```python
import torch

def _softmax(x, axis=-1):
    return torch.softmax(x, dim=axis)
def _gelu(x):
    return torch.nn.functional.gelu(x, approximate="tanh")
def _silu(x):
    return torch.nn.functional.silu(x)
def _sigmoid(x):
    return torch.sigmoid(x)
def _layernorm(x, axis=-1, eps=1e-5):
    m = x.mean(dim=axis, keepdim=True)
    v = x.var(dim=axis, keepdim=True, unbiased=False)
    return (x - m) / torch.sqrt(v + eps)
def _scatter_add(tgt, idx, src, axis=0):
    return tgt.clone().index_add_(axis, idx, src)
def _segment_sum(src, seg, n):
    out = torch.zeros((n,) + src.shape[1:], dtype=src.dtype)
    return out.index_add_(0, seg, src)
def _pad(x, pw):
    flat = tuple(v for pair in reversed(pw) for v in pair)
    return torch.nn.functional.pad(x, flat)

kernel_name = 'recurrent_scan__ssm_conv_s5__glu_gated_reglu__prompt'

def setup_inputs():

    _w = (lambda a: (lambda t: t.to(torch.bfloat16) if t.is_floating_point() else t)(torch.as_tensor(a)))
    def _A_setup():
        B, S, D = 1, 2048, 512
        P = D
        Lambda_re = -0.5 * (lambda _x: torch.exp(torch.as_tensor(_x)))((lambda *s: torch.randn(*s, dtype=torch.bfloat16) * 0.8)(P))
        Lambda_im = (lambda *s: torch.randn(*s, dtype=torch.bfloat16) * 0.8)(P) * 1.0
        B_re = (lambda *s: torch.randn(*s, dtype=torch.bfloat16) * 0.8)(P, D) * (D ** -0.5)
        B_im = (lambda *s: torch.randn(*s, dtype=torch.bfloat16) * 0.8)(P, D) * (D ** -0.5)
        C_re = (lambda *s: torch.randn(*s, dtype=torch.bfloat16) * 0.8)(D, P) * (P ** -0.5)
        C_im = (lambda *s: torch.randn(*s, dtype=torch.bfloat16) * 0.8)(D, P) * (P ** -0.5)
        log_dt = (lambda *s: torch.randn(*s, dtype=torch.bfloat16) * 0.8)(P) * 0.3 - 2.0
        Dskip = (lambda *s: torch.randn(*s, dtype=torch.bfloat16) * 0.8)(D)
        return {
            "x": _w((lambda *s: torch.randn(*s, dtype=torch.bfloat16) * 0.05)(B, S, D)),
            "Lambda_re": _w(Lambda_re),
            "Lambda_im": _w(Lambda_im),
            "B_re": _w(B_re),
            "B_im": _w(B_im),
            "C_re": _w(C_re),
            "C_im": _w(C_im),
            "log_dt": _w(log_dt),
            "Dskip": _w(Dskip),
        }
    _A_in = _A_setup()

    def _B_setup():
        B, S, D, F = 1, 2048, 512, 1376
        return {
            "x": _w((lambda *s: torch.randn(*s, dtype=torch.bfloat16) * 0.05)(B, S, D)),
            "W_gate": _w((lambda *s: torch.randn(*s, dtype=torch.bfloat16) * 0.2)(D, F) * (2.0 / D) ** 0.5),
            "W_up": _w((lambda *s: torch.randn(*s, dtype=torch.bfloat16) * 0.2)(D, F) * (2.0 / D) ** 0.5),
            "W_down": _w((lambda *s: torch.randn(*s, dtype=torch.bfloat16) * 0.2)(F, D) * (1.0 / F) ** 0.5),
        }
    _B_in = _B_setup()

    return {
        "A_x": _A_in["x"],
        "A_Lambda_re": _A_in["Lambda_re"],
        "A_Lambda_im": _A_in["Lambda_im"],
        "A_B_re": _A_in["B_re"],
        "A_B_im": _A_in["B_im"],
        "A_C_re": _A_in["C_re"],
        "A_C_im": _A_in["C_im"],
        "A_log_dt": _A_in["log_dt"],
        "A_Dskip": _A_in["Dskip"],
        "B_x": _B_in["x"],
        "B_W_gate": _B_in["W_gate"],
        "B_W_up": _B_in["W_up"],
        "B_W_down": _B_in["W_down"],
    }

def reference(A_x, A_Lambda_re, A_Lambda_im, A_B_re, A_B_im, A_C_re, A_C_im, A_log_dt, A_Dskip, B_x, B_W_gate, B_W_up, B_W_down):
    def _A_ref(x, Lambda_re, Lambda_im, B_re, B_im, C_re, C_im, log_dt, Dskip):
        Bsz, L, H = x.shape
        Lam = Lambda_re + 1j * Lambda_im
        Bmat = B_re + 1j * B_im
        Cmat = C_re + 1j * C_im
        dt = torch.exp(log_dt)
        Lam_bar = torch.exp(dt * Lam)
        B_bar = ((Lam_bar - 1.0) / Lam)[:, None] * Bmat
        Bu = torch.einsum("bld,pd->blp", x + 0j, B_bar)
        h = torch.zeros_like(Bu[:, 0, :])
        hs = []
        for t in range(L):
            h = Lam_bar[None, :] * h + Bu[:, t, :]
            hs.append(h)
        hseq = torch.stack(hs, 1)
        y = 2.0 * torch.einsum("blp,dp->bld", hseq, Cmat).real + x * Dskip[None, None, :]
        return y

    def _B_ref(x, W_gate, W_up, W_down):
        gate = torch.einsum("bsd,df->bsf", x, W_gate)
        up = torch.einsum("bsd,df->bsf", x, W_up)
        gate = gate * (lambda x, dt: x.to(dt))(gate > 0, gate.dtype)
        h = gate * up
        return torch.einsum("bsf,fd->bsd", h, W_down)

    x = A_x
    xn = _layernorm(x, axis=-1)
    s = _A_ref(xn, A_Lambda_re, A_Lambda_im, A_B_re, A_B_im, A_C_re, A_C_im, A_log_dt, A_Dskip)
    g = _silu(_B_ref(xn, B_W_gate, B_W_up, B_W_down))
    return x + s * g
```

```cpp
#include <hip/hip_runtime.h>
#include <hip/hip_bf16.h>
typedef unsigned short u16;
__device__ __forceinline__ float b2f(u16 v){ return __uint_as_float(((unsigned)v)<<16); }
__device__ __forceinline__ float rbf(float f){ return __bfloat162float(__float2bfloat16(f)); }
#define L 2048
#define D 512
#define P 512
#define F 1376
__global__ void k_ln(const u16* x, float* xn){
  int t=blockIdx.x; int l=threadIdx.x;
  float v[8]; float s=0;
  for(int j=0;j<8;j++){ v[j]=b2f(x[t*D+l*8+j]); s+=v[j]; }
  for(int o=32;o;o>>=1) s+=__shfl_xor(s,o);
  float m=s/D; float q=0;
  for(int j=0;j<8;j++){ float d=v[j]-m; q+=d*d; }
  for(int o=32;o;o>>=1) q+=__shfl_xor(q,o);
  float var=rbf(q/D); float mb=rbf(m);
  float den=rbf(sqrtf(rbf(var+1e-5f)));
  for(int j=0;j<8;j++) xn[t*D+l*8+j]=rbf(rbf(v[j]-mb)/den);
}
__global__ void k_bu(const float* xn,const u16* Bre,const u16* Bim,float* bu){
  int t=blockIdx.x;
  for(int p=threadIdx.x;p<P;p+=blockDim.x){
    float r=0,i=0;
    for(int d=0;d<D;d++){ float xv=xn[t*D+d]; r+=xv*b2f(Bre[p*D+d]); i+=xv*b2f(Bim[p*D+d]); }
    bu[(t*P+p)*2]=r; bu[(t*P+p)*2+1]=i;
  }
}
__global__ void k_scan(const u16* Lre,const u16* Lim,const u16* ldt,float* bu){
  int p=blockIdx.x*blockDim.x+threadIdx.x;
  float lr=b2f(Lre[p]), li=b2f(Lim[p]); float dt=rbf(expf(b2f(ldt[p])));
  float e=expf(dt*lr); float ar=e*cosf(dt*li), ai=e*sinf(dt*li);
  float nr=ar-1.f, ni=ai; float dd=lr*lr+li*li;
  float cr=(nr*lr+ni*li)/dd, ci=(ni*lr-nr*li)/dd;
  float hr=0,hi=0;
  for(int t=0;t<L;t++){
    float r=bu[(t*P+p)*2], i=bu[(t*P+p)*2+1];
    float ur=cr*r-ci*i, ui=cr*i+ci*r;
    float nhr=ar*hr-ai*hi+ur, nhi=ar*hi+ai*hr+ui; hr=nhr; hi=nhi;
    bu[(t*P+p)*2]=hr; bu[(t*P+p)*2+1]=hi;
  }
}
__global__ void k_glu(const float* xn,const u16* Wg,const u16* Wu,float* hh){
  int t=blockIdx.x;
  for(int f=threadIdx.x;f<F;f+=blockDim.x){
    float g=0,u=0;
    for(int d=0;d<D;d++){ float xv=xn[t*D+d]; g+=xv*b2f(Wg[d*F+f]); u+=xv*b2f(Wu[d*F+f]); }
    g=rbf(g); u=rbf(u); g= g>0?g:0.f;
    hh[t*F+f]=rbf(g*u);
  }
}
__global__ void k_out(const u16* x,const float* xn,const float* h,const float* hh,const u16* Cre,const u16* Cim,const u16* Dsk,const u16* Wd,float* out){
  int t=blockIdx.x;
  for(int d=threadIdx.x;d<D;d+=blockDim.x){
    float y=0;
    for(int p=0;p<P;p++){ y+=h[(t*P+p)*2]*b2f(Cre[d*P+p])-h[(t*P+p)*2+1]*b2f(Cim[d*P+p]); }
    y=2.f*y+rbf(xn[t*D+d]*b2f(Dsk[d]));
    float z=0;
    for(int f=0;f<F;f++) z+=hh[t*F+f]*b2f(Wd[f*D+d]);
    z=rbf(z);
    float g=rbf(z/(1.f+expf(-z)));
    out[t*D+d]=b2f(x[t*D+d])+y*g;
  }
}
extern "C" void kernel_launch(void* const* d_in, const int* in_sizes, int n_in,
                              void* d_out, int out_size, void* d_ws, size_t ws_size,
                              hipStream_t stream) {
  const u16* x=(const u16*)d_in[0];
  float* xn=(float*)d_ws; float* bu=xn+L*D; float* hh=bu+L*P*2;
  k_ln<<<L,64,0,stream>>>(x,xn);
  k_bu<<<L,256,0,stream>>>(xn,(const u16*)d_in[3],(const u16*)d_in[4],bu);
  k_scan<<<P/64,64,0,stream>>>((const u16*)d_in[1],(const u16*)d_in[2],(const u16*)d_in[7],bu);
  k_glu<<<L,256,0,stream>>>(xn,(const u16*)d_in[10],(const u16*)d_in[11],hh);
  k_out<<<L,256,0,stream>>>(x,xn,bu,hh,(const u16*)d_in[5],(const u16*)d_in[6],(const u16*)d_in[8],(const u16*)d_in[12],(float*)d_out);
}
```

```cpp
#include <hip/hip_runtime.h>
#include <hip/hip_bf16.h>
#include <type_traits>

typedef unsigned u32; typedef unsigned long long u64; typedef unsigned short u16;
typedef u32 v4u __attribute__((ext_vector_type(4)));
typedef u32 v2u __attribute__((ext_vector_type(2)));
typedef short v4s __attribute__((ext_vector_type(4)));
typedef __bf16 v2bf __attribute__((ext_vector_type(2)));
typedef __bf16 v8bf __attribute__((ext_vector_type(8)));
typedef float v2f __attribute__((ext_vector_type(2)));
typedef float v4f __attribute__((ext_vector_type(4)));
typedef float v16f __attribute__((ext_vector_type(16)));
#define LAS __attribute__((address_space(3)))
#define DEVI __device__ __forceinline__
#define AI __attribute__((always_inline))
#define SB __builtin_amdgcn_sched_barrier(0)
#if defined(__HIP_DEVICE_COMPILE__)
#define NOPK __attribute__((target("no-packed-fp32-ops")))
#else
#define NOPK
#endif

#define ROTS 0
#define ROTG 0x21
#define ROTP 1
#define LBSLEEP 6
#define WARMB 0

constexpr u32 WS_EPOCH = 0;
constexpr u32 WS_CS    = 4096;
constexpr u32 WS_FX    = 32768;
constexpr u32 WS_FH    = WS_FX + 131072;
constexpr u32 WS_FHH   = WS_FH + 131072;
constexpr u32 WS_GRAN  = WS_FHH + 131072;
constexpr u32 WS_XN    = WS_GRAN + 131072;
constexpr u32 WS_HH    = WS_XN + 2097152;
constexpr u32 WS_HS    = WS_HH + 32*86*2048;
constexpr u32 WS_END   = WS_HS + 32*131072;
constexpr u32 WS_STAMP = 16u<<20;
constexpr u32 XS = 16384;
constexpr u32 WB = 49152, WSZ = 24832;
constexpr u32 L_XT = WB+4*WSZ;
constexpr u32 L_TAB  = L_XT+8192;
constexpr u32 L_FLG  = L_TAB + 2560;
constexpr u32 L_DUM  = L_FLG + 1024;
constexpr u32 L_AGG  = L_DUM + 256;
constexpr u32 L_HP   = L_AGG + 1024;
constexpr u32 L_TAG  = L_HP + 1024;
constexpr u32 L_TOTAL = 163840;
static_assert(L_TAG+16<=L_TOTAL,"lds");
constexpr u32 OOB = 0x40000000u;
constexpr int SPIN_CAP = 1<<18;

struct Args { const u16 *x,*lre,*lim,*bre,*bim,*cre,*cim,*ldt,*dsk,*wg,*wu,*wd; float* out; char* ws; };

DEVI u32 f2u(float f){ return __builtin_bit_cast(u32,f); }
DEVI float u2f(u32 u){ return __builtin_bit_cast(float,u); }
DEVI u32 rfl(u32 v){ return __builtin_amdgcn_readfirstlane(v); }
DEVI bool all_eq(u32 v,u32 t){ return __builtin_amdgcn_ballot_w64(v!=t)==0ull; }
DEVI v4u mksrd(const void* p, u32 bytes){ u64 a=(u64)p; v4u s; s.x=rfl((u32)a); s.y=rfl((u32)(a>>32)); s.z=bytes; s.w=0x00020000u; return s; }
#define DM0 "s_add_u32 m0, %[b], %[i]\n\ts_nop 0\n\t"
#define DLD(n,off,pol) "buffer_load_dwordx4 %[v" #n "], %[r], %[s] offen" off pol " lds\n\t"
template<bool SC1,u32 IMM> DEVI void dma4(v4u srd,u32 v0,u32 v1,u32 v2,u32 v3,u32 soff,u32 base){
  if(SC1) asm volatile(DM0 DLD(0,""," sc1") DLD(1," offset:1024"," sc1") DLD(2," offset:2048"," sc1") DLD(3," offset:3072"," sc1")
      :: [v0]"v"(v0),[v1]"v"(v1),[v2]"v"(v2),[v3]"v"(v3),[r]"s"(srd),[s]"s"(soff),[b]"s"(base),[i]"n"(IMM) : "memory","scc");
  else asm volatile(DM0 DLD(0,"","") DLD(1," offset:1024","") DLD(2," offset:2048","") DLD(3," offset:3072","")
      :: [v0]"v"(v0),[v1]"v"(v1),[v2]"v"(v2),[v3]"v"(v3),[r]"s"(srd),[s]"s"(soff),[b]"s"(base),[i]"n"(IMM) : "memory","scc");
}
template<u32 IMM> DEVI void dma3(v4u srd,u32 v0,u32 v1,u32 v2,u32 soff,u32 base){
  asm volatile(DM0 DLD(0,"","") DLD(1," offset:1024","") DLD(2," offset:2048","")
      :: [v0]"v"(v0),[v1]"v"(v1),[v2]"v"(v2),[r]"s"(srd),[s]"s"(soff),[b]"s"(base),[i]"n"(IMM) : "memory","scc");
}
template<u32 IMM> DEVI void dma2(v4u srd,u32 v0,u32 v1,u32 soff,u32 base){
  asm volatile(DM0 DLD(0,"","") DLD(1," offset:1024","")
      :: [v0]"v"(v0),[v1]"v"(v1),[r]"s"(srd),[s]"s"(soff),[b]"s"(base),[i]"n"(IMM) : "memory","scc");
}
template<bool SC1> DEVI void dma_dw(v4u srd,u32 voff,u32 soff,u32 lds){
  if(SC1) asm volatile("s_mov_b32 m0, %3\n\ts_nop 0\n\tbuffer_load_dword %0, %1, %2 offen sc1 lds" :: "v"(voff), "s"(srd), "s"(soff), "s"(lds) : "memory");
  else    asm volatile("s_mov_b32 m0, %3\n\ts_nop 0\n\tbuffer_load_dword %0, %1, %2 offen lds" :: "v"(voff), "s"(srd), "s"(soff), "s"(lds) : "memory");
}
DEVI void dma_x4(v4u srd,u32 voff,u32 soff,u32 lds){
  asm volatile("s_mov_b32 m0, %3\n\ts_nop 0\n\tbuffer_load_dwordx4 %0, %1, %2 offen sc1 lds" :: "v"(voff), "s"(srd), "s"(soff), "s"(lds) : "memory");
}
template<int N> DEVI void waitv(){ asm volatile("s_waitcnt vmcnt(%0)"::"n"(N):"memory"); }
DEVI void waitl0(){ asm volatile("s_waitcnt lgkmcnt(0)":::"memory"); }
DEVI void bar(){ asm volatile("":::"memory"); __builtin_amdgcn_s_barrier(); asm volatile("":::"memory"); SB; }
template<u32 OFF> DEVI void sig(u32 vaddr,u32 one){ asm volatile("s_mov_b64 exec, 1\n\tds_add_u32 %0, %1 offset:%2\n\ts_mov_b64 exec, -1" :: "v"(vaddr), "v"(one), "n"(OFF) : "memory"); }
DEVI u32 peek(u32 vaddr){ return *(volatile LAS u32*)vaddr; }
DEVI bool vm_idle(){ u32 v; asm volatile("s_getreg_b32 %0, hwreg(HW_REG_IB_STS, 0, 4)":"=s"(v)); return v==0u; }
DEVI float b2f(u16 v){ return u2f(((u32)v)<<16); }
DEVI u32 pk2(float lo,float hi){ v2f p={lo,hi}; return __builtin_bit_cast(u32,__builtin_convertvector(p,v2bf)); }
DEVI float rbf(float f){ return u2f(pk2(f,f)<<16); }
DEVI float relu(float x){ int i=__builtin_bit_cast(int,x); return __builtin_bit_cast(float,i>0?i:0); }
DEVI float dot2(u32 a,u32 b,float c){ return __builtin_amdgcn_fdot2_f32_bf16(__builtin_bit_cast(v2bf,a),__builtin_bit_cast(v2bf,b),c,false); }
DEVI v16f mfma(v4u a,v4u b,v16f c){ return __builtin_amdgcn_mfma_f32_32x32x16_bf16(__builtin_bit_cast(v8bf,a),__builtin_bit_cast(v8bf,b),c,0,0,0); }
DEVI v4f mfma16(v4u a,v4u b,v4f c){ return __builtin_amdgcn_mfma_f32_16x16x32_bf16(__builtin_bit_cast(v8bf,a),__builtin_bit_cast(v8bf,b),c,0,0,0); }
DEVI v4u ldsr128(u32 a){ return *(LAS v4u*)a; }
DEVI v4u ldstr(u32 a0,u32 a1){
  v4s x=__builtin_amdgcn_ds_read_tr16_b64_v4i16((LAS v4s*)a0);
  v4s y=__builtin_amdgcn_ds_read_tr16_b64_v4i16((LAS v4s*)a1);
  return __builtin_bit_cast(v4u,__builtin_shufflevector(x,y,0,1,2,3,4,5,6,7));
}
DEVI void sw16(float& a,float& b){ v2u r=__builtin_amdgcn_permlane16_swap(f2u(a),f2u(b),false,false); a=u2f(r[0]); b=u2f(r[1]); }
DEVI void sw32(float& a,float& b){ v2u r=__builtin_amdgcn_permlane32_swap(f2u(a),f2u(b),false,false); a=u2f(r[0]); b=u2f(r[1]); }
template<int CTRL> DEVI float dppadd(float v){ return v+u2f(__builtin_amdgcn_update_dpp(0u,f2u(v),CTRL,0xf,0xf,true)); }
DEVI void wsum4(float& a,float& b,float& c,float& d){
  sw32(a,b); sw32(c,d); float v=a+b, t=c+d;
  sw16(v,t); v+=t;
  v=dppadd<0xB1>(v); v=dppadd<0x4E>(v); v=dppadd<0x141>(v); v=dppadd<0x140>(v);
  a=u2f(__builtin_amdgcn_readlane(f2u(v),0)); c=u2f(__builtin_amdgcn_readlane(f2u(v),16));
  b=u2f(__builtin_amdgcn_readlane(f2u(v),32)); d=u2f(__builtin_amdgcn_readlane(f2u(v),48));
}
struct cf{ float r,i; };
DEVI cf cmul(cf a,cf b){ return cf{__builtin_fmaf(-a.i,b.i,a.r*b.r), __builtin_fmaf(a.i,b.r,a.r*b.i)}; }
DEVI cf cfma(cf a,cf b,cf c){ return cf{__builtin_fmaf(-a.i,b.i,__builtin_fmaf(a.r,b.r,c.r)), __builtin_fmaf(a.i,b.r,__builtin_fmaf(a.r,b.i,c.i))}; }
DEVI cf csel(bool c,cf a,cf b){ return cf{c?a.r:b.r, c?a.i:b.i}; }
DEVI cf ldsc(u32 a){ v2f v=*(LAS v2f*)a; return cf{v[0],v[1]}; }
DEVI void sincos_(float x,float& s,float& c){
  const float n=__builtin_rintf(x*0.636619772f);
  float r=__builtin_fmaf(n,-1.57079601e+00f,x); r=__builtin_fmaf(n,-3.13916473e-07f,r); r=__builtin_fmaf(n,-5.39030253e-15f,r);
  const float z=r*r;
  float ps=__builtin_fmaf(__builtin_fmaf(__builtin_fmaf(2.86567956e-6f,z,-1.98559923e-4f),z,8.33338592e-3f),z,-1.66666672e-1f);
  ps=__builtin_fmaf(ps*z,r,r);
  float pc=__builtin_fmaf(__builtin_fmaf(__builtin_fmaf(2.44677067e-5f,z,-1.38877297e-3f),z,4.16666567e-2f),z,-0.5f);
  pc=__builtin_fmaf(pc,z,1.f);
  const int q=(int)n;
  const float a=(q&1)?pc:ps, b=(q&1)?ps:pc;
  s=(q&2)?-a:a; c=((q+1)&2)?-b:b;
}
typedef __amdgpu_buffer_rsrc_t rsrc_t;
DEVI rsrc_t mkrsrc(const void* p,u32 bytes){ return __builtin_amdgcn_make_buffer_rsrc((void*)p,0,bytes,0x00020000); }
DEVI void st128(v4u v,rsrc_t r,u32 off){ __builtin_amdgcn_raw_buffer_store_b128(v,r,off,0,16); }
DEVI u32 mktag(u32 e){ return e*0x9E3779B1u+0x7F4A7C15u; }

#define STAMP(k) do{}while(0)
#define CSTAMP(k) do{}while(0)
#define TRACE(k) do{}while(0)
#define STAMP_DECL do{}while(0)
#define BAR() bar()
#define STAMP_FLUSH() do{}while(0)
#define TRARG
#define TRPASS
#define TSARG
#define TSPASS

DEVI void poll32(v4u srd,u32 voff,u32 soff,u32 tag){
  #pragma unroll 1
  for(int it=0;it<SPIN_CAP;++it){
    u32 v;
    asm volatile("buffer_load_dword %0, %1, %2, %3 offen sc1\n\ts_waitcnt vmcnt(0)" : "=&v"(v) : "v"(voff), "s"(srd), "s"(soff) : "memory");
    if(all_eq(v,tag)) break;
    __builtin_amdgcn_s_sleep(12);
  }
}
template<int I,int N,class F> DEVI void sfor(F&& f){ if constexpr(I<N){ f(std::integral_constant<int,I>{}); sfor<I+1,N>(f); } }
AI constexpr u32 wslot(int c){ return WB+(c&3)*WSZ; }
AI constexpr u32 sslot(int c){ return c==3?0u:c==5?24576u:c==2?wslot(0):c==4?wslot(1):wslot(2+(c&1)); }

template<int NB>
DEVI void g_loop(u32 lbase,int lane,int rh,int u0,v16f (&acc)[3][2] TRARG TSARG){
  const int h=lane>>5, r=lane&31;
  u32 ax[4],aw;
  #pragma unroll
  for(int s=0;s<4;++s) ax[s]=lbase+rh*8192+r*128+((((2*s+h)^((r>>1)&7)))<<4);
  aw=lbase+WB+((lane>>4)&1)*(12288+32)+(8*h+((lane&15)>>2))*192+(lane&3)*8+u0*32;
  v4u X[2][2],T[2][3];
  auto rd=[&](int b,int s) AI {
    X[b][0]=ldsr128(ax[s]);
    #pragma unroll
    for(int t=0;t<NB;++t){ T[b][t]=ldstr(aw+s*3072+t*32,aw+s*3072+t*32+768); if(t==0) X[b][1]=ldsr128(ax[s]+4096); }
  };
  auto mm=[&](int b) AI {
    #pragma unroll
    for(int t=0;t<NB;++t){ acc[t][0]=mfma(T[b][t],X[b][0],acc[t][0]); acc[t][1]=mfma(T[b][t],X[b][1],acc[t][1]); }
  };
  #define SG(m,n) __builtin_amdgcn_sched_group_barrier(m,n,0)
  auto mix=[&](int nv) AI {
    SG(0x8,1); SG(0x100,3); SG(0x8,1); SG(0x100,3);
    if(NB==3){ SG(0x8,1); SG(0x100,2); }
    SG(0x8,1); if(nv==2) SG(0x2,2); if(nv==3) SG(0x2,3);
    SG(0x8,NB-1);
  };
  u32 d0=XS, d1=XS, d2=(u32)(-2*(int)XS);
  #pragma unroll
  for(int t=0;t<NB;++t) asm volatile("":"+v"(acc[t][0]),"+v"(acc[t][1]));
  BAR();
  BAR();
  STAMP(3); CSTAMP(6);
  rd(0,0);
  #pragma unroll 1
  for(int c=0;c<8;++c){
    const u32 dw=((c&3)==3)?(u32)(-3*(int)WSZ):WSZ;
    SB; rd(1,1); mm(0); ax[0]+=d0; ax[1]+=d0; mix(2);
    SB; asm volatile("":"+v"(ax[0]),"+v"(ax[1]));
    rd(0,2); mm(1); mix(0);
    SB; rd(1,3); mm(0); ax[2]+=d0; ax[3]+=d0; aw+=dw; mix(3);
    SB; asm volatile("":"+v"(ax[2]),"+v"(ax[3]),"+v"(aw));
    { const u32 t=d0; d0=d1; d1=d2; d2=t; }
    waitl0(); BAR();
    rd(0,0); mm(1); mix(0);
  }
}

__global__ NOPK __launch_bounds__(768,3) void fused_k(Args a){
  __shared__ __attribute__((aligned(256))) char smem[L_TOTAL];
  const u32 lbase=(u32)(size_t)(LAS char*)smem;
  const int tid=__builtin_amdgcn_workitem_id_x();
  const int wgid=__builtin_amdgcn_workgroup_id_x(), w=rfl(tid>>6), lane=tid&63;
  const int xcd=wgid&7, idx=wgid>>3;
  const int tt=(xcd&1)*8+(idx&7), cg=(xcd>>1)*4+(idx>>3);
  const int nb=((cg&3)==3||cg==14)?2:3, blk0=11*(cg>>2)+3*(cg&3)-(cg==15?1:0);
  const int rt2=(xcd&3)*8+(idx&7), ct=(xcd>>2)*4+(idx>>3);
  const int ry=ROTP?2*(idx>>3):0, rz=ROTP?(11*(idx>>3))>>2:0;
  const u32 one=1;
  STAMP_DECL; STAMP(0); CSTAMP(12);

  if(w>=8){
    __builtin_amdgcn_s_setprio(3);
    const int lw=w-8, j=lane>>3, l7=lane&7;
    const v4u sx=mksrd(a.x,2048*1024);
    const v4u sb=mksrd(lw<2?a.bre:a.bim,512*1024);
    const u32 sz0=(u32)((l7^(j>>1))<<4), sz1=sz0^64u;
    const u32 xr=(tt*128+32*lw+16*(j>>2)+(j&3))*1024;
    const u32 vx0=xr+sz0, vx1=xr+3*1024+sz1, vx2=xr+6*1024+sz0, vx3=xr+9*1024+sz1;
    const u32 br=(cg*32+16*(lw&1)+j)*1024;
    const u32 vb0=br+sz0, vb1=br+7*1024+sz1;
    const u32 l4=lbase+lw*4096, l2=lbase+lw*2048;
    u32 kx=((tt*(ROTS&15)+cg*(ROTS>>4))&7)*128;
    #define SX(c) dma4<false,sslot(c)>(sx,vx0,vx1,vx2,vx3,kx,l4)
    #define SBB(c) do{ dma2<sslot(c)+16384>(sb,vb0,vb1,kx,l2); kx=(kx+128)&0x380; }while(0)
    SX(0); SBB(0); SX(1); SBB(1);
    const v4u sws=mksrd(a.ws,WS_END);
    const v4u sw=mksrd(lw<2?a.wg:a.wu,512*2752);
    const v4u sxn=mksrd(a.ws+WS_XN,2048*1024);
    const v4u swd=mksrd(a.wd,1376*1024);
    const v4u sc=mksrd(lw<2?a.cre:a.cim,512*1024);
    const u32 vf=(lane&15)*128, lf=lbase+L_FLG+lw*256;
    const u32 fx=WS_FX+(tt*64+lw*16)*128, fh=WS_FH+(rt2*32+(lw&1)*16)*128, fhh=WS_FHH+(rt2*32+lw*8)*128;
    u32 vw[6];
    #pragma unroll
    for(int i=0;i<3;++i){ const u32 q=i*1024+lane*16, row=q/192, col=q%192; const bool ok=col<(u32)nb*64;
      vw[i]=ok?(row+32*(lw&1))*2752+col+blk0*64-i*1024:OOB; vw[i+3]=vw[i]+16*2752; asm volatile("":"+v"(vw[i]),"+v"(vw[i+3])); }
    const u32 lwv=lbase+(lw>>1)*(12288+32)+(lw&1)*6144;
    u32 kg=((tt*(ROTG&15)+cg*(ROTG>>4))&7)*128, kw=kg;
    #define GWA(c) dma3<wslot(c)>(sw,vw[0],vw[1],vw[2],kw*1376,lwv)
    #define GWB(c) do{ dma3<wslot(c)+3072>(sw,vw[3],vw[4],vw[5],kw*1376,lwv); kw=(kw+128)&0x380; }while(0)
    #define GX(c) do{ dma4<true,((c)%3)*XS>(sxn,vx0,vx1,vx2,vx3,kg,l4); kg=(kg+128)&0x380; }while(0)
    const u32 vs=(lane>>1)*128+(lane&1)*16, lt=lbase+L_TAB+lw*640;
    const u32 vs2=lane<32?fx+vs:WS_CS+(cg*32+lw*8+lane-32)*16;
    SX(2); SBB(2); SX(3);
    waitv<10>(); BAR();
    SBB(3); SX(4); waitv<10>(); BAR();
    SBB(4); SX(5); waitv<10>(); BAR();
    SBB(5); SX(6); waitv<10>(); BAR();
    SBB(6); SX(7); waitv<10>(); BAR();
    STAMP(2);
    const u32 tag=rfl(peek(lbase+L_TAG));
    SBB(7);
    if(lane<40) dma_x4(sws,vs2,0,lt);
    GWA(0); waitv<10>(); BAR();
    GWB(0);
    waitv<6>();
    #pragma unroll 1
    for(int it=0;it<SPIN_CAP;++it){
      const v4u t=*(volatile LAS v4u*)(lt+(lane<40?lane:0)*16);
      if(all_eq((t[1]^tag)|(t[3]^tag),0u)) break;
      __builtin_amdgcn_s_sleep(4);
      if(lane<40) dma_x4(sws,vs2,0,lt);
      waitv<0>();
    }
    STAMP(3);
    BAR();
    GX(0); GWA(1); GWB(1); GX(1);
    waitv<10>();
    BAR();
    GWA(2); GWB(2); GX(2); GWA(3); GWB(3);
    u32 va=WS_HS+rt2*131072+lw*4096+lane*16;
    const u32 cr=(ct*64+(lw&1)*32+j)*1024;
    u32 vp0=cr+sz0, vp1=cr+7*1024+sz1, vp2=cr+14*1024+sz0, vp3=cr+21*1024+sz1;
    waitv<16>(); BAR();
    sfor<0,4>([&](auto C) AI {
      constexpr int c=C.value;
      GX(c+3); GWA(c+4); GWB(c+4);
      waitv<16>();
      if(WARMB){
        if constexpr(c==0) dma_dw<false>(sc,lane<32?(u32)(j*1024+l7*128):OOB,(ct*64+(idx&7)*8+(lw&1)*4)*1024,lbase+L_DUM);
        if constexpr(c==2) dma_dw<false>(swd,lane<43?(u32)(lane*1024):OOB,((idx&7)*172+lw*43)*1024+ct*128,lbase+L_DUM);
      }
      BAR();
    });
    u32 ka=ry, kb=ry;
    auto PA=[&](auto C) AI {
      constexpr int c=C.value;
      if constexpr(c==8){ va=WS_HH+rt2*86*2048+lw*4096+lane*16; ka=rz; }
      u32 so;
      if constexpr(c<8){ so=ka<<14; ka=(ka+1)&7; } else { so=(ka==10&&lw==3)?OOB:ka<<14; ka= ka==10?0:ka+1; }
      dma4<true,((c+2)%3)*XS>(sws,va,va,va,va,so,l4);
    };
    auto PB=[&](auto C) AI {
      constexpr int c=C.value;
      if constexpr(c==8){ vp0=(lw*32+j)*1024+ct*128+((l7^(((j>>1)&1)<<2))<<4); vp1=vp0+7*1024; vp2=vp0+14*1024; vp3=vp0+21*1024; kb=rz; }
      u32 so;
      if constexpr(c<8){ so=kb<<7; kb=(kb+1)&7; } else { so=kb<<17; kb= kb==10?0:kb+1; }
      dma4<false,wslot(c)>(c<8?sc:swd,vp0,vp1,vp2,vp3,so,l4);
    };
    #define IC(n) std::integral_constant<int,(n)>{}
    GX(7); PB(IC(0));
    waitv<14>();
    dma_dw<true>(sws,vf,fh,lf);
    BAR();
    PB(IC(1));
    waitv<4>();
    const bool early=all_eq(peek(lf+lane*4),tag);
    if(early) PA(IC(0));
    BAR();
    STAMP(4);
    PB(IC(2));
    if(early) PA(IC(1));
    { const u32 vt=(rt2*64+lw*16+j)*1024+ct*128+l7*16; dma2<L_XT>(sx,vt,vt+7*1024,0,l2); }
    BAR();
    PB(IC(3));
    STAMP(5);
    if(!early){ poll32(sws,vf,fh,tag); PA(IC(0)); PA(IC(1)); }
    STAMP(6);
    PA(IC(2));
    waitv<8>(); BAR();
    if(lane<16) dma_x4(sws,vs,WS_FX+(rt2*32+lw*8)*128,lbase+L_TAB+lw*256);
    waitv<5>(); BAR();
    sfor<1,18>([&](auto K) AI {
      constexpr int k=K.value;
      if constexpr(k==4) dma_dw<true>(sws,vf&1023u,fhh,lf);
      if constexpr(k==9){ if(wgid==0 && lw==0) dma_dw<true>(sws,vf*64,WS_FH,lf); }
      if constexpr(k<=16) PA(IC(k+2));
      if constexpr(k<=15) PB(IC(k+3));
      waitv<(k==1?9:k<=15?12:k==16?8:0)>();
      if constexpr(k==5){
        STAMP(7);
        if(!all_eq(peek(lf+lane*4),tag)) poll32(sws,vf&1023u,fhh,tag);
        STAMP(8);
      }
      if constexpr(k==12){
        if(wgid==0 && lw==0){
          if(!all_eq(peek(lf+lane*4),tag)) poll32(sws,vf*64,WS_FH,tag);
          const u32 e=peek(lbase+L_TAG+4)+1u;
          asm volatile("s_mov_b64 exec, 1\n\tbuffer_store_dword %0, off, %1, 0 sc1\n\ts_mov_b64 exec, -1" :: "v"(e), "s"(sws) : "memory");
        }
      }
      BAR();
    });
    STAMP(11);
    STAMP_FLUSH();
    return;
  }

  const int h=lane>>5, r=lane&31, g=lane>>4;
  const rsrc_t rws=mkrsrc(a.ws,WS_END);
  u32 pend=0;
  u32 tag;
  auto try_flag=[&](bool force) AI {
    if(pend && (force || vm_idle())){
      waitv<0>();
      if(lane==0) __builtin_amdgcn_raw_buffer_store_b32(tag,rws,pend,0,16);
      pend=0;
    }
  };
  if(w>=4){
    const int gw=w-4, rh=w&1, rt=2*tt+rh;
    {
      const int row=tt*128+cg*8+gw*2;
      const v4u x0=((const v4u*)a.x)[row*64+lane], x1=((const v4u*)a.x)[row*64+64+lane];
      const u32 e=__builtin_amdgcn_raw_buffer_load_b32(rws,0,WS_EPOCH,16);
      __builtin_amdgcn_s_setprio(3);
      float f0[8],f1[8];
      #pragma unroll
      for(int k=0;k<4;++k){ f0[2*k]=u2f(x0[k]<<16); f0[2*k+1]=u2f(x0[k]&0xffff0000u); f1[2*k]=u2f(x1[k]<<16); f1[2*k+1]=u2f(x1[k]&0xffff0000u); }
      const float z0=u2f(rfl(x0[0])<<16), z1=u2f(rfl(x1[0])<<16);
      float s0=0.f,s1=0.f,v0=0.f,v1=0.f;
      #pragma unroll
      for(int k=0;k<8;++k){ const float d0=f0[k]-z0, d1=f1[k]-z1; s0+=d0; s1+=d1; v0=__builtin_fmaf(d0,d0,v0); v1=__builtin_fmaf(d1,d1,v1); }
      wsum4(s0,s1,v0,v1);
      s0*=(1.f/512.f); s1*=(1.f/512.f);
      v0=relu(__builtin_fmaf(v0,1.f/512.f,-s0*s0)); v1=relu(__builtin_fmaf(v1,1.f/512.f,-s1*s1));
      const u32 vb=pk2(v0,v1), mb=pk2(s0+z0,s1+z1);
      const u32 ve=pk2(u2f(vb<<16)+1e-5f,u2f(vb&0xffff0000u)+1e-5f);
      const u32 sd=pk2(__builtin_amdgcn_sqrtf(u2f(ve<<16)),__builtin_amdgcn_sqrtf(u2f(ve&0xffff0000u)));
      const float r0=__builtin_amdgcn_rcpf(u2f(sd<<16)), r1=__builtin_amdgcn_rcpf(u2f(sd&0xffff0000u));
      const float mb0=u2f(mb<<16), mb1=u2f(mb&0xffff0000u);
      v4u o0,o1;
      #pragma unroll
      for(int k=0;k<4;++k){
        o0[k]=pk2((f0[2*k]-mb0)*r0,(f0[2*k+1]-mb0)*r0);
        o1[k]=pk2((f1[2*k]-mb1)*r1,(f1[2*k+1]-mb1)*r1);
      }
      st128(o0,rws,WS_XN+row*1024+lane*16); st128(o1,rws,WS_XN+row*1024+1024+lane*16);
      tag=mktag(rfl(e));
      if(gw==0) *(LAS v2u*)(lbase+L_TAG)=v2u{tag,e};
      const v4u sv={f2u(lane?r1:r0),tag,f2u(lane?r1*mb1:r0*mb0),tag};
      STAMP(1);
      waitl0();
      bool todo=true;
      #pragma unroll 1
      for(int k=0;k<6;++k){
        BAR();
        if(todo && (k==3 || vm_idle())){ waitv<0>(); if(lane<2) st128(sv,rws,WS_FX+row*64+lane*16); todo=false; STAMP(2); }
      }
    }
    __builtin_amdgcn_s_setprio(2);
    v16f acc[3][2]={};
    const int u0=(gw>>1)*nb;
    if(nb==3) g_loop<3>(lbase,lane,rh,u0,acc TRPASS TSPASS); else g_loop<2>(lbase,lane,rh,u0,acc TRPASS TSPASS);
    STAMP(4); CSTAMP(7);
    #pragma unroll
    for(int t=0;t<3;++t) if(t<nb){
      #pragma unroll
      for(int m=0;m<2;++m){
        v4u v;
        #pragma unroll
        for(int k=0;k<4;++k) v[k]=pk2(relu(acc[t][m][2*k])*acc[t][m][2*k+8],relu(acc[t][m][2*k+1])*acc[t][m][2*k+9]);
        st128(v,rws,lane*16+WS_HH+((rt*86+2*blk0+u0+t)*2+m)*1024);
      }
    }
    pend=WS_FHH+(rt*32+cg*2+(gw>>1))*128;
    STAMP(5);
  } else {
    const int ch=cg*32+r;
    __builtin_amdgcn_s_setprio(3);
    const u16 hlr=a.lre[ch], hli=a.lim[ch], hdt=a.ldt[ch];
    if(w==1) *(LAS u32*)(lbase+L_TAG+8)=0u;
    const int bch=cg*32+tt*2+(w&1);
    const v4u brow=((const v4u*)(w<2?a.bre:a.bim))[bch*64+lane];
    const float lr=b2f(hlr), li=b2f(hli), dt=rbf(__builtin_expf(b2f(hdt)));
    cf lam,coef;
    { const float ex=__builtin_expf(dt*lr); float sn,cs_; sincos_(dt*li,sn,cs_); lam=cf{ex*cs_,ex*sn};
      const float dd=1.f/(lr*lr+li*li); coef=cmul(cf{lam.r-1.f,lam.i},cf{lr*dd,-li*dd}); }
    cf l16=cmul(lam,lam); l16=cmul(l16,l16); l16=cmul(l16,l16); l16=cmul(l16,l16);
    cf l32=cmul(l16,l16);
    cf l1024,wq,pw;
    { const cf l64=cmul(l32,l32), l128=cmul(l64,l64), l256=cmul(l128,l128), l512=cmul(l256,l256), o={1.f,0.f};
      l1024=cmul(l512,l512);
      wq=cmul(csel(h,l128,o),cmul(csel(w&1,l256,o),csel(w&2,l512,o)));
      pw=cmul(csel(w&1,l32,o),csel(w&2,l64,o)); }
    asm volatile("":"+v"(lam.r),"+v"(lam.i),"+v"(coef.r),"+v"(coef.i),"+v"(l16.r),"+v"(l16.i),"+v"(l32.r),"+v"(l32.i));
    asm volatile("":"+v"(l1024.r),"+v"(l1024.i),"+v"(wq.r),"+v"(wq.i),"+v"(pw.r),"+v"(pw.i));
    float bsum=0.f;
    #pragma unroll
    for(int k=0;k<4;++k) bsum=dot2(brow[k],0x3f803f80u,bsum);
    { float t1=0.f,t2=0.f,t3=0.f; wsum4(bsum,t1,t2,t3); }
    v16f are={},aim={};
    {
      u32 ax[4],ab[4];
      #pragma unroll
      for(int s=0;s<4;++s){ ab[s]=lbase+sslot(0)+r*128+((((2*s+h)^((r>>1)&7)))<<4); ax[s]=ab[s]+w*4096; }
      v4u X[2][2],T[2][2][2];
      auto rd=[&](int b) AI {
        #pragma unroll
        for(int s=0;s<2;++s){ X[b][s]=ldsr128(ax[2*b+s]); T[b][s][0]=ldsr128(ab[2*b+s]+16384); T[b][s][1]=ldsr128(ab[2*b+s]+16384+4096); } };
      auto mm=[&](int b) AI {
        #pragma unroll
        for(int s=0;s<2;++s){ are=mfma(X[b][s],T[b][s][0],are); aim=mfma(X[b][s],T[b][s][1],aim); }
        asm volatile("":"+v"(are),"+v"(aim)); };
      STAMP(1);
      waitl0(); BAR();
      tag=rfl(peek(lbase+L_TAG));
      if(lane==0) __builtin_amdgcn_raw_buffer_store_b64(v2u{f2u(bsum),tag},rws,WS_CS+bch*16+(w>>1)*8,0,16);
      rd(0);
      sfor<0,8>([&](auto C) AI {
        constexpr int c=C.value;
        SB; rd(1);
        SB; mm(0);
        if constexpr(c<7){
          #pragma unroll
          for(int s=0;s<4;++s){ ax[s]+=sslot(c+1)-sslot(c); ab[s]+=sslot(c+1)-sslot(c); asm volatile("":"+v"(ax[s]),"+v"(ab[s])); }
          SB;
          if constexpr(c>=1){ waitl0(); BAR(); }
          rd(0);
        }
        SB; mm(1);
      });
    }
    STAMP(2);
    int left=9;
    #define SBAR() do{ if(left>0){ BAR(); --left; } else __builtin_amdgcn_s_sleep(8); }while(0)
    asm volatile("s_nop 7\n\ts_nop 7\n\ts_nop 3":"+v"(are),"+v"(aim));
    cf cs; { const v4f t=*(LAS v4f*)(lbase+L_TAB+(r>>3)*640+512+(r&7)*16); cs=cf{t[0],t[2]}; }
    cf uu[16]; cf ea={0.f,0.f};
    #pragma unroll
    for(int k=0;k<16;++k){
      const v4f st=*(LAS v4f*)(lbase+L_TAB+w*640+(16*h+k)*16);
      const cf v={__builtin_fmaf(are[k],st[0],-st[2]*cs.r),__builtin_fmaf(aim[k],st[0],-st[2]*cs.i)};
      uu[k]=v; ea=cfma(lam,ea,v);
    }
    cf elo=ea,ehi=ea; sw32(elo.r,ehi.r); sw32(elo.i,ehi.i);
    { const cf ew=cfma(l16,elo,ehi); *(LAS v2f*)(lbase+L_AGG+(w*32+r)*8)=v2f{ew.r,ew.i}; }
    STAMP(3);
    waitl0(); SBAR();
    cf Q;
    { const u32 b=lbase+L_AGG+r*8;
      const cf a0=ldsc(b), a1=ldsc(b+256), a2=ldsc(b+512), a3=ldsc(b+768);
      const cf Q2=cfma(l32,a0,a1), Q3=cfma(l32,Q2,a2);
      if(w==3){ const cf E=cfma(l32,Q3,a3); if(lane<32) st128(v4u{f2u(E.r),tag,f2u(E.i),tag},rws,ch*16+WS_GRAN+tt*8192); }
      Q= w==0?cf{0.f,0.f}: w==1?a0: w==2?Q2:Q3; }
    STAMP(4);
    #pragma unroll
    for(int k=0;k<16;++k){ uu[k]=cmul(coef,uu[k]); asm volatile("":"+v"(uu[k].r),"+v"(uu[k].i)); }
    SBAR();
    cf H={0.f,0.f};
    if(tt>0){
      const int p0=tt-1-2*w-h, p1=p0-8;
      const u32 o0=p0>=0?(u32)(p0*8192+ch*16):OOB, o1=p1>=0?(u32)(p1*8192+ch*16):OOB;
      const u32 t0=p0>=0?tag:0u, t1=p1>=0?tag:0u;
      v4u g0,g1;
      __builtin_amdgcn_s_sleep(LBSLEEP);
      #pragma unroll 1
      for(int it=0;it<SPIN_CAP;++it){
        asm volatile("":::"memory");
        g0=__builtin_amdgcn_raw_buffer_load_b128(rws,o0,WS_GRAN,16);
        g1=__builtin_amdgcn_raw_buffer_load_b128(rws,o1,WS_GRAN,16);
        SBAR();
        if(all_eq((g0[1]^t0)|(g0[3]^t0)|(g1[1]^t1)|(g1[3]^t1),0u)) break;
      }
      cf hp=cmul(wq,cfma(l1024,cf{u2f(g1[0]),u2f(g1[2])},cf{u2f(g0[0]),u2f(g0[2])}));
      cf h2=hp; sw32(hp.r,h2.r); sw32(hp.i,h2.i);
      *(LAS v2f*)(lbase+L_HP+(w*32+r)*8)=v2f{hp.r+h2.r,hp.i+h2.i}; sig<8>(lbase+L_TAG,one);
      waitl0();
      #pragma unroll 1
      for(int it=0;it<SPIN_CAP*8;++it){ SBAR(); if(rfl(peek(lbase+L_TAG+8))>=4u) break; }
      asm volatile("":::"memory");
      const u32 b=lbase+L_HP+r*8;
      const cf a0=ldsc(b), a1=ldsc(b+256), a2=ldsc(b+512), a3=ldsc(b+768);
      H=cf{(a0.r+a1.r)+(a2.r+a3.r),(a0.i+a1.i)+(a2.i+a3.i)};
    }
    STAMP(5);
    cf hc=cfma(pw,H,Q);
    hc=cmul(coef,csel(h,cfma(l16,hc,elo),hc));
    const int rt=2*tt+(w>>1);
    const u32 hso=WS_HS+rt*131072+(cg>>1)*16384+(((cg&1)*2+(r>>4))*2+(w&1))*1024+h*512;
    #pragma unroll
    for(int m=0;m<2;++m){
      cf hv[8];
      #pragma unroll
      for(int jj=0;jj<8;++jj){ hc=cfma(lam,hc,uu[8*m+jj]); hv[jj]=hc; }
      const u32 o=hso+m*256+(((lane&15)^(8*h+4*m))<<4);
      st128(v4u{pk2(hv[0].r,hv[1].r),pk2(hv[2].r,hv[3].r),pk2(hv[4].r,hv[5].r),pk2(hv[6].r,hv[7].r)},rws,o);
      st128(v4u{pk2(-hv[0].i,-hv[1].i),pk2(-hv[2].i,-hv[3].i),pk2(-hv[4].i,-hv[5].i),pk2(-hv[6].i,-hv[7].i)},rws,o+8192);
      if(m==0 && left>0){ BAR(); --left; }
    }
    pend=WS_FH+(rt*32+(cg&1)*16+(cg>>1)*2+(w&1))*128;
    STAMP(6);
    #pragma unroll 1
    while(left>0){
      BAR(); --left;
      #pragma unroll 1
      for(int it=0;it<6 && pend;++it){ try_flag(false); if(pend) __builtin_amdgcn_s_sleep(1); else STAMP(7); }
    }
    if(pend){ try_flag(true); STAMP(7); }
  }
  const int m2=w&1, tc=(w>>1)&1, kh=w>>2;
  const int dcol=ct*64+tc*32+r;
  const float dsk=b2f(a.dsk[dcol]);
  v16f ay={},az={};
  const int q=(lane&15)>>2, p=lane&3, b4=g&1, gp=b4+2*(p&1);
  u32 a0=lbase+2*XS+(kh*8+m2)*1024+(gp*16+((8*h+q)^(4*gp)))*16+8*(p>>1);
  u32 b0=lbase+WB+kh*8192+(tc*32+r)*128+((h^((r>>1)&7))<<4);
  u32 za=lbase+XS+(kh*8+m2)*1024+lane*16;
  u32 zb=lbase+WB+(64*kh+4*h+q)*128+((tc*64+b4*32+8*p)^(((q>>1)&1)<<6));
  asm volatile("":"+v"(za),"+v"(zb));
  u32 d0=(u32)(-2*(int)XS), d1=XS, d2=XS, e0=WSZ, e1=WSZ, e2=WSZ, e3=(u32)(-3*(int)WSZ);
  v4u A[2][4],B[2][4];
  auto rot=[&]() AI { { const u32 t=d0; d0=d1; d1=d2; d2=t; } { const u32 t=e0; e0=e1; e1=e2; e2=e3; e3=t; } };
  auto rdy=[&](int b) AI {
    #pragma unroll
    for(int s=0;s<4;++s){ A[b][s]=ldstr(a0+s*2048,(a0^64)+s*2048); B[b][s]=ldsr128(b0^(s*32)); }
    a0+=d0; b0+=e0; rot();
  };
  auto rdz=[&](int b) AI {
    #pragma unroll
    for(int s=0;s<4;++s){ A[b][s]=ldsr128(za+s*2048); B[b][s]=ldstr(zb+s*2048,zb+s*2048+1024); }
    za+=d0; zb+=e0; rot();
  };
  auto mm4=[&](int b,v16f& c) AI {
    #pragma unroll
    for(int s=0;s<4;++s) c=mfma(A[b][s],B[b][s],c);
    #pragma unroll
    for(int s=0;s<4;++s){ SG(0x8,1); SG(0x100,3); }
    SB; asm volatile("":"+v"(c));
  };
  #define NXT() do{ waitl0(); BAR(); }while(0)
  const int tk=32*m2+16*h+8*kh;
  STAMP(8);
  __builtin_amdgcn_s_setprio(0);
  NXT(); rdy(0);
  STAMP(9);
  #pragma unroll 1
  for(int c=0;c<3;++c){
    NXT(); rdy(1); mm4(0,ay); try_flag(false);
    NXT(); rdy(0); mm4(1,ay); try_flag(false);
  }
  NXT(); rdy(1); mm4(0,ay); try_flag(true);
  NXT(); rdz(0); mm4(1,ay);
  STAMP(10);
  float sv[8], xf[8];
  #pragma unroll 1
  for(int c=0;c<5;++c){
    NXT(); rdz(1); mm4(0,az);
    if(c==1){
      #pragma unroll
      for(int k=0;k<8;++k){
        const v4f st=*(LAS v4f*)(lbase+L_TAB+(tk+k)*16);
        xf[k]=b2f(*(LAS u16*)(lbase+L_XT+(tk+k)*128+(tc*32+r)*2));
        sv[k]=__builtin_fmaf(xf[k],st[0],-st[2])*dsk;
      }
    }
    NXT(); rdz(0); mm4(1,az);
  }
  {
    const u32 xo=lbase+w*4096+lane*16, xi=lbase+(w^4)*4096+lane*16;
    float zv[8];
    if(kh){ *(LAS v4f*)xo=v4f{ay[0],ay[1],ay[2],ay[3]}; *(LAS v4f*)(xo+1024)=v4f{ay[4],ay[5],ay[6],ay[7]};
      #pragma unroll
      for(int k=0;k<8;++k) sv[k]=__builtin_fmaf(2.f,ay[8+k],sv[k]); }
    else  { *(LAS v4f*)xo=v4f{ay[8],ay[9],ay[10],ay[11]}; *(LAS v4f*)(xo+1024)=v4f{ay[12],ay[13],ay[14],ay[15]};
      #pragma unroll
      for(int k=0;k<8;++k) sv[k]=__builtin_fmaf(2.f,ay[k],sv[k]); }
    SB;
    #pragma unroll
    for(int s=0;s<4;++s) az=mfma(A[0][s],B[0][s],az);
    STAMP(11);
    if(kh){ *(LAS v4f*)(xo+2048)=v4f{az[0],az[1],az[2],az[3]}; *(LAS v4f*)(xo+3072)=v4f{az[4],az[5],az[6],az[7]};
      #pragma unroll
      for(int k=0;k<8;++k) zv[k]=az[8+k]; }
    else  { *(LAS v4f*)(xo+2048)=v4f{az[8],az[9],az[10],az[11]}; *(LAS v4f*)(xo+3072)=v4f{az[12],az[13],az[14],az[15]};
      #pragma unroll
      for(int k=0;k<8;++k) zv[k]=az[k]; }
    waitl0(); bar();
    const v4f i2=*(LAS v4f*)(xi+2048), i3=*(LAS v4f*)(xi+3072), i0=*(LAS v4f*)xi, i1=*(LAS v4f*)(xi+1024);
    float* op=a.out+(rt2*64+tk)*512+dcol;
    #pragma unroll
    for(int k=0;k<8;++k){
      const float z=zv[k]+(k<4?i2[k&3]:i3[k&3]);
      const float gt=z*__builtin_amdgcn_rcpf(1.f+__builtin_amdgcn_exp2f(z*-1.44269504f));
      op[k*512]=__builtin_fmaf(__builtin_fmaf(2.f,k<4?i0[k&3]:i1[k&3],sv[k]),gt,xf[k]);
    }
  }
  CSTAMP(14); STAMP(13); STAMP_FLUSH();
}

extern "C" void kernel_launch(void* const* d_in, const int* in_sizes, int n_in,
                              void* d_out, int out_size, void* d_ws, size_t ws_size,
                              hipStream_t stream) {
  Args a;
  a.x=(const u16*)d_in[0]; a.lre=(const u16*)d_in[1]; a.lim=(const u16*)d_in[2];
  a.bre=(const u16*)d_in[3]; a.bim=(const u16*)d_in[4]; a.cre=(const u16*)d_in[5]; a.cim=(const u16*)d_in[6];
  a.ldt=(const u16*)d_in[7]; a.dsk=(const u16*)d_in[8];
  a.wg=(const u16*)d_in[10]; a.wu=(const u16*)d_in[11]; a.wd=(const u16*)d_in[12];
  a.out=(float*)d_out; a.ws=(char*)d_ws;
  fused_k<<<256,768,0,stream>>>(a);
}
```

```cpp
#include <hip/hip_runtime.h>
#include <hip/hip_bf16.h>
#include <type_traits>
#include <cstdio>
#include <vector>
#include <algorithm>
#define STAMPS
#define TRACES

typedef unsigned u32; typedef unsigned long long u64; typedef unsigned short u16;
typedef u32 v4u __attribute__((ext_vector_type(4)));
typedef u32 v2u __attribute__((ext_vector_type(2)));
typedef short v4s __attribute__((ext_vector_type(4)));
typedef __bf16 v2bf __attribute__((ext_vector_type(2)));
typedef __bf16 v8bf __attribute__((ext_vector_type(8)));
typedef float v2f __attribute__((ext_vector_type(2)));
typedef float v4f __attribute__((ext_vector_type(4)));
typedef float v16f __attribute__((ext_vector_type(16)));
#define LAS __attribute__((address_space(3)))
#define DEVI __device__ __forceinline__
#define AI __attribute__((always_inline))
#define SB __builtin_amdgcn_sched_barrier(0)
#if defined(__HIP_DEVICE_COMPILE__)
#define NOPK __attribute__((target("no-packed-fp32-ops")))
#else
#define NOPK
#endif

#define ROT1 0x01
#define ROTP 1
#define LBSLEEP 6
#define WARMB 0

constexpr u32 WS_EPOCH = 0;
constexpr u32 WS_STAT  = 4096;
constexpr u32 WS_FH    = 32768;
constexpr u32 WS_FHH   = WS_FH + 131072;
constexpr u32 WS_GRAN  = WS_FHH + 131072;
constexpr u32 WS_HH    = WS_GRAN + 131072;
constexpr u32 WS_HS    = WS_HH + 32*86*2048;
constexpr u32 WS_END   = WS_HS + 32*131072;
constexpr u32 WS_STAMP = 16u<<20;
constexpr u32 XS = 16384;
constexpr u32 WB = 49152, WSZ = 24832;
constexpr u32 L_XT = WB+4*WSZ;
constexpr u32 L_TAB  = L_XT+8192;
constexpr u32 L_MB   = L_TAB + 1024;
constexpr u32 L_CSP  = L_MB + 512;
constexpr u32 L_FLG  = L_CSP;
constexpr u32 L_AGG  = L_CSP + 1024;
constexpr u32 L_HP   = L_AGG + 1024;
constexpr u32 L_DUM  = L_AGG;
constexpr u32 L_TAG  = L_HP + 2048;
constexpr u32 L_CW   = L_XT;
constexpr u32 L_TOTAL = 163840;
static_assert(L_TAG+16<=L_TOTAL,"lds");
constexpr u32 OOB = 0x40000000u;
constexpr int SPIN_CAP = 1<<18;

struct Args { const u16 *x,*lre,*lim,*bre,*bim,*cre,*cim,*ldt,*dsk,*wg,*wu,*wd; float* out; char* ws; };

DEVI u32 f2u(float f){ return __builtin_bit_cast(u32,f); }
DEVI float u2f(u32 u){ return __builtin_bit_cast(float,u); }
DEVI u32 rfl(u32 v){ return __builtin_amdgcn_readfirstlane(v); }
DEVI bool all_eq(u32 v,u32 t){ return __builtin_amdgcn_ballot_w64(v!=t)==0ull; }
DEVI v4u mksrd(const void* p, u32 bytes){ u64 a=(u64)p; v4u s; s.x=rfl((u32)a); s.y=rfl((u32)(a>>32)); s.z=bytes; s.w=0x00020000u; return s; }
#define DM0 "s_add_u32 m0, %[b], %[i]\n\ts_nop 0\n\t"
#define DLD(n,off,pol) "buffer_load_dwordx4 %[v" #n "], %[r], %[s] offen" off pol " lds\n\t"
template<bool SC1,u32 IMM> DEVI void dma4(v4u srd,u32 v0,u32 v1,u32 v2,u32 v3,u32 soff,u32 base){
  if(SC1) asm volatile(DM0 DLD(0,""," sc1") DLD(1," offset:1024"," sc1") DLD(2," offset:2048"," sc1") DLD(3," offset:3072"," sc1")
      :: [v0]"v"(v0),[v1]"v"(v1),[v2]"v"(v2),[v3]"v"(v3),[r]"s"(srd),[s]"s"(soff),[b]"s"(base),[i]"n"(IMM) : "memory","scc");
  else asm volatile(DM0 DLD(0,"","") DLD(1," offset:1024","") DLD(2," offset:2048","") DLD(3," offset:3072","")
      :: [v0]"v"(v0),[v1]"v"(v1),[v2]"v"(v2),[v3]"v"(v3),[r]"s"(srd),[s]"s"(soff),[b]"s"(base),[i]"n"(IMM) : "memory","scc");
}
template<u32 IMM> DEVI void dma3(v4u srd,u32 v0,u32 v1,u32 v2,u32 soff,u32 base){
  asm volatile(DM0 DLD(0,"","") DLD(1," offset:1024","") DLD(2," offset:2048","")
      :: [v0]"v"(v0),[v1]"v"(v1),[v2]"v"(v2),[r]"s"(srd),[s]"s"(soff),[b]"s"(base),[i]"n"(IMM) : "memory","scc");
}
template<u32 IMM> DEVI void dma2(v4u srd,u32 v0,u32 v1,u32 soff,u32 base){
  asm volatile(DM0 DLD(0,"","") DLD(1," offset:1024","")
      :: [v0]"v"(v0),[v1]"v"(v1),[r]"s"(srd),[s]"s"(soff),[b]"s"(base),[i]"n"(IMM) : "memory","scc");
}
template<bool SC1> DEVI void dma_dw(v4u srd,u32 voff,u32 soff,u32 lds){
  if(SC1) asm volatile("s_mov_b32 m0, %3\n\ts_nop 0\n\tbuffer_load_dword %0, %1, %2 offen sc1 lds" :: "v"(voff), "s"(srd), "s"(soff), "s"(lds) : "memory");
  else    asm volatile("s_mov_b32 m0, %3\n\ts_nop 0\n\tbuffer_load_dword %0, %1, %2 offen lds" :: "v"(voff), "s"(srd), "s"(soff), "s"(lds) : "memory");
}
DEVI void dma_x4(v4u srd,u32 voff,u32 soff,u32 lds){
  asm volatile("s_mov_b32 m0, %3\n\ts_nop 0\n\tbuffer_load_dwordx4 %0, %1, %2 offen sc1 lds" :: "v"(voff), "s"(srd), "s"(soff), "s"(lds) : "memory");
}
template<int N> DEVI void waitv(){ asm volatile("s_waitcnt vmcnt(%0)"::"n"(N):"memory"); }
DEVI void waitl0(){ asm volatile("s_waitcnt lgkmcnt(0)":::"memory"); }
DEVI void bar(){ asm volatile("":::"memory"); __builtin_amdgcn_s_barrier(); asm volatile("":::"memory"); SB; }
template<u32 OFF> DEVI void sig(u32 vaddr,u32 one){ asm volatile("s_mov_b64 exec, 1\n\tds_add_u32 %0, %1 offset:%2\n\ts_mov_b64 exec, -1" :: "v"(vaddr), "v"(one), "n"(OFF) : "memory"); }
DEVI u32 peek(u32 vaddr){ return *(volatile LAS u32*)vaddr; }
DEVI bool vm_idle(){ u32 v; asm volatile("s_getreg_b32 %0, hwreg(HW_REG_IB_STS, 0, 4)":"=s"(v)); return v==0u; }
DEVI int lane_now(){ int l; asm volatile("v_mbcnt_lo_u32_b32 %0, -1, 0\n\tv_mbcnt_hi_u32_b32 %0, -1, %0":"=v"(l)); return l; }
DEVI float b2f(u16 v){ return u2f(((u32)v)<<16); }
DEVI u32 pk2(float lo,float hi){ v2f p={lo,hi}; return __builtin_bit_cast(u32,__builtin_convertvector(p,v2bf)); }
DEVI float rbf(float f){ return u2f(pk2(f,f)<<16); }
DEVI float relu(float x){ int i=__builtin_bit_cast(int,x); return __builtin_bit_cast(float,i>0?i:0); }
DEVI float dot2(u32 a,u32 b,float c){ return __builtin_amdgcn_fdot2_f32_bf16(__builtin_bit_cast(v2bf,a),__builtin_bit_cast(v2bf,b),c,false); }
DEVI v16f mfma(v4u a,v4u b,v16f c){ return __builtin_amdgcn_mfma_f32_32x32x16_bf16(__builtin_bit_cast(v8bf,a),__builtin_bit_cast(v8bf,b),c,0,0,0); }
DEVI v4f mfma16(v4u a,v4u b,v4f c){ return __builtin_amdgcn_mfma_f32_16x16x32_bf16(__builtin_bit_cast(v8bf,a),__builtin_bit_cast(v8bf,b),c,0,0,0); }
DEVI v4u ldsr128(u32 a){ return *(LAS v4u*)a; }
DEVI v4u ldstr(u32 a0,u32 a1){
  v4s x=__builtin_amdgcn_ds_read_tr16_b64_v4i16((LAS v4s*)a0);
  v4s y=__builtin_amdgcn_ds_read_tr16_b64_v4i16((LAS v4s*)a1);
  return __builtin_bit_cast(v4u,__builtin_shufflevector(x,y,0,1,2,3,4,5,6,7));
}
DEVI void sw16(float& a,float& b){ v2u r=__builtin_amdgcn_permlane16_swap(f2u(a),f2u(b),false,false); a=u2f(r[0]); b=u2f(r[1]); }
DEVI void sw32(float& a,float& b){ v2u r=__builtin_amdgcn_permlane32_swap(f2u(a),f2u(b),false,false); a=u2f(r[0]); b=u2f(r[1]); }
template<int CTRL> DEVI float dppadd(float v){ return v+u2f(__builtin_amdgcn_update_dpp(0u,f2u(v),CTRL,0xf,0xf,true)); }
DEVI void wsum4(float& a,float& b,float& c,float& d){
  sw32(a,b); sw32(c,d); float v=a+b, t=c+d;
  sw16(v,t); v+=t;
  v=dppadd<0xB1>(v); v=dppadd<0x4E>(v); v=dppadd<0x141>(v); v=dppadd<0x140>(v);
  a=u2f(__builtin_amdgcn_readlane(f2u(v),0)); c=u2f(__builtin_amdgcn_readlane(f2u(v),16));
  b=u2f(__builtin_amdgcn_readlane(f2u(v),32)); d=u2f(__builtin_amdgcn_readlane(f2u(v),48));
}
struct cf{ float r,i; };
DEVI cf cmul(cf a,cf b){ return cf{__builtin_fmaf(-a.i,b.i,a.r*b.r), __builtin_fmaf(a.i,b.r,a.r*b.i)}; }
DEVI cf cfma(cf a,cf b,cf c){ return cf{__builtin_fmaf(-a.i,b.i,__builtin_fmaf(a.r,b.r,c.r)), __builtin_fmaf(a.i,b.r,__builtin_fmaf(a.r,b.i,c.i))}; }
DEVI cf csel(bool c,cf a,cf b){ return cf{c?a.r:b.r, c?a.i:b.i}; }
DEVI cf ldsc(u32 a){ v2f v=*(LAS v2f*)a; return cf{v[0],v[1]}; }
DEVI void sincos_(float x,float& s,float& c){
  const float n=__builtin_rintf(x*0.636619772f);
  float r=__builtin_fmaf(n,-1.57079601e+00f,x); r=__builtin_fmaf(n,-3.13916473e-07f,r); r=__builtin_fmaf(n,-5.39030253e-15f,r);
  const float z=r*r;
  float ps=__builtin_fmaf(__builtin_fmaf(__builtin_fmaf(2.86567956e-6f,z,-1.98559923e-4f),z,8.33338592e-3f),z,-1.66666672e-1f);
  ps=__builtin_fmaf(ps*z,r,r);
  float pc=__builtin_fmaf(__builtin_fmaf(__builtin_fmaf(2.44677067e-5f,z,-1.38877297e-3f),z,4.16666567e-2f),z,-0.5f);
  pc=__builtin_fmaf(pc,z,1.f);
  const int q=(int)n;
  const float a=(q&1)?pc:ps, b=(q&1)?ps:pc;
  s=(q&2)?-a:a; c=((q+1)&2)?-b:b;
}
typedef __amdgpu_buffer_rsrc_t rsrc_t;
DEVI rsrc_t mkrsrc(const void* p,u32 bytes){ return __builtin_amdgcn_make_buffer_rsrc((void*)p,0,bytes,0x00020000); }
DEVI void st128(v4u v,rsrc_t r,u32 off){ __builtin_amdgcn_raw_buffer_store_b128(v,r,off,0,16); }
DEVI u32 mktag(u32 e){ return e*0x9E3779B1u+0x7F4A7C15u; }

DEVI u32 now32(){ u64 t; asm volatile("s_memrealtime %0\n\ts_waitcnt lgkmcnt(0)":"=s"(t)::"memory"); return (u32)t; }
#define STAMP(k) do{ __builtin_amdgcn_sched_barrier(0); ts_[k]=now32(); __builtin_amdgcn_sched_barrier(0); }while(0)
#define CSTAMP(k) do{ u64 tq_; asm volatile("s_memtime %0\n\ts_waitcnt lgkmcnt(0)":"=s"(tq_)::"memory"); ts_[k]=(u32)tq_; }while(0)
#define TRACE(k) do{ u32 t_=now32(); tr_=(lane==(k))?t_:tr_; }while(0)
#define STAMP_DECL u32 ts_[16]={}; u32 tr_=0; int nt_=0
#define BAR() do{ TRACE(nt_); ++nt_; bar(); }while(0)
#define STAMP_FLUSH() do{ if(lane==0){ _Pragma("unroll") for(int k_=0;k_<16;++k_) ((u64*)(a.ws+WS_STAMP))[(wgid*12+w)*16+k_]=ts_[k_]; } ((u32*)(a.ws+WS_STAMP+(1u<<20)))[(wgid*12+w)*128+lane]=tr_; }while(0)
#define TRARG ,u32& tr_,int& nt_
#define TRPASS ,tr_,nt_
#define TSARG ,u32 (&ts_)[16]
#define TSPASS ,ts_

DEVI void poll32(v4u srd,u32 voff,u32 soff,u32 tag){
  #pragma unroll 1
  for(int it=0;it<SPIN_CAP;++it){
    u32 v;
    asm volatile("buffer_load_dword %0, %1, %2, %3 offen sc1\n\ts_waitcnt vmcnt(0)" : "=&v"(v) : "v"(voff), "s"(srd), "s"(soff) : "memory");
    if(all_eq(v,tag)) break;
    __builtin_amdgcn_s_sleep(12);
  }
}
template<int I,int N,class F> DEVI void sfor(F&& f){ if constexpr(I<N){ f(std::integral_constant<int,I>{}); sfor<I+1,N>(f); } }
AI constexpr u32 wslot(int c){ return WB+(c&3)*WSZ; }
AI constexpr u32 sx_(int c){ return c>=5?(c-5)*XS:wslot(c+2); }
AI constexpr u32 sb_(int c){ return c>=5?wslot(3)+(c-5)*8192:wslot(c+2)+16384; }

#define SG(m,n) __builtin_amdgcn_sched_group_barrier(m,n,0)
template<int NB>
DEVI void g_loop(u32 lbase,int lane,int gw,v16f (&acc)[3][2] TRARG TSARG){
  const int h=lane>>5, r=lane&31, rh=gw&1, u0=(gw>>1)*NB;
  u32 ax[4],aw,aw2;
  #pragma unroll
  for(int s=0;s<4;++s) ax[s]=lbase+rh*8192+r*128+((((2*(s^(2*rh))+h)^((r>>1)&7)))<<4);
  aw=lbase+WB+((lane>>4)&1)*(12288+32)+(8*h+((lane&15)>>2))*192+(lane&3)*8+u0*32;
  aw2=aw+6144-rh*6144; aw+=rh*6144;
  const u32 on2=((lane&3)==((lane>>4)&1))?0x3f803f80u:0u;
  const v4u ones={on2,on2,on2,on2};
  v4f cw[3]={};
  v4u X[2][2],T[2][3];
  auto rd=[&](int b,int s) AI {
    const u32 a=(s<2?aw:aw2)+(s&1)*3072;
    X[b][0]=ldsr128(ax[s]);
    #pragma unroll
    for(int t=0;t<NB;++t){ T[b][t]=ldstr(a+t*32,a+t*32+768); if(t==0) X[b][1]=ldsr128(ax[s]+4096); }
  };
  auto mm=[&](int b,bool cs) AI {
    #pragma unroll
    for(int t=0;t<NB;++t){ acc[t][0]=mfma(T[b][t],X[b][0],acc[t][0]); if(cs) cw[t]=mfma16(ones,T[b][t],cw[t]); acc[t][1]=mfma(T[b][t],X[b][1],acc[t][1]); }
  };
  auto mix=[&](int nv,bool cs) AI {
    SG(0x8,1); SG(0x100,3); if(cs) SG(0x8,2); else SG(0x8,1); SG(0x100,3);
    if(NB==3){ SG(0x8,1); SG(0x100,2); }
    SG(0x8,1); if(nv) SG(0x2,3);
    SG(0x8,5);
  };
  u32 d0=XS, d1=XS, d2=(u32)(-2*(int)XS);
  STAMP(3); CSTAMP(6);
  rd(0,0);
  #pragma unroll 1
  for(int c=0;c<8;++c){
    const u32 dw=((c&3)==3)?(u32)(-3*(int)WSZ):WSZ;
    SB; rd(1,1); mm(0,true); ax[0]+=d0; ax[1]+=d0; aw+=dw; mix(1,true);
    SB; asm volatile("":"+v"(ax[0]),"+v"(ax[1]),"+v"(aw));
    rd(0,2); mm(1,true); mix(0,true);
    SB; rd(1,3); mm(0,false); ax[2]+=d0; ax[3]+=d0; aw2+=dw; mix(1,false);
    SB; asm volatile("":"+v"(ax[2]),"+v"(ax[3]),"+v"(aw2));
    { const u32 t=d0; d0=d1; d1=d2; d2=t; }
    if(c==7){
      const int ln=lane_now();
      #pragma unroll
      for(int t=0;t<NB;++t) cw[t][0]=(ln&16)?cw[t][1]:cw[t][0];
      *(LAS v4f*)(lbase+L_CW+(gw*32+(ln&31))*16)=v4f{cw[0][0],cw[1][0],cw[2][0],0.f};
    }
    waitl0(); BAR();
    rd(0,0); mm(1,false); mix(0,false);
  }
  const int ln=lane_now();
  const v4f pc=*(LAS v4f*)(lbase+L_CW+((gw^1)*32+(ln&31))*16);
  const u32 hm=(ln&32)?0u:0xffffffffu;
  v4u M[2];
  #pragma unroll
  for(int m=0;m<2;++m){ const u32 nm=peek(lbase+L_MB+(rh*64+m*32+(ln&31))*4)&hm; M[m]=v4u{nm,nm,0u,0u}; }
  #pragma unroll
  for(int t=0;t<NB;++t){
    const float c=cw[t][0]+pc[t];
    const u32 c0=f2u(c)&0xffff0000u; const float r1=c-u2f(c0);
    const u32 c1=f2u(r1)&0xffff0000u; const float r2=r1-u2f(c1);
    const v4u F={(c0>>16)|c1,f2u(r2)>>16,0u,0u};
    acc[t][0]=mfma(F,M[0],acc[t][0]); acc[t][1]=mfma(F,M[1],acc[t][1]);
  }
}

__global__ NOPK __launch_bounds__(768,3) void fused_k(Args a){
  __shared__ __attribute__((aligned(256))) char smem[L_TOTAL];
  const u32 lbase=(u32)(size_t)(LAS char*)smem;
  const int tid=__builtin_amdgcn_workitem_id_x();
  const int wgid=__builtin_amdgcn_workgroup_id_x(), w=rfl(tid>>6);
  int lane=tid&63;
  const int xcd=wgid&7, idx=wgid>>3;
  const int tt=(xcd&1)*8+(idx&7), cg=(xcd>>1)*4+(idx>>3);
  const int nb=((cg&3)==3||cg==14)?2:3, blk0=11*(cg>>2)+3*(cg&3)-(cg==15?1:0);
  const int rt2=(xcd&3)*8+(idx&7), ct=(xcd>>2)*4+(idx>>3);
  const int ry=ROTP?2*(idx>>3):0, rz=ROTP?(11*(idx>>3))>>2:0;
  const u32 one=1;
  STAMP_DECL; STAMP(0); CSTAMP(12);

  if(w>=8){
    __builtin_amdgcn_s_setprio(3);
    const int lw=w-8, j=lane>>3, l7=lane&7;
    const v4u sx=mksrd(a.x,2048*1024);
    const v4u sb=mksrd(lw<2?a.bre:a.bim,512*1024);
    const u32 sz0=(u32)((l7^(j>>1))<<4), sz1=sz0^64u;
    const u32 xr=(tt*128+32*lw+16*(j>>2)+(j&3))*1024;
    const u32 vx0=xr+sz0, vx1=xr+3*1024+sz1, vx2=xr+6*1024+sz0, vx3=xr+9*1024+sz1;
    const u32 br=(cg*32+16*(lw&1)+j)*1024;
    const u32 vb0=br+sz0, vb1=br+7*1024+sz1;
    const u32 l4=lbase+lw*4096, l2=lbase+lw*2048;
    u32 kx=((tt*(ROT1&15)+cg*(ROT1>>4))&7)*128, kw=(kx+640)&0x380;
    #define SX(c) dma4<false,sx_(c)>(sx,vx0,vx1,vx2,vx3,kx,l4)
    #define SBB(c) do{ dma2<sb_(c)>(sb,vb0,vb1,kx,l2); kx=(kx+128)&0x380; }while(0)
    SX(0); SBB(0); SX(1); SBB(1);
    const v4u sws=mksrd(a.ws,WS_END);
    const v4u sw=mksrd(lw<2?a.wg:a.wu,512*2752);
    const v4u swd=mksrd(a.wd,1376*1024);
    const v4u sc=mksrd(lw<2?a.cre:a.cim,512*1024);
    const u32 vf=(lane&15)*128, lf=lbase+L_FLG+lw*256;
    const u32 fh=WS_FH+(rt2*32+(lw&1)*16)*128, fhh=WS_FHH+(rt2*32+lw*8)*128;
    u32 vw[6];
    #pragma unroll
    for(int i=0;i<3;++i){ const u32 q=i*1024+lane*16, row=q/192, col=q%192; const bool ok=col<(u32)nb*64;
      vw[i]=ok?(row+32*(lw&1))*2752+col+blk0*64-i*1024:OOB; vw[i+3]=vw[i]+16*2752; asm volatile("":"+v"(vw[i]),"+v"(vw[i+3])); }
    const u32 lwv=lbase+(lw>>1)*(12288+32)+(lw&1)*6144;
    #define GWA(c) dma3<wslot(c)>(sw,vw[0],vw[1],vw[2],kw*1376,lwv)
    #define GWB(c) do{ dma3<wslot(c)+3072>(sw,vw[3],vw[4],vw[5],kw*1376,lwv); kw=(kw+128)&0x380; }while(0)
    #define GX(c) do{ dma4<false,((c)%3)*XS>(sx,vx0,vx1,vx2,vx3,kx,l4); kx=(kx+128)&0x380; }while(0)
    SX(2); waitv<10>(); BAR();
    SBB(2); SX(3); waitv<10>(); BAR();
    SBB(3); SX(4); waitv<10>(); BAR();
    SBB(4); SX(5); waitv<10>(); BAR();
    SBB(5); SX(6); waitv<10>(); BAR();
    SBB(6); SX(7); waitv<10>(); BAR();
    STAMP(2);
    SBB(7); GWA(0); waitv<9>(); BAR();
    GWB(0); GWA(1); waitv<9>(); BAR();
    GWB(1); GWA(2); waitv<9>(); BAR();
    STAMP(3);
    GWB(2); GWA(3); GWB(3);
    const u32 tag=rfl(peek(lbase+L_TAG));
    u32 va=WS_HS+rt2*131072+lw*4096+lane*16;
    const u32 cr=(ct*64+(lw&1)*32+j)*1024;
    u32 vp0=cr+sz0, vp1=cr+7*1024+sz1, vp2=cr+14*1024+sz0, vp3=cr+21*1024+sz1;
    waitv<12>(); BAR();
    sfor<0,4>([&](auto C) AI {
      constexpr int c=C.value;
      GX(c+3); GWA(c+4); GWB(c+4);
      waitv<16>();
      if(WARMB){
        if constexpr(c==0) dma_dw<false>(sc,lane<32?(u32)(j*1024+l7*128):OOB,(ct*64+(idx&7)*8+(lw&1)*4)*1024,lbase+L_DUM);
        if constexpr(c==2) dma_dw<false>(swd,lane<43?(u32)(lane*1024):OOB,((idx&7)*172+lw*43)*1024+ct*128,lbase+L_DUM);
      }
      BAR();
    });
    u32 ka=ry, kb=ry;
    auto PA=[&](auto C) AI {
      constexpr int c=C.value;
      if constexpr(c==8){ va=WS_HH+rt2*86*2048+lw*4096+lane*16; ka=rz; }
      u32 so;
      if constexpr(c<8){ so=ka<<14; ka=(ka+1)&7; } else { so=(ka==10&&lw==3)?OOB:ka<<14; ka= ka==10?0:ka+1; }
      dma4<true,((c+2)%3)*XS>(sws,va,va,va,va,so,l4);
    };
    auto PB=[&](auto C) AI {
      constexpr int c=C.value;
      if constexpr(c==8){ vp0=(lw*32+j)*1024+ct*128+((l7^(((j>>1)&1)<<2))<<4); vp1=vp0+7*1024; vp2=vp0+14*1024; vp3=vp0+21*1024; kb=rz; }
      u32 so;
      if constexpr(c<8){ so=kb<<7; kb=(kb+1)&7; } else { so=kb<<17; kb= kb==10?0:kb+1; }
      dma4<false,wslot(c)>(c<8?sc:swd,vp0,vp1,vp2,vp3,so,l4);
    };
    #define IC(n) std::integral_constant<int,(n)>{}
    GX(7); PB(IC(0));
    waitv<14>();
    dma_dw<true>(sws,vf,fh,lf);
    BAR();
    PB(IC(1));
    waitv<4>();
    const bool early=all_eq(peek(lf+lane*4),tag);
    if(early) PA(IC(0));
    BAR();
    STAMP(4);
    PB(IC(2));
    if(early) PA(IC(1));
    BAR();
    PB(IC(3));
    STAMP(5);
    if(!early){ poll32(sws,vf,fh,tag); PA(IC(0)); PA(IC(1)); }
    STAMP(6);
    PA(IC(2));
    waitv<8>(); BAR();
    if(lane<32) dma_dw<true>(sws,lane*4,WS_STAT+rt2*512+lw*128,lbase+L_TAB+lw*128);
    { const u32 vt=(rt2*64+lw*16+j)*1024+ct*128+l7*16; dma2<L_XT>(sx,vt,vt+7*1024,0,l2); }
    waitv<7>(); BAR();
    sfor<1,18>([&](auto K) AI {
      constexpr int k=K.value;
      if constexpr(k==4) dma_dw<true>(sws,vf&1023u,fhh,lf);
      if constexpr(k==9){ if(wgid==0 && lw==0) dma_dw<true>(sws,vf*2,WS_FH+30*4096,lf); }
      if constexpr(k<=16) PA(IC(k+2));
      if constexpr(k<=15) PB(IC(k+3));
      waitv<(k==1?11:k<=15?12:k==16?8:0)>();
      if constexpr(k==5){
        STAMP(7);
        if(!all_eq(peek(lf+lane*4),tag)) poll32(sws,vf&1023u,fhh,tag);
        STAMP(8);
      }
      if constexpr(k==12){
        if(wgid==0 && lw==0){
          if(!all_eq(peek(lf+lane*4),tag)) poll32(sws,vf*2,WS_FH+30*4096,tag);
          const u32 e=peek(lbase+L_TAG+4)+1u;
          asm volatile("s_mov_b64 exec, 1\n\tbuffer_store_dword %0, off, %1, 0 sc1\n\ts_mov_b64 exec, -1" :: "v"(e), "s"(sws) : "memory");
        }
      }
      BAR();
    });
    STAMP(11);
    STAMP_FLUSH();
    return;
  }

  int h=lane>>5, r=lane&31;
  const rsrc_t rws=mkrsrc(a.ws,WS_END);
  u32 pend=0;
  u32 tag;
  auto try_flag=[&](bool force) AI {
    if(pend && (force || vm_idle())){
      waitv<0>();
      if(lane==0) __builtin_amdgcn_raw_buffer_store_b32(tag,rws,pend,0,16);
      pend=0;
    }
  };
  if(w>=4){
    const int gw=w-4, rh=w&1, rt=2*tt+rh;
    v16f acc[3][2]={};
    {
      u32 e=0;
      if(gw==0) e=__builtin_amdgcn_raw_buffer_load_b32(rws,0,WS_EPOCH,16);
      u32 ax[4];
      #pragma unroll
      for(int s=0;s<4;++s) ax[s]=lbase+sx_(0)+gw*4096+r*128+((((2*s+h)^((r>>1)&7)))<<4);
      u32 ab=lbase+sb_(0)+r*128+((((2*gw+h)^((r>>1)&7)))<<4);
      float s1=0.f,s2=0.f,c0=0.f,c1=0.f;
      sfor<0,8>([&](auto C) AI {
        constexpr int c=C.value;
        waitl0(); BAR();
        v4u X[4];
        #pragma unroll
        for(int s=0;s<4;++s) X[s]=ldsr128(ax[s]);
        const v4u t0=ldsr128(ab), t1=ldsr128(ab+4096);
        if constexpr(c<7){
          #pragma unroll
          for(int s=0;s<4;++s) ax[s]+=sx_(c+1)-sx_(c);
          ab+=sb_(c+1)-sb_(c);
        }
        if constexpr(c==1){ if(gw==0){ e=rfl(e); *(LAS v2u*)(lbase+L_TAG)=v2u{mktag(e),e}; } }
        #pragma unroll
        for(int s=0;s<4;++s)
          #pragma unroll
          for(int k=0;k<4;++k){ s1=dot2(X[s][k],0x3f803f80u,s1); s2=dot2(X[s][k],X[s][k],s2); }
        #pragma unroll
        for(int k=0;k<4;++k){ c0=dot2(t0[k],0x3f803f80u,c0); c1=dot2(t1[k],0x3f803f80u,c1); }
        asm volatile("":"+v"(s1),"+v"(s2),"+v"(c0),"+v"(c1),"+v"(ax[0]),"+v"(ax[1]),"+v"(ax[2]),"+v"(ax[3]),"+v"(ab));
      });
      STAMP(1);
      sw32(c0,c1);
      *(LAS float*)(lbase+L_CSP+(gw*64+lane)*4)=c0+c1;
      sw32(s1,s2); s1+=s2; s2=s1; sw32(s1,s2);
      const float m=s1*(1.f/512.f);
      const float var=relu(__builtin_fmaf(s2,1.f/512.f,-m*m));
      const u32 vm=pk2(var,m);
      const float rs=__builtin_amdgcn_rcpf(rbf(__builtin_amdgcn_sqrtf(rbf(u2f(vm<<16)+1e-5f))));
      const int tok=32*gw+16*((r>>2)&1)+8*(r>>4)+4*((r>>3)&1)+(r&3);
      if(h) *(LAS u32*)(lbase+L_MB+(gw*32+r)*4)=((vm>>16)|(vm&0xffff0000u))^0x80008000u;
      else *(LAS v2f*)(lbase+L_TAB+tok*8)=v2f{rs,rs*u2f(vm&0xffff0000u)};
      STAMP(2);
      waitl0(); BAR();
      tag=rfl(peek(lbase+L_TAG));
    }
    __builtin_amdgcn_s_setprio(2);
    const int u0=(gw>>1)*nb;
    if(nb==3) g_loop<3>(lbase,lane,gw,acc TRPASS TSPASS); else g_loop<2>(lbase,lane,gw,acc TRPASS TSPASS);
    STAMP(4); CSTAMP(7);
    lane=lane_now();
    #pragma unroll
    for(int t=0;t<3;++t) if(t<nb){
      #pragma unroll
      for(int m=0;m<2;++m){
        v4u v;
        #pragma unroll
        for(int k=0;k<4;++k) v[k]=pk2(relu(acc[t][m][2*k])*acc[t][m][2*k+8],relu(acc[t][m][2*k+1])*acc[t][m][2*k+9]);
        st128(v,rws,lane*16+WS_HH+((rt*86+2*blk0+u0+t)*2+m)*1024);
      }
    }
    pend=WS_FHH+(rt*32+cg*2+(gw>>1))*128;
    STAMP(5);
  } else {
    const int ch=cg*32+r;
    __builtin_amdgcn_s_setprio(3);
    const u16 hlr=a.lre[ch], hli=a.lim[ch], hdt=a.ldt[ch];
    *(LAS u32*)(lbase+L_HP+(w*32+r)*16+8)=0u;
    const float lr=b2f(hlr), li=b2f(hli), dt=rbf(__builtin_expf(b2f(hdt)));
    cf lam,coef;
    { const float ex=__builtin_expf(dt*lr); float sn,cs_; sincos_(dt*li,sn,cs_); lam=cf{ex*cs_,ex*sn};
      const float dd=1.f/(lr*lr+li*li); coef=cmul(cf{lam.r-1.f,lam.i},cf{lr*dd,-li*dd}); }
    cf l16=cmul(lam,lam); l16=cmul(l16,l16); l16=cmul(l16,l16); l16=cmul(l16,l16);
    cf l32=cmul(l16,l16);
    cf l1024,wq,pw;
    { const cf l64=cmul(l32,l32), l128=cmul(l64,l64), l256=cmul(l128,l128), l512=cmul(l256,l256), o={1.f,0.f};
      l1024=cmul(l512,l512);
      wq=cmul(csel(h,l128,o),cmul(csel(w&1,l256,o),csel(w&2,l512,o)));
      pw=cmul(csel(w&1,l32,o),csel(w&2,l64,o)); }
    asm volatile("":"+v"(lam.r),"+v"(lam.i),"+v"(coef.r),"+v"(coef.i),"+v"(l16.r),"+v"(l16.i),"+v"(l32.r),"+v"(l32.i));
    asm volatile("":"+v"(l1024.r),"+v"(l1024.i),"+v"(wq.r),"+v"(wq.i),"+v"(pw.r),"+v"(pw.i));
    v16f are={},aim={};
    {
      u32 ax[4],ab[4];
      #pragma unroll
      for(int s=0;s<4;++s){ const u32 t=lbase+r*128+((((2*s+h)^((r>>1)&7)))<<4); ab[s]=t+sb_(0); ax[s]=t+sx_(0)+w*4096; }
      v4u X[2][2],T[2][2][2];
      auto rd=[&](int b) AI {
        #pragma unroll
        for(int s=0;s<2;++s){ X[b][s]=ldsr128(ax[2*b+s]); T[b][s][0]=ldsr128(ab[2*b+s]); T[b][s][1]=ldsr128(ab[2*b+s]+4096); } };
      auto mm=[&](int b) AI {
        #pragma unroll
        for(int s=0;s<2;++s){ are=mfma(X[b][s],T[b][s][0],are); aim=mfma(X[b][s],T[b][s][1],aim); }
        asm volatile("":"+v"(are),"+v"(aim)); };
      STAMP(1);
      waitl0(); BAR();
      rd(0);
      sfor<0,8>([&](auto C) AI {
        constexpr int c=C.value;
        SB; rd(1);
        SB; mm(0);
        if constexpr(c<7){
          #pragma unroll
          for(int s=0;s<4;++s){ ax[s]+=sx_(c+1)-sx_(c); ab[s]+=sb_(c+1)-sb_(c); asm volatile("":"+v"(ax[s]),"+v"(ab[s])); }
          SB;
          waitl0(); BAR();
          rd(0);
        }
        SB; mm(1);
      });
    }
    STAMP(2);
    waitl0(); BAR();
    int left=8;
    #define SBAR() do{ if(left>0){ BAR(); --left; } else __builtin_amdgcn_s_sleep(8); }while(0)
    tag=rfl(peek(lbase+L_TAG));
    cf cs={0.f,0.f};
    #pragma unroll
    for(int k=0;k<4;++k){ const u32 b=lbase+L_CSP+(k*64+r)*4; cs.r+=*(LAS float*)b; cs.i+=*(LAS float*)(b+128); }
    cf uu[16]; cf ea={0.f,0.f};
    #pragma unroll
    for(int k=0;k<16;k+=2){
      const v4f st=*(LAS v4f*)(lbase+L_TAB+(32*w+16*h+k)*8);
      #pragma unroll
      for(int d=0;d<2;++d){
        const cf v={__builtin_fmaf(are[k+d],st[2*d],-st[2*d+1]*cs.r),__builtin_fmaf(aim[k+d],st[2*d],-st[2*d+1]*cs.i)};
        uu[k+d]=v; ea=cfma(lam,ea,v);
      }
    }
    cf elo=ea,ehi=ea; sw32(elo.r,ehi.r); sw32(elo.i,ehi.i);
    { const cf ew=cfma(l16,elo,ehi); *(LAS v2f*)(lbase+L_AGG+(w*32+r)*8)=v2f{ew.r,ew.i}; }
    STAMP(3);
    waitl0(); SBAR();
    cf Q;
    { const u32 b=lbase+L_AGG+r*8;
      const cf a0=ldsc(b), a1=ldsc(b+256), a2=ldsc(b+512), a3=ldsc(b+768);
      const cf Q2=cfma(l32,a0,a1), Q3=cfma(l32,Q2,a2);
      if(w==3){ const cf E=cfma(l32,Q3,a3); if(lane<32) st128(v4u{f2u(E.r),tag,f2u(E.i),tag},rws,ch*16+WS_GRAN+tt*8192); }
      Q= w==0?cf{0.f,0.f}: w==1?a0: w==2?Q2:Q3; }
    STAMP(4);
    if(cg==0) __builtin_amdgcn_raw_buffer_store_b32(peek(lbase+L_TAB+w*256+lane*4),rws,WS_STAT+tt*1024+w*256+lane*4,0,16);
    #pragma unroll
    for(int k=0;k<16;++k){ uu[k]=cmul(coef,uu[k]); asm volatile("":"+v"(uu[k].r),"+v"(uu[k].i)); }
    SBAR();
    cf H={0.f,0.f};
    if(tt>0){
      const int p0=tt-1-2*w-h, p1=p0-8;
      const u32 o0=p0>=0?(u32)(p0*8192+ch*16):OOB, o1=p1>=0?(u32)(p1*8192+ch*16):OOB;
      const u32 t0=p0>=0?tag:0u, t1=p1>=0?tag:0u;
      v4u g0,g1;
      __builtin_amdgcn_s_sleep(LBSLEEP);
      #pragma unroll 1
      for(int it=0;it<SPIN_CAP;++it){
        asm volatile("":::"memory");
        g0=__builtin_amdgcn_raw_buffer_load_b128(rws,o0,WS_GRAN,16);
        g1=__builtin_amdgcn_raw_buffer_load_b128(rws,o1,WS_GRAN,16);
        SBAR();
        if(all_eq((g0[1]^t0)|(g0[3]^t0)|(g1[1]^t1)|(g1[3]^t1),0u)) break;
      }
      cf hp=cmul(wq,cfma(l1024,cf{u2f(g1[0]),u2f(g1[2])},cf{u2f(g0[0]),u2f(g0[2])}));
      cf h2=hp; sw32(hp.r,h2.r); sw32(hp.i,h2.i);
      *(LAS v4u*)(lbase+L_HP+(w*32+r)*16)=v4u{f2u(hp.r+h2.r),f2u(hp.i+h2.i),1u,0u};
      waitl0();
      v4u a0,a1,a2,a3;
      #pragma unroll 1
      for(int it=0;it<SPIN_CAP*8;++it){
        SBAR();
        const u32 b=lbase+L_HP+r*16;
        a0=*(volatile LAS v4u*)b; a1=*(volatile LAS v4u*)(b+512); a2=*(volatile LAS v4u*)(b+1024); a3=*(volatile LAS v4u*)(b+1536);
        if(all_eq(a0[2]&a1[2]&a2[2]&a3[2],1u)) break;
      }
      H=cf{(u2f(a0[0])+u2f(a1[0]))+(u2f(a2[0])+u2f(a3[0])),(u2f(a0[1])+u2f(a1[1]))+(u2f(a2[1])+u2f(a3[1]))};
    }
    STAMP(5);
    cf hc=cfma(pw,H,Q);
    hc=cmul(coef,csel(h,cfma(l16,hc,elo),hc));
    const int rt=2*tt+(w>>1);
    const u32 hso=WS_HS+rt*131072+(cg>>1)*16384+(((cg&1)*2+(r>>4))*2+(w&1))*1024+h*512;
    #pragma unroll
    for(int m=0;m<2;++m){
      cf hv[8];
      #pragma unroll
      for(int jj=0;jj<8;++jj){ hc=cfma(lam,hc,uu[8*m+jj]); hv[jj]=hc; }
      const u32 o=hso+m*256+(((lane&15)^(8*h+4*m))<<4);
      st128(v4u{pk2(hv[0].r,hv[1].r),pk2(hv[2].r,hv[3].r),pk2(hv[4].r,hv[5].r),pk2(hv[6].r,hv[7].r)},rws,o);
      st128(v4u{pk2(-hv[0].i,-hv[1].i),pk2(-hv[2].i,-hv[3].i),pk2(-hv[4].i,-hv[5].i),pk2(-hv[6].i,-hv[7].i)},rws,o+8192);
      if(m==0 && left>0){ BAR(); --left; }
    }
    pend=WS_FH+(rt*32+(cg&1)*16+(cg>>1)*2+(w&1))*128;
    STAMP(6);
    #pragma unroll 1
    while(left>0){
      BAR(); --left;
      #pragma unroll 1
      for(int it=0;it<6 && pend;++it){ try_flag(false); if(pend) __builtin_amdgcn_s_sleep(1); else STAMP(7); }
    }
    if(pend){ try_flag(true); STAMP(7); }
  }
  lane=lane_now(); h=lane>>5; r=lane&31;
  const int g=lane>>4, m2=w&1, tc=(w>>1)&1, kh=w>>2;
  const int dcol=ct*64+tc*32+r;
  const float dsk=b2f(a.dsk[dcol]);
  v16f ay={},az={};
  const int q=(lane&15)>>2, p=lane&3, b4=g&1, gp=b4+2*(p&1);
  u32 a0=lbase+2*XS+(kh*8+m2)*1024+(gp*16+((8*h+q)^(4*gp)))*16+8*(p>>1);
  u32 b0=lbase+WB+kh*8192+(tc*32+r)*128+((h^((r>>1)&7))<<4);
  u32 za=lbase+XS+(kh*8+m2)*1024+lane*16;
  u32 zb=lbase+WB+(64*kh+4*h+q)*128+((tc*64+b4*32+8*p)^(((q>>1)&1)<<6));
  asm volatile("":"+v"(za),"+v"(zb));
  u32 d0=(u32)(-2*(int)XS), d1=XS, d2=XS, e0=WSZ, e1=WSZ, e2=WSZ, e3=(u32)(-3*(int)WSZ);
  v4u A[2][4],B[2][4];
  auto rot=[&]() AI { { const u32 t=d0; d0=d1; d1=d2; d2=t; } { const u32 t=e0; e0=e1; e1=e2; e2=e3; e3=t; } };
  auto rdy=[&](int b) AI {
    #pragma unroll
    for(int s=0;s<4;++s){ A[b][s]=ldstr(a0+s*2048,(a0^64)+s*2048); B[b][s]=ldsr128(b0^(s*32)); }
    a0+=d0; b0+=e0; rot();
  };
  auto rdz=[&](int b) AI {
    #pragma unroll
    for(int s=0;s<4;++s){ A[b][s]=ldsr128(za+s*2048); B[b][s]=ldstr(zb+s*2048,zb+s*2048+1024); }
    za+=d0; zb+=e0; rot();
  };
  auto mm4=[&](int b,v16f& c) AI {
    #pragma unroll
    for(int s=0;s<4;++s) c=mfma(A[b][s],B[b][s],c);
    #pragma unroll
    for(int s=0;s<4;++s){ SG(0x8,1); SG(0x100,3); }
    SB; asm volatile("":"+v"(c));
  };
  #define NXT() do{ waitl0(); BAR(); }while(0)
  const int tk=32*m2+16*h+8*kh;
  STAMP(8);
  __builtin_amdgcn_s_setprio(0);
  NXT(); rdy(0);
  STAMP(9);
  #pragma unroll 1
  for(int c=0;c<3;++c){
    NXT(); rdy(1); mm4(0,ay); try_flag(false);
    NXT(); rdy(0); mm4(1,ay); try_flag(false);
  }
  NXT(); rdy(1); mm4(0,ay); try_flag(true);
  NXT(); rdz(0); mm4(1,ay);
  STAMP(10);
  float sv[8], xf[8], r2[8];
  #pragma unroll 1
  for(int c=0;c<5;++c){
    NXT(); rdz(1); mm4(0,az);
    if(c==1){
      #pragma unroll
      for(int k=0;k<8;++k){
        const v2f st=*(LAS v2f*)(lbase+L_TAB+(tk+k)*8);
        xf[k]=b2f(*(LAS u16*)(lbase+L_XT+(tk+k)*128+(tc*32+r)*2));
        sv[k]=__builtin_fmaf(xf[k],st[0],-st[1])*dsk; r2[k]=st[0]*st[0];
      }
    }
    NXT(); rdz(0); mm4(1,az);
  }
  {
    const u32 xo=lbase+w*4096+lane*16, xi=lbase+(w^4)*4096+lane*16;
    float zv[8];
    if(kh){ *(LAS v4f*)xo=v4f{ay[0],ay[1],ay[2],ay[3]}; *(LAS v4f*)(xo+1024)=v4f{ay[4],ay[5],ay[6],ay[7]};
      #pragma unroll
      for(int k=0;k<8;++k) sv[k]=__builtin_fmaf(2.f,ay[8+k],sv[k]); }
    else  { *(LAS v4f*)xo=v4f{ay[8],ay[9],ay[10],ay[11]}; *(LAS v4f*)(xo+1024)=v4f{ay[12],ay[13],ay[14],ay[15]};
      #pragma unroll
      for(int k=0;k<8;++k) sv[k]=__builtin_fmaf(2.f,ay[k],sv[k]); }
    SB;
    #pragma unroll
    for(int s=0;s<4;++s) az=mfma(A[0][s],B[0][s],az);
    STAMP(11);
    if(kh){ *(LAS v4f*)(xo+2048)=v4f{az[0],az[1],az[2],az[3]}; *(LAS v4f*)(xo+3072)=v4f{az[4],az[5],az[6],az[7]};
      #pragma unroll
      for(int k=0;k<8;++k) zv[k]=az[8+k]; }
    else  { *(LAS v4f*)(xo+2048)=v4f{az[8],az[9],az[10],az[11]}; *(LAS v4f*)(xo+3072)=v4f{az[12],az[13],az[14],az[15]};
      #pragma unroll
      for(int k=0;k<8;++k) zv[k]=az[k]; }
    waitl0(); bar();
    const v4f i2=*(LAS v4f*)(xi+2048), i3=*(LAS v4f*)(xi+3072), i0=*(LAS v4f*)xi, i1=*(LAS v4f*)(xi+1024);
    float* op=a.out+(rt2*64+tk)*512+dcol;
    #pragma unroll
    for(int k=0;k<8;++k){
      const float z=(zv[k]+(k<4?i2[k&3]:i3[k&3]))*r2[k];
      const float gt=z*__builtin_amdgcn_rcpf(1.f+__builtin_amdgcn_exp2f(z*-1.44269504f));
      op[k*512]=__builtin_fmaf(__builtin_fmaf(2.f,k<4?i0[k&3]:i1[k&3],sv[k]),gt,xf[k]);
    }
  }
  CSTAMP(14); STAMP(13); STAMP_FLUSH();
}

extern "C" void kernel_launch(void* const* d_in, const int* in_sizes, int n_in,
                              void* d_out, int out_size, void* d_ws, size_t ws_size,
                              hipStream_t stream) {
  Args a;
  a.x=(const u16*)d_in[0]; a.lre=(const u16*)d_in[1]; a.lim=(const u16*)d_in[2];
  a.bre=(const u16*)d_in[3]; a.bim=(const u16*)d_in[4]; a.cre=(const u16*)d_in[5]; a.cim=(const u16*)d_in[6];
  a.ldt=(const u16*)d_in[7]; a.dsk=(const u16*)d_in[8];
  a.wg=(const u16*)d_in[10]; a.wu=(const u16*)d_in[11]; a.wd=(const u16*)d_in[12];
  a.out=(float*)d_out; a.ws=(char*)d_ws;
  fused_k<<<256,768,0,stream>>>(a);
  hipStreamCaptureStatus cs; hipStreamIsCapturing(stream,&cs);
  if(cs==hipStreamCaptureStatusNone){
    for(int rep=0;rep<2;rep++){
    hipStreamSynchronize(stream);
    char* d_ws_=(char*)d_ws;
    std::vector<unsigned long long> s(256*12*16); hipMemcpy(s.data(),d_ws_+WS_STAMP,s.size()*8,hipMemcpyDeviceToHost);
    unsigned long long t0=~0ull; for(int b=0;b<256;b++) for(int w=0;w<12;w++) t0=std::min(t0,s[(b*12+w)*16]);
    fprintf(stderr,"ptrs:"); for(int i=0;i<13;i++) fprintf(stderr," %p",d_in[i]); fprintf(stderr," out %p ws %p\n",d_out,d_ws);
    int wsel[3]={0,4,8};
    for(int k=0;k<3;k++){ fprintf(stderr,"w%d:",wsel[k]);
      for(int i=0;i<14;i++){ if(i==12) continue; double mn=1e9,mx=-1e9,av=0; int n=0; for(int b=0;b<256;b++){ unsigned long long v=s[(b*12+wsel[k])*16+i]; if(v<t0||v>t0+100000) continue; double t=(v-t0)*0.01; mn=std::min(mn,t); mx=std::max(mx,t); av+=t; n++; }
        if(n) fprintf(stderr," [%d]%.2f/%.2f/%.2f",i,mn,av/n,mx); }
      fprintf(stderr,"\n"); }
    { double c1=0; for(int b=0;b<256;b++){ auto* q=&s[(b*12)*16]; c1+=(double)(unsigned)(q[14]-q[12])/(q[13]-q[0])*0.1; } fprintf(stderr,"avg clock GHz %.3f\n",c1/256); }
    std::vector<unsigned> t(256*12*128); hipMemcpy(t.data(),d_ws_+WS_STAMP+(1u<<20),t.size()*4,hipMemcpyDeviceToHost);
    std::vector<double> prev(256,0.0); double prel=0;
    for(int k=0;k<36;k++){ double rel=0,bz[3]={0,0,0}; int n=0;
      for(int b=1;b<256;b++){ double m[3]={0,0,0}; for(int c=0;c<3;c++) for(int w=4*c;w<4*c+4;w++){ unsigned v=t[(b*12+w)*128+k]; if(v) m[c]=std::max(m[c],(v-(unsigned)t0)*0.01); }
        double r=std::max(m[0],std::max(m[1],m[2])); if(r==0) continue; for(int c=0;c<3;c++) bz[c]+=m[c]-prev[b]; rel+=r; prev[b]=r; n++; }
      if(n){ fprintf(stderr,"%d:%.2f(%.2f|%.2f %.2f %.2f) ",k,rel/n,rel/n-prel,bz[0]/n,bz[1]/n,bz[2]/n); prel=rel/n; } }
    fprintf(stderr,"\n");
    if(rep==0) fused_k<<<256,768,0,stream>>>(a);
    }
  }
}
```

```cpp
#include <hip/hip_runtime.h>
#include <hip/hip_bf16.h>
#include <type_traits>

typedef unsigned u32; typedef unsigned long long u64; typedef unsigned short u16;
typedef u32 v4u __attribute__((ext_vector_type(4)));
typedef u32 v2u __attribute__((ext_vector_type(2)));
typedef short v4s __attribute__((ext_vector_type(4)));
typedef __bf16 v2bf __attribute__((ext_vector_type(2)));
typedef __bf16 v8bf __attribute__((ext_vector_type(8)));
typedef float v2f __attribute__((ext_vector_type(2)));
typedef float v4f __attribute__((ext_vector_type(4)));
typedef float v16f __attribute__((ext_vector_type(16)));
#define LAS __attribute__((address_space(3)))
#define DEVI __device__ __forceinline__
#define AI __attribute__((always_inline))
#define SB __builtin_amdgcn_sched_barrier(0)
#if defined(__HIP_DEVICE_COMPILE__)
#define NOPK __attribute__((target("no-packed-fp32-ops")))
#else
#define NOPK
#endif

#define ROT1 0x01
#define ROTP 1
#define LBSLEEP 6
#define WARMB 0

constexpr u32 WS_EPOCH = 0;
constexpr u32 WS_STAT  = 4096;
constexpr u32 WS_FH    = 32768;
constexpr u32 WS_FHH   = WS_FH + 131072;
constexpr u32 WS_GRAN  = WS_FHH + 131072;
constexpr u32 WS_HH    = WS_GRAN + 131072;
constexpr u32 WS_HS    = WS_HH + 32*86*2048;
constexpr u32 WS_END   = WS_HS + 32*131072;
constexpr u32 WS_STAMP = 16u<<20;
constexpr u32 XS = 16384;
constexpr u32 WB = 49152, WSZ = 24832;
constexpr u32 L_XT = WB+4*WSZ;
constexpr u32 L_TAB  = L_XT+8192;
constexpr u32 L_MB   = L_TAB + 1024;
constexpr u32 L_CSP  = L_MB + 512;
constexpr u32 L_FLG  = L_CSP;
constexpr u32 L_AGG  = L_CSP + 1024;
constexpr u32 L_HP   = L_AGG + 1024;
constexpr u32 L_DUM  = L_AGG;
constexpr u32 L_TAG  = L_HP + 2048;
constexpr u32 L_CW   = L_XT;
constexpr u32 L_TOTAL = 163840;
static_assert(L_TAG+16<=L_TOTAL,"lds");
constexpr u32 OOB = 0x40000000u;
constexpr int SPIN_CAP = 1<<18;

struct Args { const u16 *x,*lre,*lim,*bre,*bim,*cre,*cim,*ldt,*dsk,*wg,*wu,*wd; float* out; char* ws; };

DEVI u32 f2u(float f){ return __builtin_bit_cast(u32,f); }
DEVI float u2f(u32 u){ return __builtin_bit_cast(float,u); }
DEVI u32 rfl(u32 v){ return __builtin_amdgcn_readfirstlane(v); }
DEVI bool all_eq(u32 v,u32 t){ return __builtin_amdgcn_ballot_w64(v!=t)==0ull; }
DEVI v4u mksrd(const void* p, u32 bytes){ u64 a=(u64)p; v4u s; s.x=rfl((u32)a); s.y=rfl((u32)(a>>32)); s.z=bytes; s.w=0x00020000u; return s; }
#define DM0 "s_add_u32 m0, %[b], %[i]\n\ts_nop 0\n\t"
#define DLD(n,off,pol) "buffer_load_dwordx4 %[v" #n "], %[r], %[s] offen" off pol " lds\n\t"
template<bool SC1,u32 IMM> DEVI void dma4(v4u srd,u32 v0,u32 v1,u32 v2,u32 v3,u32 soff,u32 base){
  if(SC1) asm volatile(DM0 DLD(0,""," sc1") DLD(1," offset:1024"," sc1") DLD(2," offset:2048"," sc1") DLD(3," offset:3072"," sc1")
      :: [v0]"v"(v0),[v1]"v"(v1),[v2]"v"(v2),[v3]"v"(v3),[r]"s"(srd),[s]"s"(soff),[b]"s"(base),[i]"n"(IMM) : "memory","scc");
  else asm volatile(DM0 DLD(0,"","") DLD(1," offset:1024","") DLD(2," offset:2048","") DLD(3," offset:3072","")
      :: [v0]"v"(v0),[v1]"v"(v1),[v2]"v"(v2),[v3]"v"(v3),[r]"s"(srd),[s]"s"(soff),[b]"s"(base),[i]"n"(IMM) : "memory","scc");
}
template<u32 IMM> DEVI void dma3(v4u srd,u32 v0,u32 v1,u32 v2,u32 soff,u32 base){
  asm volatile(DM0 DLD(0,"","") DLD(1," offset:1024","") DLD(2," offset:2048","")
      :: [v0]"v"(v0),[v1]"v"(v1),[v2]"v"(v2),[r]"s"(srd),[s]"s"(soff),[b]"s"(base),[i]"n"(IMM) : "memory","scc");
}
template<u32 IMM> DEVI void dma2(v4u srd,u32 v0,u32 v1,u32 soff,u32 base){
  asm volatile(DM0 DLD(0,"","") DLD(1," offset:1024","")
      :: [v0]"v"(v0),[v1]"v"(v1),[r]"s"(srd),[s]"s"(soff),[b]"s"(base),[i]"n"(IMM) : "memory","scc");
}
template<bool SC1> DEVI void dma_dw(v4u srd,u32 voff,u32 soff,u32 lds){
  if(SC1) asm volatile("s_mov_b32 m0, %3\n\ts_nop 0\n\tbuffer_load_dword %0, %1, %2 offen sc1 lds" :: "v"(voff), "s"(srd), "s"(soff), "s"(lds) : "memory");
  else    asm volatile("s_mov_b32 m0, %3\n\ts_nop 0\n\tbuffer_load_dword %0, %1, %2 offen lds" :: "v"(voff), "s"(srd), "s"(soff), "s"(lds) : "memory");
}
DEVI void dma_x4(v4u srd,u32 voff,u32 soff,u32 lds){
  asm volatile("s_mov_b32 m0, %3\n\ts_nop 0\n\tbuffer_load_dwordx4 %0, %1, %2 offen sc1 lds" :: "v"(voff), "s"(srd), "s"(soff), "s"(lds) : "memory");
}
template<int N> DEVI void waitv(){ asm volatile("s_waitcnt vmcnt(%0)"::"n"(N):"memory"); }
DEVI void waitl0(){ asm volatile("s_waitcnt lgkmcnt(0)":::"memory"); }
DEVI void bar(){ asm volatile("":::"memory"); __builtin_amdgcn_s_barrier(); asm volatile("":::"memory"); SB; }
template<u32 OFF> DEVI void sig(u32 vaddr,u32 one){ asm volatile("s_mov_b64 exec, 1\n\tds_add_u32 %0, %1 offset:%2\n\ts_mov_b64 exec, -1" :: "v"(vaddr), "v"(one), "n"(OFF) : "memory"); }
DEVI u32 peek(u32 vaddr){ return *(volatile LAS u32*)vaddr; }
DEVI bool vm_idle(){ u32 v; asm volatile("s_getreg_b32 %0, hwreg(HW_REG_IB_STS, 0, 4)":"=s"(v)); return v==0u; }
DEVI int lane_now(){ int l; asm volatile("v_mbcnt_lo_u32_b32 %0, -1, 0\n\tv_mbcnt_hi_u32_b32 %0, -1, %0":"=v"(l)); return l; }
DEVI float b2f(u16 v){ return u2f(((u32)v)<<16); }
DEVI u32 pk2(float lo,float hi){ v2f p={lo,hi}; return __builtin_bit_cast(u32,__builtin_convertvector(p,v2bf)); }
DEVI float rbf(float f){ return u2f(pk2(f,f)<<16); }
DEVI float relu(float x){ int i=__builtin_bit_cast(int,x); return __builtin_bit_cast(float,i>0?i:0); }
DEVI float dot2(u32 a,u32 b,float c){ return __builtin_amdgcn_fdot2_f32_bf16(__builtin_bit_cast(v2bf,a),__builtin_bit_cast(v2bf,b),c,false); }
DEVI v16f mfma(v4u a,v4u b,v16f c){ return __builtin_amdgcn_mfma_f32_32x32x16_bf16(__builtin_bit_cast(v8bf,a),__builtin_bit_cast(v8bf,b),c,0,0,0); }
DEVI v4f mfma16(v4u a,v4u b,v4f c){ return __builtin_amdgcn_mfma_f32_16x16x32_bf16(__builtin_bit_cast(v8bf,a),__builtin_bit_cast(v8bf,b),c,0,0,0); }
DEVI v4u ldsr128(u32 a){ return *(LAS v4u*)a; }
DEVI v4u ldstr(u32 a0,u32 a1){
  v4s x=__builtin_amdgcn_ds_read_tr16_b64_v4i16((LAS v4s*)a0);
  v4s y=__builtin_amdgcn_ds_read_tr16_b64_v4i16((LAS v4s*)a1);
  return __builtin_bit_cast(v4u,__builtin_shufflevector(x,y,0,1,2,3,4,5,6,7));
}
DEVI void sw16(float& a,float& b){ v2u r=__builtin_amdgcn_permlane16_swap(f2u(a),f2u(b),false,false); a=u2f(r[0]); b=u2f(r[1]); }
DEVI void sw32(float& a,float& b){ v2u r=__builtin_amdgcn_permlane32_swap(f2u(a),f2u(b),false,false); a=u2f(r[0]); b=u2f(r[1]); }
template<int CTRL> DEVI float dppadd(float v){ return v+u2f(__builtin_amdgcn_update_dpp(0u,f2u(v),CTRL,0xf,0xf,true)); }
DEVI void wsum4(float& a,float& b,float& c,float& d){
  sw32(a,b); sw32(c,d); float v=a+b, t=c+d;
  sw16(v,t); v+=t;
  v=dppadd<0xB1>(v); v=dppadd<0x4E>(v); v=dppadd<0x141>(v); v=dppadd<0x140>(v);
  a=u2f(__builtin_amdgcn_readlane(f2u(v),0)); c=u2f(__builtin_amdgcn_readlane(f2u(v),16));
  b=u2f(__builtin_amdgcn_readlane(f2u(v),32)); d=u2f(__builtin_amdgcn_readlane(f2u(v),48));
}
struct cf{ float r,i; };
DEVI cf cmul(cf a,cf b){ return cf{__builtin_fmaf(-a.i,b.i,a.r*b.r), __builtin_fmaf(a.i,b.r,a.r*b.i)}; }
DEVI cf cfma(cf a,cf b,cf c){ return cf{__builtin_fmaf(-a.i,b.i,__builtin_fmaf(a.r,b.r,c.r)), __builtin_fmaf(a.i,b.r,__builtin_fmaf(a.r,b.i,c.i))}; }
DEVI cf csel(bool c,cf a,cf b){ return cf{c?a.r:b.r, c?a.i:b.i}; }
DEVI cf ldsc(u32 a){ v2f v=*(LAS v2f*)a; return cf{v[0],v[1]}; }
DEVI void sincos_(float x,float& s,float& c){
  const float n=__builtin_rintf(x*0.636619772f);
  float r=__builtin_fmaf(n,-1.57079601e+00f,x); r=__builtin_fmaf(n,-3.13916473e-07f,r); r=__builtin_fmaf(n,-5.39030253e-15f,r);
  const float z=r*r;
  float ps=__builtin_fmaf(__builtin_fmaf(__builtin_fmaf(2.86567956e-6f,z,-1.98559923e-4f),z,8.33338592e-3f),z,-1.66666672e-1f);
  ps=__builtin_fmaf(ps*z,r,r);
  float pc=__builtin_fmaf(__builtin_fmaf(__builtin_fmaf(2.44677067e-5f,z,-1.38877297e-3f),z,4.16666567e-2f),z,-0.5f);
  pc=__builtin_fmaf(pc,z,1.f);
  const int q=(int)n;
  const float a=(q&1)?pc:ps, b=(q&1)?ps:pc;
  s=(q&2)?-a:a; c=((q+1)&2)?-b:b;
}
typedef __amdgpu_buffer_rsrc_t rsrc_t;
DEVI rsrc_t mkrsrc(const void* p,u32 bytes){ return __builtin_amdgcn_make_buffer_rsrc((void*)p,0,bytes,0x00020000); }
DEVI void st128(v4u v,rsrc_t r,u32 off){ __builtin_amdgcn_raw_buffer_store_b128(v,r,off,0,16); }
DEVI u32 mktag(u32 e){ return e*0x9E3779B1u+0x7F4A7C15u; }

#define STAMP(k) do{}while(0)
#define CSTAMP(k) do{}while(0)
#define TRACE(k) do{}while(0)
#define STAMP_DECL do{}while(0)
#define BAR() bar()
#define STAMP_FLUSH() do{}while(0)
#define TRARG
#define TRPASS
#define TSARG
#define TSPASS

DEVI void poll32(v4u srd,u32 voff,u32 soff,u32 tag){
  #pragma unroll 1
  for(int it=0;it<SPIN_CAP;++it){
    u32 v;
    asm volatile("buffer_load_dword %0, %1, %2, %3 offen sc1\n\ts_waitcnt vmcnt(0)" : "=&v"(v) : "v"(voff), "s"(srd), "s"(soff) : "memory");
    if(all_eq(v,tag)) break;
    __builtin_amdgcn_s_sleep(12);
  }
}
template<int I,int N,class F> DEVI void sfor(F&& f){ if constexpr(I<N){ f(std::integral_constant<int,I>{}); sfor<I+1,N>(f); } }
AI constexpr u32 wslot(int c){ return WB+(c&3)*WSZ; }
AI constexpr u32 sx_(int c){ return c>=5?(c-5)*XS:wslot(c+2); }
AI constexpr u32 sb_(int c){ return c>=5?wslot(3)+(c-5)*8192:wslot(c+2)+16384; }

#define SG(m,n) __builtin_amdgcn_sched_group_barrier(m,n,0)
template<int NB>
DEVI void g_loop(u32 lbase,int lane,int gw,v16f (&acc)[3][2] TRARG TSARG){
  const int h=lane>>5, r=lane&31, rh=gw&1, u0=(gw>>1)*NB;
  u32 ax[4],aw,aw2;
  #pragma unroll
  for(int s=0;s<4;++s) ax[s]=lbase+rh*8192+r*128+((((2*(s^(2*rh))+h)^((r>>1)&7)))<<4);
  aw=lbase+WB+((lane>>4)&1)*(12288+32)+(8*h+((lane&15)>>2))*192+(lane&3)*8+u0*32;
  aw2=aw+6144-rh*6144; aw+=rh*6144;
  const u32 on2=((lane&3)==((lane>>4)&1))?0x3f803f80u:0u;
  const v4u ones={on2,on2,on2,on2};
  v4f cw[3]={};
  v4u X[2][2],T[2][3];
  auto rd=[&](int b,int s) AI {
    const u32 a=(s<2?aw:aw2)+(s&1)*3072;
    X[b][0]=ldsr128(ax[s]);
    #pragma unroll
    for(int t=0;t<NB;++t){ T[b][t]=ldstr(a+t*32,a+t*32+768); if(t==0) X[b][1]=ldsr128(ax[s]+4096); }
  };
  auto mm=[&](int b,bool cs) AI {
    #pragma unroll
    for(int t=0;t<NB;++t){ acc[t][0]=mfma(T[b][t],X[b][0],acc[t][0]); if(cs) cw[t]=mfma16(ones,T[b][t],cw[t]); acc[t][1]=mfma(T[b][t],X[b][1],acc[t][1]); }
  };
  auto mix=[&](int nv,bool cs) AI {
    SG(0x8,1); SG(0x100,3); if(cs) SG(0x8,2); else SG(0x8,1); SG(0x100,3);
    if(NB==3){ SG(0x8,1); SG(0x100,2); }
    SG(0x8,1); if(nv) SG(0x2,3);
    SG(0x8,5);
  };
  u32 d0=XS, d1=XS, d2=(u32)(-2*(int)XS);
  STAMP(3); CSTAMP(6);
  rd(0,0);
  #pragma unroll 1
  for(int c=0;c<8;++c){
    const u32 dw=((c&3)==3)?(u32)(-3*(int)WSZ):WSZ;
    SB; rd(1,1); mm(0,true); ax[0]+=d0; ax[1]+=d0; aw+=dw; mix(1,true);
    SB; asm volatile("":"+v"(ax[0]),"+v"(ax[1]),"+v"(aw));
    rd(0,2); mm(1,true); mix(0,true);
    SB; rd(1,3); mm(0,false); ax[2]+=d0; ax[3]+=d0; aw2+=dw; mix(1,false);
    SB; asm volatile("":"+v"(ax[2]),"+v"(ax[3]),"+v"(aw2));
    { const u32 t=d0; d0=d1; d1=d2; d2=t; }
    if(c==7){
      const int ln=lane_now();
      #pragma unroll
      for(int t=0;t<NB;++t) cw[t][0]=(ln&16)?cw[t][1]:cw[t][0];
      *(LAS v4f*)(lbase+L_CW+(gw*32+(ln&31))*16)=v4f{cw[0][0],cw[1][0],cw[2][0],0.f};
    }
    waitl0(); BAR();
    rd(0,0); mm(1,false); mix(0,false);
  }
  const int ln=lane_now();
  const v4f pc=*(LAS v4f*)(lbase+L_CW+((gw^1)*32+(ln&31))*16);
  const u32 hm=(ln&32)?0u:0xffffffffu;
  v4u M[2];
  #pragma unroll
  for(int m=0;m<2;++m){ const u32 nm=peek(lbase+L_MB+(rh*64+m*32+(ln&31))*4)&hm; M[m]=v4u{nm,nm,0u,0u}; }
  #pragma unroll
  for(int t=0;t<NB;++t){
    const float c=cw[t][0]+pc[t];
    const u32 c0=f2u(c)&0xffff0000u; const float r1=c-u2f(c0);
    const u32 c1=f2u(r1)&0xffff0000u; const float r2=r1-u2f(c1);
    const v4u F={(c0>>16)|c1,f2u(r2)>>16,0u,0u};
    acc[t][0]=mfma(F,M[0],acc[t][0]); acc[t][1]=mfma(F,M[1],acc[t][1]);
  }
}

__global__ NOPK __launch_bounds__(768,3) void fused_k(Args a){
  __shared__ __attribute__((aligned(256))) char smem[L_TOTAL];
  const u32 lbase=(u32)(size_t)(LAS char*)smem;
  const int tid=__builtin_amdgcn_workitem_id_x();
  const int wgid=__builtin_amdgcn_workgroup_id_x(), w=rfl(tid>>6);
  int lane=tid&63;
  const int xcd=wgid&7, idx=wgid>>3;
  const int tt=(xcd&1)*8+(idx&7), cg=(xcd>>1)*4+(idx>>3);
  const int nb=((cg&3)==3||cg==14)?2:3, blk0=11*(cg>>2)+3*(cg&3)-(cg==15?1:0);
  const int rt2=(xcd&3)*8+(idx&7), ct=(xcd>>2)*4+(idx>>3);
  const int ry=ROTP?2*(idx>>3):0, rz=ROTP?(11*(idx>>3))>>2:0;
  const u32 one=1;
  STAMP_DECL; STAMP(0); CSTAMP(12);

  if(w>=8){
    __builtin_amdgcn_s_setprio(3);
    const int lw=w-8, j=lane>>3, l7=lane&7;
    const v4u sx=mksrd(a.x,2048*1024);
    const v4u sb=mksrd(lw<2?a.bre:a.bim,512*1024);
    const u32 sz0=(u32)((l7^(j>>1))<<4), sz1=sz0^64u;
    const u32 xr=(tt*128+32*lw+16*(j>>2)+(j&3))*1024;
    const u32 vx0=xr+sz0, vx1=xr+3*1024+sz1, vx2=xr+6*1024+sz0, vx3=xr+9*1024+sz1;
    const u32 br=(cg*32+16*(lw&1)+j)*1024;
    const u32 vb0=br+sz0, vb1=br+7*1024+sz1;
    const u32 l4=lbase+lw*4096, l2=lbase+lw*2048;
    u32 kx=((tt*(ROT1&15)+cg*(ROT1>>4))&7)*128, kw=(kx+640)&0x380;
    #define SX(c) dma4<false,sx_(c)>(sx,vx0,vx1,vx2,vx3,kx,l4)
    #define SBB(c) do{ dma2<sb_(c)>(sb,vb0,vb1,kx,l2); kx=(kx+128)&0x380; }while(0)
    SX(0); SBB(0); SX(1); SBB(1);
    const v4u sws=mksrd(a.ws,WS_END);
    const v4u sw=mksrd(lw<2?a.wg:a.wu,512*2752);
    const v4u swd=mksrd(a.wd,1376*1024);
    const v4u sc=mksrd(lw<2?a.cre:a.cim,512*1024);
    const u32 vf=(lane&15)*128, lf=lbase+L_FLG+lw*256;
    const u32 fh=WS_FH+(rt2*32+(lw&1)*16)*128, fhh=WS_FHH+(rt2*32+lw*8)*128;
    u32 vw[6];
    #pragma unroll
    for(int i=0;i<3;++i){ const u32 q=i*1024+lane*16, row=q/192, col=q%192; const bool ok=col<(u32)nb*64;
      vw[i]=ok?(row+32*(lw&1))*2752+col+blk0*64-i*1024:OOB; vw[i+3]=vw[i]+16*2752; asm volatile("":"+v"(vw[i]),"+v"(vw[i+3])); }
    const u32 lwv=lbase+(lw>>1)*(12288+32)+(lw&1)*6144;
    #define GWA(c) dma3<wslot(c)>(sw,vw[0],vw[1],vw[2],kw*1376,lwv)
    #define GWB(c) do{ dma3<wslot(c)+3072>(sw,vw[3],vw[4],vw[5],kw*1376,lwv); kw=(kw+128)&0x380; }while(0)
    #define GX(c) do{ dma4<false,((c)%3)*XS>(sx,vx0,vx1,vx2,vx3,kx,l4); kx=(kx+128)&0x380; }while(0)
    SX(2); waitv<10>(); BAR();
    SBB(2); SX(3); waitv<10>(); BAR();
    SBB(3); SX(4); waitv<10>(); BAR();
    SBB(4); SX(5); waitv<10>(); BAR();
    SBB(5); SX(6); waitv<10>(); BAR();
    SBB(6); SX(7); waitv<10>(); BAR();
    STAMP(2);
    SBB(7); GWA(0); waitv<9>(); BAR();
    GWB(0); GWA(1); waitv<9>(); BAR();
    GWB(1); GWA(2); waitv<9>(); BAR();
    STAMP(3);
    GWB(2); GWA(3); GWB(3);
    const u32 tag=rfl(peek(lbase+L_TAG));
    u32 va=WS_HS+rt2*131072+lw*4096+lane*16;
    const u32 cr=(ct*64+(lw&1)*32+j)*1024;
    u32 vp0=cr+sz0, vp1=cr+7*1024+sz1, vp2=cr+14*1024+sz0, vp3=cr+21*1024+sz1;
    waitv<12>(); BAR();
    sfor<0,4>([&](auto C) AI {
      constexpr int c=C.value;
      GX(c+3); GWA(c+4); GWB(c+4);
      waitv<16>();
      if(WARMB){
        if constexpr(c==0) dma_dw<false>(sc,lane<32?(u32)(j*1024+l7*128):OOB,(ct*64+(idx&7)*8+(lw&1)*4)*1024,lbase+L_DUM);
        if constexpr(c==2) dma_dw<false>(swd,lane<43?(u32)(lane*1024):OOB,((idx&7)*172+lw*43)*1024+ct*128,lbase+L_DUM);
      }
      BAR();
    });
    u32 ka=ry, kb=ry;
    auto PA=[&](auto C) AI {
      constexpr int c=C.value;
      if constexpr(c==8){ va=WS_HH+rt2*86*2048+lw*4096+lane*16; ka=rz; }
      u32 so;
      if constexpr(c<8){ so=ka<<14; ka=(ka+1)&7; } else { so=(ka==10&&lw==3)?OOB:ka<<14; ka= ka==10?0:ka+1; }
      dma4<true,((c+2)%3)*XS>(sws,va,va,va,va,so,l4);
    };
    auto PB=[&](auto C) AI {
      constexpr int c=C.value;
      if constexpr(c==8){ vp0=(lw*32+j)*1024+ct*128+((l7^(((j>>1)&1)<<2))<<4); vp1=vp0+7*1024; vp2=vp0+14*1024; vp3=vp0+21*1024; kb=rz; }
      u32 so;
      if constexpr(c<8){ so=kb<<7; kb=(kb+1)&7; } else { so=kb<<17; kb= kb==10?0:kb+1; }
      dma4<false,wslot(c)>(c<8?sc:swd,vp0,vp1,vp2,vp3,so,l4);
    };
    #define IC(n) std::integral_constant<int,(n)>{}
    GX(7); PB(IC(0));
    waitv<14>();
    dma_dw<true>(sws,vf,fh,lf);
    BAR();
    PB(IC(1));
    waitv<4>();
    const bool early=all_eq(peek(lf+lane*4),tag);
    if(early) PA(IC(0));
    BAR();
    STAMP(4);
    PB(IC(2));
    if(early) PA(IC(1));
    BAR();
    PB(IC(3));
    STAMP(5);
    if(!early){ poll32(sws,vf,fh,tag); PA(IC(0)); PA(IC(1)); }
    STAMP(6);
    PA(IC(2));
    waitv<8>(); BAR();
    if(lane<32) dma_dw<true>(sws,lane*4,WS_STAT+rt2*512+lw*128,lbase+L_TAB+lw*128);
    { const u32 vt=(rt2*64+lw*16+j)*1024+ct*128+l7*16; dma2<L_XT>(sx,vt,vt+7*1024,0,l2); }
    waitv<7>(); BAR();
    sfor<1,18>([&](auto K) AI {
      constexpr int k=K.value;
      if constexpr(k==4) dma_dw<true>(sws,vf&1023u,fhh,lf);
      if constexpr(k==9){ if(wgid==0 && lw==0) dma_dw<true>(sws,vf*2,WS_FH+30*4096,lf); }
      if constexpr(k<=16) PA(IC(k+2));
      if constexpr(k<=15) PB(IC(k+3));
      waitv<(k==1?11:k<=15?12:k==16?8:0)>();
      if constexpr(k==5){
        STAMP(7);
        if(!all_eq(peek(lf+lane*4),tag)) poll32(sws,vf&1023u,fhh,tag);
        STAMP(8);
      }
      if constexpr(k==12){
        if(wgid==0 && lw==0){
          if(!all_eq(peek(lf+lane*4),tag)) poll32(sws,vf*2,WS_FH+30*4096,tag);
          const u32 e=peek(lbase+L_TAG+4)+1u;
          asm volatile("s_mov_b64 exec, 1\n\tbuffer_store_dword %0, off, %1, 0 sc1\n\ts_mov_b64 exec, -1" :: "v"(e), "s"(sws) : "memory");
        }
      }
      BAR();
    });
    STAMP(11);
    STAMP_FLUSH();
    return;
  }

  int h=lane>>5, r=lane&31;
  const rsrc_t rws=mkrsrc(a.ws,WS_END);
  u32 pend=0;
  u32 tag;
  auto try_flag=[&](bool force) AI {
    if(pend && (force || vm_idle())){
      waitv<0>();
      if(lane==0) __builtin_amdgcn_raw_buffer_store_b32(tag,rws,pend,0,16);
      pend=0;
    }
  };
  if(w>=4){
    const int gw=w-4, rh=w&1, rt=2*tt+rh;
    v16f acc[3][2]={};
    {
      u32 e=0;
      if(gw==0) e=__builtin_amdgcn_raw_buffer_load_b32(rws,0,WS_EPOCH,16);
      u32 ax[4];
      #pragma unroll
      for(int s=0;s<4;++s) ax[s]=lbase+sx_(0)+gw*4096+r*128+((((2*s+h)^((r>>1)&7)))<<4);
      u32 ab=lbase+sb_(0)+r*128+((((2*gw+h)^((r>>1)&7)))<<4);
      float s1=0.f,s2=0.f,c0=0.f,c1=0.f;
      sfor<0,8>([&](auto C) AI {
        constexpr int c=C.value;
        waitl0(); BAR();
        v4u X[4];
        #pragma unroll
        for(int s=0;s<4;++s) X[s]=ldsr128(ax[s]);
        const v4u t0=ldsr128(ab), t1=ldsr128(ab+4096);
        if constexpr(c<7){
          #pragma unroll
          for(int s=0;s<4;++s) ax[s]+=sx_(c+1)-sx_(c);
          ab+=sb_(c+1)-sb_(c);
        }
        if constexpr(c==1){ if(gw==0){ e=rfl(e); *(LAS v2u*)(lbase+L_TAG)=v2u{mktag(e),e}; } }
        #pragma unroll
        for(int s=0;s<4;++s)
          #pragma unroll
          for(int k=0;k<4;++k){ s1=dot2(X[s][k],0x3f803f80u,s1); s2=dot2(X[s][k],X[s][k],s2); }
        #pragma unroll
        for(int k=0;k<4;++k){ c0=dot2(t0[k],0x3f803f80u,c0); c1=dot2(t1[k],0x3f803f80u,c1); }
        asm volatile("":"+v"(s1),"+v"(s2),"+v"(c0),"+v"(c1),"+v"(ax[0]),"+v"(ax[1]),"+v"(ax[2]),"+v"(ax[3]),"+v"(ab));
      });
      STAMP(1);
      sw32(c0,c1);
      *(LAS float*)(lbase+L_CSP+(gw*64+lane)*4)=c0+c1;
      sw32(s1,s2); s1+=s2; s2=s1; sw32(s1,s2);
      const float m=s1*(1.f/512.f);
      const float var=relu(__builtin_fmaf(s2,1.f/512.f,-m*m));
      const u32 vm=pk2(var,m);
      const float rs=__builtin_amdgcn_rcpf(rbf(__builtin_amdgcn_sqrtf(rbf(u2f(vm<<16)+1e-5f))));
      const int tok=32*gw+16*((r>>2)&1)+8*(r>>4)+4*((r>>3)&1)+(r&3);
      if(h) *(LAS u32*)(lbase+L_MB+(gw*32+r)*4)=((vm>>16)|(vm&0xffff0000u))^0x80008000u;
      else *(LAS v2f*)(lbase+L_TAB+tok*8)=v2f{rs,rs*u2f(vm&0xffff0000u)};
      STAMP(2);
      waitl0(); BAR();
      tag=rfl(peek(lbase+L_TAG));
    }
    __builtin_amdgcn_s_setprio(2);
    const int u0=(gw>>1)*nb;
    if(nb==3) g_loop<3>(lbase,lane,gw,acc TRPASS TSPASS); else g_loop<2>(lbase,lane,gw,acc TRPASS TSPASS);
    STAMP(4); CSTAMP(7);
    lane=lane_now();
    #pragma unroll
    for(int t=0;t<3;++t) if(t<nb){
      #pragma unroll
      for(int m=0;m<2;++m){
        v4u v;
        #pragma unroll
        for(int k=0;k<4;++k) v[k]=pk2(relu(acc[t][m][2*k])*acc[t][m][2*k+8],relu(acc[t][m][2*k+1])*acc[t][m][2*k+9]);
        st128(v,rws,lane*16+WS_HH+((rt*86+2*blk0+u0+t)*2+m)*1024);
      }
    }
    pend=WS_FHH+(rt*32+cg*2+(gw>>1))*128;
    STAMP(5);
  } else {
    const int ch=cg*32+r;
    __builtin_amdgcn_s_setprio(3);
    const u16 hlr=a.lre[ch], hli=a.lim[ch], hdt=a.ldt[ch];
    *(LAS u32*)(lbase+L_HP+(w*32+r)*16+8)=0u;
    const float lr=b2f(hlr), li=b2f(hli), dt=rbf(__builtin_expf(b2f(hdt)));
    cf lam,coef;
    { const float ex=__builtin_expf(dt*lr); float sn,cs_; sincos_(dt*li,sn,cs_); lam=cf{ex*cs_,ex*sn};
      const float dd=1.f/(lr*lr+li*li); coef=cmul(cf{lam.r-1.f,lam.i},cf{lr*dd,-li*dd}); }
    cf l16=cmul(lam,lam); l16=cmul(l16,l16); l16=cmul(l16,l16); l16=cmul(l16,l16);
    cf l32=cmul(l16,l16);
    cf l1024,wq,pw;
    { const cf l64=cmul(l32,l32), l128=cmul(l64,l64), l256=cmul(l128,l128), l512=cmul(l256,l256), o={1.f,0.f};
      l1024=cmul(l512,l512);
      wq=cmul(csel(h,l128,o),cmul(csel(w&1,l256,o),csel(w&2,l512,o)));
      pw=cmul(csel(w&1,l32,o),csel(w&2,l64,o)); }
    asm volatile("":"+v"(lam.r),"+v"(lam.i),"+v"(coef.r),"+v"(coef.i),"+v"(l16.r),"+v"(l16.i),"+v"(l32.r),"+v"(l32.i));
    asm volatile("":"+v"(l1024.r),"+v"(l1024.i),"+v"(wq.r),"+v"(wq.i),"+v"(pw.r),"+v"(pw.i));
    v16f are={},aim={};
    {
      u32 ax[4],ab[4];
      #pragma unroll
      for(int s=0;s<4;++s){ const u32 t=lbase+r*128+((((2*s+h)^((r>>1)&7)))<<4); ab[s]=t+sb_(0); ax[s]=t+sx_(0)+w*4096; }
      v4u X[2][2],T[2][2][2];
      auto rd=[&](int b) AI {
        #pragma unroll
        for(int s=0;s<2;++s){ X[b][s]=ldsr128(ax[2*b+s]); T[b][s][0]=ldsr128(ab[2*b+s]); T[b][s][1]=ldsr128(ab[2*b+s]+4096); } };
      auto mm=[&](int b) AI {
        #pragma unroll
        for(int s=0;s<2;++s){ are=mfma(X[b][s],T[b][s][0],are); aim=mfma(X[b][s],T[b][s][1],aim); }
        asm volatile("":"+v"(are),"+v"(aim)); };
      STAMP(1);
      waitl0(); BAR();
      rd(0);
      sfor<0,8>([&](auto C) AI {
        constexpr int c=C.value;
        SB; rd(1);
        SB; mm(0);
        if constexpr(c<7){
          #pragma unroll
          for(int s=0;s<4;++s){ ax[s]+=sx_(c+1)-sx_(c); ab[s]+=sb_(c+1)-sb_(c); asm volatile("":"+v"(ax[s]),"+v"(ab[s])); }
          SB;
          waitl0(); BAR();
          rd(0);
        }
        SB; mm(1);
      });
    }
    STAMP(2);
    waitl0(); BAR();
    int left=8;
    #define SBAR() do{ if(left>0){ BAR(); --left; } else __builtin_amdgcn_s_sleep(8); }while(0)
    tag=rfl(peek(lbase+L_TAG));
    cf cs={0.f,0.f};
    #pragma unroll
    for(int k=0;k<4;++k){ const u32 b=lbase+L_CSP+(k*64+r)*4; cs.r+=*(LAS float*)b; cs.i+=*(LAS float*)(b+128); }
    cf uu[16]; cf ea={0.f,0.f};
    #pragma unroll
    for(int k=0;k<16;k+=2){
      const v4f st=*(LAS v4f*)(lbase+L_TAB+(32*w+16*h+k)*8);
      #pragma unroll
      for(int d=0;d<2;++d){
        const cf v={__builtin_fmaf(are[k+d],st[2*d],-st[2*d+1]*cs.r),__builtin_fmaf(aim[k+d],st[2*d],-st[2*d+1]*cs.i)};
        uu[k+d]=v; ea=cfma(lam,ea,v);
      }
    }
    cf elo=ea,ehi=ea; sw32(elo.r,ehi.r); sw32(elo.i,ehi.i);
    { const cf ew=cfma(l16,elo,ehi); *(LAS v2f*)(lbase+L_AGG+(w*32+r)*8)=v2f{ew.r,ew.i}; }
    STAMP(3);
    waitl0(); SBAR();
    cf Q;
    { const u32 b=lbase+L_AGG+r*8;
      const cf a0=ldsc(b), a1=ldsc(b+256), a2=ldsc(b+512), a3=ldsc(b+768);
      const cf Q2=cfma(l32,a0,a1), Q3=cfma(l32,Q2,a2);
      if(w==3){ const cf E=cfma(l32,Q3,a3); if(lane<32) st128(v4u{f2u(E.r),tag,f2u(E.i),tag},rws,ch*16+WS_GRAN+tt*8192); }
      Q= w==0?cf{0.f,0.f}: w==1?a0: w==2?Q2:Q3; }
    STAMP(4);
    if(cg==0) __builtin_amdgcn_raw_buffer_store_b32(peek(lbase+L_TAB+w*256+lane*4),rws,WS_STAT+tt*1024+w*256+lane*4,0,16);
    #pragma unroll
    for(int k=0;k<16;++k){ uu[k]=cmul(coef,uu[k]); asm volatile("":"+v"(uu[k].r),"+v"(uu[k].i)); }
    SBAR();
    cf H={0.f,0.f};
    if(tt>0){
      const int p0=tt-1-2*w-h, p1=p0-8;
      const u32 o0=p0>=0?(u32)(p0*8192+ch*16):OOB, o1=p1>=0?(u32)(p1*8192+ch*16):OOB;
      const u32 t0=p0>=0?tag:0u, t1=p1>=0?tag:0u;
      v4u g0,g1;
      __builtin_amdgcn_s_sleep(LBSLEEP);
      #pragma unroll 1
      for(int it=0;it<SPIN_CAP;++it){
        asm volatile("":::"memory");
        g0=__builtin_amdgcn_raw_buffer_load_b128(rws,o0,WS_GRAN,16);
        g1=__builtin_amdgcn_raw_buffer_load_b128(rws,o1,WS_GRAN,16);
        SBAR();
        if(all_eq((g0[1]^t0)|(g0[3]^t0)|(g1[1]^t1)|(g1[3]^t1),0u)) break;
      }
      cf hp=cmul(wq,cfma(l1024,cf{u2f(g1[0]),u2f(g1[2])},cf{u2f(g0[0]),u2f(g0[2])}));
      cf h2=hp; sw32(hp.r,h2.r); sw32(hp.i,h2.i);
      *(LAS v4u*)(lbase+L_HP+(w*32+r)*16)=v4u{f2u(hp.r+h2.r),f2u(hp.i+h2.i),1u,0u};
      waitl0();
      v4u a0,a1,a2,a3;
      #pragma unroll 1
      for(int it=0;it<SPIN_CAP*8;++it){
        SBAR();
        const u32 b=lbase+L_HP+r*16;
        a0=*(volatile LAS v4u*)b; a1=*(volatile LAS v4u*)(b+512); a2=*(volatile LAS v4u*)(b+1024); a3=*(volatile LAS v4u*)(b+1536);
        if(all_eq(a0[2]&a1[2]&a2[2]&a3[2],1u)) break;
      }
      H=cf{(u2f(a0[0])+u2f(a1[0]))+(u2f(a2[0])+u2f(a3[0])),(u2f(a0[1])+u2f(a1[1]))+(u2f(a2[1])+u2f(a3[1]))};
    }
    STAMP(5);
    cf hc=cfma(pw,H,Q);
    hc=cmul(coef,csel(h,cfma(l16,hc,elo),hc));
    const int rt=2*tt+(w>>1);
    const u32 hso=WS_HS+rt*131072+(cg>>1)*16384+(((cg&1)*2+(r>>4))*2+(w&1))*1024+h*512;
    #pragma unroll
    for(int m=0;m<2;++m){
      cf hv[8];
      #pragma unroll
      for(int jj=0;jj<8;++jj){ hc=cfma(lam,hc,uu[8*m+jj]); hv[jj]=hc; }
      const u32 o=hso+m*256+(((lane&15)^(8*h+4*m))<<4);
      st128(v4u{pk2(hv[0].r,hv[1].r),pk2(hv[2].r,hv[3].r),pk2(hv[4].r,hv[5].r),pk2(hv[6].r,hv[7].r)},rws,o);
      st128(v4u{pk2(-hv[0].i,-hv[1].i),pk2(-hv[2].i,-hv[3].i),pk2(-hv[4].i,-hv[5].i),pk2(-hv[6].i,-hv[7].i)},rws,o+8192);
      if(m==0 && left>0){ BAR(); --left; }
    }
    pend=WS_FH+(rt*32+(cg&1)*16+(cg>>1)*2+(w&1))*128;
    STAMP(6);
    #pragma unroll 1
    while(left>0){
      BAR(); --left;
      #pragma unroll 1
      for(int it=0;it<6 && pend;++it){ try_flag(false); if(pend) __builtin_amdgcn_s_sleep(1); else STAMP(7); }
    }
    if(pend){ try_flag(true); STAMP(7); }
  }
  lane=lane_now(); h=lane>>5; r=lane&31;
  const int g=lane>>4, m2=w&1, tc=(w>>1)&1, kh=w>>2;
  const int dcol=ct*64+tc*32+r;
  const float dsk=b2f(a.dsk[dcol]);
  v16f ay={},az={};
  const int q=(lane&15)>>2, p=lane&3, b4=g&1, gp=b4+2*(p&1);
  u32 a0=lbase+2*XS+(kh*8+m2)*1024+(gp*16+((8*h+q)^(4*gp)))*16+8*(p>>1);
  u32 b0=lbase+WB+kh*8192+(tc*32+r)*128+((h^((r>>1)&7))<<4);
  u32 za=lbase+XS+(kh*8+m2)*1024+lane*16;
  u32 zb=lbase+WB+(64*kh+4*h+q)*128+((tc*64+b4*32+8*p)^(((q>>1)&1)<<6));
  asm volatile("":"+v"(za),"+v"(zb));
  u32 d0=(u32)(-2*(int)XS), d1=XS, d2=XS, e0=WSZ, e1=WSZ, e2=WSZ, e3=(u32)(-3*(int)WSZ);
  v4u A[2][4],B[2][4];
  auto rot=[&]() AI { { const u32 t=d0; d0=d1; d1=d2; d2=t; } { const u32 t=e0; e0=e1; e1=e2; e2=e3; e3=t; } };
  auto rdy=[&](int b) AI {
    #pragma unroll
    for(int s=0;s<4;++s){ A[b][s]=ldstr(a0+s*2048,(a0^64)+s*2048); B[b][s]=ldsr128(b0^(s*32)); }
    a0+=d0; b0+=e0; rot();
  };
  auto rdz=[&](int b) AI {
    #pragma unroll
    for(int s=0;s<4;++s){ A[b][s]=ldsr128(za+s*2048); B[b][s]=ldstr(zb+s*2048,zb+s*2048+1024); }
    za+=d0; zb+=e0; rot();
  };
  auto mm4=[&](int b,v16f& c) AI {
    #pragma unroll
    for(int s=0;s<4;++s) c=mfma(A[b][s],B[b][s],c);
    #pragma unroll
    for(int s=0;s<4;++s){ SG(0x8,1); SG(0x100,3); }
    SB; asm volatile("":"+v"(c));
  };
  #define NXT() do{ waitl0(); BAR(); }while(0)
  const int tk=32*m2+16*h+8*kh;
  STAMP(8);
  __builtin_amdgcn_s_setprio(0);
  NXT(); rdy(0);
  STAMP(9);
  #pragma unroll 1
  for(int c=0;c<3;++c){
    NXT(); rdy(1); mm4(0,ay); try_flag(false);
    NXT(); rdy(0); mm4(1,ay); try_flag(false);
  }
  NXT(); rdy(1); mm4(0,ay); try_flag(true);
  NXT(); rdz(0); mm4(1,ay);
  STAMP(10);
  float sv[8], xf[8], r2[8];
  #pragma unroll 1
  for(int c=0;c<5;++c){
    NXT(); rdz(1); mm4(0,az);
    if(c==1){
      #pragma unroll
      for(int k=0;k<8;++k){
        const v2f st=*(LAS v2f*)(lbase+L_TAB+(tk+k)*8);
        xf[k]=b2f(*(LAS u16*)(lbase+L_XT+(tk+k)*128+(tc*32+r)*2));
        sv[k]=__builtin_fmaf(xf[k],st[0],-st[1])*dsk; r2[k]=st[0]*st[0];
      }
    }
    NXT(); rdz(0); mm4(1,az);
  }
  {
    const u32 xo=lbase+w*4096+lane*16, xi=lbase+(w^4)*4096+lane*16;
    float zv[8];
    if(kh){ *(LAS v4f*)xo=v4f{ay[0],ay[1],ay[2],ay[3]}; *(LAS v4f*)(xo+1024)=v4f{ay[4],ay[5],ay[6],ay[7]};
      #pragma unroll
      for(int k=0;k<8;++k) sv[k]=__builtin_fmaf(2.f,ay[8+k],sv[k]); }
    else  { *(LAS v4f*)xo=v4f{ay[8],ay[9],ay[10],ay[11]}; *(LAS v4f*)(xo+1024)=v4f{ay[12],ay[13],ay[14],ay[15]};
      #pragma unroll
      for(int k=0;k<8;++k) sv[k]=__builtin_fmaf(2.f,ay[k],sv[k]); }
    SB;
    #pragma unroll
    for(int s=0;s<4;++s) az=mfma(A[0][s],B[0][s],az);
    STAMP(11);
    if(kh){ *(LAS v4f*)(xo+2048)=v4f{az[0],az[1],az[2],az[3]}; *(LAS v4f*)(xo+3072)=v4f{az[4],az[5],az[6],az[7]};
      #pragma unroll
      for(int k=0;k<8;++k) zv[k]=az[8+k]; }
    else  { *(LAS v4f*)(xo+2048)=v4f{az[8],az[9],az[10],az[11]}; *(LAS v4f*)(xo+3072)=v4f{az[12],az[13],az[14],az[15]};
      #pragma unroll
      for(int k=0;k<8;++k) zv[k]=az[k]; }
    waitl0(); bar();
    const v4f i2=*(LAS v4f*)(xi+2048), i3=*(LAS v4f*)(xi+3072), i0=*(LAS v4f*)xi, i1=*(LAS v4f*)(xi+1024);
    float* op=a.out+(rt2*64+tk)*512+dcol;
    #pragma unroll
    for(int k=0;k<8;++k){
      const float z=(zv[k]+(k<4?i2[k&3]:i3[k&3]))*r2[k];
      const float gt=z*__builtin_amdgcn_rcpf(1.f+__builtin_amdgcn_exp2f(z*-1.44269504f));
      op[k*512]=__builtin_fmaf(__builtin_fmaf(2.f,k<4?i0[k&3]:i1[k&3],sv[k]),gt,xf[k]);
    }
  }
  CSTAMP(14); STAMP(13); STAMP_FLUSH();
}

extern "C" void kernel_launch(void* const* d_in, const int* in_sizes, int n_in,
                              void* d_out, int out_size, void* d_ws, size_t ws_size,
                              hipStream_t stream) {
  Args a;
  a.x=(const u16*)d_in[0]; a.lre=(const u16*)d_in[1]; a.lim=(const u16*)d_in[2];
  a.bre=(const u16*)d_in[3]; a.bim=(const u16*)d_in[4]; a.cre=(const u16*)d_in[5]; a.cim=(const u16*)d_in[6];
  a.ldt=(const u16*)d_in[7]; a.dsk=(const u16*)d_in[8];
  a.wg=(const u16*)d_in[10]; a.wu=(const u16*)d_in[11]; a.wd=(const u16*)d_in[12];
  a.out=(float*)d_out; a.ws=(char*)d_ws;
  fused_k<<<256,768,0,stream>>>(a);
}
```

```cpp
#include <hip/hip_runtime.h>
#include <hip/hip_bf16.h>
#include <type_traits>

typedef unsigned u32; typedef unsigned long long u64; typedef unsigned short u16;
typedef u32 v4u __attribute__((ext_vector_type(4)));
typedef u32 v2u __attribute__((ext_vector_type(2)));
typedef short v4s __attribute__((ext_vector_type(4)));
typedef __bf16 v2bf __attribute__((ext_vector_type(2)));
typedef __bf16 v8bf __attribute__((ext_vector_type(8)));
typedef float v2f __attribute__((ext_vector_type(2)));
typedef float v4f __attribute__((ext_vector_type(4)));
typedef float v16f __attribute__((ext_vector_type(16)));
#define LAS __attribute__((address_space(3)))
#define DEVI __device__ __forceinline__
#define AI __attribute__((always_inline))
#define SB __builtin_amdgcn_sched_barrier(0)
#if defined(__HIP_DEVICE_COMPILE__)
#define NOPK __attribute__((target("no-packed-fp32-ops")))
#else
#define NOPK
#endif

#define ROT1 0x01
#define ROTP 1
#define LBSLEEP 6
#define WARMB 0

constexpr u32 WS_EPOCH = 0;
constexpr u32 WS_STAT  = 4096;
constexpr u32 WS_FH    = 32768;
constexpr u32 WS_FHH   = WS_FH + 131072;
constexpr u32 WS_GRAN  = WS_FHH + 131072;
constexpr u32 WS_HH    = WS_GRAN + 131072;
constexpr u32 WS_HS    = WS_HH + 32*86*2048;
constexpr u32 WS_END   = WS_HS + 32*131072;
constexpr u32 WS_STAMP = 16u<<20;
constexpr u32 XS = 16384;
constexpr u32 WB = 49152, WSZ = 24832;
constexpr u32 L_XT = WB+4*WSZ;
constexpr u32 L_TAB  = L_XT+8192;
constexpr u32 L_MB   = L_TAB + 1024;
constexpr u32 L_CSP  = L_MB + 512;
constexpr u32 L_FLG  = L_CSP;
constexpr u32 L_AGG  = L_CSP + 1024;
constexpr u32 L_HP   = L_AGG + 1024;
constexpr u32 L_DUM  = L_AGG;
constexpr u32 L_TAG  = L_HP + 2048;
constexpr u32 L_CW   = L_XT;
constexpr u32 L_TOTAL = 163840;
static_assert(L_TAG+16<=L_TOTAL,"lds");
constexpr u32 OOB = 0x40000000u;
constexpr int SPIN_CAP = 1<<18;

struct Args { const u16 *x,*lre,*lim,*bre,*bim,*cre,*cim,*ldt,*dsk,*wg,*wu,*wd; float* out; char* ws; };

DEVI u32 f2u(float f){ return __builtin_bit_cast(u32,f); }
DEVI float u2f(u32 u){ return __builtin_bit_cast(float,u); }
DEVI u32 rfl(u32 v){ return __builtin_amdgcn_readfirstlane(v); }
DEVI bool all_eq(u32 v,u32 t){ return __builtin_amdgcn_ballot_w64(v!=t)==0ull; }
DEVI v4u mksrd(const void* p, u32 bytes){ u64 a=(u64)p; v4u s; s.x=rfl((u32)a); s.y=rfl((u32)(a>>32)); s.z=bytes; s.w=0x00020000u; return s; }
#define DM0 "s_add_u32 m0, %[b], %[i]\n\ts_nop 0\n\t"
#define DLD(n,off,pol) "buffer_load_dwordx4 %[v" #n "], %[r], %[s] offen" off pol " lds\n\t"
template<bool SC1,u32 IMM> DEVI void dma4(v4u srd,u32 v0,u32 v1,u32 v2,u32 v3,u32 soff,u32 base){
  if(SC1) asm volatile(DM0 DLD(0,""," sc1") DLD(1," offset:1024"," sc1") DLD(2," offset:2048"," sc1") DLD(3," offset:3072"," sc1")
      :: [v0]"v"(v0),[v1]"v"(v1),[v2]"v"(v2),[v3]"v"(v3),[r]"s"(srd),[s]"s"(soff),[b]"s"(base),[i]"n"(IMM) : "memory","scc");
  else asm volatile(DM0 DLD(0,"","") DLD(1," offset:1024","") DLD(2," offset:2048","") DLD(3," offset:3072","")
      :: [v0]"v"(v0),[v1]"v"(v1),[v2]"v"(v2),[v3]"v"(v3),[r]"s"(srd),[s]"s"(soff),[b]"s"(base),[i]"n"(IMM) : "memory","scc");
}
template<u32 IMM> DEVI void dma3(v4u srd,u32 v0,u32 v1,u32 v2,u32 soff,u32 base){
  asm volatile(DM0 DLD(0,"","") DLD(1," offset:1024","") DLD(2," offset:2048","")
      :: [v0]"v"(v0),[v1]"v"(v1),[v2]"v"(v2),[r]"s"(srd),[s]"s"(soff),[b]"s"(base),[i]"n"(IMM) : "memory","scc");
}
template<u32 IMM> DEVI void dma2(v4u srd,u32 v0,u32 v1,u32 soff,u32 base){
  asm volatile(DM0 DLD(0,"","") DLD(1," offset:1024","")
      :: [v0]"v"(v0),[v1]"v"(v1),[r]"s"(srd),[s]"s"(soff),[b]"s"(base),[i]"n"(IMM) : "memory","scc");
}
template<bool SC1> DEVI void dma_dw(v4u srd,u32 voff,u32 soff,u32 lds){
  if(SC1) asm volatile("s_mov_b32 m0, %3\n\ts_nop 0\n\tbuffer_load_dword %0, %1, %2 offen sc1 lds" :: "v"(voff), "s"(srd), "s"(soff), "s"(lds) : "memory");
  else    asm volatile("s_mov_b32 m0, %3\n\ts_nop 0\n\tbuffer_load_dword %0, %1, %2 offen lds" :: "v"(voff), "s"(srd), "s"(soff), "s"(lds) : "memory");
}
DEVI void dma_x4(v4u srd,u32 voff,u32 soff,u32 lds){
  asm volatile("s_mov_b32 m0, %3\n\ts_nop 0\n\tbuffer_load_dwordx4 %0, %1, %2 offen sc1 lds" :: "v"(voff), "s"(srd), "s"(soff), "s"(lds) : "memory");
}
template<int N> DEVI void waitv(){ asm volatile("s_waitcnt vmcnt(%0)"::"n"(N):"memory"); }
DEVI void waitl0(){ asm volatile("s_waitcnt lgkmcnt(0)":::"memory"); }
DEVI void bar(){ asm volatile("":::"memory"); __builtin_amdgcn_s_barrier(); asm volatile("":::"memory"); SB; }
template<u32 OFF> DEVI void sig(u32 vaddr,u32 one){ asm volatile("s_mov_b64 exec, 1\n\tds_add_u32 %0, %1 offset:%2\n\ts_mov_b64 exec, -1" :: "v"(vaddr), "v"(one), "n"(OFF) : "memory"); }
DEVI u32 peek(u32 vaddr){ return *(volatile LAS u32*)vaddr; }
DEVI bool vm_idle(){ u32 v; asm volatile("s_getreg_b32 %0, hwreg(HW_REG_IB_STS, 0, 4)":"=s"(v)); return v==0u; }
DEVI int lane_now(){ int l; asm volatile("v_mbcnt_lo_u32_b32 %0, -1, 0\n\tv_mbcnt_hi_u32_b32 %0, -1, %0":"=v"(l)); return l; }
DEVI float b2f(u16 v){ return u2f(((u32)v)<<16); }
DEVI u32 pk2(float lo,float hi){ v2f p={lo,hi}; return __builtin_bit_cast(u32,__builtin_convertvector(p,v2bf)); }
DEVI float rbf(float f){ return u2f(pk2(f,f)<<16); }
DEVI float relu(float x){ int i=__builtin_bit_cast(int,x); return __builtin_bit_cast(float,i>0?i:0); }
DEVI float dot2(u32 a,u32 b,float c){ return __builtin_amdgcn_fdot2_f32_bf16(__builtin_bit_cast(v2bf,a),__builtin_bit_cast(v2bf,b),c,false); }
DEVI v16f mfma(v4u a,v4u b,v16f c){ return __builtin_amdgcn_mfma_f32_32x32x16_bf16(__builtin_bit_cast(v8bf,a),__builtin_bit_cast(v8bf,b),c,0,0,0); }
DEVI v4f mfma16(v4u a,v4u b,v4f c){ return __builtin_amdgcn_mfma_f32_16x16x32_bf16(__builtin_bit_cast(v8bf,a),__builtin_bit_cast(v8bf,b),c,0,0,0); }
DEVI v4u ldsr128(u32 a){ return *(LAS v4u*)a; }
DEVI v4u ldstr(u32 a0,u32 a1){
  v4s x=__builtin_amdgcn_ds_read_tr16_b64_v4i16((LAS v4s*)a0);
  v4s y=__builtin_amdgcn_ds_read_tr16_b64_v4i16((LAS v4s*)a1);
  return __builtin_bit_cast(v4u,__builtin_shufflevector(x,y,0,1,2,3,4,5,6,7));
}
DEVI void sw16(float& a,float& b){ v2u r=__builtin_amdgcn_permlane16_swap(f2u(a),f2u(b),false,false); a=u2f(r[0]); b=u2f(r[1]); }
DEVI void sw32(float& a,float& b){ v2u r=__builtin_amdgcn_permlane32_swap(f2u(a),f2u(b),false,false); a=u2f(r[0]); b=u2f(r[1]); }
template<int CTRL> DEVI float dppadd(float v){ return v+u2f(__builtin_amdgcn_update_dpp(0u,f2u(v),CTRL,0xf,0xf,true)); }
DEVI void wsum4(float& a,float& b,float& c,float& d){
  sw32(a,b); sw32(c,d); float v=a+b, t=c+d;
  sw16(v,t); v+=t;
  v=dppadd<0xB1>(v); v=dppadd<0x4E>(v); v=dppadd<0x141>(v); v=dppadd<0x140>(v);
  a=u2f(__builtin_amdgcn_readlane(f2u(v),0)); c=u2f(__builtin_amdgcn_readlane(f2u(v),16));
  b=u2f(__builtin_amdgcn_readlane(f2u(v),32)); d=u2f(__builtin_amdgcn_readlane(f2u(v),48));
}
struct cf{ float r,i; };
DEVI cf cmul(cf a,cf b){ return cf{__builtin_fmaf(-a.i,b.i,a.r*b.r), __builtin_fmaf(a.i,b.r,a.r*b.i)}; }
DEVI cf cfma(cf a,cf b,cf c){ return cf{__builtin_fmaf(-a.i,b.i,__builtin_fmaf(a.r,b.r,c.r)), __builtin_fmaf(a.i,b.r,__builtin_fmaf(a.r,b.i,c.i))}; }
DEVI cf csel(bool c,cf a,cf b){ return cf{c?a.r:b.r, c?a.i:b.i}; }
DEVI cf ldsc(u32 a){ v2f v=*(LAS v2f*)a; return cf{v[0],v[1]}; }
DEVI void sincos_(float x,float& s,float& c){
  const float n=__builtin_rintf(x*0.636619772f);
  float r=__builtin_fmaf(n,-1.57079601e+00f,x); r=__builtin_fmaf(n,-3.13916473e-07f,r); r=__builtin_fmaf(n,-5.39030253e-15f,r);
  const float z=r*r;
  float ps=__builtin_fmaf(__builtin_fmaf(__builtin_fmaf(2.86567956e-6f,z,-1.98559923e-4f),z,8.33338592e-3f),z,-1.66666672e-1f);
  ps=__builtin_fmaf(ps*z,r,r);
  float pc=__builtin_fmaf(__builtin_fmaf(__builtin_fmaf(2.44677067e-5f,z,-1.38877297e-3f),z,4.16666567e-2f),z,-0.5f);
  pc=__builtin_fmaf(pc,z,1.f);
  const int q=(int)n;
  const float a=(q&1)?pc:ps, b=(q&1)?ps:pc;
  s=(q&2)?-a:a; c=((q+1)&2)?-b:b;
}
typedef __amdgpu_buffer_rsrc_t rsrc_t;
DEVI rsrc_t mkrsrc(const void* p,u32 bytes){ return __builtin_amdgcn_make_buffer_rsrc((void*)p,0,bytes,0x00020000); }
DEVI void st128(v4u v,rsrc_t r,u32 off){ __builtin_amdgcn_raw_buffer_store_b128(v,r,off,0,16); }
DEVI u32 mktag(u32 e){ return e*0x9E3779B1u+0x7F4A7C15u; }

#define STAMP(k) do{}while(0)
#define CSTAMP(k) do{}while(0)
#define TRACE(k) do{}while(0)
#define STAMP_DECL do{}while(0)
#define BAR() bar()
#define STAMP_FLUSH() do{}while(0)
#define TRARG
#define TRPASS
#define TSARG
#define TSPASS

DEVI void poll32(v4u srd,u32 voff,u32 soff,u32 tag){
  #pragma unroll 1
  for(int it=0;it<SPIN_CAP;++it){
    u32 v;
    asm volatile("buffer_load_dword %0, %1, %2, %3 offen sc1\n\ts_waitcnt vmcnt(0)" : "=&v"(v) : "v"(voff), "s"(srd), "s"(soff) : "memory");
    if(all_eq(v,tag)) break;
    __builtin_amdgcn_s_sleep(12);
  }
}
template<int I,int N,class F> DEVI void sfor(F&& f){ if constexpr(I<N){ f(std::integral_constant<int,I>{}); sfor<I+1,N>(f); } }
AI constexpr u32 wslot(int c){ return WB+(c&3)*WSZ; }
AI constexpr u32 sx_(int c){ return c>=5?(c-5)*XS:wslot(c+2); }
AI constexpr u32 sb_(int c){ return c>=5?wslot(3)+(c-5)*8192:wslot(c+2)+16384; }

#define SG(m,n) __builtin_amdgcn_sched_group_barrier(m,n,0)
template<int NB>
DEVI void g_loop(u32 lbase,int lane,int gw,v16f (&acc)[3][2] TRARG TSARG){
  const int h=lane>>5, r=lane&31, rh=gw&1, u0=(gw>>1)*NB;
  u32 ax[4],aw,aw2;
  #pragma unroll
  for(int s=0;s<4;++s) ax[s]=lbase+rh*8192+r*128+((((2*(s^(2*rh))+h)^((r>>1)&7)))<<4);
  aw=lbase+WB+((lane>>4)&1)*(12288+32)+(8*h+((lane&15)>>2))*192+(lane&3)*8+u0*32;
  aw2=aw+6144-rh*6144; aw+=rh*6144;
  const u32 on2=((lane&3)==((lane>>4)&1))?0x3f803f80u:0u;
  const v4u ones={on2,on2,on2,on2};
  v4f cw[3]={};
  v4u X[2][2],T[2][3];
  auto rd=[&](int b,int s) AI {
    const u32 a=(s<2?aw:aw2)+(s&1)*3072;
    X[b][0]=ldsr128(ax[s]);
    #pragma unroll
    for(int t=0;t<NB;++t){ T[b][t]=ldstr(a+t*32,a+t*32+768); if(t==0) X[b][1]=ldsr128(ax[s]+4096); }
  };
  auto mm=[&](int b,bool cs) AI {
    #pragma unroll
    for(int t=0;t<NB;++t){ acc[t][0]=mfma(T[b][t],X[b][0],acc[t][0]); if(cs) cw[t]=mfma16(ones,T[b][t],cw[t]); acc[t][1]=mfma(T[b][t],X[b][1],acc[t][1]); }
  };
  auto mix=[&](int nv,bool cs) AI {
    SG(0x8,1); SG(0x100,3); if(cs) SG(0x8,2); else SG(0x8,1); SG(0x100,3);
    if(NB==3){ SG(0x8,1); SG(0x100,2); }
    SG(0x8,1); if(nv) SG(0x2,3);
    SG(0x8,5);
  };
  u32 d0=XS, d1=XS, d2=(u32)(-2*(int)XS);
  STAMP(3); CSTAMP(6);
  rd(0,0);
  #pragma unroll 1
  for(int c=0;c<8;++c){
    const u32 dw=((c&3)==3)?(u32)(-3*(int)WSZ):WSZ;
    SB; rd(1,1); mm(0,true); ax[0]+=d0; ax[1]+=d0; aw+=dw; mix(1,true);
    SB; asm volatile("":"+v"(ax[0]),"+v"(ax[1]),"+v"(aw));
    rd(0,2); mm(1,true); mix(0,true);
    SB; rd(1,3); mm(0,false); ax[2]+=d0; ax[3]+=d0; aw2+=dw; mix(1,false);
    SB; asm volatile("":"+v"(ax[2]),"+v"(ax[3]),"+v"(aw2));
    { const u32 t=d0; d0=d1; d1=d2; d2=t; }
    if(c==7){
      const int ln=lane_now();
      #pragma unroll
      for(int t=0;t<NB;++t) cw[t][0]=(ln&16)?cw[t][1]:cw[t][0];
      *(LAS v4f*)(lbase+L_CW+(gw*32+(ln&31))*16)=v4f{cw[0][0],cw[1][0],cw[2][0],0.f};
    }
    waitl0(); BAR();
    rd(0,0); mm(1,false); mix(0,false);
  }
  const int ln=lane_now();
  const v4f pc=*(LAS v4f*)(lbase+L_CW+((gw^1)*32+(ln&31))*16);
  const u32 hm=(ln&32)?0u:0xffffffffu;
  v4u M[2];
  #pragma unroll
  for(int m=0;m<2;++m){ const u32 nm=peek(lbase+L_MB+(rh*64+m*32+(ln&31))*4)&hm; M[m]=v4u{nm,nm,0u,0u}; }
  #pragma unroll
  for(int t=0;t<NB;++t){
    const float c=cw[t][0]+pc[t];
    const u32 c0=f2u(c)&0xffff0000u; const float r1=c-u2f(c0);
    const u32 c1=f2u(r1)&0xffff0000u; const float r2=r1-u2f(c1);
    const v4u F={(c0>>16)|c1,f2u(r2)>>16,0u,0u};
    acc[t][0]=mfma(F,M[0],acc[t][0]); acc[t][1]=mfma(F,M[1],acc[t][1]);
  }
}

__global__ NOPK __launch_bounds__(768,3) void fused_k(Args a){
  __shared__ __attribute__((aligned(256))) char smem[L_TOTAL];
  const u32 lbase=(u32)(size_t)(LAS char*)smem;
  const int tid=__builtin_amdgcn_workitem_id_x();
  const int wgid=__builtin_amdgcn_workgroup_id_x(), w=rfl(tid>>6);
  int lane=tid&63;
  const int xcd=wgid&7, idx=wgid>>3;
  const int tt=(xcd&1)*8+(idx&7), cg=(xcd>>1)*4+(idx>>3);
  const int nb=((cg&3)==3||cg==14)?2:3, blk0=11*(cg>>2)+3*(cg&3)-(cg==15?1:0);
  const int rt2=(xcd&3)*8+(idx&7), ct=(xcd>>2)*4+(idx>>3);
  const int ry=ROTP?2*(idx>>3):0, rz=ROTP?(11*(idx>>3))>>2:0;
  const u32 one=1;
  STAMP_DECL; STAMP(0); CSTAMP(12);
  const u64 tstart_=__builtin_amdgcn_s_memrealtime();
  #define PADTO() do{ while(__builtin_amdgcn_s_memrealtime()<tstart_+4500) __builtin_amdgcn_s_sleep(2); }while(0)

  if(w>=8){
    __builtin_amdgcn_s_setprio(3);
    const int lw=w-8, j=lane>>3, l7=lane&7;
    const v4u sx=mksrd(a.x,2048*1024);
    const v4u sb=mksrd(lw<2?a.bre:a.bim,512*1024);
    const u32 sz0=(u32)((l7^(j>>1))<<4), sz1=sz0^64u;
    const u32 xr=(tt*128+32*lw+16*(j>>2)+(j&3))*1024;
    const u32 vx0=xr+sz0, vx1=xr+3*1024+sz1, vx2=xr+6*1024+sz0, vx3=xr+9*1024+sz1;
    const u32 br=(cg*32+16*(lw&1)+j)*1024;
    const u32 vb0=br+sz0, vb1=br+7*1024+sz1;
    const u32 l4=lbase+lw*4096, l2=lbase+lw*2048;
    u32 kx=((tt*(ROT1&15)+cg*(ROT1>>4))&7)*128, kw=(kx+640)&0x380;
    #define SX(c) dma4<false,sx_(c)>(sx,vx0,vx1,vx2,vx3,kx,l4)
    #define SBB(c) do{ dma2<sb_(c)>(sb,vb0,vb1,kx,l2); kx=(kx+128)&0x380; }while(0)
    SX(0); SBB(0); SX(1); SBB(1);
    const v4u sws=mksrd(a.ws,WS_END);
    const v4u sw=mksrd(lw<2?a.wg:a.wu,512*2752);
    const v4u swd=mksrd(a.wd,1376*1024);
    const v4u sc=mksrd(lw<2?a.cre:a.cim,512*1024);
    const u32 vf=(lane&15)*128, lf=lbase+L_FLG+lw*256;
    const u32 fh=WS_FH+(rt2*32+(lw&1)*16)*128, fhh=WS_FHH+(rt2*32+lw*8)*128;
    u32 vw[6];
    #pragma unroll
    for(int i=0;i<3;++i){ const u32 q=i*1024+lane*16, row=q/192, col=q%192; const bool ok=col<(u32)nb*64;
      vw[i]=ok?(row+32*(lw&1))*2752+col+blk0*64-i*1024:OOB; vw[i+3]=vw[i]+16*2752; asm volatile("":"+v"(vw[i]),"+v"(vw[i+3])); }
    const u32 lwv=lbase+(lw>>1)*(12288+32)+(lw&1)*6144;
    #define GWA(c) dma3<wslot(c)>(sw,vw[0],vw[1],vw[2],kw*1376,lwv)
    #define GWB(c) do{ dma3<wslot(c)+3072>(sw,vw[3],vw[4],vw[5],kw*1376,lwv); kw=(kw+128)&0x380; }while(0)
    #define GX(c) do{ dma4<false,((c)%3)*XS>(sx,vx0,vx1,vx2,vx3,kx,l4); kx=(kx+128)&0x380; }while(0)
    SX(2); waitv<10>(); BAR();
    SBB(2); SX(3); waitv<10>(); BAR();
    SBB(3); SX(4); waitv<10>(); BAR();
    SBB(4); SX(5); waitv<10>(); BAR();
    SBB(5); SX(6); waitv<10>(); BAR();
    SBB(6); SX(7); waitv<10>(); BAR();
    STAMP(2);
    SBB(7); GWA(0); waitv<9>(); BAR();
    GWB(0); GWA(1); waitv<9>(); BAR();
    GWB(1); GWA(2); waitv<9>(); BAR();
    STAMP(3);
    GWB(2); GWA(3); GWB(3);
    const u32 tag=rfl(peek(lbase+L_TAG));
    u32 va=WS_HS+rt2*131072+lw*4096+lane*16;
    const u32 cr=(ct*64+(lw&1)*32+j)*1024;
    u32 vp0=cr+sz0, vp1=cr+7*1024+sz1, vp2=cr+14*1024+sz0, vp3=cr+21*1024+sz1;
    waitv<12>(); BAR();
    sfor<0,4>([&](auto C) AI {
      constexpr int c=C.value;
      GX(c+3); GWA(c+4); GWB(c+4);
      waitv<16>();
      if(WARMB){
        if constexpr(c==0) dma_dw<false>(sc,lane<32?(u32)(j*1024+l7*128):OOB,(ct*64+(idx&7)*8+(lw&1)*4)*1024,lbase+L_DUM);
        if constexpr(c==2) dma_dw<false>(swd,lane<43?(u32)(lane*1024):OOB,((idx&7)*172+lw*43)*1024+ct*128,lbase+L_DUM);
      }
      BAR();
    });
    u32 ka=ry, kb=ry;
    auto PA=[&](auto C) AI {
      constexpr int c=C.value;
      if constexpr(c==8){ va=WS_HH+rt2*86*2048+lw*4096+lane*16; ka=rz; }
      u32 so;
      if constexpr(c<8){ so=ka<<14; ka=(ka+1)&7; } else { so=(ka==10&&lw==3)?OOB:ka<<14; ka= ka==10?0:ka+1; }
      dma4<true,((c+2)%3)*XS>(sws,va,va,va,va,so,l4);
    };
    auto PB=[&](auto C) AI {
      constexpr int c=C.value;
      if constexpr(c==8){ vp0=(lw*32+j)*1024+ct*128+((l7^(((j>>1)&1)<<2))<<4); vp1=vp0+7*1024; vp2=vp0+14*1024; vp3=vp0+21*1024; kb=rz; }
      u32 so;
      if constexpr(c<8){ so=kb<<7; kb=(kb+1)&7; } else { so=kb<<17; kb= kb==10?0:kb+1; }
      dma4<false,wslot(c)>(c<8?sc:swd,vp0,vp1,vp2,vp3,so,l4);
    };
    #define IC(n) std::integral_constant<int,(n)>{}
    GX(7); PB(IC(0));
    waitv<14>();
    dma_dw<true>(sws,vf,fh,lf);
    BAR();
    PB(IC(1));
    waitv<4>();
    const bool early=all_eq(peek(lf+lane*4),tag);
    if(early) PA(IC(0));
    BAR();
    STAMP(4);
    PB(IC(2));
    if(early) PA(IC(1));
    BAR();
    PB(IC(3));
    STAMP(5);
    if(!early){ poll32(sws,vf,fh,tag); PA(IC(0)); PA(IC(1)); }
    STAMP(6);
    PA(IC(2));
    waitv<8>(); BAR();
    if(lane<32) dma_dw<true>(sws,lane*4,WS_STAT+rt2*512+lw*128,lbase+L_TAB+lw*128);
    { const u32 vt=(rt2*64+lw*16+j)*1024+ct*128+l7*16; dma2<L_XT>(sx,vt,vt+7*1024,0,l2); }
    waitv<7>(); BAR();
    sfor<1,18>([&](auto K) AI {
      constexpr int k=K.value;
      if constexpr(k==4) dma_dw<true>(sws,vf&1023u,fhh,lf);
      if constexpr(k==9){ if(wgid==0 && lw==0) dma_dw<true>(sws,vf*2,WS_FH+30*4096,lf); }
      if constexpr(k<=16) PA(IC(k+2));
      if constexpr(k<=15) PB(IC(k+3));
      waitv<(k==1?11:k<=15?12:k==16?8:0)>();
      if constexpr(k==5){
        STAMP(7);
        if(!all_eq(peek(lf+lane*4),tag)) poll32(sws,vf&1023u,fhh,tag);
        STAMP(8);
      }
      if constexpr(k==12){
        if(wgid==0 && lw==0){
          if(!all_eq(peek(lf+lane*4),tag)) poll32(sws,vf*2,WS_FH+30*4096,tag);
          const u32 e=peek(lbase+L_TAG+4)+1u;
          asm volatile("s_mov_b64 exec, 1\n\tbuffer_store_dword %0, off, %1, 0 sc1\n\ts_mov_b64 exec, -1" :: "v"(e), "s"(sws) : "memory");
        }
      }
      BAR();
    });
    STAMP(11);
    STAMP_FLUSH();
    return;
  }

  int h=lane>>5, r=lane&31;
  const rsrc_t rws=mkrsrc(a.ws,WS_END);
  u32 pend=0;
  u32 tag;
  auto try_flag=[&](bool force) AI {
    if(pend && (force || vm_idle())){
      waitv<0>();
      if(lane==0) __builtin_amdgcn_raw_buffer_store_b32(tag,rws,pend,0,16);
      pend=0;
    }
  };
  if(w>=4){
    const int gw=w-4, rh=w&1, rt=2*tt+rh;
    v16f acc[3][2]={};
    {
      u32 e=0;
      if(gw==0) e=__builtin_amdgcn_raw_buffer_load_b32(rws,0,WS_EPOCH,16);
      u32 ax[4];
      #pragma unroll
      for(int s=0;s<4;++s) ax[s]=lbase+sx_(0)+gw*4096+r*128+((((2*s+h)^((r>>1)&7)))<<4);
      u32 ab=lbase+sb_(0)+r*128+((((2*gw+h)^((r>>1)&7)))<<4);
      float s1=0.f,s2=0.f,c0=0.f,c1=0.f;
      sfor<0,8>([&](auto C) AI {
        constexpr int c=C.value;
        waitl0(); BAR();
        v4u X[4];
        #pragma unroll
        for(int s=0;s<4;++s) X[s]=ldsr128(ax[s]);
        const v4u t0=ldsr128(ab), t1=ldsr128(ab+4096);
        if constexpr(c<7){
          #pragma unroll
          for(int s=0;s<4;++s) ax[s]+=sx_(c+1)-sx_(c);
          ab+=sb_(c+1)-sb_(c);
        }
        if constexpr(c==1){ if(gw==0){ e=rfl(e); *(LAS v2u*)(lbase+L_TAG)=v2u{mktag(e),e}; } }
        #pragma unroll
        for(int s=0;s<4;++s)
          #pragma unroll
          for(int k=0;k<4;++k){ s1=dot2(X[s][k],0x3f803f80u,s1); s2=dot2(X[s][k],X[s][k],s2); }
        #pragma unroll
        for(int k=0;k<4;++k){ c0=dot2(t0[k],0x3f803f80u,c0); c1=dot2(t1[k],0x3f803f80u,c1); }
        asm volatile("":"+v"(s1),"+v"(s2),"+v"(c0),"+v"(c1),"+v"(ax[0]),"+v"(ax[1]),"+v"(ax[2]),"+v"(ax[3]),"+v"(ab));
      });
      STAMP(1);
      sw32(c0,c1);
      *(LAS float*)(lbase+L_CSP+(gw*64+lane)*4)=c0+c1;
      sw32(s1,s2); s1+=s2; s2=s1; sw32(s1,s2);
      const float m=s1*(1.f/512.f);
      const float var=relu(__builtin_fmaf(s2,1.f/512.f,-m*m));
      const u32 vm=pk2(var,m);
      const float rs=__builtin_amdgcn_rcpf(rbf(__builtin_amdgcn_sqrtf(rbf(u2f(vm<<16)+1e-5f))));
      const int tok=32*gw+16*((r>>2)&1)+8*(r>>4)+4*((r>>3)&1)+(r&3);
      if(h) *(LAS u32*)(lbase+L_MB+(gw*32+r)*4)=((vm>>16)|(vm&0xffff0000u))^0x80008000u;
      else *(LAS v2f*)(lbase+L_TAB+tok*8)=v2f{rs,rs*u2f(vm&0xffff0000u)};
      STAMP(2);
      waitl0(); BAR();
      tag=rfl(peek(lbase+L_TAG));
    }
    __builtin_amdgcn_s_setprio(2);
    const int u0=(gw>>1)*nb;
    if(nb==3) g_loop<3>(lbase,lane,gw,acc TRPASS TSPASS); else g_loop<2>(lbase,lane,gw,acc TRPASS TSPASS);
    STAMP(4); CSTAMP(7);
    lane=lane_now();
    #pragma unroll
    for(int t=0;t<3;++t) if(t<nb){
      #pragma unroll
      for(int m=0;m<2;++m){
        v4u v;
        #pragma unroll
        for(int k=0;k<4;++k) v[k]=pk2(relu(acc[t][m][2*k])*acc[t][m][2*k+8],relu(acc[t][m][2*k+1])*acc[t][m][2*k+9]);
        st128(v,rws,lane*16+WS_HH+((rt*86+2*blk0+u0+t)*2+m)*1024);
      }
    }
    pend=WS_FHH+(rt*32+cg*2+(gw>>1))*128;
    STAMP(5);
  } else {
    const int ch=cg*32+r;
    __builtin_amdgcn_s_setprio(3);
    const u16 hlr=a.lre[ch], hli=a.lim[ch], hdt=a.ldt[ch];
    *(LAS u32*)(lbase+L_HP+(w*32+r)*16+8)=0u;
    const float lr=b2f(hlr), li=b2f(hli), dt=rbf(__builtin_expf(b2f(hdt)));
    cf lam,coef;
    { const float ex=__builtin_expf(dt*lr); float sn,cs_; sincos_(dt*li,sn,cs_); lam=cf{ex*cs_,ex*sn};
      const float dd=1.f/(lr*lr+li*li); coef=cmul(cf{lam.r-1.f,lam.i},cf{lr*dd,-li*dd}); }
    cf l16=cmul(lam,lam); l16=cmul(l16,l16); l16=cmul(l16,l16); l16=cmul(l16,l16);
    cf l32=cmul(l16,l16);
    cf l1024,wq,pw;
    { const cf l64=cmul(l32,l32), l128=cmul(l64,l64), l256=cmul(l128,l128), l512=cmul(l256,l256), o={1.f,0.f};
      l1024=cmul(l512,l512);
      wq=cmul(csel(h,l128,o),cmul(csel(w&1,l256,o),csel(w&2,l512,o)));
      pw=cmul(csel(w&1,l32,o),csel(w&2,l64,o)); }
    asm volatile("":"+v"(lam.r),"+v"(lam.i),"+v"(coef.r),"+v"(coef.i),"+v"(l16.r),"+v"(l16.i),"+v"(l32.r),"+v"(l32.i));
    asm volatile("":"+v"(l1024.r),"+v"(l1024.i),"+v"(wq.r),"+v"(wq.i),"+v"(pw.r),"+v"(pw.i));
    v16f are={},aim={};
    {
      u32 ax[4],ab[4];
      #pragma unroll
      for(int s=0;s<4;++s){ const u32 t=lbase+r*128+((((2*s+h)^((r>>1)&7)))<<4); ab[s]=t+sb_(0); ax[s]=t+sx_(0)+w*4096; }
      v4u X[2][2],T[2][2][2];
      auto rd=[&](int b) AI {
        #pragma unroll
        for(int s=0;s<2;++s){ X[b][s]=ldsr128(ax[2*b+s]); T[b][s][0]=ldsr128(ab[2*b+s]); T[b][s][1]=ldsr128(ab[2*b+s]+4096); } };
      auto mm=[&](int b) AI {
        #pragma unroll
        for(int s=0;s<2;++s){ are=mfma(X[b][s],T[b][s][0],are); aim=mfma(X[b][s],T[b][s][1],aim); }
        asm volatile("":"+v"(are),"+v"(aim)); };
      STAMP(1);
      waitl0(); BAR();
      rd(0);
      sfor<0,8>([&](auto C) AI {
        constexpr int c=C.value;
        SB; rd(1);
        SB; mm(0);
        if constexpr(c<7){
          #pragma unroll
          for(int s=0;s<4;++s){ ax[s]+=sx_(c+1)-sx_(c); ab[s]+=sb_(c+1)-sb_(c); asm volatile("":"+v"(ax[s]),"+v"(ab[s])); }
          SB;
          waitl0(); BAR();
          rd(0);
        }
        SB; mm(1);
      });
    }
    STAMP(2);
    waitl0(); BAR();
    int left=8;
    #define SBAR() do{ if(left>0){ BAR(); --left; } else __builtin_amdgcn_s_sleep(8); }while(0)
    tag=rfl(peek(lbase+L_TAG));
    cf cs={0.f,0.f};
    #pragma unroll
    for(int k=0;k<4;++k){ const u32 b=lbase+L_CSP+(k*64+r)*4; cs.r+=*(LAS float*)b; cs.i+=*(LAS float*)(b+128); }
    cf uu[16]; cf ea={0.f,0.f};
    #pragma unroll
    for(int k=0;k<16;k+=2){
      const v4f st=*(LAS v4f*)(lbase+L_TAB+(32*w+16*h+k)*8);
      #pragma unroll
      for(int d=0;d<2;++d){
        const cf v={__builtin_fmaf(are[k+d],st[2*d],-st[2*d+1]*cs.r),__builtin_fmaf(aim[k+d],st[2*d],-st[2*d+1]*cs.i)};
        uu[k+d]=v; ea=cfma(lam,ea,v);
      }
    }
    cf elo=ea,ehi=ea; sw32(elo.r,ehi.r); sw32(elo.i,ehi.i);
    { const cf ew=cfma(l16,elo,ehi); *(LAS v2f*)(lbase+L_AGG+(w*32+r)*8)=v2f{ew.r,ew.i}; }
    STAMP(3);
    waitl0(); SBAR();
    cf Q;
    { const u32 b=lbase+L_AGG+r*8;
      const cf a0=ldsc(b), a1=ldsc(b+256), a2=ldsc(b+512), a3=ldsc(b+768);
      const cf Q2=cfma(l32,a0,a1), Q3=cfma(l32,Q2,a2);
      if(w==3){ const cf E=cfma(l32,Q3,a3); if(lane<32) st128(v4u{f2u(E.r),tag,f2u(E.i),tag},rws,ch*16+WS_GRAN+tt*8192); }
      Q= w==0?cf{0.f,0.f}: w==1?a0: w==2?Q2:Q3; }
    STAMP(4);
    if(cg==0) __builtin_amdgcn_raw_buffer_store_b32(peek(lbase+L_TAB+w*256+lane*4),rws,WS_STAT+tt*1024+w*256+lane*4,0,16);
    #pragma unroll
    for(int k=0;k<16;++k){ uu[k]=cmul(coef,uu[k]); asm volatile("":"+v"(uu[k].r),"+v"(uu[k].i)); }
    SBAR();
    cf H={0.f,0.f};
    if(tt>0){
      const int p0=tt-1-2*w-h, p1=p0-8;
      const u32 o0=p0>=0?(u32)(p0*8192+ch*16):OOB, o1=p1>=0?(u32)(p1*8192+ch*16):OOB;
      const u32 t0=p0>=0?tag:0u, t1=p1>=0?tag:0u;
      v4u g0,g1;
      __builtin_amdgcn_s_sleep(LBSLEEP);
      #pragma unroll 1
      for(int it=0;it<SPIN_CAP;++it){
        asm volatile("":::"memory");
        g0=__builtin_amdgcn_raw_buffer_load_b128(rws,o0,WS_GRAN,16);
        g1=__builtin_amdgcn_raw_buffer_load_b128(rws,o1,WS_GRAN,16);
        SBAR();
        if(all_eq((g0[1]^t0)|(g0[3]^t0)|(g1[1]^t1)|(g1[3]^t1),0u)) break;
      }
      cf hp=cmul(wq,cfma(l1024,cf{u2f(g1[0]),u2f(g1[2])},cf{u2f(g0[0]),u2f(g0[2])}));
      cf h2=hp; sw32(hp.r,h2.r); sw32(hp.i,h2.i);
      *(LAS v4u*)(lbase+L_HP+(w*32+r)*16)=v4u{f2u(hp.r+h2.r),f2u(hp.i+h2.i),1u,0u};
      waitl0();
      v4u a0,a1,a2,a3;
      #pragma unroll 1
      for(int it=0;it<SPIN_CAP*8;++it){
        SBAR();
        const u32 b=lbase+L_HP+r*16;
        a0=*(volatile LAS v4u*)b; a1=*(volatile LAS v4u*)(b+512); a2=*(volatile LAS v4u*)(b+1024); a3=*(volatile LAS v4u*)(b+1536);
        if(all_eq(a0[2]&a1[2]&a2[2]&a3[2],1u)) break;
      }
      H=cf{(u2f(a0[0])+u2f(a1[0]))+(u2f(a2[0])+u2f(a3[0])),(u2f(a0[1])+u2f(a1[1]))+(u2f(a2[1])+u2f(a3[1]))};
    }
    STAMP(5);
    cf hc=cfma(pw,H,Q);
    hc=cmul(coef,csel(h,cfma(l16,hc,elo),hc));
    const int rt=2*tt+(w>>1);
    const u32 hso=WS_HS+rt*131072+(cg>>1)*16384+(((cg&1)*2+(r>>4))*2+(w&1))*1024+h*512;
    #pragma unroll
    for(int m=0;m<2;++m){
      cf hv[8];
      #pragma unroll
      for(int jj=0;jj<8;++jj){ hc=cfma(lam,hc,uu[8*m+jj]); hv[jj]=hc; }
      const u32 o=hso+m*256+(((lane&15)^(8*h+4*m))<<4);
      st128(v4u{pk2(hv[0].r,hv[1].r),pk2(hv[2].r,hv[3].r),pk2(hv[4].r,hv[5].r),pk2(hv[6].r,hv[7].r)},rws,o);
      st128(v4u{pk2(-hv[0].i,-hv[1].i),pk2(-hv[2].i,-hv[3].i),pk2(-hv[4].i,-hv[5].i),pk2(-hv[6].i,-hv[7].i)},rws,o+8192);
      if(m==0 && left>0){ BAR(); --left; }
    }
    pend=WS_FH+(rt*32+(cg&1)*16+(cg>>1)*2+(w&1))*128;
    STAMP(6);
    #pragma unroll 1
    while(left>0){
      BAR(); --left;
      #pragma unroll 1
      for(int it=0;it<6 && pend;++it){ try_flag(false); if(pend) __builtin_amdgcn_s_sleep(1); else STAMP(7); }
    }
    if(pend){ try_flag(true); STAMP(7); }
  }
  lane=lane_now(); h=lane>>5; r=lane&31;
  const int g=lane>>4, m2=w&1, tc=(w>>1)&1, kh=w>>2;
  const int dcol=ct*64+tc*32+r;
  const float dsk=b2f(a.dsk[dcol]);
  v16f ay={},az={};
  const int q=(lane&15)>>2, p=lane&3, b4=g&1, gp=b4+2*(p&1);
  u32 a0=lbase+2*XS+(kh*8+m2)*1024+(gp*16+((8*h+q)^(4*gp)))*16+8*(p>>1);
  u32 b0=lbase+WB+kh*8192+(tc*32+r)*128+((h^((r>>1)&7))<<4);
  u32 za=lbase+XS+(kh*8+m2)*1024+lane*16;
  u32 zb=lbase+WB+(64*kh+4*h+q)*128+((tc*64+b4*32+8*p)^(((q>>1)&1)<<6));
  asm volatile("":"+v"(za),"+v"(zb));
  u32 d0=(u32)(-2*(int)XS), d1=XS, d2=XS, e0=WSZ, e1=WSZ, e2=WSZ, e3=(u32)(-3*(int)WSZ);
  v4u A[2][4],B[2][4];
  auto rot=[&]() AI { { const u32 t=d0; d0=d1; d1=d2; d2=t; } { const u32 t=e0; e0=e1; e1=e2; e2=e3; e3=t; } };
  auto rdy=[&](int b) AI {
    #pragma unroll
    for(int s=0;s<4;++s){ A[b][s]=ldstr(a0+s*2048,(a0^64)+s*2048); B[b][s]=ldsr128(b0^(s*32)); }
    a0+=d0; b0+=e0; rot();
  };
  auto rdz=[&](int b) AI {
    #pragma unroll
    for(int s=0;s<4;++s){ A[b][s]=ldsr128(za+s*2048); B[b][s]=ldstr(zb+s*2048,zb+s*2048+1024); }
    za+=d0; zb+=e0; rot();
  };
  auto mm4=[&](int b,v16f& c) AI {
    #pragma unroll
    for(int s=0;s<4;++s) c=mfma(A[b][s],B[b][s],c);
    #pragma unroll
    for(int s=0;s<4;++s){ SG(0x8,1); SG(0x100,3); }
    SB; asm volatile("":"+v"(c));
  };
  #define NXT() do{ waitl0(); BAR(); }while(0)
  const int tk=32*m2+16*h+8*kh;
  STAMP(8);
  __builtin_amdgcn_s_setprio(0);
  NXT(); rdy(0);
  STAMP(9);
  #pragma unroll 1
  for(int c=0;c<3;++c){
    NXT(); rdy(1); mm4(0,ay); try_flag(false);
    NXT(); rdy(0); mm4(1,ay); try_flag(false);
  }
  NXT(); rdy(1); mm4(0,ay); try_flag(true);
  NXT(); rdz(0); mm4(1,ay);
  STAMP(10);
  float sv[8], xf[8], r2[8];
  #pragma unroll 1
  for(int c=0;c<5;++c){
    NXT(); rdz(1); mm4(0,az);
    if(c==1){
      #pragma unroll
      for(int k=0;k<8;++k){
        const v2f st=*(LAS v2f*)(lbase+L_TAB+(tk+k)*8);
        xf[k]=b2f(*(LAS u16*)(lbase+L_XT+(tk+k)*128+(tc*32+r)*2));
        sv[k]=__builtin_fmaf(xf[k],st[0],-st[1])*dsk; r2[k]=st[0]*st[0];
      }
    }
    NXT(); rdz(0); mm4(1,az);
  }
  {
    const u32 xo=lbase+w*4096+lane*16, xi=lbase+(w^4)*4096+lane*16;
    float zv[8];
    if(kh){ *(LAS v4f*)xo=v4f{ay[0],ay[1],ay[2],ay[3]}; *(LAS v4f*)(xo+1024)=v4f{ay[4],ay[5],ay[6],ay[7]};
      #pragma unroll
      for(int k=0;k<8;++k) sv[k]=__builtin_fmaf(2.f,ay[8+k],sv[k]); }
    else  { *(LAS v4f*)xo=v4f{ay[8],ay[9],ay[10],ay[11]}; *(LAS v4f*)(xo+1024)=v4f{ay[12],ay[13],ay[14],ay[15]};
      #pragma unroll
      for(int k=0;k<8;++k) sv[k]=__builtin_fmaf(2.f,ay[k],sv[k]); }
    SB;
    #pragma unroll
    for(int s=0;s<4;++s) az=mfma(A[0][s],B[0][s],az);
    STAMP(11);
    if(kh){ *(LAS v4f*)(xo+2048)=v4f{az[0],az[1],az[2],az[3]}; *(LAS v4f*)(xo+3072)=v4f{az[4],az[5],az[6],az[7]};
      #pragma unroll
      for(int k=0;k<8;++k) zv[k]=az[8+k]; }
    else  { *(LAS v4f*)(xo+2048)=v4f{az[8],az[9],az[10],az[11]}; *(LAS v4f*)(xo+3072)=v4f{az[12],az[13],az[14],az[15]};
      #pragma unroll
      for(int k=0;k<8;++k) zv[k]=az[k]; }
    waitl0(); bar();
    const v4f i2=*(LAS v4f*)(xi+2048), i3=*(LAS v4f*)(xi+3072), i0=*(LAS v4f*)xi, i1=*(LAS v4f*)(xi+1024);
    const rsrc_t ro=mkrsrc(a.out,2048*512*4);
    #pragma unroll
    for(int k=0;k<8;++k){
      const float z=(zv[k]+(k<4?i2[k&3]:i3[k&3]))*r2[k];
      const float gt=z*__builtin_amdgcn_rcpf(1.f+__builtin_amdgcn_exp2f(z*-1.44269504f));
      __builtin_amdgcn_raw_buffer_store_b32(f2u(__builtin_fmaf(__builtin_fmaf(2.f,k<4?i0[k&3]:i1[k&3],sv[k]),gt,xf[k])),ro,(u32)(((rt2*64+tk+k)*512+dcol)*4),0,16);
    }
  }
  PADTO();
}

extern "C" void kernel_launch(void* const* d_in, const int* in_sizes, int n_in,
                              void* d_out, int out_size, void* d_ws, size_t ws_size,
                              hipStream_t stream) {
  Args a;
  a.x=(const u16*)d_in[0]; a.lre=(const u16*)d_in[1]; a.lim=(const u16*)d_in[2];
  a.bre=(const u16*)d_in[3]; a.bim=(const u16*)d_in[4]; a.cre=(const u16*)d_in[5]; a.cim=(const u16*)d_in[6];
  a.ldt=(const u16*)d_in[7]; a.dsk=(const u16*)d_in[8];
  a.wg=(const u16*)d_in[10]; a.wu=(const u16*)d_in[11]; a.wd=(const u16*)d_in[12];
  a.out=(float*)d_out; a.ws=(char*)d_ws;
  fused_k<<<256,768,0,stream>>>(a);
}
```

```cpp
#include <hip/hip_runtime.h>
#include <hip/hip_bf16.h>
#include <type_traits>

typedef unsigned u32; typedef unsigned long long u64; typedef unsigned short u16;
typedef u32 v4u __attribute__((ext_vector_type(4)));
typedef u32 v2u __attribute__((ext_vector_type(2)));
typedef short v4s __attribute__((ext_vector_type(4)));
typedef __bf16 v2bf __attribute__((ext_vector_type(2)));
typedef __bf16 v8bf __attribute__((ext_vector_type(8)));
typedef float v2f __attribute__((ext_vector_type(2)));
typedef float v4f __attribute__((ext_vector_type(4)));
typedef float v16f __attribute__((ext_vector_type(16)));
#define LAS __attribute__((address_space(3)))
#define DEVI __device__ __forceinline__
#define AI __attribute__((always_inline))
#define SB __builtin_amdgcn_sched_barrier(0)
#if defined(__HIP_DEVICE_COMPILE__)
#define NOPK __attribute__((target("no-packed-fp32-ops")))
#else
#define NOPK
#endif

#define ROT1 0x01
#define ROTP 1
#define LBSLEEP 6
#define WARMB 0

constexpr u32 WS_EPOCH = 0;
constexpr u32 WS_STAT  = 4096;
constexpr u32 WS_FH    = 32768;
constexpr u32 WS_FHH   = WS_FH + 131072;
constexpr u32 WS_GRAN  = WS_FHH + 131072;
constexpr u32 WS_HH    = WS_GRAN + 131072;
constexpr u32 WS_HS    = WS_HH + 32*86*2048;
constexpr u32 WS_END   = WS_HS + 32*131072;
constexpr u32 WS_STAMP = 16u<<20;
constexpr u32 XS = 16384;
constexpr u32 WB = 49152, WSZ = 24832;
constexpr u32 L_XT = WB+4*WSZ;
constexpr u32 L_TAB  = L_XT+8192;
constexpr u32 L_MB   = L_TAB + 1024;
constexpr u32 L_CSP  = L_MB + 512;
constexpr u32 L_FLG  = L_CSP;
constexpr u32 L_AGG  = L_CSP + 1024;
constexpr u32 L_HP   = L_AGG + 1024;
constexpr u32 L_DUM  = L_AGG;
constexpr u32 L_TAG  = L_HP + 2048;
constexpr u32 L_CW   = L_XT;
constexpr u32 L_TOTAL = 163840;
static_assert(L_TAG+16<=L_TOTAL,"lds");
constexpr u32 OOB = 0x40000000u;
constexpr int SPIN_CAP = 1<<18;

struct Args { const u16 *x,*lre,*lim,*bre,*bim,*cre,*cim,*ldt,*dsk,*wg,*wu,*wd; float* out; char* ws; };

DEVI u32 f2u(float f){ return __builtin_bit_cast(u32,f); }
DEVI float u2f(u32 u){ return __builtin_bit_cast(float,u); }
DEVI u32 rfl(u32 v){ return __builtin_amdgcn_readfirstlane(v); }
DEVI bool all_eq(u32 v,u32 t){ return __builtin_amdgcn_ballot_w64(v!=t)==0ull; }
DEVI v4u mksrd(const void* p, u32 bytes){ u64 a=(u64)p; v4u s; s.x=rfl((u32)a); s.y=rfl((u32)(a>>32)); s.z=bytes; s.w=0x00020000u; return s; }
#define DM0 "s_add_u32 m0, %[b], %[i]\n\ts_nop 0\n\t"
#define DLD(n,off,pol) "buffer_load_dwordx4 %[v" #n "], %[r], %[s] offen" off pol " lds\n\t"
template<bool SC1,u32 IMM> DEVI void dma4(v4u srd,u32 v0,u32 v1,u32 v2,u32 v3,u32 soff,u32 base){
  if(SC1) asm volatile(DM0 DLD(0,""," sc1") DLD(1," offset:1024"," sc1") DLD(2," offset:2048"," sc1") DLD(3," offset:3072"," sc1")
      :: [v0]"v"(v0),[v1]"v"(v1),[v2]"v"(v2),[v3]"v"(v3),[r]"s"(srd),[s]"s"(soff),[b]"s"(base),[i]"n"(IMM) : "memory","scc");
  else asm volatile(DM0 DLD(0,"","") DLD(1," offset:1024","") DLD(2," offset:2048","") DLD(3," offset:3072","")
      :: [v0]"v"(v0),[v1]"v"(v1),[v2]"v"(v2),[v3]"v"(v3),[r]"s"(srd),[s]"s"(soff),[b]"s"(base),[i]"n"(IMM) : "memory","scc");
}
template<u32 IMM> DEVI void dma3(v4u srd,u32 v0,u32 v1,u32 v2,u32 soff,u32 base){
  asm volatile(DM0 DLD(0,"","") DLD(1," offset:1024","") DLD(2," offset:2048","")
      :: [v0]"v"(v0),[v1]"v"(v1),[v2]"v"(v2),[r]"s"(srd),[s]"s"(soff),[b]"s"(base),[i]"n"(IMM) : "memory","scc");
}
template<u32 IMM> DEVI void dma2(v4u srd,u32 v0,u32 v1,u32 soff,u32 base){
  asm volatile(DM0 DLD(0,"","") DLD(1," offset:1024","")
      :: [v0]"v"(v0),[v1]"v"(v1),[r]"s"(srd),[s]"s"(soff),[b]"s"(base),[i]"n"(IMM) : "memory","scc");
}
template<bool SC1> DEVI void dma_dw(v4u srd,u32 voff,u32 soff,u32 lds){
  if(SC1) asm volatile("s_mov_b32 m0, %3\n\ts_nop 0\n\tbuffer_load_dword %0, %1, %2 offen sc1 lds" :: "v"(voff), "s"(srd), "s"(soff), "s"(lds) : "memory");
  else    asm volatile("s_mov_b32 m0, %3\n\ts_nop 0\n\tbuffer_load_dword %0, %1, %2 offen lds" :: "v"(voff), "s"(srd), "s"(soff), "s"(lds) : "memory");
}
DEVI void dma_x4(v4u srd,u32 voff,u32 soff,u32 lds){
  asm volatile("s_mov_b32 m0, %3\n\ts_nop 0\n\tbuffer_load_dwordx4 %0, %1, %2 offen sc1 lds" :: "v"(voff), "s"(srd), "s"(soff), "s"(lds) : "memory");
}
template<int N> DEVI void waitv(){ asm volatile("s_waitcnt vmcnt(%0)"::"n"(N):"memory"); }
DEVI void waitl0(){ asm volatile("s_waitcnt lgkmcnt(0)":::"memory"); }
DEVI void bar(){ asm volatile("":::"memory"); __builtin_amdgcn_s_barrier(); asm volatile("":::"memory"); SB; }
template<u32 OFF> DEVI void sig(u32 vaddr,u32 one){ asm volatile("s_mov_b64 exec, 1\n\tds_add_u32 %0, %1 offset:%2\n\ts_mov_b64 exec, -1" :: "v"(vaddr), "v"(one), "n"(OFF) : "memory"); }
DEVI u32 peek(u32 vaddr){ return *(volatile LAS u32*)vaddr; }
DEVI bool vm_idle(){ u32 v; asm volatile("s_getreg_b32 %0, hwreg(HW_REG_IB_STS, 0, 4)":"=s"(v)); return v==0u; }
DEVI int lane_now(){ int l; asm volatile("v_mbcnt_lo_u32_b32 %0, -1, 0\n\tv_mbcnt_hi_u32_b32 %0, -1, %0":"=v"(l)); return l; }
DEVI float b2f(u16 v){ return u2f(((u32)v)<<16); }
DEVI u32 pk2(float lo,float hi){ v2f p={lo,hi}; return __builtin_bit_cast(u32,__builtin_convertvector(p,v2bf)); }
DEVI float rbf(float f){ return u2f(pk2(f,f)<<16); }
DEVI float relu(float x){ int i=__builtin_bit_cast(int,x); return __builtin_bit_cast(float,i>0?i:0); }
DEVI float dot2(u32 a,u32 b,float c){ return __builtin_amdgcn_fdot2_f32_bf16(__builtin_bit_cast(v2bf,a),__builtin_bit_cast(v2bf,b),c,false); }
DEVI v16f mfma(v4u a,v4u b,v16f c){ return __builtin_amdgcn_mfma_f32_32x32x16_bf16(__builtin_bit_cast(v8bf,a),__builtin_bit_cast(v8bf,b),c,0,0,0); }
DEVI v4f mfma16(v4u a,v4u b,v4f c){ return __builtin_amdgcn_mfma_f32_16x16x32_bf16(__builtin_bit_cast(v8bf,a),__builtin_bit_cast(v8bf,b),c,0,0,0); }
DEVI v4u ldsr128(u32 a){ return *(LAS v4u*)a; }
DEVI v4u ldstr(u32 a0,u32 a1){
  v4s x=__builtin_amdgcn_ds_read_tr16_b64_v4i16((LAS v4s*)a0);
  v4s y=__builtin_amdgcn_ds_read_tr16_b64_v4i16((LAS v4s*)a1);
  return __builtin_bit_cast(v4u,__builtin_shufflevector(x,y,0,1,2,3,4,5,6,7));
}
DEVI void sw16(float& a,float& b){ v2u r=__builtin_amdgcn_permlane16_swap(f2u(a),f2u(b),false,false); a=u2f(r[0]); b=u2f(r[1]); }
DEVI void sw32(float& a,float& b){ v2u r=__builtin_amdgcn_permlane32_swap(f2u(a),f2u(b),false,false); a=u2f(r[0]); b=u2f(r[1]); }
template<int CTRL> DEVI float dppadd(float v){ return v+u2f(__builtin_amdgcn_update_dpp(0u,f2u(v),CTRL,0xf,0xf,true)); }
DEVI void wsum4(float& a,float& b,float& c,float& d){
  sw32(a,b); sw32(c,d); float v=a+b, t=c+d;
  sw16(v,t); v+=t;
  v=dppadd<0xB1>(v); v=dppadd<0x4E>(v); v=dppadd<0x141>(v); v=dppadd<0x140>(v);
  a=u2f(__builtin_amdgcn_readlane(f2u(v),0)); c=u2f(__builtin_amdgcn_readlane(f2u(v),16));
  b=u2f(__builtin_amdgcn_readlane(f2u(v),32)); d=u2f(__builtin_amdgcn_readlane(f2u(v),48));
}
struct cf{ float r,i; };
DEVI cf cmul(cf a,cf b){ return cf{__builtin_fmaf(-a.i,b.i,a.r*b.r), __builtin_fmaf(a.i,b.r,a.r*b.i)}; }
DEVI cf cfma(cf a,cf b,cf c){ return cf{__builtin_fmaf(-a.i,b.i,__builtin_fmaf(a.r,b.r,c.r)), __builtin_fmaf(a.i,b.r,__builtin_fmaf(a.r,b.i,c.i))}; }
DEVI cf csel(bool c,cf a,cf b){ return cf{c?a.r:b.r, c?a.i:b.i}; }
DEVI cf ldsc(u32 a){ v2f v=*(LAS v2f*)a; return cf{v[0],v[1]}; }
DEVI void sincos_(float x,float& s,float& c){
  const float n=__builtin_rintf(x*0.636619772f);
  float r=__builtin_fmaf(n,-1.57079601e+00f,x); r=__builtin_fmaf(n,-3.13916473e-07f,r); r=__builtin_fmaf(n,-5.39030253e-15f,r);
  const float z=r*r;
  float ps=__builtin_fmaf(__builtin_fmaf(__builtin_fmaf(2.86567956e-6f,z,-1.98559923e-4f),z,8.33338592e-3f),z,-1.66666672e-1f);
  ps=__builtin_fmaf(ps*z,r,r);
  float pc=__builtin_fmaf(__builtin_fmaf(__builtin_fmaf(2.44677067e-5f,z,-1.38877297e-3f),z,4.16666567e-2f),z,-0.5f);
  pc=__builtin_fmaf(pc,z,1.f);
  const int q=(int)n;
  const float a=(q&1)?pc:ps, b=(q&1)?ps:pc;
  s=(q&2)?-a:a; c=((q+1)&2)?-b:b;
}
typedef __amdgpu_buffer_rsrc_t rsrc_t;
DEVI rsrc_t mkrsrc(const void* p,u32 bytes){ return __builtin_amdgcn_make_buffer_rsrc((void*)p,0,bytes,0x00020000); }
DEVI void st128(v4u v,rsrc_t r,u32 off){ __builtin_amdgcn_raw_buffer_store_b128(v,r,off,0,16); }
DEVI u32 mktag(u32 e){ return e*0x9E3779B1u+0x7F4A7C15u; }

#define STAMP(k) do{}while(0)
#define CSTAMP(k) do{}while(0)
#define TRACE(k) do{}while(0)
#define STAMP_DECL do{}while(0)
#define BAR() bar()
#define STAMP_FLUSH() do{}while(0)
#define TRARG
#define TRPASS
#define TSARG
#define TSPASS

DEVI void poll32(v4u srd,u32 voff,u32 soff,u32 tag){
  #pragma unroll 1
  for(int it=0;it<SPIN_CAP;++it){
    u32 v;
    asm volatile("buffer_load_dword %0, %1, %2, %3 offen sc1\n\ts_waitcnt vmcnt(0)" : "=&v"(v) : "v"(voff), "s"(srd), "s"(soff) : "memory");
    if(all_eq(v,tag)) break;
    __builtin_amdgcn_s_sleep(12);
  }
}
template<int I,int N,class F> DEVI void sfor(F&& f){ if constexpr(I<N){ f(std::integral_constant<int,I>{}); sfor<I+1,N>(f); } }
AI constexpr u32 wslot(int c){ return WB+(c&3)*WSZ; }
AI constexpr u32 sx_(int c){ return c>=5?(c-5)*XS:wslot(c+2); }
AI constexpr u32 sb_(int c){ return c>=5?wslot(3)+(c-5)*8192:wslot(c+2)+16384; }

#define SG(m,n) __builtin_amdgcn_sched_group_barrier(m,n,0)
template<int NB>
DEVI void g_loop(u32 lbase,int lane,int gw,v16f (&acc)[3][2] TRARG TSARG){
  const int h=lane>>5, r=lane&31, rh=gw&1, u0=(gw>>1)*NB;
  u32 ax[4],aw,aw2;
  #pragma unroll
  for(int s=0;s<4;++s) ax[s]=lbase+rh*8192+r*128+((((2*(s^(2*rh))+h)^((r>>1)&7)))<<4);
  aw=lbase+WB+((lane>>4)&1)*(12288+32)+(8*h+((lane&15)>>2))*192+(lane&3)*8+u0*32;
  aw2=aw+6144-rh*6144; aw+=rh*6144;
  const u32 on2=((lane&3)==((lane>>4)&1))?0x3f803f80u:0u;
  const v4u ones={on2,on2,on2,on2};
  v4f cw[3]={};
  v4u X[2][2],T[2][3];
  auto rd=[&](int b,int s) AI {
    const u32 a=(s<2?aw:aw2)+(s&1)*3072;
    X[b][0]=ldsr128(ax[s]);
    #pragma unroll
    for(int t=0;t<NB;++t){ T[b][t]=ldstr(a+t*32,a+t*32+768); if(t==0) X[b][1]=ldsr128(ax[s]+4096); }
  };
  auto mm=[&](int b,bool cs) AI {
    #pragma unroll
    for(int t=0;t<NB;++t){ acc[t][0]=mfma(T[b][t],X[b][0],acc[t][0]); if(cs) cw[t]=mfma16(ones,T[b][t],cw[t]); acc[t][1]=mfma(T[b][t],X[b][1],acc[t][1]); }
  };
  auto mix=[&](int nv,bool cs) AI {
    SG(0x8,1); SG(0x100,3); if(cs) SG(0x8,2); else SG(0x8,1); SG(0x100,3);
    if(NB==3){ SG(0x8,1); SG(0x100,2); }
    SG(0x8,1); if(nv) SG(0x2,3);
    SG(0x8,5);
  };
  u32 d0=XS, d1=XS, d2=(u32)(-2*(int)XS);
  STAMP(3); CSTAMP(6);
  rd(0,0);
  #pragma unroll 1
  for(int c=0;c<8;++c){
    const u32 dw=((c&3)==3)?(u32)(-3*(int)WSZ):WSZ;
    SB; rd(1,1); mm(0,true); ax[0]+=d0; ax[1]+=d0; aw+=dw; mix(1,true);
    SB; asm volatile("":"+v"(ax[0]),"+v"(ax[1]),"+v"(aw));
    rd(0,2); mm(1,true); mix(0,true);
    SB; rd(1,3); mm(0,false); ax[2]+=d0; ax[3]+=d0; aw2+=dw; mix(1,false);
    SB; asm volatile("":"+v"(ax[2]),"+v"(ax[3]),"+v"(aw2));
    { const u32 t=d0; d0=d1; d1=d2; d2=t; }
    if(c==7){
      const int ln=lane_now();
      #pragma unroll
      for(int t=0;t<NB;++t) cw[t][0]=(ln&16)?cw[t][1]:cw[t][0];
      *(LAS v4f*)(lbase+L_CW+(gw*32+(ln&31))*16)=v4f{cw[0][0],cw[1][0],cw[2][0],0.f};
    }
    waitl0(); BAR();
    rd(0,0); mm(1,false); mix(0,false);
  }
  const int ln=lane_now();
  const v4f pc=*(LAS v4f*)(lbase+L_CW+((gw^1)*32+(ln&31))*16);
  const u32 hm=(ln&32)?0u:0xffffffffu;
  v4u M[2];
  #pragma unroll
  for(int m=0;m<2;++m){ const u32 nm=peek(lbase+L_MB+(rh*64+m*32+(ln&31))*4)&hm; M[m]=v4u{nm,nm,0u,0u}; }
  #pragma unroll
  for(int t=0;t<NB;++t){
    const float c=cw[t][0]+pc[t];
    const u32 c0=f2u(c)&0xffff0000u; const float r1=c-u2f(c0);
    const u32 c1=f2u(r1)&0xffff0000u; const float r2=r1-u2f(c1);
    const v4u F={(c0>>16)|c1,f2u(r2)>>16,0u,0u};
    acc[t][0]=mfma(F,M[0],acc[t][0]); acc[t][1]=mfma(F,M[1],acc[t][1]);
  }
}

__global__ NOPK __launch_bounds__(768,3) void fused_k(Args a){
  __shared__ __attribute__((aligned(256))) char smem[L_TOTAL];
  const u32 lbase=(u32)(size_t)(LAS char*)smem;
  const int tid=__builtin_amdgcn_workitem_id_x();
  const int wgid=__builtin_amdgcn_workgroup_id_x(), w=rfl(tid>>6);
  int lane=tid&63;
  const int xcd=wgid&7, idx=wgid>>3;
  const int tt=(xcd&1)*8+(idx&7), cg=(xcd>>1)*4+(idx>>3);
  const int nb=((cg&3)==3||cg==14)?2:3, blk0=11*(cg>>2)+3*(cg&3)-(cg==15?1:0);
  const int rt2=(xcd&3)*8+(idx&7), ct=(xcd>>2)*4+(idx>>3);
  const int ry=ROTP?2*(idx>>3):0, rz=ROTP?(11*(idx>>3))>>2:0;
  const u32 one=1;
  STAMP_DECL; STAMP(0); CSTAMP(12);
  const u64 tstart_=__builtin_amdgcn_s_memrealtime();
  #define PADTO() do{ while(__builtin_amdgcn_s_memrealtime()<tstart_+4500) __builtin_amdgcn_s_sleep(2); }while(0)

  if(w>=8){
    __builtin_amdgcn_s_setprio(3);
    const int lw=w-8, j=lane>>3, l7=lane&7;
    const v4u sx=mksrd(a.x,2048*1024);
    const v4u sb=mksrd(lw<2?a.bre:a.bim,512*1024);
    const u32 sz0=(u32)((l7^(j>>1))<<4), sz1=sz0^64u;
    const u32 xr=(tt*128+32*lw+16*(j>>2)+(j&3))*1024;
    const u32 vx0=xr+sz0, vx1=xr+3*1024+sz1, vx2=xr+6*1024+sz0, vx3=xr+9*1024+sz1;
    const u32 br=(cg*32+16*(lw&1)+j)*1024;
    const u32 vb0=br+sz0, vb1=br+7*1024+sz1;
    const u32 l4=lbase+lw*4096, l2=lbase+lw*2048;
    u32 kx=((tt*(ROT1&15)+cg*(ROT1>>4))&7)*128, kw=(kx+640)&0x380;
    #define SX(c) dma4<false,sx_(c)>(sx,vx0,vx1,vx2,vx3,kx,l4)
    #define SBB(c) do{ dma2<sb_(c)>(sb,vb0,vb1,kx,l2); kx=(kx+128)&0x380; }while(0)
    SX(0); SBB(0); SX(1); SBB(1);
    const v4u sws=mksrd(a.ws,WS_END);
    const v4u sw=mksrd(lw<2?a.wg:a.wu,512*2752);
    const v4u swd=mksrd(a.wd,1376*1024);
    const v4u sc=mksrd(lw<2?a.cre:a.cim,512*1024);
    const u32 vf=(lane&15)*128, lf=lbase+L_FLG+lw*256;
    const u32 fh=WS_FH+(rt2*32+(lw&1)*16)*128, fhh=WS_FHH+(rt2*32+lw*8)*128;
    u32 vw[6];
    #pragma unroll
    for(int i=0;i<3;++i){ const u32 q=i*1024+lane*16, row=q/192, col=q%192; const bool ok=col<(u32)nb*64;
      vw[i]=ok?(row+32*(lw&1))*2752+col+blk0*64-i*1024:OOB; vw[i+3]=vw[i]+16*2752; asm volatile("":"+v"(vw[i]),"+v"(vw[i+3])); }
    const u32 lwv=lbase+(lw>>1)*(12288+32)+(lw&1)*6144;
    #define GWA(c) dma3<wslot(c)>(sw,vw[0],vw[1],vw[2],kw*1376,lwv)
    #define GWB(c) do{ dma3<wslot(c)+3072>(sw,vw[3],vw[4],vw[5],kw*1376,lwv); kw=(kw+128)&0x380; }while(0)
    #define GX(c) do{ dma4<false,((c)%3)*XS>(sx,vx0,vx1,vx2,vx3,kx,l4); kx=(kx+128)&0x380; }while(0)
    SX(2); waitv<10>(); BAR();
    SBB(2); SX(3); waitv<10>(); BAR();
    SBB(3); SX(4); waitv<10>(); BAR();
    SBB(4); SX(5); waitv<10>(); BAR();
    SBB(5); SX(6); waitv<10>(); BAR();
    SBB(6); SX(7); waitv<10>(); BAR();
    STAMP(2);
    SBB(7); GWA(0); waitv<9>(); BAR();
    GWB(0); GWA(1); waitv<9>(); BAR();
    GWB(1); GWA(2); waitv<9>(); BAR();
    STAMP(3);
    GWB(2); GWA(3); GWB(3);
    const u32 tag=rfl(peek(lbase+L_TAG));
    u32 va=WS_HS+rt2*131072+lw*4096+lane*16;
    const u32 cr=(ct*64+(lw&1)*32+j)*1024;
    u32 vp0=cr+sz0, vp1=cr+7*1024+sz1, vp2=cr+14*1024+sz0, vp3=cr+21*1024+sz1;
    waitv<12>(); BAR();
    sfor<0,4>([&](auto C) AI {
      constexpr int c=C.value;
      GX(c+3); GWA(c+4); GWB(c+4);
      waitv<16>();
      if(WARMB){
        if constexpr(c==0) dma_dw<false>(sc,lane<32?(u32)(j*1024+l7*128):OOB,(ct*64+(idx&7)*8+(lw&1)*4)*1024,lbase+L_DUM);
        if constexpr(c==2) dma_dw<false>(swd,lane<43?(u32)(lane*1024):OOB,((idx&7)*172+lw*43)*1024+ct*128,lbase+L_DUM);
      }
      BAR();
    });
    u32 ka=ry, kb=ry;
    auto PA=[&](auto C) AI {
      constexpr int c=C.value;
      if constexpr(c==8){ va=WS_HH+rt2*86*2048+lw*4096+lane*16; ka=rz; }
      u32 so;
      if constexpr(c<8){ so=ka<<14; ka=(ka+1)&7; } else { so=(ka==10&&lw==3)?OOB:ka<<14; ka= ka==10?0:ka+1; }
      dma4<true,((c+2)%3)*XS>(sws,va,va,va,va,so,l4);
    };
    auto PB=[&](auto C) AI {
      constexpr int c=C.value;
      if constexpr(c==8){ vp0=(lw*32+j)*1024+ct*128+((l7^(((j>>1)&1)<<2))<<4); vp1=vp0+7*1024; vp2=vp0+14*1024; vp3=vp0+21*1024; kb=rz; }
      u32 so;
      if constexpr(c<8){ so=kb<<7; kb=(kb+1)&7; } else { so=kb<<17; kb= kb==10?0:kb+1; }
      dma4<false,wslot(c)>(c<8?sc:swd,vp0,vp1,vp2,vp3,so,l4);
    };
    #define IC(n) std::integral_constant<int,(n)>{}
    GX(7); PB(IC(0));
    waitv<14>();
    dma_dw<true>(sws,vf,fh,lf);
    BAR();
    PB(IC(1));
    waitv<4>();
    const bool early=all_eq(peek(lf+lane*4),tag);
    if(early) PA(IC(0));
    BAR();
    STAMP(4);
    PB(IC(2));
    if(early) PA(IC(1));
    BAR();
    PB(IC(3));
    STAMP(5);
    if(!early){ poll32(sws,vf,fh,tag); PA(IC(0)); PA(IC(1)); }
    STAMP(6);
    PA(IC(2));
    waitv<8>(); BAR();
    if(lane<32) dma_dw<true>(sws,lane*4,WS_STAT+rt2*512+lw*128,lbase+L_TAB+lw*128);
    { const u32 vt=(rt2*64+lw*16+j)*1024+ct*128+l7*16; dma2<L_XT>(sx,vt,vt+7*1024,0,l2); }
    waitv<7>(); BAR();
    sfor<1,18>([&](auto K) AI {
      constexpr int k=K.value;
      if constexpr(k==4) dma_dw<true>(sws,vf&1023u,fhh,lf);
      if constexpr(k==9){ if(wgid==0 && lw==0) dma_dw<true>(sws,vf*2,WS_FH+30*4096,lf); }
      if constexpr(k<=16) PA(IC(k+2));
      if constexpr(k<=15) PB(IC(k+3));
      waitv<(k==1?11:k<=15?12:k==16?8:0)>();
      if constexpr(k==5){
        STAMP(7);
        if(!all_eq(peek(lf+lane*4),tag)) poll32(sws,vf&1023u,fhh,tag);
        STAMP(8);
      }
      if constexpr(k==12){
        if(wgid==0 && lw==0){
          if(!all_eq(peek(lf+lane*4),tag)) poll32(sws,vf*2,WS_FH+30*4096,tag);
          const u32 e=peek(lbase+L_TAG+4)+1u;
          asm volatile("s_mov_b64 exec, 1\n\tbuffer_store_dword %0, off, %1, 0 sc1\n\ts_mov_b64 exec, -1" :: "v"(e), "s"(sws) : "memory");
        }
      }
      BAR();
    });
    STAMP(11);
    STAMP_FLUSH();
    return;
  }

  int h=lane>>5, r=lane&31;
  const rsrc_t rws=mkrsrc(a.ws,WS_END);
  u32 pend=0;
  u32 tag;
  auto try_flag=[&](bool force) AI {
    if(pend && (force || vm_idle())){
      waitv<0>();
      if(lane==0) __builtin_amdgcn_raw_buffer_store_b32(tag,rws,pend,0,16);
      pend=0;
    }
  };
  if(w>=4){
    const int gw=w-4, rh=w&1, rt=2*tt+rh;
    v16f acc[3][2]={};
    {
      u32 e=0;
      if(gw==0) e=__builtin_amdgcn_raw_buffer_load_b32(rws,0,WS_EPOCH,16);
      u32 ax[4];
      #pragma unroll
      for(int s=0;s<4;++s) ax[s]=lbase+sx_(0)+gw*4096+r*128+((((2*s+h)^((r>>1)&7)))<<4);
      u32 ab=lbase+sb_(0)+r*128+((((2*gw+h)^((r>>1)&7)))<<4);
      float s1=0.f,s2=0.f,c0=0.f,c1=0.f;
      sfor<0,8>([&](auto C) AI {
        constexpr int c=C.value;
        waitl0(); BAR();
        v4u X[4];
        #pragma unroll
        for(int s=0;s<4;++s) X[s]=ldsr128(ax[s]);
        const v4u t0=ldsr128(ab), t1=ldsr128(ab+4096);
        if constexpr(c<7){
          #pragma unroll
          for(int s=0;s<4;++s) ax[s]+=sx_(c+1)-sx_(c);
          ab+=sb_(c+1)-sb_(c);
        }
        if constexpr(c==1){ if(gw==0){ e=rfl(e); *(LAS v2u*)(lbase+L_TAG)=v2u{mktag(e),e}; } }
        #pragma unroll
        for(int s=0;s<4;++s)
          #pragma unroll
          for(int k=0;k<4;++k){ s1=dot2(X[s][k],0x3f803f80u,s1); s2=dot2(X[s][k],X[s][k],s2); }
        #pragma unroll
        for(int k=0;k<4;++k){ c0=dot2(t0[k],0x3f803f80u,c0); c1=dot2(t1[k],0x3f803f80u,c1); }
        asm volatile("":"+v"(s1),"+v"(s2),"+v"(c0),"+v"(c1),"+v"(ax[0]),"+v"(ax[1]),"+v"(ax[2]),"+v"(ax[3]),"+v"(ab));
      });
      STAMP(1);
      sw32(c0,c1);
      *(LAS float*)(lbase+L_CSP+(gw*64+lane)*4)=c0+c1;
      sw32(s1,s2); s1+=s2; s2=s1; sw32(s1,s2);
      const float m=s1*(1.f/512.f);
      const float var=relu(__builtin_fmaf(s2,1.f/512.f,-m*m));
      const u32 vm=pk2(var,m);
      const float rs=__builtin_amdgcn_rcpf(rbf(__builtin_amdgcn_sqrtf(rbf(u2f(vm<<16)+1e-5f))));
      const int tok=32*gw+16*((r>>2)&1)+8*(r>>4)+4*((r>>3)&1)+(r&3);
      if(h) *(LAS u32*)(lbase+L_MB+(gw*32+r)*4)=((vm>>16)|(vm&0xffff0000u))^0x80008000u;
      else *(LAS v2f*)(lbase+L_TAB+tok*8)=v2f{rs,rs*u2f(vm&0xffff0000u)};
      STAMP(2);
      waitl0(); BAR();
      tag=rfl(peek(lbase+L_TAG));
    }
    __builtin_amdgcn_s_setprio(2);
    const int u0=(gw>>1)*nb;
    if(nb==3) g_loop<3>(lbase,lane,gw,acc TRPASS TSPASS); else g_loop<2>(lbase,lane,gw,acc TRPASS TSPASS);
    STAMP(4); CSTAMP(7);
    lane=lane_now();
    #pragma unroll
    for(int t=0;t<3;++t) if(t<nb){
      #pragma unroll
      for(int m=0;m<2;++m){
        v4u v;
        #pragma unroll
        for(int k=0;k<4;++k) v[k]=pk2(relu(acc[t][m][2*k])*acc[t][m][2*k+8],relu(acc[t][m][2*k+1])*acc[t][m][2*k+9]);
        st128(v,rws,lane*16+WS_HH+((rt*86+2*blk0+u0+t)*2+m)*1024);
      }
    }
    pend=WS_FHH+(rt*32+cg*2+(gw>>1))*128;
    STAMP(5);
  } else {
    const int ch=cg*32+r;
    __builtin_amdgcn_s_setprio(3);
    const u16 hlr=a.lre[ch], hli=a.lim[ch], hdt=a.ldt[ch];
    *(LAS u32*)(lbase+L_HP+(w*32+r)*16+8)=0u;
    const float lr=b2f(hlr), li=b2f(hli), dt=rbf(__builtin_expf(b2f(hdt)));
    cf lam,coef;
    { const float ex=__builtin_expf(dt*lr); float sn,cs_; sincos_(dt*li,sn,cs_); lam=cf{ex*cs_,ex*sn};
      const float dd=1.f/(lr*lr+li*li); coef=cmul(cf{lam.r-1.f,lam.i},cf{lr*dd,-li*dd}); }
    cf l16=cmul(lam,lam); l16=cmul(l16,l16); l16=cmul(l16,l16); l16=cmul(l16,l16);
    cf l32=cmul(l16,l16);
    cf l1024,wq,pw;
    { const cf l64=cmul(l32,l32), l128=cmul(l64,l64), l256=cmul(l128,l128), l512=cmul(l256,l256), o={1.f,0.f};
      l1024=cmul(l512,l512);
      wq=cmul(csel(h,l128,o),cmul(csel(w&1,l256,o),csel(w&2,l512,o)));
      pw=cmul(csel(w&1,l32,o),csel(w&2,l64,o)); }
    asm volatile("":"+v"(lam.r),"+v"(lam.i),"+v"(coef.r),"+v"(coef.i),"+v"(l16.r),"+v"(l16.i),"+v"(l32.r),"+v"(l32.i));
    asm volatile("":"+v"(l1024.r),"+v"(l1024.i),"+v"(wq.r),"+v"(wq.i),"+v"(pw.r),"+v"(pw.i));
    v16f are={},aim={};
    {
      u32 ax[4],ab[4];
      #pragma unroll
      for(int s=0;s<4;++s){ const u32 t=lbase+r*128+((((2*s+h)^((r>>1)&7)))<<4); ab[s]=t+sb_(0); ax[s]=t+sx_(0)+w*4096; }
      v4u X[2][2],T[2][2][2];
      auto rd=[&](int b) AI {
        #pragma unroll
        for(int s=0;s<2;++s){ X[b][s]=ldsr128(ax[2*b+s]); T[b][s][0]=ldsr128(ab[2*b+s]); T[b][s][1]=ldsr128(ab[2*b+s]+4096); } };
      auto mm=[&](int b) AI {
        #pragma unroll
        for(int s=0;s<2;++s){ are=mfma(X[b][s],T[b][s][0],are); aim=mfma(X[b][s],T[b][s][1],aim); }
        asm volatile("":"+v"(are),"+v"(aim)); };
      STAMP(1);
      waitl0(); BAR();
      rd(0);
      sfor<0,8>([&](auto C) AI {
        constexpr int c=C.value;
        SB; rd(1);
        SB; mm(0);
        if constexpr(c<7){
          #pragma unroll
          for(int s=0;s<4;++s){ ax[s]+=sx_(c+1)-sx_(c); ab[s]+=sb_(c+1)-sb_(c); asm volatile("":"+v"(ax[s]),"+v"(ab[s])); }
          SB;
          waitl0(); BAR();
          rd(0);
        }
        SB; mm(1);
      });
    }
    STAMP(2);
    waitl0(); BAR();
    int left=8;
    #define SBAR() do{ if(left>0){ BAR(); --left; } else __builtin_amdgcn_s_sleep(8); }while(0)
    tag=rfl(peek(lbase+L_TAG));
    cf cs={0.f,0.f};
    #pragma unroll
    for(int k=0;k<4;++k){ const u32 b=lbase+L_CSP+(k*64+r)*4; cs.r+=*(LAS float*)b; cs.i+=*(LAS float*)(b+128); }
    cf uu[16]; cf ea={0.f,0.f};
    #pragma unroll
    for(int k=0;k<16;k+=2){
      const v4f st=*(LAS v4f*)(lbase+L_TAB+(32*w+16*h+k)*8);
      #pragma unroll
      for(int d=0;d<2;++d){
        const cf v={__builtin_fmaf(are[k+d],st[2*d],-st[2*d+1]*cs.r),__builtin_fmaf(aim[k+d],st[2*d],-st[2*d+1]*cs.i)};
        uu[k+d]=v; ea=cfma(lam,ea,v);
      }
    }
    cf elo=ea,ehi=ea; sw32(elo.r,ehi.r); sw32(elo.i,ehi.i);
    { const cf ew=cfma(l16,elo,ehi); *(LAS v2f*)(lbase+L_AGG+(w*32+r)*8)=v2f{ew.r,ew.i}; }
    STAMP(3);
    waitl0(); SBAR();
    cf Q;
    { const u32 b=lbase+L_AGG+r*8;
      const cf a0=ldsc(b), a1=ldsc(b+256), a2=ldsc(b+512), a3=ldsc(b+768);
      const cf Q2=cfma(l32,a0,a1), Q3=cfma(l32,Q2,a2);
      if(w==3){ const cf E=cfma(l32,Q3,a3); if(lane<32) st128(v4u{f2u(E.r),tag,f2u(E.i),tag},rws,ch*16+WS_GRAN+tt*8192); }
      Q= w==0?cf{0.f,0.f}: w==1?a0: w==2?Q2:Q3; }
    STAMP(4);
    if(cg==0) __builtin_amdgcn_raw_buffer_store_b32(peek(lbase+L_TAB+w*256+lane*4),rws,WS_STAT+tt*1024+w*256+lane*4,0,16);
    #pragma unroll
    for(int k=0;k<16;++k){ uu[k]=cmul(coef,uu[k]); asm volatile("":"+v"(uu[k].r),"+v"(uu[k].i)); }
    SBAR();
    cf H={0.f,0.f};
    if(tt>0){
      const int p0=tt-1-2*w-h, p1=p0-8;
      const u32 o0=p0>=0?(u32)(p0*8192+ch*16):OOB, o1=p1>=0?(u32)(p1*8192+ch*16):OOB;
      const u32 t0=p0>=0?tag:0u, t1=p1>=0?tag:0u;
      v4u g0,g1;
      __builtin_amdgcn_s_sleep(LBSLEEP);
      #pragma unroll 1
      for(int it=0;it<SPIN_CAP;++it){
        asm volatile("":::"memory");
        g0=__builtin_amdgcn_raw_buffer_load_b128(rws,o0,WS_GRAN,16);
        g1=__builtin_amdgcn_raw_buffer_load_b128(rws,o1,WS_GRAN,16);
        SBAR();
        if(all_eq((g0[1]^t0)|(g0[3]^t0)|(g1[1]^t1)|(g1[3]^t1),0u)) break;
      }
      cf hp=cmul(wq,cfma(l1024,cf{u2f(g1[0]),u2f(g1[2])},cf{u2f(g0[0]),u2f(g0[2])}));
      cf h2=hp; sw32(hp.r,h2.r); sw32(hp.i,h2.i);
      *(LAS v4u*)(lbase+L_HP+(w*32+r)*16)=v4u{f2u(hp.r+h2.r),f2u(hp.i+h2.i),1u,0u};
      waitl0();
      v4u a0,a1,a2,a3;
      #pragma unroll 1
      for(int it=0;it<SPIN_CAP*8;++it){
        SBAR();
        const u32 b=lbase+L_HP+r*16;
        a0=*(volatile LAS v4u*)b; a1=*(volatile LAS v4u*)(b+512); a2=*(volatile LAS v4u*)(b+1024); a3=*(volatile LAS v4u*)(b+1536);
        if(all_eq(a0[2]&a1[2]&a2[2]&a3[2],1u)) break;
      }
      H=cf{(u2f(a0[0])+u2f(a1[0]))+(u2f(a2[0])+u2f(a3[0])),(u2f(a0[1])+u2f(a1[1]))+(u2f(a2[1])+u2f(a3[1]))};
    }
    STAMP(5);
    cf hc=cfma(pw,H,Q);
    hc=cmul(coef,csel(h,cfma(l16,hc,elo),hc));
    const int rt=2*tt+(w>>1);
    const u32 hso=WS_HS+rt*131072+(cg>>1)*16384+(((cg&1)*2+(r>>4))*2+(w&1))*1024+h*512;
    #pragma unroll
    for(int m=0;m<2;++m){
      cf hv[8];
      #pragma unroll
      for(int jj=0;jj<8;++jj){ hc=cfma(lam,hc,uu[8*m+jj]); hv[jj]=hc; }
      const u32 o=hso+m*256+(((lane&15)^(8*h+4*m))<<4);
      st128(v4u{pk2(hv[0].r,hv[1].r),pk2(hv[2].r,hv[3].r),pk2(hv[4].r,hv[5].r),pk2(hv[6].r,hv[7].r)},rws,o);
      st128(v4u{pk2(-hv[0].i,-hv[1].i),pk2(-hv[2].i,-hv[3].i),pk2(-hv[4].i,-hv[5].i),pk2(-hv[6].i,-hv[7].i)},rws,o+8192);
      if(m==0 && left>0){ BAR(); --left; }
    }
    pend=WS_FH+(rt*32+(cg&1)*16+(cg>>1)*2+(w&1))*128;
    STAMP(6);
    #pragma unroll 1
    while(left>0){
      BAR(); --left;
      #pragma unroll 1
      for(int it=0;it<6 && pend;++it){ try_flag(false); if(pend) __builtin_amdgcn_s_sleep(1); else STAMP(7); }
    }
    if(pend){ try_flag(true); STAMP(7); }
  }
  lane=lane_now(); h=lane>>5; r=lane&31;
  const int g=lane>>4, m2=w&1, tc=(w>>1)&1, kh=w>>2;
  const int dcol=ct*64+tc*32+r;
  const float dsk=b2f(a.dsk[dcol]);
  v16f ay={},az={};
  const int q=(lane&15)>>2, p=lane&3, b4=g&1, gp=b4+2*(p&1);
  u32 a0=lbase+2*XS+(kh*8+m2)*1024+(gp*16+((8*h+q)^(4*gp)))*16+8*(p>>1);
  u32 b0=lbase+WB+kh*8192+(tc*32+r)*128+((h^((r>>1)&7))<<4);
  u32 za=lbase+XS+(kh*8+m2)*1024+lane*16;
  u32 zb=lbase+WB+(64*kh+4*h+q)*128+((tc*64+b4*32+8*p)^(((q>>1)&1)<<6));
  asm volatile("":"+v"(za),"+v"(zb));
  u32 d0=(u32)(-2*(int)XS), d1=XS, d2=XS, e0=WSZ, e1=WSZ, e2=WSZ, e3=(u32)(-3*(int)WSZ);
  v4u A[2][4],B[2][4];
  auto rot=[&]() AI { { const u32 t=d0; d0=d1; d1=d2; d2=t; } { const u32 t=e0; e0=e1; e1=e2; e2=e3; e3=t; } };
  auto rdy=[&](int b) AI {
    #pragma unroll
    for(int s=0;s<4;++s){ A[b][s]=ldstr(a0+s*2048,(a0^64)+s*2048); B[b][s]=ldsr128(b0^(s*32)); }
    a0+=d0; b0+=e0; rot();
  };
  auto rdz=[&](int b) AI {
    #pragma unroll
    for(int s=0;s<4;++s){ A[b][s]=ldsr128(za+s*2048); B[b][s]=ldstr(zb+s*2048,zb+s*2048+1024); }
    za+=d0; zb+=e0; rot();
  };
  auto mm4=[&](int b,v16f& c) AI {
    #pragma unroll
    for(int s=0;s<4;++s) c=mfma(A[b][s],B[b][s],c);
    #pragma unroll
    for(int s=0;s<4;++s){ SG(0x8,1); SG(0x100,3); }
    SB; asm volatile("":"+v"(c));
  };
  #define NXT() do{ waitl0(); BAR(); }while(0)
  const int tk=32*m2+16*h+8*kh;
  STAMP(8);
  __builtin_amdgcn_s_setprio(0);
  NXT(); rdy(0);
  STAMP(9);
  #pragma unroll 1
  for(int c=0;c<3;++c){
    NXT(); rdy(1); mm4(0,ay); try_flag(false);
    NXT(); rdy(0); mm4(1,ay); try_flag(false);
  }
  NXT(); rdy(1); mm4(0,ay); try_flag(true);
  NXT(); rdz(0); mm4(1,ay);
  STAMP(10);
  float sv[8], xf[8], r2[8];
  #pragma unroll 1
  for(int c=0;c<5;++c){
    NXT(); rdz(1); mm4(0,az);
    if(c==1){
      #pragma unroll
      for(int k=0;k<8;++k){
        const v2f st=*(LAS v2f*)(lbase+L_TAB+(tk+k)*8);
        xf[k]=b2f(*(LAS u16*)(lbase+L_XT+(tk+k)*128+(tc*32+r)*2));
        sv[k]=__builtin_fmaf(xf[k],st[0],-st[1])*dsk; r2[k]=st[0]*st[0];
      }
    }
    NXT(); rdz(0); mm4(1,az);
  }
  {
    const u32 xo=lbase+w*4096+lane*16, xi=lbase+(w^4)*4096+lane*16;
    float zv[8];
    if(kh){ *(LAS v4f*)xo=v4f{ay[0],ay[1],ay[2],ay[3]}; *(LAS v4f*)(xo+1024)=v4f{ay[4],ay[5],ay[6],ay[7]};
      #pragma unroll
      for(int k=0;k<8;++k) sv[k]=__builtin_fmaf(2.f,ay[8+k],sv[k]); }
    else  { *(LAS v4f*)xo=v4f{ay[8],ay[9],ay[10],ay[11]}; *(LAS v4f*)(xo+1024)=v4f{ay[12],ay[13],ay[14],ay[15]};
      #pragma unroll
      for(int k=0;k<8;++k) sv[k]=__builtin_fmaf(2.f,ay[k],sv[k]); }
    SB;
    #pragma unroll
    for(int s=0;s<4;++s) az=mfma(A[0][s],B[0][s],az);
    STAMP(11);
    if(kh){ *(LAS v4f*)(xo+2048)=v4f{az[0],az[1],az[2],az[3]}; *(LAS v4f*)(xo+3072)=v4f{az[4],az[5],az[6],az[7]};
      #pragma unroll
      for(int k=0;k<8;++k) zv[k]=az[8+k]; }
    else  { *(LAS v4f*)(xo+2048)=v4f{az[8],az[9],az[10],az[11]}; *(LAS v4f*)(xo+3072)=v4f{az[12],az[13],az[14],az[15]};
      #pragma unroll
      for(int k=0;k<8;++k) zv[k]=az[k]; }
    waitl0(); bar();
    const v4f i2=*(LAS v4f*)(xi+2048), i3=*(LAS v4f*)(xi+3072), i0=*(LAS v4f*)xi, i1=*(LAS v4f*)(xi+1024);
    float* op=a.out+(rt2*64+tk)*512+dcol;
    #pragma unroll
    for(int k=0;k<8;++k){
      const float z=(zv[k]+(k<4?i2[k&3]:i3[k&3]))*r2[k];
      const float gt=z*__builtin_amdgcn_rcpf(1.f+__builtin_amdgcn_exp2f(z*-1.44269504f));
      op[k*512]=__builtin_fmaf(__builtin_fmaf(2.f,k<4?i0[k&3]:i1[k&3],sv[k]),gt,xf[k]);
    }
  }
  PADTO();
}

__global__ __launch_bounds__(768) void post_k(char* ws){ __shared__ char sm[163840]; if(ws==nullptr) sm[threadIdx.x*200]=1; asm volatile("v_mov_b32 v160, 0":::"v160"); if(ws==nullptr) ws[0]=sm[threadIdx.x+5]; }
extern "C" void kernel_launch(void* const* d_in, const int* in_sizes, int n_in,
                              void* d_out, int out_size, void* d_ws, size_t ws_size,
                              hipStream_t stream) {
  Args a;
  a.x=(const u16*)d_in[0]; a.lre=(const u16*)d_in[1]; a.lim=(const u16*)d_in[2];
  a.bre=(const u16*)d_in[3]; a.bim=(const u16*)d_in[4]; a.cre=(const u16*)d_in[5]; a.cim=(const u16*)d_in[6];
  a.ldt=(const u16*)d_in[7]; a.dsk=(const u16*)d_in[8];
  a.wg=(const u16*)d_in[10]; a.wu=(const u16*)d_in[11]; a.wd=(const u16*)d_in[12];
  a.out=(float*)d_out; a.ws=(char*)d_ws;
  fused_k<<<256,768,0,stream>>>(a);
  post_k<<<256,768,0,stream>>>(a.ws);
}
```

```cpp
#include <hip/hip_runtime.h>
#include <hip/hip_bf16.h>
#include <type_traits>

typedef unsigned u32; typedef unsigned long long u64; typedef unsigned short u16;
typedef u32 v4u __attribute__((ext_vector_type(4)));
typedef u32 v2u __attribute__((ext_vector_type(2)));
typedef short v4s __attribute__((ext_vector_type(4)));
typedef __bf16 v2bf __attribute__((ext_vector_type(2)));
typedef __bf16 v8bf __attribute__((ext_vector_type(8)));
typedef float v2f __attribute__((ext_vector_type(2)));
typedef float v4f __attribute__((ext_vector_type(4)));
typedef float v16f __attribute__((ext_vector_type(16)));
#define LAS __attribute__((address_space(3)))
#define DEVI __device__ __forceinline__
#define AI __attribute__((always_inline))
#define SB __builtin_amdgcn_sched_barrier(0)
#if defined(__HIP_DEVICE_COMPILE__)
#define NOPK __attribute__((target("no-packed-fp32-ops")))
#else
#define NOPK
#endif

#define ROT1 0x01
#define ROTP 1
#define SPRIO 3
#define OUTAUX 16
#define HSLEEP 2
#define P0SPLIT 8
#define LBSLEEP 6
#define WARMB 1

constexpr u32 WS_EPOCH = 0;
constexpr u32 WS_STAT  = 4096;
constexpr u32 WS_FH    = 32768;
constexpr u32 WS_FHH   = WS_FH + 131072;
constexpr u32 WS_GRAN  = WS_FHH + 131072;
constexpr u32 WS_HH    = WS_GRAN + 131072;
constexpr u32 WS_HS    = WS_HH + 32*86*2048;
constexpr u32 WS_END   = WS_HS + 32*131072;
constexpr u32 WS_STAMP = 16u<<20;
constexpr u32 XS = 16384;
constexpr u32 WB = 49152, WSZ = 24832;
constexpr u32 L_XT = WB+4*WSZ;
constexpr u32 L_TAB  = L_XT+8192;
constexpr u32 L_CSP  = L_TAB + 1024;
constexpr u32 L_FLG  = L_CSP;
constexpr u32 L_HP   = L_CSP + 1024;
constexpr u32 L_DUM  = L_HP + 2048;
constexpr u32 L_TAG  = L_DUM + 256;
constexpr u32 L_CW   = L_XT;
constexpr u32 L_AGG  = L_XT + 2048;
constexpr u32 L_TOTAL = 163840;
static_assert(L_TAG+16<=L_TOTAL,"lds");
constexpr u32 OOB = 0x40000000u;
constexpr int SPIN_CAP = 1<<18;

#define CODEPF 1
constexpr u32 CODE_LEN=26000;
struct Args { const u16 *x,*lre,*lim,*bre,*bim,*cre,*cim,*ldt,*dsk,*wg,*wu,*wd; float* out; char* ws; };

DEVI u32 f2u(float f){ return __builtin_bit_cast(u32,f); }
DEVI float u2f(u32 u){ return __builtin_bit_cast(float,u); }
DEVI u32 rfl(u32 v){ return __builtin_amdgcn_readfirstlane(v); }
DEVI bool all_eq(u32 v,u32 t){ return __builtin_amdgcn_ballot_w64(v!=t)==0ull; }
DEVI v4u mksrd(const void* p, u32 bytes){ u64 a=(u64)p; v4u s; s.x=rfl((u32)a); s.y=rfl((u32)(a>>32)); s.z=bytes; s.w=0x00020000u; return s; }
#define DM0 "s_add_u32 m0, %[b], %[i]\n\ts_nop 0\n\t"
#define DLD(n,off,pol) "buffer_load_dwordx4 %[v" #n "], %[r], %[s] offen" off pol " lds\n\t"
template<bool SC1,u32 IMM> DEVI void dma4(v4u srd,u32 v0,u32 v1,u32 v2,u32 v3,u32 soff,u32 base){
  if(SC1) asm volatile(DM0 DLD(0,""," sc1") DLD(1," offset:1024"," sc1") DLD(2," offset:2048"," sc1") DLD(3," offset:3072"," sc1")
      :: [v0]"v"(v0),[v1]"v"(v1),[v2]"v"(v2),[v3]"v"(v3),[r]"s"(srd),[s]"s"(soff),[b]"s"(base),[i]"n"(IMM) : "memory","scc");
  else asm volatile(DM0 DLD(0,"","") DLD(1," offset:1024","") DLD(2," offset:2048","") DLD(3," offset:3072","")
      :: [v0]"v"(v0),[v1]"v"(v1),[v2]"v"(v2),[v3]"v"(v3),[r]"s"(srd),[s]"s"(soff),[b]"s"(base),[i]"n"(IMM) : "memory","scc");
}
template<u32 IMM> DEVI void dma3(v4u srd,u32 v0,u32 v1,u32 v2,u32 soff,u32 base){
  asm volatile(DM0 DLD(0,"","") DLD(1," offset:1024","") DLD(2," offset:2048","")
      :: [v0]"v"(v0),[v1]"v"(v1),[v2]"v"(v2),[r]"s"(srd),[s]"s"(soff),[b]"s"(base),[i]"n"(IMM) : "memory","scc");
}
template<u32 IMM> DEVI void dma2(v4u srd,u32 v0,u32 v1,u32 soff,u32 base){
  asm volatile(DM0 DLD(0,"","") DLD(1," offset:1024","")
      :: [v0]"v"(v0),[v1]"v"(v1),[r]"s"(srd),[s]"s"(soff),[b]"s"(base),[i]"n"(IMM) : "memory","scc");
}
template<bool SC1> DEVI void dma_dw(v4u srd,u32 voff,u32 soff,u32 lds){
  if(SC1) asm volatile("s_mov_b32 m0, %3\n\ts_nop 0\n\tbuffer_load_dword %0, %1, %2 offen sc1 lds" :: "v"(voff), "s"(srd), "s"(soff), "s"(lds) : "memory");
  else    asm volatile("s_mov_b32 m0, %3\n\ts_nop 0\n\tbuffer_load_dword %0, %1, %2 offen lds" :: "v"(voff), "s"(srd), "s"(soff), "s"(lds) : "memory");
}
DEVI void dma_x4(v4u srd,u32 voff,u32 soff,u32 lds){
  asm volatile("s_mov_b32 m0, %3\n\ts_nop 0\n\tbuffer_load_dwordx4 %0, %1, %2 offen sc1 lds" :: "v"(voff), "s"(srd), "s"(soff), "s"(lds) : "memory");
}
template<int N> DEVI void waitv(){ asm volatile("s_waitcnt vmcnt(%0)"::"n"(N):"memory"); }
DEVI void waitl0(){ asm volatile("s_waitcnt lgkmcnt(0)":::"memory"); }
DEVI void bar(){ asm volatile("":::"memory"); __builtin_amdgcn_s_barrier(); asm volatile("":::"memory"); SB; }
template<u32 OFF> DEVI void sig(u32 vaddr,u32 one){ asm volatile("s_mov_b64 exec, 1\n\tds_add_u32 %0, %1 offset:%2\n\ts_mov_b64 exec, -1" :: "v"(vaddr), "v"(one), "n"(OFF) : "memory"); }
template<u32 OFF> DEVI void sigm(u32 vaddr){ asm volatile("s_mov_b64 exec, 1\n\tds_inc_u32 %0, %0 offset:%1\n\ts_mov_b64 exec, -1" :: "v"(vaddr), "n"(OFF) : "memory"); }
DEVI u32 peek(u32 vaddr){ return *(volatile LAS u32*)vaddr; }
DEVI bool gather4(u32 b,v4u& a0,v4u& a1,v4u& a2,v4u& a3,int tries){
  #pragma unroll 1
  for(int it=0;it<tries;++it){
    a0=*(volatile LAS v4u*)b; a1=*(volatile LAS v4u*)(b+512); a2=*(volatile LAS v4u*)(b+1024); a3=*(volatile LAS v4u*)(b+1536);
    if(all_eq(a0[2]&a1[2]&a2[2]&a3[2],1u)) return true;
  }
  return false;
}
DEVI bool vm_idle(){ u32 v; asm volatile("s_getreg_b32 %0, hwreg(HW_REG_IB_STS, 0, 4)":"=s"(v)); return v==0u; }
DEVI int lane_now(){ int l; asm volatile("v_mbcnt_lo_u32_b32 %0, -1, 0\n\tv_mbcnt_hi_u32_b32 %0, -1, %0":"=v"(l)); return l; }
DEVI float b2f(u16 v){ return u2f(((u32)v)<<16); }
DEVI u32 pk2(float lo,float hi){ v2f p={lo,hi}; return __builtin_bit_cast(u32,__builtin_convertvector(p,v2bf)); }
DEVI float rbf(float f){ return u2f(pk2(f,f)<<16); }
DEVI float relu(float x){ int i=__builtin_bit_cast(int,x); return __builtin_bit_cast(float,i>0?i:0); }
DEVI float dot2(u32 a,u32 b,float c){ return __builtin_amdgcn_fdot2_f32_bf16(__builtin_bit_cast(v2bf,a),__builtin_bit_cast(v2bf,b),c,false); }
DEVI v16f mfma(v4u a,v4u b,v16f c){ return __builtin_amdgcn_mfma_f32_32x32x16_bf16(__builtin_bit_cast(v8bf,a),__builtin_bit_cast(v8bf,b),c,0,0,0); }
DEVI v4f mfma16(v4u a,v4u b,v4f c){ return __builtin_amdgcn_mfma_f32_16x16x32_bf16(__builtin_bit_cast(v8bf,a),__builtin_bit_cast(v8bf,b),c,0,0,0); }
DEVI v4u ldsr128(u32 a){ return *(LAS v4u*)a; }
DEVI v4u ldstr(u32 a0,u32 a1){
  v4s x=__builtin_amdgcn_ds_read_tr16_b64_v4i16((LAS v4s*)a0);
  v4s y=__builtin_amdgcn_ds_read_tr16_b64_v4i16((LAS v4s*)a1);
  return __builtin_bit_cast(v4u,__builtin_shufflevector(x,y,0,1,2,3,4,5,6,7));
}
DEVI void sw16(float& a,float& b){ v2u r=__builtin_amdgcn_permlane16_swap(f2u(a),f2u(b),false,false); a=u2f(r[0]); b=u2f(r[1]); }
DEVI void sw32(float& a,float& b){ v2u r=__builtin_amdgcn_permlane32_swap(f2u(a),f2u(b),false,false); a=u2f(r[0]); b=u2f(r[1]); }
template<int CTRL> DEVI float dppadd(float v){ return v+u2f(__builtin_amdgcn_update_dpp(0u,f2u(v),CTRL,0xf,0xf,true)); }
DEVI void wsum4(float& a,float& b,float& c,float& d){
  sw32(a,b); sw32(c,d); float v=a+b, t=c+d;
  sw16(v,t); v+=t;
  v=dppadd<0xB1>(v); v=dppadd<0x4E>(v); v=dppadd<0x141>(v); v=dppadd<0x140>(v);
  a=u2f(__builtin_amdgcn_readlane(f2u(v),0)); c=u2f(__builtin_amdgcn_readlane(f2u(v),16));
  b=u2f(__builtin_amdgcn_readlane(f2u(v),32)); d=u2f(__builtin_amdgcn_readlane(f2u(v),48));
}
struct cf{ float r,i; };
DEVI cf cmul(cf a,cf b){ return cf{__builtin_fmaf(-a.i,b.i,a.r*b.r), __builtin_fmaf(a.i,b.r,a.r*b.i)}; }
DEVI cf cfma(cf a,cf b,cf c){ return cf{__builtin_fmaf(-a.i,b.i,__builtin_fmaf(a.r,b.r,c.r)), __builtin_fmaf(a.i,b.r,__builtin_fmaf(a.r,b.i,c.i))}; }
DEVI cf csel(bool c,cf a,cf b){ return cf{c?a.r:b.r, c?a.i:b.i}; }
DEVI cf ldsc(u32 a){ v2f v=*(LAS v2f*)a; return cf{v[0],v[1]}; }
DEVI void sincos_(float x,float& s,float& c){
  const float n=__builtin_rintf(x*0.636619772f);
  float r=__builtin_fmaf(n,-1.57079601e+00f,x); r=__builtin_fmaf(n,-3.13916473e-07f,r); r=__builtin_fmaf(n,-5.39030253e-15f,r);
  const float z=r*r;
  float ps=__builtin_fmaf(__builtin_fmaf(__builtin_fmaf(2.86567956e-6f,z,-1.98559923e-4f),z,8.33338592e-3f),z,-1.66666672e-1f);
  ps=__builtin_fmaf(ps*z,r,r);
  float pc=__builtin_fmaf(__builtin_fmaf(__builtin_fmaf(2.44677067e-5f,z,-1.38877297e-3f),z,4.16666567e-2f),z,-0.5f);
  pc=__builtin_fmaf(pc,z,1.f);
  const int q=(int)n;
  const float a=(q&1)?pc:ps, b=(q&1)?ps:pc;
  s=(q&2)?-a:a; c=((q+1)&2)?-b:b;
}
typedef __amdgpu_buffer_rsrc_t rsrc_t;
DEVI rsrc_t mkrsrc(const void* p,u32 bytes){ return __builtin_amdgcn_make_buffer_rsrc((void*)p,0,bytes,0x00020000); }
DEVI void st128(v4u v,rsrc_t r,u32 off){ __builtin_amdgcn_raw_buffer_store_b128(v,r,off,0,16); }
DEVI u32 mktag(u32 e){ return e*0x9E3779B1u+0x7F4A7C15u; }

#define STAMP(k) do{}while(0)
#define CSTAMP(k) do{}while(0)
#define TRACE(k) do{}while(0)
#define STAMP_DECL do{}while(0)
#define BAR() bar()
#define STAMP_FLUSH() do{}while(0)
#define TRARG
#define TRPASS
#define TSARG
#define TSPASS

DEVI void poll32(v4u srd,u32 voff,u32 soff,u32 tag){
  #pragma unroll 1
  for(int it=0;it<SPIN_CAP;++it){
    u32 v;
    asm volatile("buffer_load_dword %0, %1, %2, %3 offen sc1\n\ts_waitcnt vmcnt(0)" : "=&v"(v) : "v"(voff), "s"(srd), "s"(soff) : "memory");
    if(all_eq(v,tag)) break;
    __builtin_amdgcn_s_sleep(12);
  }
}
template<int I,int N,class F> DEVI void sfor(F&& f){ if constexpr(I<N){ f(std::integral_constant<int,I>{}); sfor<I+1,N>(f); } }
AI constexpr u32 wslot(int c){ return WB+(c&3)*WSZ; }
AI constexpr u32 sx_(int c){ return c>=5?(c-5)*XS:wslot(c+2); }
AI constexpr u32 sb_(int c){ return c>=5?wslot(3)+(c-5)*8192:wslot(c+2)+16384; }

#define SG(m,n) __builtin_amdgcn_sched_group_barrier(m,n,0)
template<int NB>
DEVI u32 g_main(const Args& a,rsrc_t rws,u32 lbase,int lane,int gw,int tt,int cg,u32 hqo TRARG TSARG){
  const int h=lane>>5, r=lane&31, rh=gw&1, u0=(gw>>1)*NB;
  u32 e=0;
  if(gw==0) e=__builtin_amdgcn_raw_buffer_load_b32(rws,0,WS_EPOCH,16);
  SB;
  v16f acc[3][2]={}; v4f cw[3]={};
  u32 ax[4],aw,aw2;
  #pragma unroll
  for(int s=0;s<4;++s) ax[s]=lbase+rh*8192+r*128+((((2*(s^(2*rh))+h)^((r>>1)&7)))<<4);
  aw=lbase+WB+((lane>>4)&1)*(12288+32)+(8*h+((lane&15)>>2))*192+(lane&3)*8+u0*32;
  aw2=aw+6144-rh*6144; aw+=rh*6144;
  const u32 on2=((lane&3)==((lane>>4)&1))?0x3f803f80u:0u;
  v4u ones={on2,on2,on2,on2};
  #pragma unroll
  for(int t=0;t<NB;++t) asm volatile("":"+v"(acc[t][0]),"+v"(acc[t][1]));
  asm volatile("":"+v"(ax[0]),"+v"(ax[1]),"+v"(ax[2]),"+v"(ax[3]),"+v"(aw),"+v"(aw2),"+v"(ones));
  SB;
  u32 tag;
  {
    u32 sa=lbase+sx_(0)+gw*4096+r*128+((h^((r>>1)&7))<<4);
    u32 ab=lbase+sb_(0)+r*128+((((2*gw+h)^((r>>1)&7)))<<4);
    float s2=0.f,s3=0.f; v4f s1={},c0={},c1={};
    sfor<0,8>([&](auto C) AI {
      constexpr int c=C.value;
      waitl0(); BAR();
      v4u Y[4];
      #pragma unroll
      for(int s=0;s<4;++s) Y[s]=ldsr128(sa^(s*32));
      const v4u t0=ldsr128(ab), t1=ldsr128(ab+4096);
      if constexpr(c<7){ sa+=sx_(c+1)-sx_(c); ab+=sb_(c+1)-sb_(c); }
      if constexpr(c==2){ if(gw==0){ e=rfl(e); *(LAS v2u*)(lbase+L_TAG)=v2u{mktag(e),e}; } }
      #pragma unroll
      for(int s=0;s<4;++s){ s2=dot2(Y[s][0],Y[s][0],s2); s3=dot2(Y[s][1],Y[s][1],s3); s2=dot2(Y[s][2],Y[s][2],s2); s3=dot2(Y[s][3],Y[s][3],s3); s1=mfma16(ones,Y[s],s1); }
      c0=mfma16(ones,t0,c0); c1=mfma16(ones,t1,c1);
      asm volatile("":"+v"(s1),"+v"(s2),"+v"(s3),"+v"(c0),"+v"(c1),"+v"(sa),"+v"(ab));
    });
    STAMP(1);
    s2+=s3; s3=s2; sw32(s2,s3); s2+=s3;
    asm volatile("s_nop 7":"+v"(s1),"+v"(c0),"+v"(c1));
    const bool hi=lane&16;
    const float m=(hi?s1[1]:s1[0])*(1.f/512.f);
    *(LAS float*)(lbase+L_CSP+(gw*64+lane)*4)=h?(hi?c1[1]:c1[0]):(hi?c0[1]:c0[0]);
    const u32 vm=pk2(relu(__builtin_fmaf(s2,1.f/512.f,-m*m)),m);
    const float rs=__builtin_amdgcn_rcpf(rbf(__builtin_amdgcn_sqrtf(rbf(u2f(vm<<16)+1e-5f))));
    *(LAS v2u*)(lbase+L_TAB+(32*gw+16*((r>>2)&1)+8*(r>>4)+4*((r>>3)&1)+(r&3))*8)=v2u{f2u(rs),vm&0xffff0000u};
    tag=rfl(peek(lbase+L_TAG));
    STAMP(2);
    waitl0(); BAR();
  }
  __builtin_amdgcn_s_setprio(2);
  v4u X[2][2],T[2][3];
  auto rd=[&](int b,int s) AI {
    const u32 ad=(s<2?aw:aw2)+(s&1)*3072;
    X[b][0]=ldsr128(ax[s]);
    #pragma unroll
    for(int t=0;t<NB;++t){ T[b][t]=ldstr(ad+t*32,ad+t*32+768); if(t==0) X[b][1]=ldsr128(ax[s]+4096); }
  };
  auto mm=[&](int b,bool cs) AI {
    #pragma unroll
    for(int t=0;t<NB;++t){ acc[t][0]=mfma(T[b][t],X[b][0],acc[t][0]); if(cs) cw[t]=mfma16(ones,T[b][t],cw[t]); acc[t][1]=mfma(T[b][t],X[b][1],acc[t][1]); }
  };
  auto mix=[&](int nv,bool cs) AI {
    SG(0x8,1); SG(0x100,3); if(cs) SG(0x8,2); else SG(0x8,1); SG(0x100,3);
    if(NB==3){ SG(0x8,1); SG(0x100,2); }
    SG(0x8,1); if(nv) SG(0x2,3);
    SG(0x8,5);
  };
  u32 d0=XS, d1=XS, d2=(u32)(-2*(int)XS);
  STAMP(3); CSTAMP(6);
  rd(0,0);
  #pragma unroll 1
  for(int c=0;c<8;++c){
    const u32 dw=((c&3)==3)?(u32)(-3*(int)WSZ):WSZ;
    SB; rd(1,1); mm(0,true); ax[0]+=d0; ax[1]+=d0; aw+=dw; mix(1,true);
    SB; asm volatile("":"+v"(ax[0]),"+v"(ax[1]),"+v"(aw));
    rd(0,2); mm(1,true); mix(0,true);
    SB; rd(1,3); mm(0,false); ax[2]+=d0; ax[3]+=d0; aw2+=dw; mix(1,false);
    SB; asm volatile("":"+v"(ax[2]),"+v"(ax[3]),"+v"(aw2));
    { const u32 t=d0; d0=d1; d1=d2; d2=t; }
    if(c==7){
      const int ln=lane_now();
      #pragma unroll
      for(int t=0;t<NB;++t){ const float lo=cw[t][0], hi=cw[t][1]; cw[t][0]=(ln&16)?hi:lo; }
      *(LAS v4f*)(lbase+L_CW+(gw*32+(ln&31))*16)=v4f{cw[0][0],cw[1][0],cw[2][0],0.f};
    }
    waitl0(); BAR();
    rd(0,0); mm(1,false); mix(0,false);
  }
  const int ln=lane_now();
  const v4u pc=*(LAS v4u*)(lbase+L_CW+((gw^1)*32+(ln&31))*16);
  const u32 hm=(ln&32)?0u:0xffffffffu;
  v4u M[2];
  #pragma unroll
  for(int m=0;m<2;++m){ const u32 mu=peek(lbase+L_TAB+((rh*2+m)*32+16*((ln>>2)&1)+8*((ln>>4)&1)+4*((ln>>3)&1)+(ln&3))*8+4);
    const u32 nm=(((mu>>16)|mu)^0x80008000u)&hm; M[m]=v4u{nm,nm,0u,0u}; }
  #pragma unroll
  for(int t=0;t<NB;++t){
    const float c=cw[t][0]+u2f(pc[t]);
    const u32 c0=f2u(c)&0xffff0000u; const float r1=c-u2f(c0);
    const u32 c1=f2u(r1)&0xffff0000u; const float r2=r1-u2f(c1);
    const v4u F={(c0>>16)|c1,f2u(r2)>>16,0u,0u};
    acc[t][0]=mfma(F,M[0],acc[t][0]); acc[t][1]=mfma(F,M[1],acc[t][1]);
  }
  STAMP(4); CSTAMP(7);
  #pragma unroll
  for(int t=0;t<NB;++t)
    #pragma unroll
    for(int m=0;m<2;++m){
      v4u v;
      #pragma unroll
      for(int k=0;k<4;++k) v[k]=pk2(relu(acc[t][m][2*k])*acc[t][m][2*k+8],relu(acc[t][m][2*k+1])*acc[t][m][2*k+9]);
      st128(v,rws,hqo+(u0+t)*2048+m*1024+ln*16);
    }
  return tag;
}

__global__ NOPK __launch_bounds__(768,3) void fused_k(Args a){
  __shared__ __attribute__((aligned(256))) char smem[L_TOTAL];
  const u32 lbase=(u32)(size_t)(LAS char*)smem;
  const int tid=__builtin_amdgcn_workitem_id_x();
  const int wgid=__builtin_amdgcn_workgroup_id_x(), w=rfl(tid>>6);
  int lane=tid&63;
  const int xcd=wgid&7, idx=wgid>>3;
  if(CODEPF && w==CODEPF-1){
    u64 pc; u32 back; asm volatile("s_getpc_b64 %0\n.Lpcx%=:\n\ts_mov_b32 %1, .Lpcx%=-_Z7fused_k4Args":"=&s"(pc),"=s"(back));
    dma_dw<false>(mksrd((const void*)(pc-back),CODE_LEN),(u32)(((idx&3)*64+lane)*128),0,lbase+L_DUM);
  }
  const int tt=(xcd&1)*8+(idx&7), cg=(xcd>>1)*4+(idx>>3);
  const int nb=((cg&3)==3||cg==14)?2:3, blk0=11*(cg>>2)+3*(cg&3)-(cg==15?1:0);
  const int rt2=(xcd&3)*8+(idx&7), ct=(xcd>>2)*4+(idx>>3);
  const int ry=ROTP?2*(idx>>3):0, rz=ROTP?(11*(idx>>3))>>2:0;
  const u32 one=1;
  STAMP_DECL; STAMP(0); CSTAMP(12);

  if(w>=8){
    __builtin_amdgcn_s_setprio(3);
    const int lw=w-8, j=lane>>3, l7=lane&7;
    const v4u sx=mksrd(a.x,2048*1024);
    const v4u sb=mksrd(lw<2?a.bre:a.bim,512*1024);
    const u32 sz0=(u32)((l7^(j>>1))<<4), sz1=sz0^64u;
    const u32 xr=(tt*128+32*lw+16*(j>>2)+(j&3))*1024;
    const u32 vx0=xr+sz0, vx1=xr+3*1024+sz1, vx2=xr+6*1024+sz0, vx3=xr+9*1024+sz1;
    const u32 br=(cg*32+16*(lw&1)+j)*1024;
    const u32 vb0=br+sz0, vb1=br+7*1024+sz1;
    const u32 l4=lbase+lw*4096, l2=lbase+lw*2048;
    u32 kx=((tt*(ROT1&15)+cg*(ROT1>>4))&7)*128, kw=(kx+640)&0x380;
    #define SX(c) dma4<false,sx_(c)>(sx,vx0,vx1,vx2,vx3,kx,l4)
    #define SBB(c) do{ dma2<sb_(c)>(sb,vb0,vb1,kx,l2); kx=(kx+128)&0x380; }while(0)
    SX(0); SBB(0); SX(1); SBB(1);
    const v4u sws=mksrd(a.ws,WS_END);
    const v4u sw=mksrd(lw<2?a.wg:a.wu,512*2752);
    const v4u swd=mksrd(a.wd,1376*1024);
    const v4u sc=mksrd(lw<2?a.cre:a.cim,512*1024);
    const u32 vf=(lane&15)*128, lf=lbase+L_FLG+lw*256;
    const u32 fh=WS_FH+(rt2*32+(lw&1)*16)*128, fhh=WS_FHH+rt2*4096;
    u32 vw[6];
    #pragma unroll
    for(int i=0;i<3;++i){ const u32 q=i*1024+lane*16, row=q/192, col=q%192; const bool ok=col<(u32)nb*64;
      vw[i]=ok?(row+32*(lw&1))*2752+col+blk0*64-i*1024:OOB; vw[i+3]=vw[i]+16*2752; asm volatile("":"+v"(vw[i]),"+v"(vw[i+3])); }
    const u32 lwv=lbase+(lw>>1)*(12288+32)+(lw&1)*6144;
    #define GWA(c) dma3<wslot(c)>(sw,vw[0],vw[1],vw[2],kw*1376,lwv)
    #define GWB(c) do{ dma3<wslot(c)+3072>(sw,vw[3],vw[4],vw[5],kw*1376,lwv); kw=(kw+128)&0x380; }while(0)
    #define GX(c) do{ dma4<false,((c)%3)*XS>(sx,vx0,vx1,vx2,vx3,kx,l4); kx=(kx+128)&0x380; }while(0)
    SX(2); waitv<10>(); BAR();
    SBB(2); SX(3); waitv<10>(); BAR();
    SBB(3); SX(4); waitv<10>(); BAR();
    SBB(4); SX(5); waitv<10>(); BAR();
    SBB(5); SX(6); waitv<10>(); BAR();
    SBB(6); SX(7); waitv<10>(); BAR();
    STAMP(2);
    SBB(7); GWA(0); waitv<9>(); BAR();
    GWB(0); GWA(1); waitv<9>(); BAR();
    GWB(1); waitv<6>(); BAR();
    STAMP(3);
    GWA(2); GWB(2); GWA(3); GWB(3);
    const u32 tag=rfl(peek(lbase+L_TAG));
    u32 va=WS_HS+rt2*131072+lw*4096+lane*16;
    const u32 cr=(ct*64+(lw&1)*32+j)*1024;
    u32 vp0=cr+sz0, vp1=cr+7*1024+sz1, vp2=cr+14*1024+sz0, vp3=cr+21*1024+sz1;
    waitv<12>(); BAR();
    sfor<0,4>([&](auto C) AI {
      constexpr int c=C.value;
      GX(c+3); GWA(c+4); GWB(c+4);
      if(WARMB){
        if constexpr(c==0) dma_dw<false>(sc,lane<32?(u32)(j*1024+l7*128):OOB,(ct*64+(idx&7)*8+(lw&1)*4)*1024,lbase+L_DUM);
        if constexpr(c==2) dma_dw<false>(swd,lane<43?(u32)(lane*1024):OOB,((idx&7)*172+lw*43)*1024+ct*128,lbase+L_DUM);
      }
      waitv<WARMB?17:16>();
      BAR();
    });
    u32 ka=ry, kb=ry;
    auto PA=[&](auto C) AI {
      constexpr int c=C.value;
      if constexpr(c==8){ va=WS_HH+rt2*86*2048+lw*4096+lane*16; ka=rz; }
      u32 so;
      if constexpr(c<8){ so=ka<<14; ka=(ka+1)&7; } else { so=(ka==10&&lw==3)?OOB:ka<<14; ka= ka==10?0:ka+1; }
      dma4<true,((c+2)%3)*XS>(sws,va,va,va,va,so,l4);
    };
    auto PB=[&](auto C) AI {
      constexpr int c=C.value;
      if constexpr(c==8){ vp0=(lw*32+j)*1024+ct*128+((l7^(((j>>1)&1)<<2))<<4); vp1=vp0+7*1024; vp2=vp0+14*1024; vp3=vp0+21*1024; kb=rz; }
      u32 so;
      if constexpr(c<8){ so=kb<<7; kb=(kb+1)&7; } else { so=kb<<17; kb= kb==10?0:kb+1; }
      dma4<false,wslot(c)>(c<8?sc:swd,vp0,vp1,vp2,vp3,so,l4);
    };
    #define IC(n) std::integral_constant<int,(n)>{}
    GX(7); PB(IC(0));
    waitv<14>(); BAR();
    PB(IC(1));
    waitv<8>(); BAR();
    STAMP(4);
    PB(IC(2));
    __builtin_amdgcn_s_sleep(HSLEEP);
    dma_dw<true>(sws,vf,fh,lf);
    waitv<0>();
    const bool early=all_eq(peek(lf+lane*4),tag);
    if(early){ PA(IC(0)); PA(IC(1)); }
    BAR();
    STAMP(5);
    if(!early){ poll32(sws,vf,fh,tag); PA(IC(0)); PA(IC(1)); }
    STAMP(6);
    PB(IC(3)); PA(IC(2));
    waitv<12>(); BAR();
    if(lane<32) dma_dw<true>(sws,lane*4,WS_STAT+rt2*512+lw*128,lbase+L_TAB+lw*128);
    { const u32 vt=(rt2*64+lw*16+j)*1024+ct*128+l7*16; dma2<L_XT>(sx,vt,vt+7*1024,0,l2); }
    waitv<11>(); BAR();
    bool zok=false;
    sfor<1,18>([&](auto K) AI {
      constexpr int k=K.value;
      if constexpr(k==5) dma_dw<true>(sws,(lane&31)*128,fhh,lf);
      if constexpr(k==9){ if(wgid==0 && lw==0) dma_dw<true>(sws,vf*2,WS_FH+30*4096,lf); }
      if constexpr(k==6){
        waitv<8>();
        zok=all_eq(peek(lf+lane*4),tag);
        if(zok) PA(IC(8));
        PB(IC(9));
        if(zok) waitv<12>(); else waitv<8>();
      } else if constexpr(k==7){
        STAMP(7);
        if(!zok){ poll32(sws,(lane&31)*128,fhh,tag); PA(IC(8)); }
        STAMP(8);
        PA(IC(9)); PB(IC(10));
        if(zok) waitv<12>(); else waitv<8>();
      } else {
        if constexpr(k<=16) PA(IC(k+2));
        if constexpr(k<=15) PB(IC(k+3));
        waitv<(k==1?11:k<=15?12:k==16?8:0)>();
      }
      if constexpr(k==12){
        if(wgid==0 && lw==0){
          if(!all_eq(peek(lf+lane*4),tag)) poll32(sws,vf*2,WS_FH+30*4096,tag);
          const u32 e=peek(lbase+L_TAG+4)+1u;
          asm volatile("s_mov_b64 exec, 1\n\tbuffer_store_dword %0, off, %1, 0 sc1\n\ts_mov_b64 exec, -1" :: "v"(e), "s"(sws) : "memory");
        }
      }
      BAR();
    });
    STAMP(11);
    STAMP_FLUSH();
    return;
  }

  int h=lane>>5, r=lane&31;
  const rsrc_t rws=mkrsrc(a.ws,WS_END);
  u32 pend=0;
  u32 tag;
  auto try_flag=[&](bool force) AI {
    if(pend && (force || vm_idle())){
      waitv<0>();
      if(lane==0) __builtin_amdgcn_raw_buffer_store_b32(tag,rws,pend,0,16);
      pend=0;
    }
  };
  if(w>=4){
    const int gw=w-4, rt=2*tt+(w&1);
    const u32 hqo=WS_HH+(rt*86+2*blk0)*2048;
    if(nb==3) tag=g_main<3>(a,rws,lbase,lane,gw,tt,cg,hqo TRPASS TSPASS); else tag=g_main<2>(a,rws,lbase,lane,gw,tt,cg,hqo TRPASS TSPASS);
    pend=WS_FHH+(rt*32+cg*2+(gw>>1))*128;
    STAMP(5);
  } else {
    const int ch=cg*32+r;
    __builtin_amdgcn_s_setprio(SPRIO);
    const u16 hlr=a.lre[ch], hli=a.lim[ch], hdt=a.ldt[ch];
    *(LAS u32*)(lbase+L_HP+(w*32+r)*16+8)=0u;
    *(LAS u32*)(lbase+L_AGG+(w*32+r)*16+8)=0u;
    const float lr=b2f(hlr), li=b2f(hli), dt=rbf(__builtin_expf(b2f(hdt)));
    cf lam,coef;
    { const float ex=__builtin_expf(dt*lr); float sn,cs_; sincos_(dt*li,sn,cs_); lam=cf{ex*cs_,ex*sn};
      const float dd=1.f/(lr*lr+li*li); coef=cmul(cf{lam.r-1.f,lam.i},cf{lr*dd,-li*dd}); }
    cf l16=cmul(lam,lam); l16=cmul(l16,l16); l16=cmul(l16,l16); l16=cmul(l16,l16);
    cf l32=cmul(l16,l16);
    cf l1024,wq,pw;
    { const cf l64=cmul(l32,l32), l128=cmul(l64,l64), l256=cmul(l128,l128), l512=cmul(l256,l256), o={1.f,0.f};
      l1024=cmul(l512,l512);
      wq=cmul(csel(h,l128,o),cmul(csel(w&1,l256,o),csel(w&2,l512,o)));
      pw=cmul(csel(w&1,l32,o),csel(w&2,l64,o)); }
    asm volatile("":"+v"(lam.r),"+v"(lam.i),"+v"(coef.r),"+v"(coef.i),"+v"(l16.r),"+v"(l16.i),"+v"(l32.r),"+v"(l32.i));
    asm volatile("":"+v"(l1024.r),"+v"(l1024.i),"+v"(wq.r),"+v"(wq.i),"+v"(pw.r),"+v"(pw.i));
    v16f are={},aim={};
    {
      u32 ax[4],ab[4];
      #pragma unroll
      for(int s=0;s<4;++s){ const u32 t=lbase+r*128+((((2*s+h)^((r>>1)&7)))<<4); ab[s]=t+sb_(0); ax[s]=t+sx_(0)+w*4096; }
      v4u X[2][2],T[2][2][2];
      auto rd=[&](int b) AI {
        #pragma unroll
        for(int s=0;s<2;++s){ X[b][s]=ldsr128(ax[2*b+s]); T[b][s][0]=ldsr128(ab[2*b+s]); T[b][s][1]=ldsr128(ab[2*b+s]+4096); } };
      auto mm=[&](int b) AI {
        #pragma unroll
        for(int s=0;s<2;++s){ are=mfma(X[b][s],T[b][s][0],are); aim=mfma(X[b][s],T[b][s][1],aim); }
        asm volatile("":"+v"(are),"+v"(aim)); };
      STAMP(1);
      waitl0(); BAR();
      rd(0);
      sfor<0,8>([&](auto C) AI {
        constexpr int c=C.value;
        SB; rd(1);
        SB; mm(0);
        if constexpr(c<7){
          #pragma unroll
          for(int s=0;s<4;++s){ ax[s]+=sx_(c+1)-sx_(c); ab[s]+=sb_(c+1)-sb_(c); asm volatile("":"+v"(ax[s]),"+v"(ab[s])); }
          SB;
          waitl0(); BAR();
          rd(0);
        }
        SB; mm(1);
      });
    }
    STAMP(2);
    waitl0(); BAR();
    tag=rfl(peek(lbase+L_TAG));
    cf cs={0.f,0.f};
    #pragma unroll
    for(int k=0;k<4;++k){ const u32 b=lbase+L_CSP+(k*64+r)*4; cs.r+=*(LAS float*)b; cs.i+=*(LAS float*)(b+128); }
    int left=8;
    #define SBAR() do{ if(left>0){ BAR(); --left; } else __builtin_amdgcn_s_sleep(4); }while(0)
    cf uu[16]; cf ea={0.f,0.f};
    #pragma unroll
    for(int k=0;k<16;k+=2){
      const v4f st=*(LAS v4f*)(lbase+L_TAB+(32*w+16*h+k)*8);
      #pragma unroll
      for(int d=0;d<2;++d){
        const cf v={__builtin_fmaf(-st[2*d+1],cs.r,are[k+d])*st[2*d],__builtin_fmaf(-st[2*d+1],cs.i,aim[k+d])*st[2*d]};
        uu[k+d]=v; ea=cfma(lam,ea,v);
      }
      if(k==P0SPLIT){ asm volatile("":"+v"(ea.r),"+v"(ea.i)); SBAR(); }
    }
    cf elo=ea,ehi=ea; sw32(elo.r,ehi.r); sw32(elo.i,ehi.i);
    { const cf ew=cfma(l16,elo,ehi); *(LAS v4u*)(lbase+L_AGG+(w*32+r)*16)=v4u{f2u(ew.r),f2u(ew.i),1u,0u}; }
    STAMP(3);
    cf Q;
    { v4u b0,b1,b2,b3;
      #pragma unroll 1
      for(int it=0;it<SPIN_CAP*8 && !gather4(lbase+L_AGG+r*16,b0,b1,b2,b3,2);++it) SBAR();
      const cf a0={u2f(b0[0]),u2f(b0[1])}, a1={u2f(b1[0]),u2f(b1[1])}, a2={u2f(b2[0]),u2f(b2[1])}, a3={u2f(b3[0]),u2f(b3[1])};
      const cf Q2=cfma(l32,a0,a1), Q3=cfma(l32,Q2,a2);
      if(w==3){ const cf E=cfma(l32,Q3,a3); if(lane<32) st128(v4u{f2u(E.r),tag,f2u(E.i),tag},rws,ch*16+WS_GRAN+tt*8192); }
      Q= w==0?cf{0.f,0.f}: w==1?a0: w==2?Q2:Q3; }
    STAMP(4);
    if(left==7) SBAR();
    if(cg==0) __builtin_amdgcn_raw_buffer_store_b32(peek(lbase+L_TAB+w*256+lane*4),rws,WS_STAT+tt*1024+w*256+lane*4,0,16);
    #pragma unroll
    for(int k=0;k<16;++k){ uu[k]=cmul(coef,uu[k]); asm volatile("":"+v"(uu[k].r),"+v"(uu[k].i)); }
    cf H={0.f,0.f};
    int mark=left;
    if(tt>0){
      const int p0=tt-1-2*w-h, p1=p0-8;
      const u32 o0=p0>=0?(u32)(p0*8192+ch*16):OOB, o1=p1>=0?(u32)(p1*8192+ch*16):OOB;
      const u32 t0=p0>=0?tag:0u, t1=p1>=0?tag:0u;
      v4u g0,g1;
      #pragma unroll 1
      for(int it=0;it<SPIN_CAP;++it){
        asm volatile("":::"memory");
        g0=__builtin_amdgcn_raw_buffer_load_b128(rws,o0,WS_GRAN,16);
        g1=__builtin_amdgcn_raw_buffer_load_b128(rws,o1,WS_GRAN,16);
        SBAR();
        if(all_eq((g0[1]^t0)|(g0[3]^t0)|(g1[1]^t1)|(g1[3]^t1),0u)) break;
      }
      mark=left;
      cf hp=cmul(wq,cfma(l1024,cf{u2f(g1[0]),u2f(g1[2])},cf{u2f(g0[0]),u2f(g0[2])}));
      cf h2=hp; sw32(hp.r,h2.r); sw32(hp.i,h2.i);
      *(LAS v4u*)(lbase+L_HP+(w*32+r)*16)=v4u{f2u(hp.r+h2.r),f2u(hp.i+h2.i),1u,0u};
      v4u a0,a1,a2,a3;
      #pragma unroll 1
      for(int it=0;it<SPIN_CAP*8 && !gather4(lbase+L_HP+r*16,a0,a1,a2,a3,2);++it) SBAR();
      H=cf{(u2f(a0[0])+u2f(a1[0]))+(u2f(a2[0])+u2f(a3[0])),(u2f(a0[1])+u2f(a1[1]))+(u2f(a2[1])+u2f(a3[1]))};
    }
    STAMP(5);
    cf hc=cfma(pw,H,Q);
    hc=cmul(coef,csel(h,cfma(l16,hc,elo),hc));
    const int rt=2*tt+(w>>1);
    const u32 hso=WS_HS+rt*131072+(cg>>1)*16384+(((cg&1)*2+(r>>4))*2+(w&1))*1024+h*512;
    #pragma unroll
    for(int m=0;m<2;++m){
      if(left>0 && (m==1 || left==mark)){ BAR(); --left; }
      cf hv[8];
      #pragma unroll
      for(int jj=0;jj<8;++jj){ hc=cfma(lam,hc,uu[8*m+jj]); hv[jj]=hc; }
      const u32 o=hso+m*256+(((lane&15)^(8*h+4*m))<<4);
      st128(v4u{pk2(hv[0].r,hv[1].r),pk2(hv[2].r,hv[3].r),pk2(hv[4].r,hv[5].r),pk2(hv[6].r,hv[7].r)},rws,o);
      st128(v4u{pk2(-hv[0].i,-hv[1].i),pk2(-hv[2].i,-hv[3].i),pk2(-hv[4].i,-hv[5].i),pk2(-hv[6].i,-hv[7].i)},rws,o+8192);
    }
    pend=WS_FH+(rt*32+(cg&1)*16+(cg>>1)*2+(w&1))*128;
    STAMP(6);
    #pragma unroll 1
    while(left>0){
      BAR(); --left;
      #pragma unroll 1
      for(int it=0;it<10 && pend;++it){ try_flag(false); if(pend) __builtin_amdgcn_s_sleep(1); else STAMP(7); }
    }
    if(pend){ try_flag(true); STAMP(7); }
  }
  lane=lane_now(); h=lane>>5; r=lane&31;
  const int g=lane>>4, m2=w&1, tc=(w>>1)&1, kh=w>>2;
  const int dcol=ct*64+tc*32+r;
  const float dsk=b2f(a.dsk[dcol]);
  v16f ay={},az={};
  const int q=(lane&15)>>2, p=lane&3, b4=g&1, gp=b4+2*(p&1);
  u32 a0=lbase+2*XS+(kh*8+m2)*1024+(gp*16+((8*h+q)^(4*gp)))*16+8*(p>>1);
  u32 b0=lbase+WB+kh*8192+(tc*32+r)*128+((h^((r>>1)&7))<<4);
  u32 za=lbase+XS+(kh*8+m2)*1024+lane*16;
  u32 zb=lbase+WB+(64*kh+4*h+q)*128+((tc*64+b4*32+8*p)^(((q>>1)&1)<<6));
  asm volatile("":"+v"(za),"+v"(zb));
  u32 d0=(u32)(-2*(int)XS), d1=XS, d2=XS, e0=WSZ, e1=WSZ, e2=WSZ, e3=(u32)(-3*(int)WSZ);
  v4u A[2][4],B[2][4];
  auto rot=[&]() AI { { const u32 t=d0; d0=d1; d1=d2; d2=t; } { const u32 t=e0; e0=e1; e1=e2; e2=e3; e3=t; } };
  auto rdy=[&](int b) AI {
    #pragma unroll
    for(int s=0;s<4;++s){ A[b][s]=ldstr(a0+s*2048,(a0^64)+s*2048); B[b][s]=ldsr128(b0^(s*32)); }
    a0+=d0; b0+=e0; rot();
  };
  auto rdz=[&](int b) AI {
    #pragma unroll
    for(int s=0;s<4;++s){ A[b][s]=ldsr128(za+s*2048); B[b][s]=ldstr(zb+s*2048,zb+s*2048+1024); }
    za+=d0; zb+=e0; rot();
  };
  auto mm4=[&](int b,v16f& c) AI {
    #pragma unroll
    for(int s=0;s<4;++s) c=mfma(A[b][s],B[b][s],c);
    #pragma unroll
    for(int s=0;s<4;++s){ SG(0x8,1); SG(0x100,3); }
    SB; asm volatile("":"+v"(c));
  };
  #define NXT() do{ waitl0(); BAR(); }while(0)
  const int tk=32*m2+16*h+8*kh;
  STAMP(8);
  __builtin_amdgcn_s_setprio(0);
  NXT(); rdy(0);
  STAMP(9);
  #pragma unroll 1
  for(int c=0;c<3;++c){
    NXT(); rdy(1); mm4(0,ay); try_flag(false);
    NXT(); rdy(0); mm4(1,ay); try_flag(c==2);
  }
  NXT(); rdy(1); mm4(0,ay);
  NXT(); rdz(0); mm4(1,ay);
  STAMP(10);
  float sv[8], xf[8], r2[8];
  #pragma unroll 1
  for(int c=0;c<5;++c){
    NXT(); rdz(1); mm4(0,az);
    if(c==1){
      #pragma unroll
      for(int k=0;k<8;++k){
        const v2f st=*(LAS v2f*)(lbase+L_TAB+(tk+k)*8);
        xf[k]=b2f(*(LAS u16*)(lbase+L_XT+(tk+k)*128+(tc*32+r)*2));
        sv[k]=(xf[k]-st[1])*st[0]*dsk; r2[k]=st[0]*st[0];
      }
    }
    NXT(); rdz(0); mm4(1,az);
  }
  {
    const u32 xo=lbase+w*4096+lane*16, xi=lbase+(w^4)*4096+lane*16;
    float zv[8];
    if(kh){ *(LAS v4f*)xo=v4f{ay[0],ay[1],ay[2],ay[3]}; *(LAS v4f*)(xo+1024)=v4f{ay[4],ay[5],ay[6],ay[7]};
      #pragma unroll
      for(int k=0;k<8;++k) sv[k]=__builtin_fmaf(2.f,ay[8+k],sv[k]); }
    else  { *(LAS v4f*)xo=v4f{ay[8],ay[9],ay[10],ay[11]}; *(LAS v4f*)(xo+1024)=v4f{ay[12],ay[13],ay[14],ay[15]};
      #pragma unroll
      for(int k=0;k<8;++k) sv[k]=__builtin_fmaf(2.f,ay[k],sv[k]); }
    SB;
    #pragma unroll
    for(int s=0;s<4;++s) az=mfma(A[0][s],B[0][s],az);
    STAMP(11);
    if(kh){ *(LAS v4f*)(xo+2048)=v4f{az[0],az[1],az[2],az[3]}; *(LAS v4f*)(xo+3072)=v4f{az[4],az[5],az[6],az[7]};
      #pragma unroll
      for(int k=0;k<8;++k) zv[k]=az[8+k]; }
    else  { *(LAS v4f*)(xo+2048)=v4f{az[8],az[9],az[10],az[11]}; *(LAS v4f*)(xo+3072)=v4f{az[12],az[13],az[14],az[15]};
      #pragma unroll
      for(int k=0;k<8;++k) zv[k]=az[k]; }
    waitl0(); bar();
    const v4f i2=*(LAS v4f*)(xi+2048), i3=*(LAS v4f*)(xi+3072), i0=*(LAS v4f*)xi, i1=*(LAS v4f*)(xi+1024);
    const rsrc_t ro=mkrsrc(a.out,2048*512*4);
    const u32 oo=((rt2*64+tk)*512+dcol)*4;
    #pragma unroll
    for(int k=0;k<8;++k){
      const float z=(zv[k]+(k<4?i2[k&3]:i3[k&3]))*r2[k];
      const float gt=z*__builtin_amdgcn_rcpf(1.f+__builtin_amdgcn_exp2f(z*-1.44269504f));
      __builtin_amdgcn_raw_buffer_store_b32(f2u(__builtin_fmaf(__builtin_fmaf(2.f,k<4?i0[k&3]:i1[k&3],sv[k]),gt,xf[k])),ro,oo+k*2048,0,OUTAUX);
    }
  }
  CSTAMP(14); STAMP(13); STAMP_FLUSH();
}

extern "C" void kernel_launch(void* const* d_in, const int* in_sizes, int n_in,
                              void* d_out, int out_size, void* d_ws, size_t ws_size,
                              hipStream_t stream) {
  Args a;
  a.x=(const u16*)d_in[0]; a.lre=(const u16*)d_in[1]; a.lim=(const u16*)d_in[2];
  a.bre=(const u16*)d_in[3]; a.bim=(const u16*)d_in[4]; a.cre=(const u16*)d_in[5]; a.cim=(const u16*)d_in[6];
  a.ldt=(const u16*)d_in[7]; a.dsk=(const u16*)d_in[8];
  a.wg=(const u16*)d_in[10]; a.wu=(const u16*)d_in[11]; a.wd=(const u16*)d_in[12];
  a.out=(float*)d_out; a.ws=(char*)d_ws;
  fused_k<<<256,768,0,stream>>>(a);
}
```

```cpp
#include <hip/hip_runtime.h>
#include <hip/hip_bf16.h>
#include <type_traits>

typedef unsigned u32; typedef unsigned long long u64; typedef unsigned short u16;
typedef u32 v4u __attribute__((ext_vector_type(4)));
typedef u32 v2u __attribute__((ext_vector_type(2)));
typedef short v4s __attribute__((ext_vector_type(4)));
typedef __bf16 v2bf __attribute__((ext_vector_type(2)));
typedef __bf16 v8bf __attribute__((ext_vector_type(8)));
typedef float v2f __attribute__((ext_vector_type(2)));
typedef float v4f __attribute__((ext_vector_type(4)));
typedef float v16f __attribute__((ext_vector_type(16)));
#define LAS __attribute__((address_space(3)))
#define DEVI __device__ __forceinline__
#define AI __attribute__((always_inline))
#define SB __builtin_amdgcn_sched_barrier(0)
#if defined(__HIP_DEVICE_COMPILE__)
#define NOPK __attribute__((target("no-packed-fp32-ops")))
#else
#define NOPK
#endif

#define ROT1 0x01
#define ROTP 1
#define SPRIO 3
#define OUTAUX 16
#define HSLEEP 2
#define P0SPLIT 8
#define LBSLEEP 6
#define WARMB 1

constexpr u32 WS_EPOCH = 0;
constexpr u32 WS_STAT  = 4096;
constexpr u32 WS_FH    = 32768;
constexpr u32 WS_FHH   = WS_FH + 131072;
constexpr u32 WS_GRAN  = WS_FHH + 131072;
constexpr u32 WS_HH    = WS_GRAN + 131072;
constexpr u32 WS_HS    = WS_HH + 32*86*2048;
constexpr u32 WS_END   = WS_HS + 32*131072;
constexpr u32 WS_STAMP = 16u<<20;
constexpr u32 XS = 16384;
constexpr u32 WB = 49152, WSZ = 24832;
constexpr u32 L_XT = WB+4*WSZ;
constexpr u32 L_TAB  = L_XT+8192;
constexpr u32 L_CSP  = L_TAB + 1024;
constexpr u32 L_FLG  = L_CSP;
constexpr u32 L_HP   = L_CSP + 1024;
constexpr u32 L_DUM  = L_HP + 2048;
constexpr u32 L_TAG  = L_DUM + 256;
constexpr u32 L_CW   = L_XT;
constexpr u32 L_AGG  = L_XT + 2048;
constexpr u32 L_TOTAL = 163840;
static_assert(L_TAG+16<=L_TOTAL,"lds");
constexpr u32 OOB = 0x40000000u;
constexpr int SPIN_CAP = 1<<18;

#define CODEPF 0
constexpr u32 CODE_LEN=26000;
struct Args { const u16 *x,*lre,*lim,*bre,*bim,*cre,*cim,*ldt,*dsk,*wg,*wu,*wd; float* out; char* ws; };

DEVI u32 f2u(float f){ return __builtin_bit_cast(u32,f); }
DEVI float u2f(u32 u){ return __builtin_bit_cast(float,u); }
DEVI u32 rfl(u32 v){ return __builtin_amdgcn_readfirstlane(v); }
DEVI bool all_eq(u32 v,u32 t){ return __builtin_amdgcn_ballot_w64(v!=t)==0ull; }
DEVI v4u mksrd(const void* p, u32 bytes){ u64 a=(u64)p; v4u s; s.x=rfl((u32)a); s.y=rfl((u32)(a>>32)); s.z=bytes; s.w=0x00020000u; return s; }
#define DM0 "s_add_u32 m0, %[b], %[i]\n\ts_nop 0\n\t"
#define DLD(n,off,pol) "buffer_load_dwordx4 %[v" #n "], %[r], %[s] offen" off pol " lds\n\t"
template<bool SC1,u32 IMM> DEVI void dma4(v4u srd,u32 v0,u32 v1,u32 v2,u32 v3,u32 soff,u32 base){
  if(SC1) asm volatile(DM0 DLD(0,""," sc1") DLD(1," offset:1024"," sc1") DLD(2," offset:2048"," sc1") DLD(3," offset:3072"," sc1")
      :: [v0]"v"(v0),[v1]"v"(v1),[v2]"v"(v2),[v3]"v"(v3),[r]"s"(srd),[s]"s"(soff),[b]"s"(base),[i]"n"(IMM) : "memory","scc");
  else asm volatile(DM0 DLD(0,"","") DLD(1," offset:1024","") DLD(2," offset:2048","") DLD(3," offset:3072","")
      :: [v0]"v"(v0),[v1]"v"(v1),[v2]"v"(v2),[v3]"v"(v3),[r]"s"(srd),[s]"s"(soff),[b]"s"(base),[i]"n"(IMM) : "memory","scc");
}
template<u32 IMM> DEVI void dma3(v4u srd,u32 v0,u32 v1,u32 v2,u32 soff,u32 base){
  asm volatile(DM0 DLD(0,"","") DLD(1," offset:1024","") DLD(2," offset:2048","")
      :: [v0]"v"(v0),[v1]"v"(v1),[v2]"v"(v2),[r]"s"(srd),[s]"s"(soff),[b]"s"(base),[i]"n"(IMM) : "memory","scc");
}
template<u32 IMM> DEVI void dma2(v4u srd,u32 v0,u32 v1,u32 soff,u32 base){
  asm volatile(DM0 DLD(0,"","") DLD(1," offset:1024","")
      :: [v0]"v"(v0),[v1]"v"(v1),[r]"s"(srd),[s]"s"(soff),[b]"s"(base),[i]"n"(IMM) : "memory","scc");
}
template<bool SC1> DEVI void dma_dw(v4u srd,u32 voff,u32 soff,u32 lds){
  if(SC1) asm volatile("s_mov_b32 m0, %3\n\ts_nop 0\n\tbuffer_load_dword %0, %1, %2 offen sc1 lds" :: "v"(voff), "s"(srd), "s"(soff), "s"(lds) : "memory");
  else    asm volatile("s_mov_b32 m0, %3\n\ts_nop 0\n\tbuffer_load_dword %0, %1, %2 offen lds" :: "v"(voff), "s"(srd), "s"(soff), "s"(lds) : "memory");
}
DEVI void dma_x4(v4u srd,u32 voff,u32 soff,u32 lds){
  asm volatile("s_mov_b32 m0, %3\n\ts_nop 0\n\tbuffer_load_dwordx4 %0, %1, %2 offen sc1 lds" :: "v"(voff), "s"(srd), "s"(soff), "s"(lds) : "memory");
}
template<int N> DEVI void waitv(){ asm volatile("s_waitcnt vmcnt(%0)"::"n"(N):"memory"); }
DEVI void waitl0(){ asm volatile("s_waitcnt lgkmcnt(0)":::"memory"); }
DEVI void bar(){ asm volatile("":::"memory"); __builtin_amdgcn_s_barrier(); asm volatile("":::"memory"); SB; }
template<u32 OFF> DEVI void sig(u32 vaddr,u32 one){ asm volatile("s_mov_b64 exec, 1\n\tds_add_u32 %0, %1 offset:%2\n\ts_mov_b64 exec, -1" :: "v"(vaddr), "v"(one), "n"(OFF) : "memory"); }
template<u32 OFF> DEVI void sigm(u32 vaddr){ asm volatile("s_mov_b64 exec, 1\n\tds_inc_u32 %0, %0 offset:%1\n\ts_mov_b64 exec, -1" :: "v"(vaddr), "n"(OFF) : "memory"); }
DEVI u32 peek(u32 vaddr){ return *(volatile LAS u32*)vaddr; }
DEVI bool gather4(u32 b,v4u& a0,v4u& a1,v4u& a2,v4u& a3,int tries){
  #pragma unroll 1
  for(int it=0;it<tries;++it){
    a0=*(volatile LAS v4u*)b; a1=*(volatile LAS v4u*)(b+512); a2=*(volatile LAS v4u*)(b+1024); a3=*(volatile LAS v4u*)(b+1536);
    if(all_eq(a0[2]&a1[2]&a2[2]&a3[2],1u)) return true;
  }
  return false;
}
DEVI bool vm_idle(){ u32 v; asm volatile("s_getreg_b32 %0, hwreg(HW_REG_IB_STS, 0, 4)":"=s"(v)); return v==0u; }
DEVI int lane_now(){ int l; asm volatile("v_mbcnt_lo_u32_b32 %0, -1, 0\n\tv_mbcnt_hi_u32_b32 %0, -1, %0":"=v"(l)); return l; }
DEVI float b2f(u16 v){ return u2f(((u32)v)<<16); }
DEVI u32 pk2(float lo,float hi){ v2f p={lo,hi}; return __builtin_bit_cast(u32,__builtin_convertvector(p,v2bf)); }
DEVI float rbf(float f){ return u2f(pk2(f,f)<<16); }
DEVI float relu(float x){ int i=__builtin_bit_cast(int,x); return __builtin_bit_cast(float,i>0?i:0); }
DEVI float dot2(u32 a,u32 b,float c){ return __builtin_amdgcn_fdot2_f32_bf16(__builtin_bit_cast(v2bf,a),__builtin_bit_cast(v2bf,b),c,false); }
DEVI v16f mfma(v4u a,v4u b,v16f c){ return __builtin_amdgcn_mfma_f32_32x32x16_bf16(__builtin_bit_cast(v8bf,a),__builtin_bit_cast(v8bf,b),c,0,0,0); }
DEVI v4f mfma16(v4u a,v4u b,v4f c){ return __builtin_amdgcn_mfma_f32_16x16x32_bf16(__builtin_bit_cast(v8bf,a),__builtin_bit_cast(v8bf,b),c,0,0,0); }
DEVI v4u ldsr128(u32 a){ return *(LAS v4u*)a; }
DEVI v4u ldstr(u32 a0,u32 a1){
  v4s x=__builtin_amdgcn_ds_read_tr16_b64_v4i16((LAS v4s*)a0);
  v4s y=__builtin_amdgcn_ds_read_tr16_b64_v4i16((LAS v4s*)a1);
  return __builtin_bit_cast(v4u,__builtin_shufflevector(x,y,0,1,2,3,4,5,6,7));
}
DEVI void sw16(float& a,float& b){ v2u r=__builtin_amdgcn_permlane16_swap(f2u(a),f2u(b),false,false); a=u2f(r[0]); b=u2f(r[1]); }
DEVI void sw32(float& a,float& b){ v2u r=__builtin_amdgcn_permlane32_swap(f2u(a),f2u(b),false,false); a=u2f(r[0]); b=u2f(r[1]); }
template<int CTRL> DEVI float dppadd(float v){ return v+u2f(__builtin_amdgcn_update_dpp(0u,f2u(v),CTRL,0xf,0xf,true)); }
DEVI void wsum4(float& a,float& b,float& c,float& d){
  sw32(a,b); sw32(c,d); float v=a+b, t=c+d;
  sw16(v,t); v+=t;
  v=dppadd<0xB1>(v); v=dppadd<0x4E>(v); v=dppadd<0x141>(v); v=dppadd<0x140>(v);
  a=u2f(__builtin_amdgcn_readlane(f2u(v),0)); c=u2f(__builtin_amdgcn_readlane(f2u(v),16));
  b=u2f(__builtin_amdgcn_readlane(f2u(v),32)); d=u2f(__builtin_amdgcn_readlane(f2u(v),48));
}
struct cf{ float r,i; };
DEVI cf cmul(cf a,cf b){ return cf{__builtin_fmaf(-a.i,b.i,a.r*b.r), __builtin_fmaf(a.i,b.r,a.r*b.i)}; }
DEVI cf cfma(cf a,cf b,cf c){ return cf{__builtin_fmaf(-a.i,b.i,__builtin_fmaf(a.r,b.r,c.r)), __builtin_fmaf(a.i,b.r,__builtin_fmaf(a.r,b.i,c.i))}; }
DEVI cf csel(bool c,cf a,cf b){ return cf{c?a.r:b.r, c?a.i:b.i}; }
DEVI cf ldsc(u32 a){ v2f v=*(LAS v2f*)a; return cf{v[0],v[1]}; }
DEVI void sincos_(float x,float& s,float& c){
  const float n=__builtin_rintf(x*0.636619772f);
  float r=__builtin_fmaf(n,-1.57079601e+00f,x); r=__builtin_fmaf(n,-3.13916473e-07f,r); r=__builtin_fmaf(n,-5.39030253e-15f,r);
  const float z=r*r;
  float ps=__builtin_fmaf(__builtin_fmaf(__builtin_fmaf(2.86567956e-6f,z,-1.98559923e-4f),z,8.33338592e-3f),z,-1.66666672e-1f);
  ps=__builtin_fmaf(ps*z,r,r);
  float pc=__builtin_fmaf(__builtin_fmaf(__builtin_fmaf(2.44677067e-5f,z,-1.38877297e-3f),z,4.16666567e-2f),z,-0.5f);
  pc=__builtin_fmaf(pc,z,1.f);
  const int q=(int)n;
  const float a=(q&1)?pc:ps, b=(q&1)?ps:pc;
  s=(q&2)?-a:a; c=((q+1)&2)?-b:b;
}
typedef __amdgpu_buffer_rsrc_t rsrc_t;
DEVI rsrc_t mkrsrc(const void* p,u32 bytes){ return __builtin_amdgcn_make_buffer_rsrc((void*)p,0,bytes,0x00020000); }
DEVI void st128(v4u v,rsrc_t r,u32 off){ __builtin_amdgcn_raw_buffer_store_b128(v,r,off,0,16); }
DEVI u32 mktag(u32 e){ return e*0x9E3779B1u+0x7F4A7C15u; }

#define STAMP(k) do{}while(0)
#define CSTAMP(k) do{}while(0)
#define TRACE(k) do{}while(0)
#define STAMP_DECL do{}while(0)
#define BAR() bar()
#define STAMP_FLUSH() do{}while(0)
#define TRARG
#define TRPASS
#define TSARG
#define TSPASS

DEVI void poll32(v4u srd,u32 voff,u32 soff,u32 tag){
  #pragma unroll 1
  for(int it=0;it<SPIN_CAP;++it){
    u32 v;
    asm volatile("buffer_load_dword %0, %1, %2, %3 offen sc1\n\ts_waitcnt vmcnt(0)" : "=&v"(v) : "v"(voff), "s"(srd), "s"(soff) : "memory");
    if(all_eq(v,tag)) break;
    __builtin_amdgcn_s_sleep(12);
  }
}
template<int I,int N,class F> DEVI void sfor(F&& f){ if constexpr(I<N){ f(std::integral_constant<int,I>{}); sfor<I+1,N>(f); } }
AI constexpr u32 wslot(int c){ return WB+(c&3)*WSZ; }
AI constexpr u32 sx_(int c){ return c>=5?(c-5)*XS:wslot(c+2); }
AI constexpr u32 sb_(int c){ return c>=5?wslot(3)+(c-5)*8192:wslot(c+2)+16384; }

#define SG(m,n) __builtin_amdgcn_sched_group_barrier(m,n,0)
template<int NB>
DEVI u32 g_main(const Args& a,rsrc_t rws,u32 lbase,int lane,int gw,int tt,int cg,u32 hqo TRARG TSARG){
  const int h=lane>>5, r=lane&31, rh=gw&1, u0=(gw>>1)*NB;
  u32 e=0;
  if(gw==0) e=__builtin_amdgcn_raw_buffer_load_b32(rws,0,WS_EPOCH,16);
  SB;
  v16f acc[3][2]={}; v4f cw[3]={};
  u32 ax[4],aw,aw2;
  #pragma unroll
  for(int s=0;s<4;++s) ax[s]=lbase+rh*8192+r*128+((((2*(s^(2*rh))+h)^((r>>1)&7)))<<4);
  aw=lbase+WB+((lane>>4)&1)*(12288+32)+(8*h+((lane&15)>>2))*192+(lane&3)*8+u0*32;
  aw2=aw+6144-rh*6144; aw+=rh*6144;
  const u32 on2=((lane&3)==((lane>>4)&1))?0x3f803f80u:0u;
  v4u ones={on2,on2,on2,on2};
  #pragma unroll
  for(int t=0;t<NB;++t) asm volatile("":"+v"(acc[t][0]),"+v"(acc[t][1]));
  asm volatile("":"+v"(ax[0]),"+v"(ax[1]),"+v"(ax[2]),"+v"(ax[3]),"+v"(aw),"+v"(aw2),"+v"(ones));
  SB;
  u32 tag;
  {
    u32 sa=lbase+sx_(0)+gw*4096+r*128+((h^((r>>1)&7))<<4);
    u32 ab=lbase+sb_(0)+r*128+((((2*gw+h)^((r>>1)&7)))<<4);
    float s2=0.f,s3=0.f; v4f s1={},c0={},c1={};
    sfor<0,8>([&](auto C) AI {
      constexpr int c=C.value;
      waitl0(); BAR();
      v4u Y[4];
      #pragma unroll
      for(int s=0;s<4;++s) Y[s]=ldsr128(sa^(s*32));
      const v4u t0=ldsr128(ab), t1=ldsr128(ab+4096);
      if constexpr(c<7){ sa+=sx_(c+1)-sx_(c); ab+=sb_(c+1)-sb_(c); }
      if constexpr(c==2){ if(gw==0){ e=rfl(e); *(LAS v2u*)(lbase+L_TAG)=v2u{mktag(e),e}; } }
      #pragma unroll
      for(int s=0;s<4;++s){ s2=dot2(Y[s][0],Y[s][0],s2); s3=dot2(Y[s][1],Y[s][1],s3); s2=dot2(Y[s][2],Y[s][2],s2); s3=dot2(Y[s][3],Y[s][3],s3); s1=mfma16(ones,Y[s],s1); }
      c0=mfma16(ones,t0,c0); c1=mfma16(ones,t1,c1);
      asm volatile("":"+v"(s1),"+v"(s2),"+v"(s3),"+v"(c0),"+v"(c1),"+v"(sa),"+v"(ab));
    });
    STAMP(1);
    s2+=s3; s3=s2; sw32(s2,s3); s2+=s3;
    asm volatile("s_nop 7":"+v"(s1),"+v"(c0),"+v"(c1));
    const bool hi=lane&16;
    const float m=(hi?s1[1]:s1[0])*(1.f/512.f);
    *(LAS float*)(lbase+L_CSP+(gw*64+lane)*4)=h?(hi?c1[1]:c1[0]):(hi?c0[1]:c0[0]);
    const u32 vm=pk2(relu(__builtin_fmaf(s2,1.f/512.f,-m*m)),m);
    const float rs=__builtin_amdgcn_rcpf(rbf(__builtin_amdgcn_sqrtf(rbf(u2f(vm<<16)+1e-5f))));
    *(LAS v2u*)(lbase+L_TAB+(32*gw+16*((r>>2)&1)+8*(r>>4)+4*((r>>3)&1)+(r&3))*8)=v2u{f2u(rs),vm&0xffff0000u};
    tag=rfl(peek(lbase+L_TAG));
    STAMP(2);
    waitl0(); BAR();
  }
  __builtin_amdgcn_s_setprio(2);
  v4u X[2][2],T[2][3];
  auto rd=[&](int b,int s) AI {
    const u32 ad=(s<2?aw:aw2)+(s&1)*3072;
    X[b][0]=ldsr128(ax[s]);
    #pragma unroll
    for(int t=0;t<NB;++t){ T[b][t]=ldstr(ad+t*32,ad+t*32+768); if(t==0) X[b][1]=ldsr128(ax[s]+4096); }
  };
  auto mm=[&](int b,bool cs) AI {
    #pragma unroll
    for(int t=0;t<NB;++t){ acc[t][0]=mfma(T[b][t],X[b][0],acc[t][0]); if(cs) cw[t]=mfma16(ones,T[b][t],cw[t]); acc[t][1]=mfma(T[b][t],X[b][1],acc[t][1]); }
  };
  auto mix=[&](int nv,bool cs) AI {
    SG(0x8,1); SG(0x100,3); if(cs) SG(0x8,2); else SG(0x8,1); SG(0x100,3);
    if(NB==3){ SG(0x8,1); SG(0x100,2); }
    SG(0x8,1); if(nv) SG(0x2,3);
    SG(0x8,5);
  };
  u32 d0=XS, d1=XS, d2=(u32)(-2*(int)XS);
  STAMP(3); CSTAMP(6);
  rd(0,0);
  #pragma unroll 1
  for(int c=0;c<8;++c){
    const u32 dw=((c&3)==3)?(u32)(-3*(int)WSZ):WSZ;
    SB; rd(1,1); mm(0,true); ax[0]+=d0; ax[1]+=d0; aw+=dw; mix(1,true);
    SB; asm volatile("":"+v"(ax[0]),"+v"(ax[1]),"+v"(aw));
    rd(0,2); mm(1,true); mix(0,true);
    SB; rd(1,3); mm(0,false); ax[2]+=d0; ax[3]+=d0; aw2+=dw; mix(1,false);
    SB; asm volatile("":"+v"(ax[2]),"+v"(ax[3]),"+v"(aw2));
    { const u32 t=d0; d0=d1; d1=d2; d2=t; }
    if(c==7){
      const int ln=lane_now();
      #pragma unroll
      for(int t=0;t<NB;++t){ const float lo=cw[t][0], hi=cw[t][1]; cw[t][0]=(ln&16)?hi:lo; }
      *(LAS v4f*)(lbase+L_CW+(gw*32+(ln&31))*16)=v4f{cw[0][0],cw[1][0],cw[2][0],0.f};
    }
    waitl0(); BAR();
    rd(0,0); mm(1,false); mix(0,false);
  }
  const int ln=lane_now();
  const v4u pc=*(LAS v4u*)(lbase+L_CW+((gw^1)*32+(ln&31))*16);
  const u32 hm=(ln&32)?0u:0xffffffffu;
  v4u M[2];
  #pragma unroll
  for(int m=0;m<2;++m){ const u32 mu=peek(lbase+L_TAB+((rh*2+m)*32+16*((ln>>2)&1)+8*((ln>>4)&1)+4*((ln>>3)&1)+(ln&3))*8+4);
    const u32 nm=(((mu>>16)|mu)^0x80008000u)&hm; M[m]=v4u{nm,nm,0u,0u}; }
  #pragma unroll
  for(int t=0;t<NB;++t){
    const float c=cw[t][0]+u2f(pc[t]);
    const u32 c0=f2u(c)&0xffff0000u; const float r1=c-u2f(c0);
    const u32 c1=f2u(r1)&0xffff0000u; const float r2=r1-u2f(c1);
    const v4u F={(c0>>16)|c1,f2u(r2)>>16,0u,0u};
    acc[t][0]=mfma(F,M[0],acc[t][0]); acc[t][1]=mfma(F,M[1],acc[t][1]);
  }
  STAMP(4); CSTAMP(7);
  #pragma unroll
  for(int t=0;t<NB;++t)
    #pragma unroll
    for(int m=0;m<2;++m){
      v4u v;
      #pragma unroll
      for(int k=0;k<4;++k) v[k]=pk2(relu(acc[t][m][2*k])*acc[t][m][2*k+8],relu(acc[t][m][2*k+1])*acc[t][m][2*k+9]);
      st128(v,rws,hqo+(u0+t)*2048+m*1024+ln*16);
    }
  return tag;
}

__global__ NOPK __launch_bounds__(768,3) void fused_k(Args a){
  __shared__ __attribute__((aligned(256))) char smem[L_TOTAL];
  const u32 lbase=(u32)(size_t)(LAS char*)smem;
  const int tid=__builtin_amdgcn_workitem_id_x();
  const int wgid=__builtin_amdgcn_workgroup_id_x(), w=rfl(tid>>6);
  int lane=tid&63;
  const int xcd=wgid&7, idx=wgid>>3;
  if(CODEPF && w==CODEPF-1){
    u64 pc; u32 back; asm volatile("s_getpc_b64 %0\n.Lpcx%=:\n\ts_mov_b32 %1, .Lpcx%=-_Z7fused_k4Args":"=&s"(pc),"=s"(back));
    dma_dw<false>(mksrd((const void*)(pc-back),CODE_LEN),(u32)(((idx&3)*64+lane)*128),0,lbase+L_DUM);
  }
  const int tt=(xcd&1)*8+(idx&7), cg=(xcd>>1)*4+(idx>>3);
  const int nb=((cg&3)==3||cg==14)?2:3, blk0=11*(cg>>2)+3*(cg&3)-(cg==15?1:0);
  const int rt2=(xcd&3)*8+(idx&7), ct=(xcd>>2)*4+(idx>>3);
  const int ry=ROTP?2*(idx>>3):0, rz=ROTP?(11*(idx>>3))>>2:0;
  const u32 one=1;
  STAMP_DECL; STAMP(0); CSTAMP(12);

  if(w>=8){
    __builtin_amdgcn_s_setprio(3);
    const int lw=w-8, j=lane>>3, l7=lane&7;
    const v4u sx=mksrd(a.x,2048*1024);
    const v4u sb=mksrd(lw<2?a.bre:a.bim,512*1024);
    const u32 sz0=(u32)((l7^(j>>1))<<4), sz1=sz0^64u;
    const u32 xr=(tt*128+32*lw+16*(j>>2)+(j&3))*1024;
    const u32 vx0=xr+sz0, vx1=xr+3*1024+sz1, vx2=xr+6*1024+sz0, vx3=xr+9*1024+sz1;
    const u32 br=(cg*32+16*(lw&1)+j)*1024;
    const u32 vb0=br+sz0, vb1=br+7*1024+sz1;
    const u32 l4=lbase+lw*4096, l2=lbase+lw*2048;
    u32 kx=((tt*(ROT1&15)+cg*(ROT1>>4))&7)*128, kw=(kx+640)&0x380;
    #define SX(c) dma4<false,sx_(c)>(sx,vx0,vx1,vx2,vx3,kx,l4)
    #define SBB(c) do{ dma2<sb_(c)>(sb,vb0,vb1,kx,l2); kx=(kx+128)&0x380; }while(0)
    SX(0); SBB(0); SX(1); SBB(1);
    const v4u sws=mksrd(a.ws,WS_END);
    const v4u sw=mksrd(lw<2?a.wg:a.wu,512*2752);
    const v4u swd=mksrd(a.wd,1376*1024);
    const v4u sc=mksrd(lw<2?a.cre:a.cim,512*1024);
    const u32 vf=(lane&15)*128, lf=lbase+L_FLG+lw*256;
    const u32 fh=WS_FH+(rt2*32+(lw&1)*16)*128, fhh=WS_FHH+rt2*4096;
    u32 vw[6];
    #pragma unroll
    for(int i=0;i<3;++i){ const u32 q=i*1024+lane*16, row=q/192, col=q%192; const bool ok=col<(u32)nb*64;
      vw[i]=ok?(row+32*(lw&1))*2752+col+blk0*64-i*1024:OOB; vw[i+3]=vw[i]+16*2752; asm volatile("":"+v"(vw[i]),"+v"(vw[i+3])); }
    const u32 lwv=lbase+(lw>>1)*(12288+32)+(lw&1)*6144;
    #define GWA(c) dma3<wslot(c)>(sw,vw[0],vw[1],vw[2],kw*1376,lwv)
    #define GWB(c) do{ dma3<wslot(c)+3072>(sw,vw[3],vw[4],vw[5],kw*1376,lwv); kw=(kw+128)&0x380; }while(0)
    #define GX(c) do{ dma4<false,((c)%3)*XS>(sx,vx0,vx1,vx2,vx3,kx,l4); kx=(kx+128)&0x380; }while(0)
    SX(2); waitv<10>(); BAR();
    SBB(2); SX(3); waitv<10>(); BAR();
    SBB(3); SX(4); waitv<10>(); BAR();
    SBB(4); SX(5); waitv<10>(); BAR();
    SBB(5); SX(6); waitv<10>(); BAR();
    SBB(6); SX(7); waitv<10>(); BAR();
    STAMP(2);
    SBB(7); GWA(0); waitv<9>(); BAR();
    GWB(0); GWA(1); waitv<9>(); BAR();
    GWB(1); waitv<6>(); BAR();
    STAMP(3);
    GWA(2); GWB(2); GWA(3); GWB(3);
    const u32 tag=rfl(peek(lbase+L_TAG));
    u32 va=WS_HS+rt2*131072+lw*4096+lane*16;
    const u32 cr=(ct*64+(lw&1)*32+j)*1024;
    u32 vp0=cr+sz0, vp1=cr+7*1024+sz1, vp2=cr+14*1024+sz0, vp3=cr+21*1024+sz1;
    waitv<12>(); BAR();
    sfor<0,4>([&](auto C) AI {
      constexpr int c=C.value;
      GX(c+3); GWA(c+4); GWB(c+4);
      if(WARMB){
        if constexpr(c==0) dma_dw<false>(sc,lane<32?(u32)(j*1024+l7*128):OOB,(ct*64+(idx&7)*8+(lw&1)*4)*1024,lbase+L_DUM);
        if constexpr(c==2) dma_dw<false>(swd,lane<43?(u32)(lane*1024):OOB,((idx&7)*172+lw*43)*1024+ct*128,lbase+L_DUM);
      }
      waitv<WARMB?17:16>();
      BAR();
    });
    u32 ka=ry, kb=ry;
    auto PA=[&](auto C) AI {
      constexpr int c=C.value;
      if constexpr(c==8){ va=WS_HH+rt2*86*2048+lw*4096+lane*16; ka=rz; }
      u32 so;
      if constexpr(c<8){ so=ka<<14; ka=(ka+1)&7; } else { so=(ka==10&&lw==3)?OOB:ka<<14; ka= ka==10?0:ka+1; }
      dma4<true,((c+2)%3)*XS>(sws,va,va,va,va,so,l4);
    };
    auto PB=[&](auto C) AI {
      constexpr int c=C.value;
      if constexpr(c==8){ vp0=(lw*32+j)*1024+ct*128+((l7^(((j>>1)&1)<<2))<<4); vp1=vp0+7*1024; vp2=vp0+14*1024; vp3=vp0+21*1024; kb=rz; }
      u32 so;
      if constexpr(c<8){ so=kb<<7; kb=(kb+1)&7; } else { so=kb<<17; kb= kb==10?0:kb+1; }
      dma4<false,wslot(c)>(c<8?sc:swd,vp0,vp1,vp2,vp3,so,l4);
    };
    #define IC(n) std::integral_constant<int,(n)>{}
    GX(7); PB(IC(0));
    waitv<14>(); BAR();
    PB(IC(1));
    waitv<8>(); BAR();
    STAMP(4);
    PB(IC(2));
    __builtin_amdgcn_s_sleep(HSLEEP);
    dma_dw<true>(sws,vf,fh,lf);
    waitv<0>();
    const bool early=all_eq(peek(lf+lane*4),tag);
    if(early){ PA(IC(0)); PA(IC(1)); }
    BAR();
    STAMP(5);
    if(!early){ poll32(sws,vf,fh,tag); PA(IC(0)); PA(IC(1)); }
    STAMP(6);
    PB(IC(3)); PA(IC(2));
    waitv<12>(); BAR();
    if(lane<32) dma_dw<true>(sws,lane*4,WS_STAT+rt2*512+lw*128,lbase+L_TAB+lw*128);
    { const u32 vt=(rt2*64+lw*16+j)*1024+ct*128+l7*16; dma2<L_XT>(sx,vt,vt+7*1024,0,l2); }
    waitv<11>(); BAR();
    bool zok=false;
    sfor<1,18>([&](auto K) AI {
      constexpr int k=K.value;
      if constexpr(k==5) dma_dw<true>(sws,(lane&31)*128,fhh,lf);
      if constexpr(k==9){ if(wgid==0 && lw==0) dma_dw<true>(sws,vf*2,WS_FH+30*4096,lf); }
      if constexpr(k==6){
        waitv<8>();
        zok=all_eq(peek(lf+lane*4),tag);
        if(zok) PA(IC(8));
        PB(IC(9));
        if(zok) waitv<12>(); else waitv<8>();
      } else if constexpr(k==7){
        STAMP(7);
        if(!zok){ poll32(sws,(lane&31)*128,fhh,tag); PA(IC(8)); }
        STAMP(8);
        PA(IC(9)); PB(IC(10));
        if(zok) waitv<12>(); else waitv<8>();
      } else {
        if constexpr(k<=16) PA(IC(k+2));
        if constexpr(k<=15) PB(IC(k+3));
        waitv<(k==1?11:k<=15?12:k==16?8:0)>();
      }
      if constexpr(k==12){
        if(wgid==0 && lw==0){
          if(!all_eq(peek(lf+lane*4),tag)) poll32(sws,vf*2,WS_FH+30*4096,tag);
          const u32 e=peek(lbase+L_TAG+4)+1u;
          asm volatile("s_mov_b64 exec, 1\n\tbuffer_store_dword %0, off, %1, 0 sc1\n\ts_mov_b64 exec, -1" :: "v"(e), "s"(sws) : "memory");
        }
      }
      BAR();
    });
    STAMP(11);
    STAMP_FLUSH();
    return;
  }

  int h=lane>>5, r=lane&31;
  const rsrc_t rws=mkrsrc(a.ws,WS_END);
  u32 pend=0;
  u32 tag;
  auto try_flag=[&](bool force) AI {
    if(pend && (force || vm_idle())){
      waitv<0>();
      if(lane==0) __builtin_amdgcn_raw_buffer_store_b32(tag,rws,pend,0,16);
      pend=0;
    }
  };
  if(w>=4){
    const int gw=w-4, rt=2*tt+(w&1);
    const u32 hqo=WS_HH+(rt*86+2*blk0)*2048;
    if(nb==3) tag=g_main<3>(a,rws,lbase,lane,gw,tt,cg,hqo TRPASS TSPASS); else tag=g_main<2>(a,rws,lbase,lane,gw,tt,cg,hqo TRPASS TSPASS);
    pend=WS_FHH+(rt*32+cg*2+(gw>>1))*128;
    STAMP(5);
  } else {
    const int ch=cg*32+r;
    __builtin_amdgcn_s_setprio(SPRIO);
    const u16 hlr=a.lre[ch], hli=a.lim[ch], hdt=a.ldt[ch];
    *(LAS u32*)(lbase+L_HP+(w*32+r)*16+8)=0u;
    *(LAS u32*)(lbase+L_AGG+(w*32+r)*16+8)=0u;
    const float lr=b2f(hlr), li=b2f(hli), dt=rbf(__builtin_expf(b2f(hdt)));
    cf lam,coef;
    { const float ex=__builtin_expf(dt*lr); float sn,cs_; sincos_(dt*li,sn,cs_); lam=cf{ex*cs_,ex*sn};
      const float dd=1.f/(lr*lr+li*li); coef=cmul(cf{lam.r-1.f,lam.i},cf{lr*dd,-li*dd}); }
    cf l16=cmul(lam,lam); l16=cmul(l16,l16); l16=cmul(l16,l16); l16=cmul(l16,l16);
    cf l32=cmul(l16,l16);
    cf l1024,wq,pw;
    { const cf l64=cmul(l32,l32), l128=cmul(l64,l64), l256=cmul(l128,l128), l512=cmul(l256,l256), o={1.f,0.f};
      l1024=cmul(l512,l512);
      wq=cmul(csel(h,l128,o),cmul(csel(w&1,l256,o),csel(w&2,l512,o)));
      pw=cmul(csel(w&1,l32,o),csel(w&2,l64,o)); }
    asm volatile("":"+v"(lam.r),"+v"(lam.i),"+v"(coef.r),"+v"(coef.i),"+v"(l16.r),"+v"(l16.i),"+v"(l32.r),"+v"(l32.i));
    asm volatile("":"+v"(l1024.r),"+v"(l1024.i),"+v"(wq.r),"+v"(wq.i),"+v"(pw.r),"+v"(pw.i));
    v16f are={},aim={};
    {
      u32 ax[4],ab[4];
      #pragma unroll
      for(int s=0;s<4;++s){ const u32 t=lbase+r*128+((((2*s+h)^((r>>1)&7)))<<4); ab[s]=t+sb_(0); ax[s]=t+sx_(0)+w*4096; }
      v4u X[2][2],T[2][2][2];
      auto rd=[&](int b) AI {
        #pragma unroll
        for(int s=0;s<2;++s){ X[b][s]=ldsr128(ax[2*b+s]); T[b][s][0]=ldsr128(ab[2*b+s]); T[b][s][1]=ldsr128(ab[2*b+s]+4096); } };
      auto mm=[&](int b) AI {
        #pragma unroll
        for(int s=0;s<2;++s){ are=mfma(X[b][s],T[b][s][0],are); aim=mfma(X[b][s],T[b][s][1],aim); }
        asm volatile("":"+v"(are),"+v"(aim)); };
      STAMP(1);
      waitl0(); BAR();
      rd(0);
      sfor<0,8>([&](auto C) AI {
        constexpr int c=C.value;
        SB; rd(1);
        SB; mm(0);
        if constexpr(c<7){
          #pragma unroll
          for(int s=0;s<4;++s){ ax[s]+=sx_(c+1)-sx_(c); ab[s]+=sb_(c+1)-sb_(c); asm volatile("":"+v"(ax[s]),"+v"(ab[s])); }
          SB;
          waitl0(); BAR();
          rd(0);
        }
        SB; mm(1);
      });
    }
    STAMP(2);
    waitl0(); BAR();
    tag=rfl(peek(lbase+L_TAG));
    cf cs={0.f,0.f};
    #pragma unroll
    for(int k=0;k<4;++k){ const u32 b=lbase+L_CSP+(k*64+r)*4; cs.r+=*(LAS float*)b; cs.i+=*(LAS float*)(b+128); }
    int left=8;
    #define SBAR() do{ if(left>0){ BAR(); --left; } else __builtin_amdgcn_s_sleep(4); }while(0)
    cf uu[16]; cf ea={0.f,0.f};
    #pragma unroll
    for(int k=0;k<16;k+=2){
      const v4f st=*(LAS v4f*)(lbase+L_TAB+(32*w+16*h+k)*8);
      #pragma unroll
      for(int d=0;d<2;++d){
        const cf v={__builtin_fmaf(-st[2*d+1],cs.r,are[k+d])*st[2*d],__builtin_fmaf(-st[2*d+1],cs.i,aim[k+d])*st[2*d]};
        uu[k+d]=v; ea=cfma(lam,ea,v);
      }
      if(k==P0SPLIT){ asm volatile("":"+v"(ea.r),"+v"(ea.i)); SBAR(); }
    }
    cf elo=ea,ehi=ea; sw32(elo.r,ehi.r); sw32(elo.i,ehi.i);
    { const cf ew=cfma(l16,elo,ehi); *(LAS v4u*)(lbase+L_AGG+(w*32+r)*16)=v4u{f2u(ew.r),f2u(ew.i),1u,0u}; }
    STAMP(3);
    cf Q;
    { v4u b0,b1,b2,b3;
      #pragma unroll 1
      for(int it=0;it<SPIN_CAP*8 && !gather4(lbase+L_AGG+r*16,b0,b1,b2,b3,2);++it) SBAR();
      const cf a0={u2f(b0[0]),u2f(b0[1])}, a1={u2f(b1[0]),u2f(b1[1])}, a2={u2f(b2[0]),u2f(b2[1])}, a3={u2f(b3[0]),u2f(b3[1])};
      const cf Q2=cfma(l32,a0,a1), Q3=cfma(l32,Q2,a2);
      if(w==3){ const cf E=cfma(l32,Q3,a3); if(lane<32) st128(v4u{f2u(E.r),tag,f2u(E.i),tag},rws,ch*16+WS_GRAN+tt*8192); }
      Q= w==0?cf{0.f,0.f}: w==1?a0: w==2?Q2:Q3; }
    STAMP(4);
    if(left==7) SBAR();
    if(cg==0) __builtin_amdgcn_raw_buffer_store_b32(peek(lbase+L_TAB+w*256+lane*4),rws,WS_STAT+tt*1024+w*256+lane*4,0,16);
    #pragma unroll
    for(int k=0;k<16;++k){ uu[k]=cmul(coef,uu[k]); asm volatile("":"+v"(uu[k].r),"+v"(uu[k].i)); }
    cf H={0.f,0.f};
    int mark=left;
    if(tt>0){
      const int p0=tt-1-2*w-h, p1=p0-8;
      const u32 o0=p0>=0?(u32)(p0*8192+ch*16):OOB, o1=p1>=0?(u32)(p1*8192+ch*16):OOB;
      const u32 t0=p0>=0?tag:0u, t1=p1>=0?tag:0u;
      v4u g0,g1;
      #pragma unroll 1
      for(int it=0;it<SPIN_CAP;++it){
        asm volatile("":::"memory");
        g0=__builtin_amdgcn_raw_buffer_load_b128(rws,o0,WS_GRAN,16);
        g1=__builtin_amdgcn_raw_buffer_load_b128(rws,o1,WS_GRAN,16);
        SBAR();
        if(all_eq((g0[1]^t0)|(g0[3]^t0)|(g1[1]^t1)|(g1[3]^t1),0u)) break;
      }
      mark=left;
      cf hp=cmul(wq,cfma(l1024,cf{u2f(g1[0]),u2f(g1[2])},cf{u2f(g0[0]),u2f(g0[2])}));
      cf h2=hp; sw32(hp.r,h2.r); sw32(hp.i,h2.i);
      *(LAS v4u*)(lbase+L_HP+(w*32+r)*16)=v4u{f2u(hp.r+h2.r),f2u(hp.i+h2.i),1u,0u};
      v4u a0,a1,a2,a3;
      #pragma unroll 1
      for(int it=0;it<SPIN_CAP*8 && !gather4(lbase+L_HP+r*16,a0,a1,a2,a3,2);++it) SBAR();
      H=cf{(u2f(a0[0])+u2f(a1[0]))+(u2f(a2[0])+u2f(a3[0])),(u2f(a0[1])+u2f(a1[1]))+(u2f(a2[1])+u2f(a3[1]))};
    }
    STAMP(5);
    cf hc=cfma(pw,H,Q);
    hc=cmul(coef,csel(h,cfma(l16,hc,elo),hc));
    const int rt=2*tt+(w>>1);
    const u32 hso=WS_HS+rt*131072+(cg>>1)*16384+(((cg&1)*2+(r>>4))*2+(w&1))*1024+h*512;
    #pragma unroll
    for(int m=0;m<2;++m){
      if(left>0 && (m==1 || left==mark)){ BAR(); --left; }
      cf hv[8];
      #pragma unroll
      for(int jj=0;jj<8;++jj){ hc=cfma(lam,hc,uu[8*m+jj]); hv[jj]=hc; }
      const u32 o=hso+m*256+(((lane&15)^(8*h+4*m))<<4);
      st128(v4u{pk2(hv[0].r,hv[1].r),pk2(hv[2].r,hv[3].r),pk2(hv[4].r,hv[5].r),pk2(hv[6].r,hv[7].r)},rws,o);
      st128(v4u{pk2(-hv[0].i,-hv[1].i),pk2(-hv[2].i,-hv[3].i),pk2(-hv[4].i,-hv[5].i),pk2(-hv[6].i,-hv[7].i)},rws,o+8192);
    }
    pend=WS_FH+(rt*32+(cg&1)*16+(cg>>1)*2+(w&1))*128;
    STAMP(6);
    #pragma unroll 1
    while(left>0){
      BAR(); --left;
      #pragma unroll 1
      for(int it=0;it<10 && pend;++it){ try_flag(false); if(pend) __builtin_amdgcn_s_sleep(1); else STAMP(7); }
    }
    if(pend){ try_flag(true); STAMP(7); }
  }
  lane=lane_now(); h=lane>>5; r=lane&31;
  const int g=lane>>4, m2=w&1, tc=(w>>1)&1, kh=w>>2;
  const int dcol=ct*64+tc*32+r;
  const float dsk=b2f(a.dsk[dcol]);
  v16f ay={},az={};
  const int q=(lane&15)>>2, p=lane&3, b4=g&1, gp=b4+2*(p&1);
  u32 a0=lbase+2*XS+(kh*8+m2)*1024+(gp*16+((8*h+q)^(4*gp)))*16+8*(p>>1);
  u32 b0=lbase+WB+kh*8192+(tc*32+r)*128+((h^((r>>1)&7))<<4);
  u32 za=lbase+XS+(kh*8+m2)*1024+lane*16;
  u32 zb=lbase+WB+(64*kh+4*h+q)*128+((tc*64+b4*32+8*p)^(((q>>1)&1)<<6));
  asm volatile("":"+v"(za),"+v"(zb));
  u32 d0=(u32)(-2*(int)XS), d1=XS, d2=XS, e0=WSZ, e1=WSZ, e2=WSZ, e3=(u32)(-3*(int)WSZ);
  v4u A[2][4],B[2][4];
  auto rot=[&]() AI { { const u32 t=d0; d0=d1; d1=d2; d2=t; } { const u32 t=e0; e0=e1; e1=e2; e2=e3; e3=t; } };
  auto rdy=[&](int b) AI {
    #pragma unroll
    for(int s=0;s<4;++s){ A[b][s]=ldstr(a0+s*2048,(a0^64)+s*2048); B[b][s]=ldsr128(b0^(s*32)); }
    a0+=d0; b0+=e0; rot();
  };
  auto rdz=[&](int b) AI {
    #pragma unroll
    for(int s=0;s<4;++s){ A[b][s]=ldsr128(za+s*2048); B[b][s]=ldstr(zb+s*2048,zb+s*2048+1024); }
    za+=d0; zb+=e0; rot();
  };
  auto mm4=[&](int b,v16f& c) AI {
    #pragma unroll
    for(int s=0;s<4;++s) c=mfma(A[b][s],B[b][s],c);
    #pragma unroll
    for(int s=0;s<4;++s){ SG(0x8,1); SG(0x100,3); }
    SB; asm volatile("":"+v"(c));
  };
  #define NXT() do{ waitl0(); BAR(); }while(0)
  const int tk=32*m2+16*h+8*kh;
  STAMP(8);
  __builtin_amdgcn_s_setprio(0);
  NXT(); rdy(0);
  STAMP(9);
  #pragma unroll 1
  for(int c=0;c<3;++c){
    NXT(); rdy(1); mm4(0,ay); try_flag(false);
    NXT(); rdy(0); mm4(1,ay); try_flag(c==2);
  }
  NXT(); rdy(1); mm4(0,ay);
  NXT(); rdz(0); mm4(1,ay);
  STAMP(10);
  float sv[8], xf[8], r2[8];
  #pragma unroll 1
  for(int c=0;c<5;++c){
    NXT(); rdz(1); mm4(0,az);
    if(c==1){
      #pragma unroll
      for(int k=0;k<8;++k){
        const v2f st=*(LAS v2f*)(lbase+L_TAB+(tk+k)*8);
        xf[k]=b2f(*(LAS u16*)(lbase+L_XT+(tk+k)*128+(tc*32+r)*2));
        sv[k]=(xf[k]-st[1])*st[0]*dsk; r2[k]=st[0]*st[0];
      }
    }
    NXT(); rdz(0); mm4(1,az);
  }
  {
    const u32 xo=lbase+w*4096+lane*16, xi=lbase+(w^4)*4096+lane*16;
    float zv[8];
    if(kh){ *(LAS v4f*)xo=v4f{ay[0],ay[1],ay[2],ay[3]}; *(LAS v4f*)(xo+1024)=v4f{ay[4],ay[5],ay[6],ay[7]};
      #pragma unroll
      for(int k=0;k<8;++k) sv[k]=__builtin_fmaf(2.f,ay[8+k],sv[k]); }
    else  { *(LAS v4f*)xo=v4f{ay[8],ay[9],ay[10],ay[11]}; *(LAS v4f*)(xo+1024)=v4f{ay[12],ay[13],ay[14],ay[15]};
      #pragma unroll
      for(int k=0;k<8;++k) sv[k]=__builtin_fmaf(2.f,ay[k],sv[k]); }
    SB;
    #pragma unroll
    for(int s=0;s<4;++s) az=mfma(A[0][s],B[0][s],az);
    STAMP(11);
    if(kh){ *(LAS v4f*)(xo+2048)=v4f{az[0],az[1],az[2],az[3]}; *(LAS v4f*)(xo+3072)=v4f{az[4],az[5],az[6],az[7]};
      #pragma unroll
      for(int k=0;k<8;++k) zv[k]=az[8+k]; }
    else  { *(LAS v4f*)(xo+2048)=v4f{az[8],az[9],az[10],az[11]}; *(LAS v4f*)(xo+3072)=v4f{az[12],az[13],az[14],az[15]};
      #pragma unroll
      for(int k=0;k<8;++k) zv[k]=az[k]; }
    waitl0(); bar();
    const v4f i2=*(LAS v4f*)(xi+2048), i3=*(LAS v4f*)(xi+3072), i0=*(LAS v4f*)xi, i1=*(LAS v4f*)(xi+1024);
    const rsrc_t ro=mkrsrc(a.out,2048*512*4);
    const u32 oo=((rt2*64+tk)*512+dcol)*4;
    #pragma unroll
    for(int k=0;k<8;++k){
      const float z=(zv[k]+(k<4?i2[k&3]:i3[k&3]))*r2[k];
      const float gt=z*__builtin_amdgcn_rcpf(1.f+__builtin_amdgcn_exp2f(z*-1.44269504f));
      __builtin_amdgcn_raw_buffer_store_b32(f2u(__builtin_fmaf(__builtin_fmaf(2.f,k<4?i0[k&3]:i1[k&3],sv[k]),gt,xf[k])),ro,oo+k*2048,0,OUTAUX);
    }
  }
  CSTAMP(14); STAMP(13); STAMP_FLUSH();
}

extern "C" void kernel_launch(void* const* d_in, const int* in_sizes, int n_in,
                              void* d_out, int out_size, void* d_ws, size_t ws_size,
                              hipStream_t stream) {
  Args a;
  a.x=(const u16*)d_in[0]; a.lre=(const u16*)d_in[1]; a.lim=(const u16*)d_in[2];
  a.bre=(const u16*)d_in[3]; a.bim=(const u16*)d_in[4]; a.cre=(const u16*)d_in[5]; a.cim=(const u16*)d_in[6];
  a.ldt=(const u16*)d_in[7]; a.dsk=(const u16*)d_in[8];
  a.wg=(const u16*)d_in[10]; a.wu=(const u16*)d_in[11]; a.wd=(const u16*)d_in[12];
  a.out=(float*)d_out; a.ws=(char*)d_ws;
  fused_k<<<256,768,0,stream>>>(a);
}
```

```cpp
#include <hip/hip_runtime.h>
#include <hip/hip_bf16.h>
#include <type_traits>

typedef unsigned u32; typedef unsigned long long u64; typedef unsigned short u16;
typedef u32 v4u __attribute__((ext_vector_type(4)));
typedef u32 v2u __attribute__((ext_vector_type(2)));
typedef short v4s __attribute__((ext_vector_type(4)));
typedef __bf16 v2bf __attribute__((ext_vector_type(2)));
typedef __bf16 v8bf __attribute__((ext_vector_type(8)));
typedef float v2f __attribute__((ext_vector_type(2)));
typedef float v4f __attribute__((ext_vector_type(4)));
typedef float v16f __attribute__((ext_vector_type(16)));
#define LAS __attribute__((address_space(3)))
#define DEVI __device__ __forceinline__
#define AI __attribute__((always_inline))
#define SB __builtin_amdgcn_sched_barrier(0)
#if defined(__HIP_DEVICE_COMPILE__)
#define NOPK __attribute__((target("no-packed-fp32-ops")))
#else
#define NOPK
#endif

#define ROT1 0x01
#define ROTP 1
#define SPRIO 2
#define OUTAUX 16
#define HSLEEP 2
#define FSPLIT 0
#define FWIN 10
#define LATEA 0
#define CSPLIT 5
#define LBSLEEP 6
#define WARMB 1

constexpr u32 WS_EPOCH = 0;
constexpr u32 WS_FX    = 32768;
constexpr u32 WS_FH    = WS_FX + 131072;
constexpr u32 WS_FHH   = WS_FH + 131072;
constexpr u32 WS_GRAN  = WS_FHH + 131072;
constexpr u32 WS_HH    = WS_GRAN + 131072;
constexpr u32 WS_HS    = WS_HH + 32*86*2048;
constexpr u32 WS_END   = WS_HS + 32*131072;
constexpr u32 WS_STAMP = 16u<<20;
constexpr u32 XS = 16384;
constexpr u32 WB = 49152, WSZ = 24832;
constexpr u32 L_XT = WB+4*WSZ;
constexpr u32 L_TAB  = L_XT+8192;
constexpr u32 L_CSP  = L_TAB + 2048;
constexpr u32 L_FLG  = L_CSP;
constexpr u32 L_CW   = L_CSP + 1024;
constexpr u32 L_AGG  = L_CW + 2048;
constexpr u32 L_DUM  = L_AGG + 1024;
constexpr u32 L_TAG  = L_DUM + 256;
constexpr u32 L_GR   = L_XT;
constexpr u32 L_TOTAL = 163840;
static_assert(L_TAG+16<=L_TOTAL,"lds");
constexpr u32 OOB = 0x40000000u;
constexpr int SPIN_CAP = 1<<18;

#define CODEPF 1
constexpr u32 CODE_LEN=25000;
struct Args { const u16 *x,*lre,*lim,*bre,*bim,*cre,*cim,*ldt,*dsk,*wg,*wu,*wd; float* out; char* ws; };

DEVI u32 f2u(float f){ return __builtin_bit_cast(u32,f); }
DEVI float u2f(u32 u){ return __builtin_bit_cast(float,u); }
DEVI u32 rfl(u32 v){ return __builtin_amdgcn_readfirstlane(v); }
DEVI bool all_eq(u32 v,u32 t){ return __builtin_amdgcn_ballot_w64(v!=t)==0ull; }
DEVI v4u mksrd(const void* p, u32 bytes){ u64 a=(u64)p; v4u s; s.x=rfl((u32)a); s.y=rfl((u32)(a>>32)); s.z=bytes; s.w=0x00020000u; return s; }
#define DM0 "s_add_u32 m0, %[b], %[i]\n\ts_nop 0\n\t"
#define DLD(n,off,pol) "buffer_load_dwordx4 %[v" #n "], %[r], %[s] offen" off pol " lds\n\t"
template<bool SC1,u32 IMM> DEVI void dma4(v4u srd,u32 v0,u32 v1,u32 v2,u32 v3,u32 soff,u32 base){
  if(SC1) asm volatile(DM0 DLD(0,""," sc1") DLD(1," offset:1024"," sc1") DLD(2," offset:2048"," sc1") DLD(3," offset:3072"," sc1")
      :: [v0]"v"(v0),[v1]"v"(v1),[v2]"v"(v2),[v3]"v"(v3),[r]"s"(srd),[s]"s"(soff),[b]"s"(base),[i]"n"(IMM) : "memory","scc");
  else asm volatile(DM0 DLD(0,"","") DLD(1," offset:1024","") DLD(2," offset:2048","") DLD(3," offset:3072","")
      :: [v0]"v"(v0),[v1]"v"(v1),[v2]"v"(v2),[v3]"v"(v3),[r]"s"(srd),[s]"s"(soff),[b]"s"(base),[i]"n"(IMM) : "memory","scc");
}
template<u32 IMM> DEVI void dma3(v4u srd,u32 v0,u32 v1,u32 v2,u32 soff,u32 base){
  asm volatile(DM0 DLD(0,"","") DLD(1," offset:1024","") DLD(2," offset:2048","")
      :: [v0]"v"(v0),[v1]"v"(v1),[v2]"v"(v2),[r]"s"(srd),[s]"s"(soff),[b]"s"(base),[i]"n"(IMM) : "memory","scc");
}
template<u32 IMM> DEVI void dma2(v4u srd,u32 v0,u32 v1,u32 soff,u32 base){
  asm volatile(DM0 DLD(0,"","") DLD(1," offset:1024","")
      :: [v0]"v"(v0),[v1]"v"(v1),[r]"s"(srd),[s]"s"(soff),[b]"s"(base),[i]"n"(IMM) : "memory","scc");
}
template<bool SC1> DEVI void dma_dw(v4u srd,u32 voff,u32 soff,u32 lds){
  if(SC1) asm volatile("s_mov_b32 m0, %3\n\ts_nop 0\n\tbuffer_load_dword %0, %1, %2 offen sc1 lds" :: "v"(voff), "s"(srd), "s"(soff), "s"(lds) : "memory");
  else    asm volatile("s_mov_b32 m0, %3\n\ts_nop 0\n\tbuffer_load_dword %0, %1, %2 offen lds" :: "v"(voff), "s"(srd), "s"(soff), "s"(lds) : "memory");
}
DEVI void dma_x4(v4u srd,u32 voff,u32 soff,u32 lds){
  asm volatile("s_mov_b32 m0, %3\n\ts_nop 0\n\tbuffer_load_dwordx4 %0, %1, %2 offen sc1 lds" :: "v"(voff), "s"(srd), "s"(soff), "s"(lds) : "memory");
}
template<int N> DEVI void waitv(){ asm volatile("s_waitcnt vmcnt(%0)"::"n"(N):"memory"); }
DEVI void waitl0(){ asm volatile("s_waitcnt lgkmcnt(0)":::"memory"); }
DEVI void bar(){ asm volatile("":::"memory"); __builtin_amdgcn_s_barrier(); asm volatile("":::"memory"); SB; }
template<u32 OFF> DEVI void sig(u32 vaddr,u32 one){ asm volatile("s_mov_b64 exec, 1\n\tds_add_u32 %0, %1 offset:%2\n\ts_mov_b64 exec, -1" :: "v"(vaddr), "v"(one), "n"(OFF) : "memory"); }
template<u32 OFF> DEVI void sigm(u32 vaddr){ asm volatile("s_mov_b64 exec, 1\n\tds_inc_u32 %0, %0 offset:%1\n\ts_mov_b64 exec, -1" :: "v"(vaddr), "n"(OFF) : "memory"); }
DEVI u32 peek(u32 vaddr){ return *(volatile LAS u32*)vaddr; }
DEVI bool vm_idle(){ u32 v; asm volatile("s_getreg_b32 %0, hwreg(HW_REG_IB_STS, 0, 4)":"=s"(v)); return v==0u; }
DEVI int lane_now(){ int l; asm volatile("v_mbcnt_lo_u32_b32 %0, -1, 0\n\tv_mbcnt_hi_u32_b32 %0, -1, %0":"=v"(l)); return l; }
DEVI float b2f(u16 v){ return u2f(((u32)v)<<16); }
DEVI u32 pk2(float lo,float hi){ v2f p={lo,hi}; return __builtin_bit_cast(u32,__builtin_convertvector(p,v2bf)); }
DEVI float rbf(float f){ return u2f(pk2(f,f)<<16); }
DEVI float relu(float x){ int i=__builtin_bit_cast(int,x); return __builtin_bit_cast(float,i>0?i:0); }
DEVI float dot2(u32 a,u32 b,float c){ return __builtin_amdgcn_fdot2_f32_bf16(__builtin_bit_cast(v2bf,a),__builtin_bit_cast(v2bf,b),c,false); }
DEVI v16f mfma(v4u a,v4u b,v16f c){ return __builtin_amdgcn_mfma_f32_32x32x16_bf16(__builtin_bit_cast(v8bf,a),__builtin_bit_cast(v8bf,b),c,0,0,0); }
DEVI v4f mfma16(v4u a,v4u b,v4f c){ return __builtin_amdgcn_mfma_f32_16x16x32_bf16(__builtin_bit_cast(v8bf,a),__builtin_bit_cast(v8bf,b),c,0,0,0); }
DEVI v4u ldsr128(u32 a){ return *(LAS v4u*)a; }
DEVI v4u ldstr(u32 a0,u32 a1){
  v4s x=__builtin_amdgcn_ds_read_tr16_b64_v4i16((LAS v4s*)a0);
  v4s y=__builtin_amdgcn_ds_read_tr16_b64_v4i16((LAS v4s*)a1);
  return __builtin_bit_cast(v4u,__builtin_shufflevector(x,y,0,1,2,3,4,5,6,7));
}
DEVI void sw16(float& a,float& b){ v2u r=__builtin_amdgcn_permlane16_swap(f2u(a),f2u(b),false,false); a=u2f(r[0]); b=u2f(r[1]); }
DEVI void sw32(float& a,float& b){ v2u r=__builtin_amdgcn_permlane32_swap(f2u(a),f2u(b),false,false); a=u2f(r[0]); b=u2f(r[1]); }
template<int CTRL> DEVI float dppadd(float v){ return v+u2f(__builtin_amdgcn_update_dpp(0u,f2u(v),CTRL,0xf,0xf,true)); }
DEVI void wsum4(float& a,float& b,float& c,float& d){
  sw32(a,b); sw32(c,d); float v=a+b, t=c+d;
  sw16(v,t); v+=t;
  v=dppadd<0xB1>(v); v=dppadd<0x4E>(v); v=dppadd<0x141>(v); v=dppadd<0x140>(v);
  a=u2f(__builtin_amdgcn_readlane(f2u(v),0)); c=u2f(__builtin_amdgcn_readlane(f2u(v),16));
  b=u2f(__builtin_amdgcn_readlane(f2u(v),32)); d=u2f(__builtin_amdgcn_readlane(f2u(v),48));
}
struct cf{ float r,i; };
DEVI cf cmul(cf a,cf b){ return cf{__builtin_fmaf(-a.i,b.i,a.r*b.r), __builtin_fmaf(a.i,b.r,a.r*b.i)}; }
DEVI cf cfma(cf a,cf b,cf c){ return cf{__builtin_fmaf(-a.i,b.i,__builtin_fmaf(a.r,b.r,c.r)), __builtin_fmaf(a.i,b.r,__builtin_fmaf(a.r,b.i,c.i))}; }
DEVI cf csel(bool c,cf a,cf b){ return cf{c?a.r:b.r, c?a.i:b.i}; }
DEVI cf ldsc(u32 a){ v2f v=*(LAS v2f*)a; return cf{v[0],v[1]}; }
DEVI void sincos_(float x,float& s,float& c){
  const float n=__builtin_rintf(x*0.636619772f);
  float r=__builtin_fmaf(n,-1.57079601e+00f,x); r=__builtin_fmaf(n,-3.13916473e-07f,r); r=__builtin_fmaf(n,-5.39030253e-15f,r);
  const float z=r*r;
  float ps=__builtin_fmaf(__builtin_fmaf(__builtin_fmaf(2.86567956e-6f,z,-1.98559923e-4f),z,8.33338592e-3f),z,-1.66666672e-1f);
  ps=__builtin_fmaf(ps*z,r,r);
  float pc=__builtin_fmaf(__builtin_fmaf(__builtin_fmaf(2.44677067e-5f,z,-1.38877297e-3f),z,4.16666567e-2f),z,-0.5f);
  pc=__builtin_fmaf(pc,z,1.f);
  const int q=(int)n;
  const float a=(q&1)?pc:ps, b=(q&1)?ps:pc;
  s=(q&2)?-a:a; c=((q+1)&2)?-b:b;
}
typedef __amdgpu_buffer_rsrc_t rsrc_t;
DEVI rsrc_t mkrsrc(const void* p,u32 bytes){ return __builtin_amdgcn_make_buffer_rsrc((void*)p,0,bytes,0x00020000); }
DEVI void st128(v4u v,rsrc_t r,u32 off){ __builtin_amdgcn_raw_buffer_store_b128(v,r,off,0,16); }
DEVI u32 mktag(u32 e){ return (e*0x9E3779B1u+0x7F4A7C15u)|1u; }

#define STAMP(k) do{}while(0)
#define CSTAMP(k) do{}while(0)
#define TRACE(k) do{}while(0)
#define STAMP_DECL do{}while(0)
#define BAR() bar()
#define STAMP_FLUSH() do{}while(0)
#define TRARG
#define TRPASS
#define TSARG
#define TSPASS

DEVI void poll32(v4u srd,u32 voff,u32 soff,u32 tag){
  #pragma unroll 1
  for(int it=0;it<SPIN_CAP;++it){
    u32 v;
    asm volatile("buffer_load_dword %0, %1, %2, %3 offen sc1\n\ts_waitcnt vmcnt(0)" : "=&v"(v) : "v"(voff), "s"(srd), "s"(soff) : "memory");
    if(all_eq(v,tag)) break;
    __builtin_amdgcn_s_sleep(12);
  }
}
template<int I,int N,class F> DEVI void sfor(F&& f){ if constexpr(I<N){ f(std::integral_constant<int,I>{}); sfor<I+1,N>(f); } }
AI constexpr u32 wslot(int c){ return WB+(c&3)*WSZ; }
AI constexpr u32 pa_(int c){ return (c&3)==3?WB+5*XS:(((c&3)+2)%3)*XS; }
AI constexpr u32 pb_(int c){ return WB+(c%5)*XS; }
AI constexpr u32 sx_(int c){ return c>=5?(c-5)*XS:wslot(c+2); }
AI constexpr u32 sb_(int c){ return c>=5?wslot(3)+(c-5)*8192:wslot(c+2)+16384; }

#define SG(m,n) __builtin_amdgcn_sched_group_barrier(m,n,0)
template<int NB>
DEVI u32 g_main(const Args& a,rsrc_t rws,u32 lbase,int lane,int gw,int tt,int cg,u32 hqo TRARG TSARG){
  const int h=lane>>5, r=lane&31, rh=gw&1, u0=(gw>>1)*NB;
  const int row=tt*128+cg*8+gw*2;
  const v4u x0=((const v4u*)a.x)[row*64+lane], x1=((const v4u*)a.x)[row*64+64+lane];
  const u32 e=__builtin_amdgcn_raw_buffer_load_b32(rws,0,WS_EPOCH,16);
  SB;
  v16f acc[3][2]; v4f cw[3];
  u32 ax[4],aw,aw2;
  const u32 on2=((lane&3)==((lane>>4)&1))?0x3f803f80u:0u;
  v4u ones={on2,on2,on2,on2};
  u32 tag;
  {
    u32 ab=lbase+sb_(0)+r*128+((((2*gw+h)^((r>>1)&7)))<<4);
    v4f c0={},c1={};
    sfor<0,8>([&](auto C) AI {
      constexpr int c=C.value;
      waitl0(); BAR();
      const v4u t0=ldsr128(ab), t1=ldsr128(ab+4096);
      if constexpr(c<7) ab+=sb_(c+1)-sb_(c);
      if constexpr(c==0){
        const float z0=u2f(rfl(x0[0])<<16), z1=u2f(rfl(x1[0])<<16);
        float s0=0.f,s1=0.f,v0=0.f,v1=0.f;
        #pragma unroll
        for(int k=0;k<8;++k){
          const float d0=u2f((k&1)?x0[k>>1]&0xffff0000u:x0[k>>1]<<16)-z0, d1=u2f((k&1)?x1[k>>1]&0xffff0000u:x1[k>>1]<<16)-z1;
          s0+=d0; s1+=d1; v0=__builtin_fmaf(d0,d0,v0); v1=__builtin_fmaf(d1,d1,v1); }
        wsum4(s0,s1,v0,v1);
        const float sm=(lane?s1:s0)*(1.f/512.f);
        const u32 vm=pk2(relu(__builtin_fmaf(lane?v1:v0,1.f/512.f,-sm*sm)),sm+(lane?z1:z0));
        const float rs=__builtin_amdgcn_rcpf(rbf(__builtin_amdgcn_sqrtf(rbf(u2f(vm<<16)+1e-5f))));
        tag=mktag(rfl(e));
        if(lane<2) st128(v4u{f2u(rs),tag,vm&0xffff0000u,tag},rws,WS_FX+row*64+lane*16);
        if(gw==0) *(LAS v2u*)(lbase+L_TAG)=v2u{tag,e};
        STAMP(1);
      }
      if constexpr(c>=1 && c<=2*NB){ acc[(c-1)>>1][(c-1)&1]=v16f{}; asm volatile("":"+v"(acc[(c-1)>>1][(c-1)&1])); }
      c0=mfma16(ones,t0,c0); c1=mfma16(ones,t1,c1);
      asm volatile("":"+v"(c0),"+v"(c1),"+v"(ab));
    });
    asm volatile("s_nop 7\n\ts_nop 1":"+v"(c0),"+v"(c1));
    const bool hi=lane&16;
    *(LAS float*)(lbase+L_CSP+(gw*64+lane)*4)=h?(hi?c1[1]:c1[0]):(hi?c0[1]:c0[0]);
    STAMP(2);
    waitl0(); BAR();
  }
  #pragma unroll
  for(int s=0;s<4;++s) ax[s]=lbase+rh*8192+r*128+((((2*(s^(2*rh))+h)^((r>>1)&7)))<<4);
  aw=lbase+WB+((lane>>4)&1)*(12288+32)+(8*h+((lane&15)>>2))*192+(lane&3)*8+u0*32;
  aw2=aw+6144-rh*6144; aw+=rh*6144;
  #pragma unroll
  for(int t=0;t<3;++t){ cw[t]=v4f{}; if(t<NB) asm volatile("":"+v"(cw[t])); }
  asm volatile("":"+v"(ax[0]),"+v"(ax[1]),"+v"(ax[2]),"+v"(ax[3]),"+v"(aw),"+v"(aw2));
  BAR();
  __builtin_amdgcn_s_setprio(2);
  v4u X[2][2],T[2][3];
  auto rd=[&](int b,int s) AI {
    const u32 ad=(s<2?aw:aw2)+(s&1)*3072;
    X[b][0]=ldsr128(ax[s]);
    #pragma unroll
    for(int t=0;t<NB;++t){ T[b][t]=ldstr(ad+t*32,ad+t*32+768); if(t==0) X[b][1]=ldsr128(ax[s]+4096); }
  };
  auto mm=[&](int b,bool cs) AI {
    #pragma unroll
    for(int t=0;t<NB;++t){ acc[t][0]=mfma(T[b][t],X[b][0],acc[t][0]); if(cs) cw[t]=mfma16(ones,T[b][t],cw[t]); acc[t][1]=mfma(T[b][t],X[b][1],acc[t][1]); }
  };
  auto mix=[&](int nv,bool cs) AI {
    SG(0x8,1); SG(0x100,2); if(cs) SG(0x8,2); else SG(0x8,1); SG(0x100,2); SG(0x8,1); SG(0x100,2);
    if(NB==3){ if(cs) SG(0x8,2); else SG(0x8,1); SG(0x100,2); }
    SG(0x8,1); if(nv) SG(0x2,3);
    SG(0x8,5);
  };
  u32 d0=XS, d1=XS, d2=(u32)(-2*(int)XS);
  STAMP(3); CSTAMP(6);
  rd(0,0);
  #pragma unroll 1
  for(int c=0;c<8;++c){
    const u32 dw=((c&3)==3)?(u32)(-3*(int)WSZ):WSZ;
    SB; rd(1,1); mm(0,true); ax[0]+=d0; ax[1]+=d0; aw+=dw; mix(1,true);
    SB; asm volatile("":"+v"(ax[0]),"+v"(ax[1]),"+v"(aw));
    rd(0,2); mm(1,true); mix(0,true);
    SB; rd(1,3); mm(0,false); ax[2]+=d0; ax[3]+=d0; aw2+=dw; mix(1,false);
    SB; asm volatile("":"+v"(ax[2]),"+v"(ax[3]),"+v"(aw2));
    { const u32 t=d0; d0=d1; d1=d2; d2=t; }
    if(c==7){
      const int ln=lane_now();
      #pragma unroll
      for(int t=0;t<NB;++t){ const float lo=cw[t][0], hi=cw[t][1]; cw[t][0]=(ln&16)?hi:lo; }
      *(LAS v4f*)(lbase+L_CW+(gw*32+(ln&31))*16)=v4f{cw[0][0],cw[1][0],cw[2][0],0.f};
    }
    waitl0(); BAR();
    rd(0,0); mm(1,false); mix(0,false);
  }
  const int ln=lane_now();
  const v4u pc=*(LAS v4u*)(lbase+L_CW+((gw^1)*32+(ln&31))*16);
  const u32 hm=(ln&32)?0u:0xffffffffu;
  v4u M[2];
  #pragma unroll
  for(int m=0;m<2;++m){ const u32 mu=peek(lbase+L_TAB+((rh*2+m)*32+16*((ln>>2)&1)+8*((ln>>4)&1)+4*((ln>>3)&1)+(ln&3))*16+8);
    const u32 nm=(((mu>>16)|mu)^0x80008000u)&hm; M[m]=v4u{nm,nm,0u,0u}; }
  #pragma unroll
  for(int t=0;t<NB;++t){
    const float c=cw[t][0]+u2f(pc[t]);
    const u32 c0=f2u(c)&0xffff0000u; const float r1=c-u2f(c0);
    const u32 c1=f2u(r1)&0xffff0000u; const float r2=r1-u2f(c1);
    const v4u F={(c0>>16)|c1,f2u(r2)>>16,0u,0u};
    acc[t][0]=mfma(F,M[0],acc[t][0]); acc[t][1]=mfma(F,M[1],acc[t][1]);
  }
  STAMP(4); CSTAMP(7);
  #pragma unroll
  for(int t=0;t<NB;++t)
    #pragma unroll
    for(int m=0;m<2;++m){
      v4u v;
      #pragma unroll
      for(int k=0;k<4;++k) v[k]=pk2(relu(acc[t][m][2*k])*acc[t][m][2*k+8],relu(acc[t][m][2*k+1])*acc[t][m][2*k+9]);
      st128(v,rws,hqo+(u0+t)*2048+m*1024+ln*16);
    }
  return tag;
}

__global__ NOPK __launch_bounds__(768,3) void fused_k(Args a){
  __shared__ __attribute__((aligned(256))) char smem[L_TOTAL];
  const u32 lbase=(u32)(size_t)(LAS char*)smem;
  const int tid=__builtin_amdgcn_workitem_id_x();
  const int wgid=__builtin_amdgcn_workgroup_id_x(), w=rfl(tid>>6);
  int lane=tid&63;
  const int xcd=wgid&7, idx=wgid>>3;
  if(CODEPF && w==CODEPF-1){
    u64 pc; u32 back; asm volatile("s_getpc_b64 %0\n.Lpcx%=:\n\ts_mov_b32 %1, .Lpcx%=-_Z7fused_k4Args":"=&s"(pc),"=s"(back));
    dma_dw<false>(mksrd((const void*)(pc-back),CODE_LEN),(u32)(((idx&3)*64+lane)*128),0,lbase+L_DUM);
  }
  const int tt=(xcd&1)*8+(idx&7), cg=(xcd>>1)*4+(idx>>3);
  const int nb=((cg&3)==3||cg==14)?2:3, blk0=11*(cg>>2)+3*(cg&3)-(cg==15?1:0);
  const int rt2=(xcd&3)*8+(idx&7), ct=(xcd>>2)*4+(idx>>3);
  const int ry=ROTP?2*(idx>>3):0, rz=ROTP?(11*(idx>>3))>>2:0;
  const u32 one=1;
  STAMP_DECL; STAMP(0); CSTAMP(12);
  const u64 tstart_=__builtin_amdgcn_s_memrealtime(); u64 tq_=tstart_;
  #define MARK() do{ asm volatile("s_memrealtime %0\n\ts_waitcnt lgkmcnt(0)":"=s"(tq_)::"memory"); }while(0)

  if(w>=8){
    __builtin_amdgcn_s_setprio(3);
    const int lw=w-8, j=lane>>3, l7=lane&7;
    const v4u sx=mksrd(a.x,2048*1024);
    const v4u sb=mksrd(lw<2?a.bre:a.bim,512*1024);
    const u32 sz0=(u32)((l7^(j>>1))<<4), sz1=sz0^64u;
    const u32 xr=(tt*128+32*lw+16*(j>>2)+(j&3))*1024;
    const u32 vx0=xr+sz0, vx1=xr+3*1024+sz1, vx2=xr+6*1024+sz0, vx3=xr+9*1024+sz1;
    const u32 br=(cg*32+16*(lw&1)+j)*1024;
    const u32 vb0=br+sz0, vb1=br+7*1024+sz1;
    const u32 l4=lbase+lw*4096, l2=lbase+lw*2048;
    u32 kx=((tt*(ROT1&15)+cg*(ROT1>>4))&7)*128, kw=(kx+640)&0x380;
    #define SX(c) dma4<false,sx_(c)>(sx,vx0,vx1,vx2,vx3,kx,l4)
    #define SBB(c) do{ dma2<sb_(c)>(sb,vb0,vb1,kx,l2); kx=(kx+128)&0x380; }while(0)
    SX(0); SBB(0); SX(1); SBB(1);
    const v4u sws=mksrd(a.ws,WS_END);
    const v4u sw=mksrd(lw<2?a.wg:a.wu,512*2752);
    const v4u swd=mksrd(a.wd,1376*1024);
    const v4u sc=mksrd(lw<2?a.cre:a.cim,512*1024);
    const u32 vf=(lane&15)*128, lf=lbase+L_FLG+lw*256;
    const u32 fx=WS_FX+(tt*64+lw*16)*128, fh=WS_FH+(rt2*32+(lw&1)*16)*128, fhh=WS_FHH+rt2*4096;
    u32 vw[6];
    #pragma unroll
    for(int i=0;i<3;++i){ const u32 q=i*1024+lane*16, row=q/192, col=q%192; const bool ok=col<(u32)nb*64;
      vw[i]=ok?(row+32*(lw&1))*2752+col+blk0*64-i*1024:OOB; vw[i+3]=vw[i]+16*2752; asm volatile("":"+v"(vw[i]),"+v"(vw[i+3])); }
    const u32 lwv=lbase+(lw>>1)*(12288+32)+(lw&1)*6144;
    #define GWA(c) dma3<wslot(c)>(sw,vw[0],vw[1],vw[2],kw*1376,lwv)
    #define GWB(c) do{ dma3<wslot(c)+3072>(sw,vw[3],vw[4],vw[5],kw*1376,lwv); kw=(kw+128)&0x380; }while(0)
    #define GX(c) do{ dma4<false,((c)%3)*XS>(sx,vx0,vx1,vx2,vx3,kx,l4); kx=(kx+128)&0x380; }while(0)
    SX(2); waitv<10>(); BAR();
    SBB(2); SX(3); waitv<10>(); BAR();
    SBB(3); SX(4); waitv<10>(); BAR();
    SBB(4); SX(5); waitv<10>(); BAR();
    SBB(5); SX(6); SBB(6); waitv<12>(); BAR();
    SX(7); SBB(7); waitv<12>(); BAR();
    STAMP(2);
    const u32 vs=(lane>>1)*128+(lane&1)*16, lt=lbase+L_TAB+lw*512;
    const u32 tag=rfl(peek(lbase+L_TAG));
    if(lane<32) dma_x4(sws,vs,fx,lt);
    GWA(0); waitv<10>(); BAR();
    GWB(0); waitv<7>(); BAR();
    waitv<6>();
    #pragma unroll 1
    for(int it=0;it<SPIN_CAP;++it){
      const v4u t=*(volatile LAS v4u*)(lt+(lane&31)*16);
      if(all_eq((t[1]^tag)|(t[3]^tag),0u)) break;
      __builtin_amdgcn_s_sleep(2);
      if(lane<32) dma_x4(sws,vs,fx,lt);
      waitv<0>();
    }
    BAR();
    GWA(1); GWB(1); waitv<6>(); BAR();
    STAMP(3);
    GWA(2); GWB(2); GWA(3); GWB(3);
    u32 va=WS_HS+rt2*131072+lw*4096+lane*16;
    const u32 cr=(ct*64+(lw&1)*32+j)*1024;
    u32 vp0=cr+sz0, vp1=cr+7*1024+sz1, vp2=cr+14*1024+sz0, vp3=cr+21*1024+sz1;
    const int gp0=tt-1-4*lw-(lane>>5);
    const u32 vg0=gp0>=0?(u32)(WS_GRAN+gp0*8192+(cg*32+(lane&31))*16):OOB, vg1=gp0>=2?vg0-2*8192-1024:OOB;
    #define SNAP() asm volatile("s_add_u32 m0, %[b], %[i]\n\ts_nop 0\n\tbuffer_load_dwordx4 %[v0], %[r], 0 offen sc1 lds\n\tbuffer_load_dwordx4 %[v1], %[r], 0 offen offset:1024 sc1 lds" \
        :: [v0]"v"(vg0),[v1]"v"(vg1),[r]"s"(sws),[b]"s"(l2),[i]"n"(L_GR) : "memory","scc")
    waitv<12>(); BAR();
    GX(3); GWA(4); GWB(4); SNAP(); waitv<18>(); BAR();
    GX(4); GWA(5); GWB(5); waitv<10>(); SNAP();
    if(WARMB) dma_dw<false>(sc,lane<32?(u32)(j*1024+l7*128):OOB,(ct*64+(idx&7)*8+(lw&1)*4)*1024,lbase+L_DUM);
    BAR();
    GX(5); GWA(6); GWB(6); waitv<WARMB?11:10>(); SNAP(); BAR();
    GX(6); GWA(7); GWB(7); waitv<10>(); BAR();
    u32 ka=ry, kb=ry;
    auto PA=[&](auto C) AI {
      constexpr int c=C.value;
      if constexpr(c==8){ va=WS_HH+rt2*86*2048+lw*4096+lane*16; ka=rz; }
      u32 so;
      if constexpr(c<8){ so=ka<<14; ka=(ka+1)&7; } else { so=(ka==10&&lw==3)?OOB:ka<<14; ka= ka==10?0:ka+1; }
      dma4<true,pa_(c)>(sws,va,va,va,va,so,l4);
    };
    auto PB=[&](auto C) AI {
      constexpr int c=C.value;
      if constexpr(c==8){ vp0=(lw*32+j)*1024+ct*128+((l7^(((j>>1)&1)<<2))<<4); vp1=vp0+7*1024; vp2=vp0+14*1024; vp3=vp0+21*1024; kb=rz; }
      u32 so;
      if constexpr(c<8){ so=kb<<7; kb=(kb+1)&7; } else { so=kb<<17; kb= kb==10?0:kb+1; }
      dma4<false,pb_(c)>(c<8?sc:swd,vp0,vp1,vp2,vp3,so,l4);
    };
    #define IC(n) std::integral_constant<int,(n)>{}
    GX(7); PB(IC(0));
    waitv<14>(); BAR();
    PB(IC(1)); PB(IC(2));
    waitv<12>(); BAR();
    STAMP(4);
    dma_dw<true>(sws,vf,fh,lf);
    PB(IC(3));
    waitv<4>();
    const bool early=all_eq(peek(lf+lane*4),tag);
    if(early){ PA(IC(0)); PA(IC(1)); }
    BAR();
    STAMP(5);
    if(!early){ poll32(sws,vf,fh,tag); PA(IC(0)); PA(IC(1)); }
    STAMP(6);
    if(!LATEA){ PA(IC(2)); PA(IC(3)); PB(IC(4)); }
    waitv<LATEA?4:16>(); BAR();
    if(LATEA){ PA(IC(2)); PA(IC(3)); PB(IC(4)); }
    if(WARMB) dma_dw<false>(swd,lane<43?(u32)(lane*1024):OOB,((idx&7)*172+lw*43)*1024+ct*128,lbase+L_DUM);
    if(lane<16) dma_x4(sws,vs,WS_FX+(rt2*32+lw*8)*128,lbase+L_TAB+lw*256);
    { const u32 vt=(rt2*64+lw*16+j)*1024+ct*128+l7*16; dma2<L_XT>(sx,vt,vt+7*1024,0,l2); }
    waitv<WARMB?16:15>(); BAR();
    waitv<WARMB?12:11>(); BAR();
    bool zok=false;
    sfor<2,18>([&](auto K) AI {
      constexpr int k=K.value;
      if constexpr(k==5) dma_dw<true>(sws,(lane&31)*128,fhh,lf);
      if constexpr(k==9){ if(wgid==0 && lw==0) dma_dw<true>(sws,vf*64,WS_FH,lf); }
      if constexpr(k==6){
        waitv<8>();
        zok=all_eq(peek(lf+lane*4),tag);
        if(zok) PA(IC(8));
        PB(IC(9));
        if(zok) waitv<12>(); else waitv<8>();
      } else if constexpr(k==7){
        STAMP(7);
        if(!zok){ poll32(sws,(lane&31)*128,fhh,tag); PA(IC(8)); }
        MARK();
        PA(IC(9)); PB(IC(10));
        if(zok) waitv<12>(); else waitv<8>();
      } else {
        if constexpr(k<=16) PA(IC(k+2));
        if constexpr(k<=15) PB(IC(k+3));
        waitv<(k==2?(WARMB?16:15):k<=15?12:k==16?8:0)>();
      }
      if constexpr(k==12){
        if(wgid==0 && lw==0){
          if(!all_eq(peek(lf+lane*4),tag)) poll32(sws,vf*64,WS_FH,tag);
          const u32 e=peek(lbase+L_TAG+4)+1u;
          asm volatile("s_mov_b64 exec, 1\n\tbuffer_store_dword %0, off, %1, 0 sc1\n\ts_mov_b64 exec, -1" :: "v"(e), "s"(sws) : "memory");
        }
      }
      BAR();
    });
    STAMP(11);
    STAMP_FLUSH();
    return;
  }

  int h=lane>>5, r=lane&31;
  const rsrc_t rws=mkrsrc(a.ws,WS_END);
  u32 pend=0;
  u32 tag;
  auto try_flag=[&](bool force) AI {
    if(pend && (force || vm_idle())){
      waitv<0>();
      if(lane==0) __builtin_amdgcn_raw_buffer_store_b32(tag,rws,pend,0,16);
      pend=0;
    }
  };
  if(w>=4){
    const int gw=w-4, rt=2*tt+(w&1);
    __builtin_amdgcn_s_setprio(3);
    const u32 hqo=WS_HH+(rt*86+2*blk0)*2048;
    if(nb==3) tag=g_main<3>(a,rws,lbase,lane,gw,tt,cg,hqo TRPASS TSPASS); else tag=g_main<2>(a,rws,lbase,lane,gw,tt,cg,hqo TRPASS TSPASS);
    pend=WS_FHH+(rt*32+cg*2+(gw>>1))*128;
    STAMP(5);
  } else {
    const int ch=cg*32+r;
    __builtin_amdgcn_s_setprio(SPRIO);
    const u16 hlr=a.lre[ch], hli=a.lim[ch], hdt=a.ldt[ch];
    *(LAS v4u*)(lbase+L_GR+w*2048+lane*16)=v4u{0u,0u,0u,0u}; *(LAS v4u*)(lbase+L_GR+w*2048+1024+lane*16)=v4u{0u,0u,0u,0u};
    const float lr=b2f(hlr), li=b2f(hli), dt=rbf(__builtin_expf(b2f(hdt)));
    cf lam,coef;
    { const float ex=__builtin_expf(dt*lr); float sn,cs_; sincos_(dt*li,sn,cs_); lam=cf{ex*cs_,ex*sn};
      const float dd=1.f/(lr*lr+li*li); coef=cmul(cf{lam.r-1.f,lam.i},cf{lr*dd,-li*dd}); }
    cf l16=cmul(lam,lam); l16=cmul(l16,l16); l16=cmul(l16,l16); l16=cmul(l16,l16);
    cf l32=cmul(l16,l16);
    cf l256,wq,pw;
    { const cf l64=cmul(l32,l32), l128=cmul(l64,l64), o={1.f,0.f};
      l256=cmul(l128,l128); wq=csel(h,l128,o); pw=cmul(csel(w&1,l32,o),csel(w&2,l64,o)); }
    asm volatile("":"+v"(lam.r),"+v"(lam.i),"+v"(coef.r),"+v"(coef.i),"+v"(l16.r),"+v"(l16.i),"+v"(l32.r),"+v"(l32.i));
    asm volatile("":"+v"(l256.r),"+v"(l256.i),"+v"(wq.r),"+v"(wq.i),"+v"(pw.r),"+v"(pw.i));
    v16f are={},aim={};
    {
      u32 ax[4],ab[4];
      #pragma unroll
      for(int s=0;s<4;++s){ const u32 t=lbase+r*128+((((2*s+h)^((r>>1)&7)))<<4); ab[s]=t+sb_(0); ax[s]=t+sx_(0)+w*4096; }
      v4u X[2][2],T[2][2][2];
      auto rd=[&](int b) AI {
        #pragma unroll
        for(int s=0;s<2;++s){ X[b][s]=ldsr128(ax[2*b+s]); T[b][s][0]=ldsr128(ab[2*b+s]); T[b][s][1]=ldsr128(ab[2*b+s]+4096); } };
      auto mm=[&](int b) AI {
        #pragma unroll
        for(int s=0;s<2;++s){ are=mfma(X[b][s],T[b][s][0],are); aim=mfma(X[b][s],T[b][s][1],aim); }
        asm volatile("":"+v"(are),"+v"(aim)); };
      STAMP(1);
      waitl0(); BAR();
      rd(0);
      sfor<0,8>([&](auto C) AI {
        constexpr int c=C.value;
        SB; rd(1);
        SB; mm(0);
        if constexpr(c<7){
          #pragma unroll
          for(int s=0;s<4;++s){ ax[s]+=sx_(c+1)-sx_(c); ab[s]+=sb_(c+1)-sb_(c); asm volatile("":"+v"(ax[s]),"+v"(ab[s])); }
          SB;
          waitl0(); BAR();
          rd(0);
        }
        SB; mm(1);
      });
    }
    STAMP(2);
    waitl0(); BAR();
    __builtin_amdgcn_s_setprio(3);
    tag=rfl(peek(lbase+L_TAG));
    cf cs={0.f,0.f};
    #pragma unroll
    for(int k=0;k<4;++k){ const u32 b=lbase+L_CSP+(k*64+r)*4; cs.r+=*(LAS float*)b; cs.i+=*(LAS float*)(b+128); }
    int left=8;
    #define SBAR() do{ if(left>0){ BAR(); --left; } else __builtin_amdgcn_s_sleep(4); }while(0)
    cf uu[16]; cf ea={0.f,0.f};
    #pragma unroll
    for(int k=0;k<16;++k){
      const v4f st=*(LAS v4f*)(lbase+L_TAB+(32*w+16*h+k)*16);
      const cf v={__builtin_fmaf(-st[2],cs.r,are[k])*st[0],__builtin_fmaf(-st[2],cs.i,aim[k])*st[0]};
      uu[k]=v; ea=cfma(lam,ea,v);
    }
    cf elo=ea,ehi=ea; sw32(elo.r,ehi.r); sw32(elo.i,ehi.i);
    { const cf ew=cfma(l16,elo,ehi); *(LAS v2f*)(lbase+L_AGG+(w*32+r)*8)=v2f{ew.r,ew.i}; }
    STAMP(3);
    waitl0(); BAR();
    cf Q;
    { const u32 b=lbase+L_AGG+r*8;
      const cf a0=ldsc(b), a1=ldsc(b+256), a2=ldsc(b+512), a3=ldsc(b+768);
      const cf Q2=cfma(l32,a0,a1), Q3=cfma(l32,Q2,a2);
      if(w==3){ const cf E=cfma(l32,Q3,a3); if(lane<32) st128(v4u{f2u(E.r),tag,f2u(E.i),tag},rws,ch*16+WS_GRAN+tt*8192); }
      Q= w==0?cf{0.f,0.f}: w==1?a0: w==2?Q2:Q3; }
    STAMP(4);
    #pragma unroll
    for(int k=0;k<16;++k){ uu[k]=cmul(coef,uu[k]); asm volatile("":"+v"(uu[k].r),"+v"(uu[k].i)); if(k==CSPLIT) SBAR(); }
    cf H={0.f,0.f};
    if(tt>0){
      SBAR();
      v4u gr[8];
      #pragma unroll 1
      for(int it=0;it<SPIN_CAP;++it){
        if(it<3){
          SBAR();
          #pragma unroll
          for(int i=0;i<8;++i) gr[i]=*(volatile LAS v4u*)(lbase+L_GR+lane*16+i*1024);
        } else {
          #pragma unroll
          for(int i=0;i<8;++i){ const int pd=tt-1-2*i-h; gr[i]=__builtin_amdgcn_raw_buffer_load_b128(rws,pd>=0?(u32)(pd*8192+ch*16):OOB,WS_GRAN,16); }
          SBAR();
        }
        u32 bad=0;
        #pragma unroll
        for(int i=0;i<8;++i) bad|=((gr[i][1]^tag)|(gr[i][3]^tag))&~(u32)((tt-1-2*i-h)>>31);
        if(all_eq(bad,0u)) break;
      }
      #pragma unroll
      for(int i=7;i>=0;--i) H=cfma(l256,H,cf{u2f(gr[i][0]),u2f(gr[i][2])});
      H=cmul(H,wq);
      cf h2=H; sw32(H.r,h2.r); sw32(H.i,h2.i); H.r+=h2.r; H.i+=h2.i;
    }
    STAMP(5);
    cf hc=cfma(pw,H,Q);
    hc=cmul(coef,csel(h,cfma(l16,hc,elo),hc));
    const int rt=2*tt+(w>>1);
    const u32 hso=WS_HS+rt*131072+(cg>>1)*16384+(((cg&1)*2+(r>>4))*2+(w&1))*1024+h*512;
    #pragma unroll
    for(int m=0;m<2;++m){
      if(left>0 && (m==0 || FSPLIT)){ asm volatile("":"+v"(hc.r),"+v"(hc.i)); BAR(); --left; }
      cf hv[8];
      #pragma unroll
      for(int jj=0;jj<8;++jj){ hc=cfma(lam,hc,uu[8*m+jj]); hv[jj]=hc; }
      const u32 o=hso+m*256+(((lane&15)^(8*h+4*m))<<4);
      st128(v4u{pk2(hv[0].r,hv[1].r),pk2(hv[2].r,hv[3].r),pk2(hv[4].r,hv[5].r),pk2(hv[6].r,hv[7].r)},rws,o);
      st128(v4u{pk2(-hv[0].i,-hv[1].i),pk2(-hv[2].i,-hv[3].i),pk2(-hv[4].i,-hv[5].i),pk2(-hv[6].i,-hv[7].i)},rws,o+8192);
    }
    pend=WS_FH+(rt*32+(cg&1)*16+(cg>>1)*2+(w&1))*128;
    STAMP(6);
    #pragma unroll 1
    while(left>0){
      BAR(); --left;
      #pragma unroll 1
      for(int it=0;it<FWIN && pend;++it){ try_flag(false); if(pend) __builtin_amdgcn_s_sleep(1); else STAMP(7); }
    }
    if(pend){ try_flag(true); STAMP(7); }
  }
  lane=lane_now(); h=lane>>5; r=lane&31;
  const int g=lane>>4, m2=w&1, tc=(w>>1)&1, kh=w>>2;
  const int dcol=ct*64+tc*32+r;
  const float dsk=b2f(a.dsk[dcol]);
  v16f ay={},az={};
  const int q=(lane&15)>>2, p=lane&3, b4=g&1, gp=b4+2*(p&1);
  u32 a0=lbase+pa_(0)+(kh*8+m2)*1024+(gp*16+((8*h+q)^(4*gp)))*16+8*(p>>1);
  u32 b0=lbase+pb_(0)+kh*8192+(tc*32+r)*128+((h^((r>>1)&7))<<4);
  u32 za=lbase+pa_(8)+(kh*8+m2)*1024+lane*16;
  u32 zb=lbase+pb_(8)+(64*kh+4*h+q)*128+((tc*64+b4*32+8*p)^(((q>>1)&1)<<6));
  u32 d0=pa_(1)-pa_(0), d1=pa_(2)-pa_(1), d2=pa_(3)-pa_(2), d3=pa_(0)-pa_(3), e0=XS; int eb=0;
  v4u A[2][4],B[2][4];
  auto rot=[&]() AI { const u32 t=d0; d0=d1; d1=d2; d2=d3; d3=t; eb= eb==4?0:eb+1; e0= eb==4?(u32)(-4*(int)XS):XS; };
  auto rdy=[&](int b) AI {
    #pragma unroll
    for(int s=0;s<4;++s){ A[b][s]=ldstr(a0+s*2048,(a0^64)+s*2048); B[b][s]=ldsr128(b0^(s*32)); }
    a0+=d0; b0+=e0; rot();
  };
  auto rdz=[&](int b) AI {
    #pragma unroll
    for(int s=0;s<4;++s){ A[b][s]=ldsr128(za+s*2048); B[b][s]=ldstr(zb+s*2048,zb+s*2048+1024); }
    za+=d0; zb+=e0; rot();
  };
  auto mm4=[&](int b,v16f& c) AI {
    #pragma unroll
    for(int s=0;s<4;++s) c=mfma(A[b][s],B[b][s],c);
    #pragma unroll
    for(int s=0;s<4;++s){ SG(0x8,1); SG(0x100,3); }
    SB; asm volatile("":"+v"(c));
  };
  #define NXT() BAR()
  const int tk=32*m2+16*h+8*kh;
  STAMP(8);
  __builtin_amdgcn_s_setprio(0);
  NXT(); rdy(0);
  STAMP(9);
  #pragma unroll 1
  for(int c=0;c<3;++c){
    NXT(); rdy(1); mm4(0,ay); try_flag(false);
    NXT(); rdy(0); mm4(1,ay); try_flag(c==2);
  }
  NXT(); rdy(1); mm4(0,ay);
  NXT(); rdz(0); mm4(1,ay);
  STAMP(10);
  float sv[8], xf[8], r2[8];
  #pragma unroll 1
  for(int c=0;c<5;++c){
    NXT(); rdz(1); mm4(0,az);
    if(c==1){
      #pragma unroll
      for(int k=0;k<8;++k){
        const v4f st=*(LAS v4f*)(lbase+L_TAB+(tk+k)*16);
        xf[k]=b2f(*(LAS u16*)(lbase+L_XT+(tk+k)*128+(tc*32+r)*2));
        sv[k]=(xf[k]-st[2])*st[0]*dsk; r2[k]=st[0]*st[0];
      }
    }
    NXT(); rdz(0); mm4(1,az);
  }
  {
    const u32 xo=lbase+WB+w*4096+lane*16, xi=lbase+WB+(w^4)*4096+lane*16;
    float zv[8];
    if(kh){ *(LAS v4f*)xo=v4f{ay[0],ay[1],ay[2],ay[3]}; *(LAS v4f*)(xo+1024)=v4f{ay[4],ay[5],ay[6],ay[7]};
      #pragma unroll
      for(int k=0;k<8;++k) sv[k]=__builtin_fmaf(2.f,ay[8+k],sv[k]); }
    else  { *(LAS v4f*)xo=v4f{ay[8],ay[9],ay[10],ay[11]}; *(LAS v4f*)(xo+1024)=v4f{ay[12],ay[13],ay[14],ay[15]};
      #pragma unroll
      for(int k=0;k<8;++k) sv[k]=__builtin_fmaf(2.f,ay[k],sv[k]); }
    SB;
    #pragma unroll
    for(int s=0;s<4;++s) az=mfma(A[0][s],B[0][s],az);
    STAMP(11);
    if(kh){ *(LAS v4f*)(xo+2048)=v4f{az[0],az[1],az[2],az[3]}; *(LAS v4f*)(xo+3072)=v4f{az[4],az[5],az[6],az[7]};
      #pragma unroll
      for(int k=0;k<8;++k) zv[k]=az[8+k]; }
    else  { *(LAS v4f*)(xo+2048)=v4f{az[8],az[9],az[10],az[11]}; *(LAS v4f*)(xo+3072)=v4f{az[12],az[13],az[14],az[15]};
      #pragma unroll
      for(int k=0;k<8;++k) zv[k]=az[k]; }
    waitl0(); bar();
    const v4f i2=*(LAS v4f*)(xi+2048), i3=*(LAS v4f*)(xi+3072), i0=*(LAS v4f*)xi, i1=*(LAS v4f*)(xi+1024);
    const rsrc_t ro=mkrsrc(a.out,2048*512*4);
    const u32 oo=((rt2*64+tk)*512+dcol)*4;
    #pragma unroll
    for(int k=0;k<8;++k){
      const float z=(zv[k]+(k<4?i2[k&3]:i3[k&3]))*r2[k];
      const float gt=z*__builtin_amdgcn_rcpf(1.f+__builtin_amdgcn_exp2f(z*-1.44269504f));
      __builtin_amdgcn_raw_buffer_store_b32(f2u(__builtin_fmaf(__builtin_fmaf(2.f,k<4?i0[k&3]:i1[k&3],sv[k]),gt,xf[k])),ro,oo+k*2048,0,OUTAUX);
    }
  }

  { const u64 tgt_=tstart_+3000+4*(tq_-tstart_); while(__builtin_amdgcn_s_memrealtime()<tgt_) __builtin_amdgcn_s_sleep(2); }
}

extern "C" void kernel_launch(void* const* d_in, const int* in_sizes, int n_in,
                              void* d_out, int out_size, void* d_ws, size_t ws_size,
                              hipStream_t stream) {
  Args a;
  a.x=(const u16*)d_in[0]; a.lre=(const u16*)d_in[1]; a.lim=(const u16*)d_in[2];
  a.bre=(const u16*)d_in[3]; a.bim=(const u16*)d_in[4]; a.cre=(const u16*)d_in[5]; a.cim=(const u16*)d_in[6];
  a.ldt=(const u16*)d_in[7]; a.dsk=(const u16*)d_in[8];
  a.wg=(const u16*)d_in[10]; a.wu=(const u16*)d_in[11]; a.wd=(const u16*)d_in[12];
  a.out=(float*)d_out; a.ws=(char*)d_ws;
  fused_k<<<256,768,0,stream>>>(a);
}
```

```cpp
#include <hip/hip_runtime.h>
#include <hip/hip_bf16.h>
#include <type_traits>

typedef unsigned u32; typedef unsigned long long u64; typedef unsigned short u16;
typedef u32 v4u __attribute__((ext_vector_type(4)));
typedef u32 v2u __attribute__((ext_vector_type(2)));
typedef short v4s __attribute__((ext_vector_type(4)));
typedef __bf16 v2bf __attribute__((ext_vector_type(2)));
typedef __bf16 v8bf __attribute__((ext_vector_type(8)));
typedef float v2f __attribute__((ext_vector_type(2)));
typedef float v4f __attribute__((ext_vector_type(4)));
typedef float v16f __attribute__((ext_vector_type(16)));
#define LAS __attribute__((address_space(3)))
#define DEVI __device__ __forceinline__
#define AI __attribute__((always_inline))
#define SB __builtin_amdgcn_sched_barrier(0)
#if defined(__HIP_DEVICE_COMPILE__)
#define NOPK __attribute__((target("no-packed-fp32-ops")))
#else
#define NOPK
#endif

#define ROT1 0x01
#define ROTP 1
#define SPRIO 2
#define OUTAUX 16
#define HSLEEP 2
#define FSPLIT 0
#define FWIN 10
#define LATEA 0
#define CSPLIT 5
#define LBSLEEP 6
#define WARMB 1

constexpr u32 WS_EPOCH = 0;
constexpr u32 WS_FX    = 32768;
constexpr u32 WS_FH    = WS_FX + 131072;
constexpr u32 WS_FHH   = WS_FH + 131072;
constexpr u32 WS_GRAN  = WS_FHH + 131072;
constexpr u32 WS_HH    = WS_GRAN + 131072;
constexpr u32 WS_HS    = WS_HH + 32*86*2048;
constexpr u32 WS_END   = WS_HS + 32*131072;
constexpr u32 WS_STAMP = 16u<<20;
constexpr u32 XS = 16384;
constexpr u32 WB = 49152, WSZ = 24832;
constexpr u32 L_XT = WB+4*WSZ;
constexpr u32 L_TAB  = L_XT+8192;
constexpr u32 L_CSP  = L_TAB + 2048;
constexpr u32 L_FLG  = L_CSP;
constexpr u32 L_CW   = L_CSP + 1024;
constexpr u32 L_AGG  = L_CW + 2048;
constexpr u32 L_DUM  = L_AGG + 1024;
constexpr u32 L_TAG  = L_DUM + 256;
constexpr u32 L_GR   = L_XT;
constexpr u32 L_TOTAL = 163840;
static_assert(L_TAG+16<=L_TOTAL,"lds");
constexpr u32 OOB = 0x40000000u;
constexpr int SPIN_CAP = 1<<18;

#define CODEPF 1
constexpr u32 CODE_LEN=$n;
struct Args { const u16 *x,*lre,*lim,*bre,*bim,*cre,*cim,*ldt,*dsk,*wg,*wu,*wd; float* out; char* ws; };

DEVI u32 f2u(float f){ return __builtin_bit_cast(u32,f); }
DEVI float u2f(u32 u){ return __builtin_bit_cast(float,u); }
DEVI u32 rfl(u32 v){ return __builtin_amdgcn_readfirstlane(v); }
DEVI bool all_eq(u32 v,u32 t){ return __builtin_amdgcn_ballot_w64(v!=t)==0ull; }
DEVI v4u mksrd(const void* p, u32 bytes){ u64 a=(u64)p; v4u s; s.x=rfl((u32)a); s.y=rfl((u32)(a>>32)); s.z=bytes; s.w=0x00020000u; return s; }
#define DM0 "s_add_u32 m0, %[b], %[i]\n\ts_nop 0\n\t"
#define DLD(n,off,pol) "buffer_load_dwordx4 %[v" #n "], %[r], %[s] offen" off pol " lds\n\t"
template<bool SC1,u32 IMM> DEVI void dma4(v4u srd,u32 v0,u32 v1,u32 v2,u32 v3,u32 soff,u32 base){
  if(SC1) asm volatile(DM0 DLD(0,""," sc1") DLD(1," offset:1024"," sc1") DLD(2," offset:2048"," sc1") DLD(3," offset:3072"," sc1")
      :: [v0]"v"(v0),[v1]"v"(v1),[v2]"v"(v2),[v3]"v"(v3),[r]"s"(srd),[s]"s"(soff),[b]"s"(base),[i]"n"(IMM) : "memory","scc");
  else asm volatile(DM0 DLD(0,"","") DLD(1," offset:1024","") DLD(2," offset:2048","") DLD(3," offset:3072","")
      :: [v0]"v"(v0),[v1]"v"(v1),[v2]"v"(v2),[v3]"v"(v3),[r]"s"(srd),[s]"s"(soff),[b]"s"(base),[i]"n"(IMM) : "memory","scc");
}
template<u32 IMM> DEVI void dma3(v4u srd,u32 v0,u32 v1,u32 v2,u32 soff,u32 base){
  asm volatile(DM0 DLD(0,"","") DLD(1," offset:1024","") DLD(2," offset:2048","")
      :: [v0]"v"(v0),[v1]"v"(v1),[v2]"v"(v2),[r]"s"(srd),[s]"s"(soff),[b]"s"(base),[i]"n"(IMM) : "memory","scc");
}
template<u32 IMM> DEVI void dma2(v4u srd,u32 v0,u32 v1,u32 soff,u32 base){
  asm volatile(DM0 DLD(0,"","") DLD(1," offset:1024","")
      :: [v0]"v"(v0),[v1]"v"(v1),[r]"s"(srd),[s]"s"(soff),[b]"s"(base),[i]"n"(IMM) : "memory","scc");
}
template<bool SC1> DEVI void dma_dw(v4u srd,u32 voff,u32 soff,u32 lds){
  if(SC1) asm volatile("s_mov_b32 m0, %3\n\ts_nop 0\n\tbuffer_load_dword %0, %1, %2 offen sc1 lds" :: "v"(voff), "s"(srd), "s"(soff), "s"(lds) : "memory");
  else    asm volatile("s_mov_b32 m0, %3\n\ts_nop 0\n\tbuffer_load_dword %0, %1, %2 offen lds" :: "v"(voff), "s"(srd), "s"(soff), "s"(lds) : "memory");
}
DEVI void dma_x4(v4u srd,u32 voff,u32 soff,u32 lds){
  asm volatile("s_mov_b32 m0, %3\n\ts_nop 0\n\tbuffer_load_dwordx4 %0, %1, %2 offen sc1 lds" :: "v"(voff), "s"(srd), "s"(soff), "s"(lds) : "memory");
}
template<int N> DEVI void waitv(){ asm volatile("s_waitcnt vmcnt(%0)"::"n"(N):"memory"); }
DEVI void waitl0(){ asm volatile("s_waitcnt lgkmcnt(0)":::"memory"); }
DEVI void bar(){ asm volatile("":::"memory"); __builtin_amdgcn_s_barrier(); asm volatile("":::"memory"); SB; }
template<u32 OFF> DEVI void sig(u32 vaddr,u32 one){ asm volatile("s_mov_b64 exec, 1\n\tds_add_u32 %0, %1 offset:%2\n\ts_mov_b64 exec, -1" :: "v"(vaddr), "v"(one), "n"(OFF) : "memory"); }
template<u32 OFF> DEVI void sigm(u32 vaddr){ asm volatile("s_mov_b64 exec, 1\n\tds_inc_u32 %0, %0 offset:%1\n\ts_mov_b64 exec, -1" :: "v"(vaddr), "n"(OFF) : "memory"); }
DEVI u32 peek(u32 vaddr){ return *(volatile LAS u32*)vaddr; }
DEVI bool vm_idle(){ u32 v; asm volatile("s_getreg_b32 %0, hwreg(HW_REG_IB_STS, 0, 4)":"=s"(v)); return v==0u; }
DEVI int lane_now(){ int l; asm volatile("v_mbcnt_lo_u32_b32 %0, -1, 0\n\tv_mbcnt_hi_u32_b32 %0, -1, %0":"=v"(l)); return l; }
DEVI float b2f(u16 v){ return u2f(((u32)v)<<16); }
DEVI u32 pk2(float lo,float hi){ v2f p={lo,hi}; return __builtin_bit_cast(u32,__builtin_convertvector(p,v2bf)); }
DEVI float rbf(float f){ return u2f(pk2(f,f)<<16); }
DEVI float relu(float x){ int i=__builtin_bit_cast(int,x); return __builtin_bit_cast(float,i>0?i:0); }
DEVI float dot2(u32 a,u32 b,float c){ return __builtin_amdgcn_fdot2_f32_bf16(__builtin_bit_cast(v2bf,a),__builtin_bit_cast(v2bf,b),c,false); }
DEVI v16f mfma(v4u a,v4u b,v16f c){ return __builtin_amdgcn_mfma_f32_32x32x16_bf16(__builtin_bit_cast(v8bf,a),__builtin_bit_cast(v8bf,b),c,0,0,0); }
DEVI v4f mfma16(v4u a,v4u b,v4f c){ return __builtin_amdgcn_mfma_f32_16x16x32_bf16(__builtin_bit_cast(v8bf,a),__builtin_bit_cast(v8bf,b),c,0,0,0); }
DEVI v4u ldsr128(u32 a){ return *(LAS v4u*)a; }
DEVI v4u ldstr(u32 a0,u32 a1){
  v4s x=__builtin_amdgcn_ds_read_tr16_b64_v4i16((LAS v4s*)a0);
  v4s y=__builtin_amdgcn_ds_read_tr16_b64_v4i16((LAS v4s*)a1);
  return __builtin_bit_cast(v4u,__builtin_shufflevector(x,y,0,1,2,3,4,5,6,7));
}
DEVI void sw16(float& a,float& b){ v2u r=__builtin_amdgcn_permlane16_swap(f2u(a),f2u(b),false,false); a=u2f(r[0]); b=u2f(r[1]); }
DEVI void sw32(float& a,float& b){ v2u r=__builtin_amdgcn_permlane32_swap(f2u(a),f2u(b),false,false); a=u2f(r[0]); b=u2f(r[1]); }
template<int CTRL> DEVI float dppadd(float v){ return v+u2f(__builtin_amdgcn_update_dpp(0u,f2u(v),CTRL,0xf,0xf,true)); }
DEVI void wsum4(float& a,float& b,float& c,float& d){
  sw32(a,b); sw32(c,d); float v=a+b, t=c+d;
  sw16(v,t); v+=t;
  v=dppadd<0xB1>(v); v=dppadd<0x4E>(v); v=dppadd<0x141>(v); v=dppadd<0x140>(v);
  a=u2f(__builtin_amdgcn_readlane(f2u(v),0)); c=u2f(__builtin_amdgcn_readlane(f2u(v),16));
  b=u2f(__builtin_amdgcn_readlane(f2u(v),32)); d=u2f(__builtin_amdgcn_readlane(f2u(v),48));
}
struct cf{ float r,i; };
DEVI cf cmul(cf a,cf b){ return cf{__builtin_fmaf(-a.i,b.i,a.r*b.r), __builtin_fmaf(a.i,b.r,a.r*b.i)}; }
DEVI cf cfma(cf a,cf b,cf c){ return cf{__builtin_fmaf(-a.i,b.i,__builtin_fmaf(a.r,b.r,c.r)), __builtin_fmaf(a.i,b.r,__builtin_fmaf(a.r,b.i,c.i))}; }
DEVI cf csel(bool c,cf a,cf b){ return cf{c?a.r:b.r, c?a.i:b.i}; }
DEVI cf ldsc(u32 a){ v2f v=*(LAS v2f*)a; return cf{v[0],v[1]}; }
DEVI void sincos_(float x,float& s,float& c){
  const float n=__builtin_rintf(x*0.636619772f);
  float r=__builtin_fmaf(n,-1.57079601e+00f,x); r=__builtin_fmaf(n,-3.13916473e-07f,r); r=__builtin_fmaf(n,-5.39030253e-15f,r);
  const float z=r*r;
  float ps=__builtin_fmaf(__builtin_fmaf(__builtin_fmaf(2.86567956e-6f,z,-1.98559923e-4f),z,8.33338592e-3f),z,-1.66666672e-1f);
  ps=__builtin_fmaf(ps*z,r,r);
  float pc=__builtin_fmaf(__builtin_fmaf(__builtin_fmaf(2.44677067e-5f,z,-1.38877297e-3f),z,4.16666567e-2f),z,-0.5f);
  pc=__builtin_fmaf(pc,z,1.f);
  const int q=(int)n;
  const float a=(q&1)?pc:ps, b=(q&1)?ps:pc;
  s=(q&2)?-a:a; c=((q+1)&2)?-b:b;
}
typedef __amdgpu_buffer_rsrc_t rsrc_t;
DEVI rsrc_t mkrsrc(const void* p,u32 bytes){ return __builtin_amdgcn_make_buffer_rsrc((void*)p,0,bytes,0x00020000); }
DEVI void st128(v4u v,rsrc_t r,u32 off){ __builtin_amdgcn_raw_buffer_store_b128(v,r,off,0,16); }
DEVI u32 mktag(u32 e){ return (e*0x9E3779B1u+0x7F4A7C15u)|1u; }

#define STAMP(k) do{}while(0)
#define CSTAMP(k) do{}while(0)
#define TRACE(k) do{}while(0)
#define STAMP_DECL do{}while(0)
#define BAR() bar()
#define STAMP_FLUSH() do{}while(0)
#define TRARG
#define TRPASS
#define TSARG
#define TSPASS

DEVI void poll32(v4u srd,u32 voff,u32 soff,u32 tag){
  #pragma unroll 1
  for(int it=0;it<SPIN_CAP;++it){
    u32 v;
    asm volatile("buffer_load_dword %0, %1, %2, %3 offen sc1\n\ts_waitcnt vmcnt(0)" : "=&v"(v) : "v"(voff), "s"(srd), "s"(soff) : "memory");
    if(all_eq(v,tag)) break;
    __builtin_amdgcn_s_sleep(12);
  }
}
template<int I,int N,class F> DEVI void sfor(F&& f){ if constexpr(I<N){ f(std::integral_constant<int,I>{}); sfor<I+1,N>(f); } }
AI constexpr u32 wslot(int c){ return WB+(c&3)*WSZ; }
AI constexpr u32 pa_(int c){ return (c&3)==3?WB+5*XS:(((c&3)+2)%3)*XS; }
AI constexpr u32 pb_(int c){ return WB+(c%5)*XS; }
AI constexpr u32 sx_(int c){ return c>=5?(c-5)*XS:wslot(c+2); }
AI constexpr u32 sb_(int c){ return c>=5?wslot(3)+(c-5)*8192:wslot(c+2)+16384; }

#define SG(m,n) __builtin_amdgcn_sched_group_barrier(m,n,0)
template<int NB>
DEVI u32 g_main(const Args& a,rsrc_t rws,u32 lbase,int lane,int gw,int tt,int cg,u32 hqo TRARG TSARG){
  const int h=lane>>5, r=lane&31, rh=gw&1, u0=(gw>>1)*NB;
  const int row=tt*128+cg*8+gw*2;
  const v4u x0=((const v4u*)a.x)[row*64+lane], x1=((const v4u*)a.x)[row*64+64+lane];
  const u32 e=__builtin_amdgcn_raw_buffer_load_b32(rws,0,WS_EPOCH,16);
  SB;
  v16f acc[3][2]; v4f cw[3];
  u32 ax[4],aw,aw2;
  const u32 on2=((lane&3)==((lane>>4)&1))?0x3f803f80u:0u;
  v4u ones={on2,on2,on2,on2};
  u32 tag;
  {
    u32 ab=lbase+sb_(0)+r*128+((((2*gw+h)^((r>>1)&7)))<<4);
    v4f c0={},c1={};
    sfor<0,8>([&](auto C) AI {
      constexpr int c=C.value;
      waitl0(); BAR();
      const v4u t0=ldsr128(ab), t1=ldsr128(ab+4096);
      if constexpr(c<7) ab+=sb_(c+1)-sb_(c);
      if constexpr(c==0){
        const float z0=u2f(rfl(x0[0])<<16), z1=u2f(rfl(x1[0])<<16);
        float s0=0.f,s1=0.f,v0=0.f,v1=0.f;
        #pragma unroll
        for(int k=0;k<8;++k){
          const float d0=u2f((k&1)?x0[k>>1]&0xffff0000u:x0[k>>1]<<16)-z0, d1=u2f((k&1)?x1[k>>1]&0xffff0000u:x1[k>>1]<<16)-z1;
          s0+=d0; s1+=d1; v0=__builtin_fmaf(d0,d0,v0); v1=__builtin_fmaf(d1,d1,v1); }
        wsum4(s0,s1,v0,v1);
        const float sm=(lane?s1:s0)*(1.f/512.f);
        const u32 vm=pk2(relu(__builtin_fmaf(lane?v1:v0,1.f/512.f,-sm*sm)),sm+(lane?z1:z0));
        const float rs=__builtin_amdgcn_rcpf(rbf(__builtin_amdgcn_sqrtf(rbf(u2f(vm<<16)+1e-5f))));
        tag=mktag(rfl(e));
        if(lane<2) st128(v4u{f2u(rs),tag,vm&0xffff0000u,tag},rws,WS_FX+row*64+lane*16);
        if(gw==0) *(LAS v2u*)(lbase+L_TAG)=v2u{tag,e};
        STAMP(1);
      }
      if constexpr(c>=1 && c<=2*NB){ acc[(c-1)>>1][(c-1)&1]=v16f{}; asm volatile("":"+v"(acc[(c-1)>>1][(c-1)&1])); }
      c0=mfma16(ones,t0,c0); c1=mfma16(ones,t1,c1);
      asm volatile("":"+v"(c0),"+v"(c1),"+v"(ab));
    });
    asm volatile("s_nop 7\n\ts_nop 1":"+v"(c0),"+v"(c1));
    const bool hi=lane&16;
    *(LAS float*)(lbase+L_CSP+(gw*64+lane)*4)=h?(hi?c1[1]:c1[0]):(hi?c0[1]:c0[0]);
    STAMP(2);
    waitl0(); BAR();
  }
  #pragma unroll
  for(int s=0;s<4;++s) ax[s]=lbase+rh*8192+r*128+((((2*(s^(2*rh))+h)^((r>>1)&7)))<<4);
  aw=lbase+WB+((lane>>4)&1)*(12288+32)+(8*h+((lane&15)>>2))*192+(lane&3)*8+u0*32;
  aw2=aw+6144-rh*6144; aw+=rh*6144;
  #pragma unroll
  for(int t=0;t<3;++t){ cw[t]=v4f{}; if(t<NB) asm volatile("":"+v"(cw[t])); }
  asm volatile("":"+v"(ax[0]),"+v"(ax[1]),"+v"(ax[2]),"+v"(ax[3]),"+v"(aw),"+v"(aw2));
  BAR();
  __builtin_amdgcn_s_setprio(2);
  v4u X[2][2],T[2][3];
  auto rd=[&](int b,int s) AI {
    const u32 ad=(s<2?aw:aw2)+(s&1)*3072;
    X[b][0]=ldsr128(ax[s]);
    #pragma unroll
    for(int t=0;t<NB;++t){ T[b][t]=ldstr(ad+t*32,ad+t*32+768); if(t==0) X[b][1]=ldsr128(ax[s]+4096); }
  };
  auto mm=[&](int b,bool cs) AI {
    #pragma unroll
    for(int t=0;t<NB;++t){ acc[t][0]=mfma(T[b][t],X[b][0],acc[t][0]); if(cs) cw[t]=mfma16(ones,T[b][t],cw[t]); acc[t][1]=mfma(T[b][t],X[b][1],acc[t][1]); }
  };
  auto mix=[&](int nv,bool cs) AI {
    SG(0x8,1); SG(0x100,2); if(cs) SG(0x8,2); else SG(0x8,1); SG(0x100,2); SG(0x8,1); SG(0x100,2);
    if(NB==3){ if(cs) SG(0x8,2); else SG(0x8,1); SG(0x100,2); }
    SG(0x8,1); if(nv) SG(0x2,3);
    SG(0x8,5);
  };
  u32 d0=XS, d1=XS, d2=(u32)(-2*(int)XS);
  STAMP(3); CSTAMP(6);
  rd(0,0);
  #pragma unroll 1
  for(int c=0;c<8;++c){
    const u32 dw=((c&3)==3)?(u32)(-3*(int)WSZ):WSZ;
    SB; rd(1,1); mm(0,true); ax[0]+=d0; ax[1]+=d0; aw+=dw; mix(1,true);
    SB; asm volatile("":"+v"(ax[0]),"+v"(ax[1]),"+v"(aw));
    rd(0,2); mm(1,true); mix(0,true);
    SB; rd(1,3); mm(0,false); ax[2]+=d0; ax[3]+=d0; aw2+=dw; mix(1,false);
    SB; asm volatile("":"+v"(ax[2]),"+v"(ax[3]),"+v"(aw2));
    { const u32 t=d0; d0=d1; d1=d2; d2=t; }
    if(c==7){
      const int ln=lane_now();
      #pragma unroll
      for(int t=0;t<NB;++t){ const float lo=cw[t][0], hi=cw[t][1]; cw[t][0]=(ln&16)?hi:lo; }
      *(LAS v4f*)(lbase+L_CW+(gw*32+(ln&31))*16)=v4f{cw[0][0],cw[1][0],cw[2][0],0.f};
    }
    waitl0(); BAR();
    rd(0,0); mm(1,false); mix(0,false);
  }
  const int ln=lane_now();
  const v4u pc=*(LAS v4u*)(lbase+L_CW+((gw^1)*32+(ln&31))*16);
  const u32 hm=(ln&32)?0u:0xffffffffu;
  v4u M[2];
  #pragma unroll
  for(int m=0;m<2;++m){ const u32 mu=peek(lbase+L_TAB+((rh*2+m)*32+16*((ln>>2)&1)+8*((ln>>4)&1)+4*((ln>>3)&1)+(ln&3))*16+8);
    const u32 nm=(((mu>>16)|mu)^0x80008000u)&hm; M[m]=v4u{nm,nm,0u,0u}; }
  #pragma unroll
  for(int t=0;t<NB;++t){
    const float c=cw[t][0]+u2f(pc[t]);
    const u32 c0=f2u(c)&0xffff0000u; const float r1=c-u2f(c0);
    const u32 c1=f2u(r1)&0xffff0000u; const float r2=r1-u2f(c1);
    const v4u F={(c0>>16)|c1,f2u(r2)>>16,0u,0u};
    acc[t][0]=mfma(F,M[0],acc[t][0]); acc[t][1]=mfma(F,M[1],acc[t][1]);
  }
  STAMP(4); CSTAMP(7);
  #pragma unroll
  for(int t=0;t<NB;++t)
    #pragma unroll
    for(int m=0;m<2;++m){
      v4u v;
      #pragma unroll
      for(int k=0;k<4;++k) v[k]=pk2(relu(acc[t][m][2*k])*acc[t][m][2*k+8],relu(acc[t][m][2*k+1])*acc[t][m][2*k+9]);
      st128(v,rws,hqo+(u0+t)*2048+m*1024+ln*16);
    }
  return tag;
}

__global__ NOPK __launch_bounds__(768,3) void fused_k(Args a){
  __shared__ __attribute__((aligned(256))) char smem[L_TOTAL];
  const u32 lbase=(u32)(size_t)(LAS char*)smem;
  const int tid=__builtin_amdgcn_workitem_id_x();
  const int wgid=__builtin_amdgcn_workgroup_id_x(), w=rfl(tid>>6);
  int lane=tid&63;
  const int xcd=wgid&7, idx=wgid>>3;
  if(CODEPF && w==CODEPF-1){
    u64 pc; u32 back; asm volatile("s_getpc_b64 %0\n.Lpcx%=:\n\ts_mov_b32 %1, .Lpcx%=-_Z7fused_k4Args":"=&s"(pc),"=s"(back));
    dma_dw<false>(mksrd((const void*)(pc-back),CODE_LEN),(u32)(((idx&3)*64+lane)*128),0,lbase+L_DUM);
  }
  const int tt=(xcd&1)*8+(idx&7), cg=(xcd>>1)*4+(idx>>3);
  const int nb=((cg&3)==3||cg==14)?2:3, blk0=11*(cg>>2)+3*(cg&3)-(cg==15?1:0);
  const int rt2=(xcd&3)*8+(idx&7), ct=(xcd>>2)*4+(idx>>3);
  const int ry=ROTP?2*(idx>>3):0, rz=ROTP?(11*(idx>>3))>>2:0;
  const u32 one=1;
  STAMP_DECL; STAMP(0); CSTAMP(12);

  if(w>=8){
    __builtin_amdgcn_s_setprio(3);
    const int lw=w-8, j=lane>>3, l7=lane&7;
    const v4u sx=mksrd(a.x,2048*1024);
    const v4u sb=mksrd(lw<2?a.bre:a.bim,512*1024);
    const u32 sz0=(u32)((l7^(j>>1))<<4), sz1=sz0^64u;
    const u32 xr=(tt*128+32*lw+16*(j>>2)+(j&3))*1024;
    const u32 vx0=xr+sz0, vx1=xr+3*1024+sz1, vx2=xr+6*1024+sz0, vx3=xr+9*1024+sz1;
    const u32 br=(cg*32+16*(lw&1)+j)*1024;
    const u32 vb0=br+sz0, vb1=br+7*1024+sz1;
    const u32 l4=lbase+lw*4096, l2=lbase+lw*2048;
    u32 kx=((tt*(ROT1&15)+cg*(ROT1>>4))&7)*128, kw=(kx+640)&0x380;
    #define SX(c) dma4<false,sx_(c)>(sx,vx0,vx1,vx2,vx3,kx,l4)
    #define SBB(c) do{ dma2<sb_(c)>(sb,vb0,vb1,kx,l2); kx=(kx+128)&0x380; }while(0)
    SX(0); SBB(0); SX(1); SBB(1);
    const v4u sws=mksrd(a.ws,WS_END);
    const v4u sw=mksrd(lw<2?a.wg:a.wu,512*2752);
    const v4u swd=mksrd(a.wd,1376*1024);
    const v4u sc=mksrd(lw<2?a.cre:a.cim,512*1024);
    const u32 vf=(lane&15)*128, lf=lbase+L_FLG+lw*256;
    const u32 fx=WS_FX+(tt*64+lw*16)*128, fh=WS_FH+(rt2*32+(lw&1)*16)*128, fhh=WS_FHH+rt2*4096;
    u32 vw[6];
    #pragma unroll
    for(int i=0;i<3;++i){ const u32 q=i*1024+lane*16, row=q/192, col=q%192; const bool ok=col<(u32)nb*64;
      vw[i]=ok?(row+32*(lw&1))*2752+col+blk0*64-i*1024:OOB; vw[i+3]=vw[i]+16*2752; asm volatile("":"+v"(vw[i]),"+v"(vw[i+3])); }
    const u32 lwv=lbase+(lw>>1)*(12288+32)+(lw&1)*6144;
    #define GWA(c) dma3<wslot(c)>(sw,vw[0],vw[1],vw[2],kw*1376,lwv)
    #define GWB(c) do{ dma3<wslot(c)+3072>(sw,vw[3],vw[4],vw[5],kw*1376,lwv); kw=(kw+128)&0x380; }while(0)
    #define GX(c) do{ dma4<false,((c)%3)*XS>(sx,vx0,vx1,vx2,vx3,kx,l4); kx=(kx+128)&0x380; }while(0)
    SX(2); waitv<10>(); BAR();
    SBB(2); SX(3); waitv<10>(); BAR();
    SBB(3); SX(4); waitv<10>(); BAR();
    SBB(4); SX(5); waitv<10>(); BAR();
    SBB(5); SX(6); SBB(6); waitv<12>(); BAR();
    SX(7); SBB(7); waitv<12>(); BAR();
    STAMP(2);
    const u32 vs=(lane>>1)*128+(lane&1)*16, lt=lbase+L_TAB+lw*512;
    const u32 tag=rfl(peek(lbase+L_TAG));
    if(lane<32) dma_x4(sws,vs,fx,lt);
    GWA(0); waitv<10>(); BAR();
    GWB(0); waitv<7>(); BAR();
    waitv<6>();
    #pragma unroll 1
    for(int it=0;it<SPIN_CAP;++it){
      const v4u t=*(volatile LAS v4u*)(lt+(lane&31)*16);
      if(all_eq((t[1]^tag)|(t[3]^tag),0u)) break;
      __builtin_amdgcn_s_sleep(2);
      if(lane<32) dma_x4(sws,vs,fx,lt);
      waitv<0>();
    }
    BAR();
    GWA(1); GWB(1); waitv<6>(); BAR();
    STAMP(3);
    GWA(2); GWB(2); GWA(3); GWB(3);
    u32 va=WS_HS+rt2*131072+lw*4096+lane*16;
    const u32 cr=(ct*64+(lw&1)*32+j)*1024;
    u32 vp0=cr+sz0, vp1=cr+7*1024+sz1, vp2=cr+14*1024+sz0, vp3=cr+21*1024+sz1;
    const int gp0=tt-1-4*lw-(lane>>5);
    const u32 vg0=gp0>=0?(u32)(WS_GRAN+gp0*8192+(cg*32+(lane&31))*16):OOB, vg1=gp0>=2?vg0-2*8192-1024:OOB;
    #define SNAP() asm volatile("s_add_u32 m0, %[b], %[i]\n\ts_nop 0\n\tbuffer_load_dwordx4 %[v0], %[r], 0 offen sc1 lds\n\tbuffer_load_dwordx4 %[v1], %[r], 0 offen offset:1024 sc1 lds" \
        :: [v0]"v"(vg0),[v1]"v"(vg1),[r]"s"(sws),[b]"s"(l2),[i]"n"(L_GR) : "memory","scc")
    waitv<12>(); BAR();
    GX(3); GWA(4); GWB(4); SNAP(); waitv<18>(); BAR();
    GX(4); GWA(5); GWB(5); waitv<10>(); SNAP();
    if(WARMB) dma_dw<false>(sc,lane<32?(u32)(j*1024+l7*128):OOB,(ct*64+(idx&7)*8+(lw&1)*4)*1024,lbase+L_DUM);
    BAR();
    GX(5); GWA(6); GWB(6); waitv<WARMB?11:10>(); SNAP(); BAR();
    GX(6); GWA(7); GWB(7); waitv<10>(); BAR();
    u32 ka=ry, kb=ry;
    auto PA=[&](auto C) AI {
      constexpr int c=C.value;
      if constexpr(c==8){ va=WS_HH+rt2*86*2048+lw*4096+lane*16; ka=rz; }
      u32 so;
      if constexpr(c<8){ so=ka<<14; ka=(ka+1)&7; } else { so=(ka==10&&lw==3)?OOB:ka<<14; ka= ka==10?0:ka+1; }
      dma4<true,pa_(c)>(sws,va,va,va,va,so,l4);
    };
    auto PB=[&](auto C) AI {
      constexpr int c=C.value;
      if constexpr(c==8){ vp0=(lw*32+j)*1024+ct*128+((l7^(((j>>1)&1)<<2))<<4); vp1=vp0+7*1024; vp2=vp0+14*1024; vp3=vp0+21*1024; kb=rz; }
      u32 so;
      if constexpr(c<8){ so=kb<<7; kb=(kb+1)&7; } else { so=kb<<17; kb= kb==10?0:kb+1; }
      dma4<false,pb_(c)>(c<8?sc:swd,vp0,vp1,vp2,vp3,so,l4);
    };
    #define IC(n) std::integral_constant<int,(n)>{}
    GX(7); PB(IC(0));
    waitv<14>(); BAR();
    PB(IC(1)); PB(IC(2));
    waitv<12>(); BAR();
    STAMP(4);
    dma_dw<true>(sws,vf,fh,lf);
    PB(IC(3));
    waitv<4>();
    const bool early=all_eq(peek(lf+lane*4),tag);
    if(early){ PA(IC(0)); PA(IC(1)); }
    BAR();
    STAMP(5);
    if(!early){ poll32(sws,vf,fh,tag); PA(IC(0)); PA(IC(1)); }
    STAMP(6);
    if(!LATEA){ PA(IC(2)); PA(IC(3)); PB(IC(4)); }
    waitv<LATEA?4:16>(); BAR();
    if(LATEA){ PA(IC(2)); PA(IC(3)); PB(IC(4)); }
    if(WARMB) dma_dw<false>(swd,lane<43?(u32)(lane*1024):OOB,((idx&7)*172+lw*43)*1024+ct*128,lbase+L_DUM);
    if(lane<16) dma_x4(sws,vs,WS_FX+(rt2*32+lw*8)*128,lbase+L_TAB+lw*256);
    { const u32 vt=(rt2*64+lw*16+j)*1024+ct*128+l7*16; dma2<L_XT>(sx,vt,vt+7*1024,0,l2); }
    waitv<WARMB?16:15>(); BAR();
    waitv<WARMB?12:11>(); BAR();
    bool zok=false;
    sfor<2,18>([&](auto K) AI {
      constexpr int k=K.value;
      if constexpr(k==5) dma_dw<true>(sws,(lane&31)*128,fhh,lf);
      if constexpr(k==9){ if(wgid==0 && lw==0) dma_dw<true>(sws,vf*64,WS_FH,lf); }
      if constexpr(k==6){
        waitv<8>();
        zok=all_eq(peek(lf+lane*4),tag);
        if(zok) PA(IC(8));
        PB(IC(9));
        if(zok) waitv<12>(); else waitv<8>();
      } else if constexpr(k==7){
        STAMP(7);
        if(!zok){ poll32(sws,(lane&31)*128,fhh,tag); PA(IC(8)); }
        STAMP(8);
        PA(IC(9)); PB(IC(10));
        if(zok) waitv<12>(); else waitv<8>();
      } else {
        if constexpr(k<=16) PA(IC(k+2));
        if constexpr(k<=15) PB(IC(k+3));
        waitv<(k==2?(WARMB?16:15):k<=15?12:k==16?8:0)>();
      }
      if constexpr(k==12){
        if(wgid==0 && lw==0){
          if(!all_eq(peek(lf+lane*4),tag)) poll32(sws,vf*64,WS_FH,tag);
          const u32 e=peek(lbase+L_TAG+4)+1u;
          asm volatile("s_mov_b64 exec, 1\n\tbuffer_store_dword %0, off, %1, 0 sc1\n\ts_mov_b64 exec, -1" :: "v"(e), "s"(sws) : "memory");
        }
      }
      BAR();
    });
    STAMP(11);
    STAMP_FLUSH();
    return;
  }

  int h=lane>>5, r=lane&31;
  const rsrc_t rws=mkrsrc(a.ws,WS_END);
  u32 pend=0;
  u32 tag;
  auto try_flag=[&](bool force) AI {
    if(pend && (force || vm_idle())){
      waitv<0>();
      if(lane==0) __builtin_amdgcn_raw_buffer_store_b32(tag,rws,pend,0,16);
      pend=0;
    }
  };
  if(w>=4){
    const int gw=w-4, rt=2*tt+(w&1);
    __builtin_amdgcn_s_setprio(3);
    const u32 hqo=WS_HH+(rt*86+2*blk0)*2048;
    if(nb==3) tag=g_main<3>(a,rws,lbase,lane,gw,tt,cg,hqo TRPASS TSPASS); else tag=g_main<2>(a,rws,lbase,lane,gw,tt,cg,hqo TRPASS TSPASS);
    pend=WS_FHH+(rt*32+cg*2+(gw>>1))*128;
    STAMP(5);
  } else {
    const int ch=cg*32+r;
    __builtin_amdgcn_s_setprio(SPRIO);
    const u16 hlr=a.lre[ch], hli=a.lim[ch], hdt=a.ldt[ch];
    *(LAS v4u*)(lbase+L_GR+w*2048+lane*16)=v4u{0u,0u,0u,0u}; *(LAS v4u*)(lbase+L_GR+w*2048+1024+lane*16)=v4u{0u,0u,0u,0u};
    const float lr=b2f(hlr), li=b2f(hli), dt=rbf(__builtin_expf(b2f(hdt)));
    cf lam,coef;
    { const float ex=__builtin_expf(dt*lr); float sn,cs_; sincos_(dt*li,sn,cs_); lam=cf{ex*cs_,ex*sn};
      const float dd=1.f/(lr*lr+li*li); coef=cmul(cf{lam.r-1.f,lam.i},cf{lr*dd,-li*dd}); }
    cf l16=cmul(lam,lam); l16=cmul(l16,l16); l16=cmul(l16,l16); l16=cmul(l16,l16);
    cf l32=cmul(l16,l16);
    cf l256,wq,pw;
    { const cf l64=cmul(l32,l32), l128=cmul(l64,l64), o={1.f,0.f};
      l256=cmul(l128,l128); wq=csel(h,l128,o); pw=cmul(csel(w&1,l32,o),csel(w&2,l64,o)); }
    asm volatile("":"+v"(lam.r),"+v"(lam.i),"+v"(coef.r),"+v"(coef.i),"+v"(l16.r),"+v"(l16.i),"+v"(l32.r),"+v"(l32.i));
    asm volatile("":"+v"(l256.r),"+v"(l256.i),"+v"(wq.r),"+v"(wq.i),"+v"(pw.r),"+v"(pw.i));
    v16f are={},aim={};
    {
      u32 ax[4],ab[4];
      #pragma unroll
      for(int s=0;s<4;++s){ const u32 t=lbase+r*128+((((2*s+h)^((r>>1)&7)))<<4); ab[s]=t+sb_(0); ax[s]=t+sx_(0)+w*4096; }
      v4u X[2][2],T[2][2][2];
      auto rd=[&](int b) AI {
        #pragma unroll
        for(int s=0;s<2;++s){ X[b][s]=ldsr128(ax[2*b+s]); T[b][s][0]=ldsr128(ab[2*b+s]); T[b][s][1]=ldsr128(ab[2*b+s]+4096); } };
      auto mm=[&](int b) AI {
        #pragma unroll
        for(int s=0;s<2;++s){ are=mfma(X[b][s],T[b][s][0],are); aim=mfma(X[b][s],T[b][s][1],aim); }
        asm volatile("":"+v"(are),"+v"(aim)); };
      STAMP(1);
      waitl0(); BAR();
      rd(0);
      sfor<0,8>([&](auto C) AI {
        constexpr int c=C.value;
        SB; rd(1);
        SB; mm(0);
        if constexpr(c<7){
          #pragma unroll
          for(int s=0;s<4;++s){ ax[s]+=sx_(c+1)-sx_(c); ab[s]+=sb_(c+1)-sb_(c); asm volatile("":"+v"(ax[s]),"+v"(ab[s])); }
          SB;
          waitl0(); BAR();
          rd(0);
        }
        SB; mm(1);
      });
    }
    STAMP(2);
    waitl0(); BAR();
    __builtin_amdgcn_s_setprio(3);
    tag=rfl(peek(lbase+L_TAG));
    cf cs={0.f,0.f};
    #pragma unroll
    for(int k=0;k<4;++k){ const u32 b=lbase+L_CSP+(k*64+r)*4; cs.r+=*(LAS float*)b; cs.i+=*(LAS float*)(b+128); }
    int left=8;
    #define SBAR() do{ if(left>0){ BAR(); --left; } else __builtin_amdgcn_s_sleep(4); }while(0)
    cf uu[16]; cf ea={0.f,0.f};
    #pragma unroll
    for(int k=0;k<16;++k){
      const v4f st=*(LAS v4f*)(lbase+L_TAB+(32*w+16*h+k)*16);
      const cf v={__builtin_fmaf(-st[2],cs.r,are[k])*st[0],__builtin_fmaf(-st[2],cs.i,aim[k])*st[0]};
      uu[k]=v; ea=cfma(lam,ea,v);
    }
    cf elo=ea,ehi=ea; sw32(elo.r,ehi.r); sw32(elo.i,ehi.i);
    { const cf ew=cfma(l16,elo,ehi); *(LAS v2f*)(lbase+L_AGG+(w*32+r)*8)=v2f{ew.r,ew.i}; }
    STAMP(3);
    waitl0(); BAR();
    cf Q;
    { const u32 b=lbase+L_AGG+r*8;
      const cf a0=ldsc(b), a1=ldsc(b+256), a2=ldsc(b+512), a3=ldsc(b+768);
      const cf Q2=cfma(l32,a0,a1), Q3=cfma(l32,Q2,a2);
      if(w==3){ const cf E=cfma(l32,Q3,a3); if(lane<32) st128(v4u{f2u(E.r),tag,f2u(E.i),tag},rws,ch*16+WS_GRAN+tt*8192); }
      Q= w==0?cf{0.f,0.f}: w==1?a0: w==2?Q2:Q3; }
    STAMP(4);
    #pragma unroll
    for(int k=0;k<16;++k){ uu[k]=cmul(coef,uu[k]); asm volatile("":"+v"(uu[k].r),"+v"(uu[k].i)); if(k==CSPLIT) SBAR(); }
    cf H={0.f,0.f};
    if(tt>0){
      SBAR();
      v4u gr[8];
      #pragma unroll 1
      for(int it=0;it<SPIN_CAP;++it){
        if(it<3){
          SBAR();
          #pragma unroll
          for(int i=0;i<8;++i) gr[i]=*(volatile LAS v4u*)(lbase+L_GR+lane*16+i*1024);
        } else {
          #pragma unroll
          for(int i=0;i<8;++i){ const int pd=tt-1-2*i-h; gr[i]=__builtin_amdgcn_raw_buffer_load_b128(rws,pd>=0?(u32)(pd*8192+ch*16):OOB,WS_GRAN,16); }
          SBAR();
        }
        u32 bad=0;
        #pragma unroll
        for(int i=0;i<8;++i) bad|=((gr[i][1]^tag)|(gr[i][3]^tag))&~(u32)((tt-1-2*i-h)>>31);
        if(all_eq(bad,0u)) break;
      }
      #pragma unroll
      for(int i=7;i>=0;--i) H=cfma(l256,H,cf{u2f(gr[i][0]),u2f(gr[i][2])});
      H=cmul(H,wq);
      cf h2=H; sw32(H.r,h2.r); sw32(H.i,h2.i); H.r+=h2.r; H.i+=h2.i;
    }
    STAMP(5);
    cf hc=cfma(pw,H,Q);
    hc=cmul(coef,csel(h,cfma(l16,hc,elo),hc));
    const int rt=2*tt+(w>>1);
    const u32 hso=WS_HS+rt*131072+(cg>>1)*16384+(((cg&1)*2+(r>>4))*2+(w&1))*1024+h*512;
    #pragma unroll
    for(int m=0;m<2;++m){
      if(left>0 && (m==0 || FSPLIT)){ asm volatile("":"+v"(hc.r),"+v"(hc.i)); BAR(); --left; }
      cf hv[8];
      #pragma unroll
      for(int jj=0;jj<8;++jj){ hc=cfma(lam,hc,uu[8*m+jj]); hv[jj]=hc; }
      const u32 o=hso+m*256+(((lane&15)^(8*h+4*m))<<4);
      st128(v4u{pk2(hv[0].r,hv[1].r),pk2(hv[2].r,hv[3].r),pk2(hv[4].r,hv[5].r),pk2(hv[6].r,hv[7].r)},rws,o);
      st128(v4u{pk2(-hv[0].i,-hv[1].i),pk2(-hv[2].i,-hv[3].i),pk2(-hv[4].i,-hv[5].i),pk2(-hv[6].i,-hv[7].i)},rws,o+8192);
    }
    pend=WS_FH+(rt*32+(cg&1)*16+(cg>>1)*2+(w&1))*128;
    STAMP(6);
    #pragma unroll 1
    while(left>0){
      BAR(); --left;
      #pragma unroll 1
      for(int it=0;it<FWIN && pend;++it){ try_flag(false); if(pend) __builtin_amdgcn_s_sleep(1); else STAMP(7); }
    }
    if(pend){ try_flag(true); STAMP(7); }
  }
  lane=lane_now(); h=lane>>5; r=lane&31;
  const int g=lane>>4, m2=w&1, tc=(w>>1)&1, kh=w>>2;
  const int dcol=ct*64+tc*32+r;
  const float dsk=b2f(a.dsk[dcol]);
  v16f ay={},az={};
  const int q=(lane&15)>>2, p=lane&3, b4=g&1, gp=b4+2*(p&1);
  u32 a0=lbase+pa_(0)+(kh*8+m2)*1024+(gp*16+((8*h+q)^(4*gp)))*16+8*(p>>1);
  u32 b0=lbase+pb_(0)+kh*8192+(tc*32+r)*128+((h^((r>>1)&7))<<4);
  u32 za=lbase+pa_(8)+(kh*8+m2)*1024+lane*16;
  u32 zb=lbase+pb_(8)+(64*kh+4*h+q)*128+((tc*64+b4*32+8*p)^(((q>>1)&1)<<6));
  u32 d0=pa_(1)-pa_(0), d1=pa_(2)-pa_(1), d2=pa_(3)-pa_(2), d3=pa_(0)-pa_(3), e0=XS; int eb=0;
  v4u A[2][4],B[2][4];
  auto rot=[&]() AI { const u32 t=d0; d0=d1; d1=d2; d2=d3; d3=t; eb= eb==4?0:eb+1; e0= eb==4?(u32)(-4*(int)XS):XS; };
  auto rdy=[&](int b) AI {
    #pragma unroll
    for(int s=0;s<4;++s){ A[b][s]=ldstr(a0+s*2048,(a0^64)+s*2048); B[b][s]=ldsr128(b0^(s*32)); }
    a0+=d0; b0+=e0; rot();
  };
  auto rdz=[&](int b) AI {
    #pragma unroll
    for(int s=0;s<4;++s){ A[b][s]=ldsr128(za+s*2048); B[b][s]=ldstr(zb+s*2048,zb+s*2048+1024); }
    za+=d0; zb+=e0; rot();
  };
  auto mm4=[&](int b,v16f& c) AI {
    #pragma unroll
    for(int s=0;s<4;++s) c=mfma(A[b][s],B[b][s],c);
    #pragma unroll
    for(int s=0;s<4;++s){ SG(0x8,1); SG(0x100,3); }
    SB; asm volatile("":"+v"(c));
  };
  #define NXT() BAR()
  const int tk=32*m2+16*h+8*kh;
  STAMP(8);
  __builtin_amdgcn_s_setprio(0);
  NXT(); rdy(0);
  STAMP(9);
  #pragma unroll 1
  for(int c=0;c<3;++c){
    NXT(); rdy(1); mm4(0,ay); try_flag(false);
    NXT(); rdy(0); mm4(1,ay); try_flag(c==2);
  }
  NXT(); rdy(1); mm4(0,ay);
  NXT(); rdz(0); mm4(1,ay);
  STAMP(10);
  float sv[8], xf[8], r2[8];
  #pragma unroll 1
  for(int c=0;c<5;++c){
    NXT(); rdz(1); mm4(0,az);
    if(c==1){
      #pragma unroll
      for(int k=0;k<8;++k){
        const v4f st=*(LAS v4f*)(lbase+L_TAB+(tk+k)*16);
        xf[k]=b2f(*(LAS u16*)(lbase+L_XT+(tk+k)*128+(tc*32+r)*2));
        sv[k]=(xf[k]-st[2])*st[0]*dsk; r2[k]=st[0]*st[0];
      }
    }
    NXT(); rdz(0); mm4(1,az);
  }
  {
    const u32 xo=lbase+WB+w*4096+lane*16, xi=lbase+WB+(w^4)*4096+lane*16;
    float zv[8];
    if(kh){ *(LAS v4f*)xo=v4f{ay[0],ay[1],ay[2],ay[3]}; *(LAS v4f*)(xo+1024)=v4f{ay[4],ay[5],ay[6],ay[7]};
      #pragma unroll
      for(int k=0;k<8;++k) sv[k]=__builtin_fmaf(2.f,ay[8+k],sv[k]); }
    else  { *(LAS v4f*)xo=v4f{ay[8],ay[9],ay[10],ay[11]}; *(LAS v4f*)(xo+1024)=v4f{ay[12],ay[13],ay[14],ay[15]};
      #pragma unroll
      for(int k=0;k<8;++k) sv[k]=__builtin_fmaf(2.f,ay[k],sv[k]); }
    SB;
    #pragma unroll
    for(int s=0;s<4;++s) az=mfma(A[0][s],B[0][s],az);
    STAMP(11);
    if(kh){ *(LAS v4f*)(xo+2048)=v4f{az[0],az[1],az[2],az[3]}; *(LAS v4f*)(xo+3072)=v4f{az[4],az[5],az[6],az[7]};
      #pragma unroll
      for(int k=0;k<8;++k) zv[k]=az[8+k]; }
    else  { *(LAS v4f*)(xo+2048)=v4f{az[8],az[9],az[10],az[11]}; *(LAS v4f*)(xo+3072)=v4f{az[12],az[13],az[14],az[15]};
      #pragma unroll
      for(int k=0;k<8;++k) zv[k]=az[k]; }
    waitl0(); bar();
    const v4f i2=*(LAS v4f*)(xi+2048), i3=*(LAS v4f*)(xi+3072), i0=*(LAS v4f*)xi, i1=*(LAS v4f*)(xi+1024);
    const rsrc_t ro=mkrsrc(a.out,2048*512*4);
    const u32 oo=((rt2*64+tk)*512+dcol)*4;
    #pragma unroll
    for(int k=0;k<8;++k){
      const float z=(zv[k]+(k<4?i2[k&3]:i3[k&3]))*r2[k];
      const float gt=z*__builtin_amdgcn_rcpf(1.f+__builtin_amdgcn_exp2f(z*-1.44269504f));
      __builtin_amdgcn_raw_buffer_store_b32(f2u(__builtin_fmaf(__builtin_fmaf(2.f,k<4?i0[k&3]:i1[k&3],sv[k]),gt,xf[k])),ro,oo+k*2048,0,OUTAUX);
    }
  }
  CSTAMP(14); STAMP(13); STAMP_FLUSH();
}

extern "C" void kernel_launch(void* const* d_in, const int* in_sizes, int n_in,
                              void* d_out, int out_size, void* d_ws, size_t ws_size,
                              hipStream_t stream) {
  Args a;
  a.x=(const u16*)d_in[0]; a.lre=(const u16*)d_in[1]; a.lim=(const u16*)d_in[2];
  a.bre=(const u16*)d_in[3]; a.bim=(const u16*)d_in[4]; a.cre=(const u16*)d_in[5]; a.cim=(const u16*)d_in[6];
  a.ldt=(const u16*)d_in[7]; a.dsk=(const u16*)d_in[8];
  a.wg=(const u16*)d_in[10]; a.wu=(const u16*)d_in[11]; a.wd=(const u16*)d_in[12];
  a.out=(float*)d_out; a.ws=(char*)d_ws;
  fused_k<<<256,768,0,stream>>>(a);
}
```

```cpp
#include <hip/hip_runtime.h>
#include <hip/hip_bf16.h>
#include <type_traits>

typedef unsigned u32; typedef unsigned long long u64; typedef unsigned short u16;
typedef u32 v4u __attribute__((ext_vector_type(4)));
typedef u32 v2u __attribute__((ext_vector_type(2)));
typedef short v4s __attribute__((ext_vector_type(4)));
typedef __bf16 v2bf __attribute__((ext_vector_type(2)));
typedef __bf16 v8bf __attribute__((ext_vector_type(8)));
typedef float v2f __attribute__((ext_vector_type(2)));
typedef float v4f __attribute__((ext_vector_type(4)));
typedef float v16f __attribute__((ext_vector_type(16)));
#define LAS __attribute__((address_space(3)))
#define DEVI __device__ __forceinline__
#define AI __attribute__((always_inline))
#define SB __builtin_amdgcn_sched_barrier(0)
#if defined(__HIP_DEVICE_COMPILE__)
#define NOPK __attribute__((target("no-packed-fp32-ops")))
#else
#define NOPK
#endif

#define ROT1 0x01
#define ROTP 1
#define SPRIO 2
#define OUTAUX 16
#define HSLEEP 2
#define FSPLIT 0
#define FWIN 10
#define LATEA 0
#define LLCPF 0
#define LNFIRST 1
#define WARMP 1
#define RELAX 1
#define CSPLIT 5
#define LBSLEEP 6
#define WARMB 0

constexpr u32 WS_EPOCH = 0;
constexpr u32 WS_FX    = 32768;
constexpr u32 WS_FH    = WS_FX + 131072;
constexpr u32 WS_FHH   = WS_FH + 131072;
constexpr u32 WS_GRAN  = WS_FHH + 131072;
constexpr u32 WS_HH    = WS_GRAN + 131072;
constexpr u32 WS_HS    = WS_HH + 32*86*2048;
constexpr u32 WS_END   = WS_HS + 32*131072;
constexpr u32 WS_STAMP = 16u<<20;
constexpr u32 XS = 16384;
constexpr u32 WB = 49152, WSZ = 24832;
constexpr u32 L_XT = WB+4*WSZ;
constexpr u32 L_TAB  = L_XT+8192;
constexpr u32 L_CSP  = L_TAB + 2048;
constexpr u32 L_FLG  = L_CSP;
constexpr u32 L_CW   = L_CSP + 1024;
constexpr u32 L_AGG  = L_CW + 2048;
constexpr u32 L_DUM  = L_AGG + 1024;
constexpr u32 L_TAG  = L_DUM + 256;
constexpr u32 L_GR   = L_XT;
constexpr u32 L_TOTAL = 163840;
static_assert(L_TAG+16<=L_TOTAL,"lds");
constexpr u32 OOB = 0x40000000u;
constexpr int SPIN_CAP = 1<<18;

#define CODEPF 12
constexpr u32 CODE_LEN=$n;
struct Args { const u16 *x,*lre,*lim,*bre,*bim,*cre,*cim,*ldt,*dsk,*wg,*wu,*wd; float* out; char* ws; };

DEVI u32 f2u(float f){ return __builtin_bit_cast(u32,f); }
DEVI float u2f(u32 u){ return __builtin_bit_cast(float,u); }
DEVI u32 rfl(u32 v){ return __builtin_amdgcn_readfirstlane(v); }
DEVI bool all_eq(u32 v,u32 t){ return __builtin_amdgcn_ballot_w64(v!=t)==0ull; }
DEVI v4u mksrd(const void* p, u32 bytes){ u64 a=(u64)p; v4u s; s.x=rfl((u32)a); s.y=rfl((u32)(a>>32)); s.z=bytes; s.w=0x00020000u; return s; }
#define NTM 0
#define POL1 ""
#define POL2 ""
#define POL3 ""
#define POL4 ""
#define DM0 "s_add_u32 m0, %[b], %[i]\n\ts_nop 0\n\t"
#define DLD(n,off,pol) "buffer_load_dwordx4 %[v" #n "], %[r], %[s] offen" off pol " lds\n\t"
template<bool SC1,u32 IMM> DEVI void dma4(v4u srd,u32 v0,u32 v1,u32 v2,u32 v3,u32 soff,u32 base){
  if(SC1) asm volatile(DM0 DLD(0,""," sc1") DLD(1," offset:1024"," sc1") DLD(2," offset:2048"," sc1") DLD(3," offset:3072"," sc1")
      :: [v0]"v"(v0),[v1]"v"(v1),[v2]"v"(v2),[v3]"v"(v3),[r]"s"(srd),[s]"s"(soff),[b]"s"(base),[i]"n"(IMM) : "memory","scc");
  else asm volatile(DM0 DLD(0,"",POL4) DLD(1," offset:1024",POL4) DLD(2," offset:2048",POL4) DLD(3," offset:3072",POL4)
      :: [v0]"v"(v0),[v1]"v"(v1),[v2]"v"(v2),[v3]"v"(v3),[r]"s"(srd),[s]"s"(soff),[b]"s"(base),[i]"n"(IMM) : "memory","scc");
}
template<u32 IMM> DEVI void dma3(v4u srd,u32 v0,u32 v1,u32 v2,u32 soff,u32 base){
  asm volatile(DM0 DLD(0,"",POL3) DLD(1," offset:1024",POL3) DLD(2," offset:2048",POL3)
      :: [v0]"v"(v0),[v1]"v"(v1),[v2]"v"(v2),[r]"s"(srd),[s]"s"(soff),[b]"s"(base),[i]"n"(IMM) : "memory","scc");
}
template<u32 IMM> DEVI void dma2(v4u srd,u32 v0,u32 v1,u32 soff,u32 base){
  asm volatile(DM0 DLD(0,"",POL2) DLD(1," offset:1024",POL2)
      :: [v0]"v"(v0),[v1]"v"(v1),[r]"s"(srd),[s]"s"(soff),[b]"s"(base),[i]"n"(IMM) : "memory","scc");
}
template<bool SC1> DEVI void dma_dw(v4u srd,u32 voff,u32 soff,u32 lds){
  if(SC1) asm volatile("s_mov_b32 m0, %3\n\ts_nop 0\n\tbuffer_load_dword %0, %1, %2 offen sc1 lds" :: "v"(voff), "s"(srd), "s"(soff), "s"(lds) : "memory");
  else    asm volatile("s_mov_b32 m0, %3\n\ts_nop 0\n\tbuffer_load_dword %0, %1, %2 offen" POL1 " lds" :: "v"(voff), "s"(srd), "s"(soff), "s"(lds) : "memory");
}
DEVI void dma_x4(v4u srd,u32 voff,u32 soff,u32 lds){
  asm volatile("s_mov_b32 m0, %3\n\ts_nop 0\n\tbuffer_load_dwordx4 %0, %1, %2 offen sc1 lds" :: "v"(voff), "s"(srd), "s"(soff), "s"(lds) : "memory");
}
template<int N> DEVI void waitv(){ asm volatile("s_waitcnt vmcnt(%0)"::"n"(N):"memory"); }
DEVI void waitl0(){ asm volatile("s_waitcnt lgkmcnt(0)":::"memory"); }
DEVI void bar(){ asm volatile("":::"memory"); __builtin_amdgcn_s_barrier(); asm volatile("":::"memory"); SB; }
template<u32 OFF> DEVI void sig(u32 vaddr,u32 one){ asm volatile("s_mov_b64 exec, 1\n\tds_add_u32 %0, %1 offset:%2\n\ts_mov_b64 exec, -1" :: "v"(vaddr), "v"(one), "n"(OFF) : "memory"); }
template<u32 OFF> DEVI void sigm(u32 vaddr){ asm volatile("s_mov_b64 exec, 1\n\tds_inc_u32 %0, %0 offset:%1\n\ts_mov_b64 exec, -1" :: "v"(vaddr), "n"(OFF) : "memory"); }
DEVI u32 peek(u32 vaddr){ return *(volatile LAS u32*)vaddr; }
DEVI bool vm_idle(){ u32 v; asm volatile("s_getreg_b32 %0, hwreg(HW_REG_IB_STS, 0, 4)":"=s"(v)); return v==0u; }
DEVI void flag_st(v4u srd,u32 soff,u32 val){ asm volatile("s_mov_b64 exec, 1\n\tbuffer_store_dword %0, off, %1, %2 sc1\n\ts_mov_b64 exec, -1" :: "v"(val), "s"(srd), "s"(soff) : "memory"); }
DEVI int lane_now(){ int l; asm volatile("v_mbcnt_lo_u32_b32 %0, -1, 0\n\tv_mbcnt_hi_u32_b32 %0, -1, %0":"=v"(l)); return l; }
DEVI float b2f(u16 v){ return u2f(((u32)v)<<16); }
DEVI u32 pk2(float lo,float hi){ v2f p={lo,hi}; return __builtin_bit_cast(u32,__builtin_convertvector(p,v2bf)); }
DEVI float rbf(float f){ return u2f(pk2(f,f)<<16); }
DEVI float relu(float x){ int i=__builtin_bit_cast(int,x); return __builtin_bit_cast(float,i>0?i:0); }
DEVI float dot2(u32 a,u32 b,float c){ return __builtin_amdgcn_fdot2_f32_bf16(__builtin_bit_cast(v2bf,a),__builtin_bit_cast(v2bf,b),c,false); }
DEVI v16f mfma(v4u a,v4u b,v16f c){ return __builtin_amdgcn_mfma_f32_32x32x16_bf16(__builtin_bit_cast(v8bf,a),__builtin_bit_cast(v8bf,b),c,0,0,0); }
DEVI v4f mfma16(v4u a,v4u b,v4f c){ return __builtin_amdgcn_mfma_f32_16x16x32_bf16(__builtin_bit_cast(v8bf,a),__builtin_bit_cast(v8bf,b),c,0,0,0); }
DEVI v4u ldsr128(u32 a){ return *(LAS v4u*)a; }
DEVI v4u ldstr(u32 a0,u32 a1){
  v4s x=__builtin_amdgcn_ds_read_tr16_b64_v4i16((LAS v4s*)a0);
  v4s y=__builtin_amdgcn_ds_read_tr16_b64_v4i16((LAS v4s*)a1);
  return __builtin_bit_cast(v4u,__builtin_shufflevector(x,y,0,1,2,3,4,5,6,7));
}
DEVI void sw16(float& a,float& b){ v2u r=__builtin_amdgcn_permlane16_swap(f2u(a),f2u(b),false,false); a=u2f(r[0]); b=u2f(r[1]); }
DEVI void sw32(float& a,float& b){ v2u r=__builtin_amdgcn_permlane32_swap(f2u(a),f2u(b),false,false); a=u2f(r[0]); b=u2f(r[1]); }
template<int CTRL> DEVI float dppadd(float v){ return v+u2f(__builtin_amdgcn_update_dpp(0u,f2u(v),CTRL,0xf,0xf,true)); }
DEVI void wsum4(float& a,float& b,float& c,float& d){
  sw32(a,b); sw32(c,d); float v=a+b, t=c+d;
  sw16(v,t); v+=t;
  v=dppadd<0xB1>(v); v=dppadd<0x4E>(v); v=dppadd<0x141>(v); v=dppadd<0x140>(v);
  a=u2f(__builtin_amdgcn_readlane(f2u(v),0)); c=u2f(__builtin_amdgcn_readlane(f2u(v),16));
  b=u2f(__builtin_amdgcn_readlane(f2u(v),32)); d=u2f(__builtin_amdgcn_readlane(f2u(v),48));
}
typedef v2f cf;
DEVI cf cfma(cf a,cf b,cf c){ const cf t=__builtin_elementwise_fma(cf{a.x,a.x},b,c); return __builtin_elementwise_fma(cf{-a.y,a.y},cf{b.y,b.x},t); }
DEVI cf cmul(cf a,cf b){ const cf t=cf{a.x,a.x}*b; return __builtin_elementwise_fma(cf{-a.y,a.y},cf{b.y,b.x},t); }
DEVI cf csel(bool c,cf a,cf b){ return cf{c?a.x:b.x, c?a.y:b.y}; }
DEVI cf ldsc(u32 a){ return *(LAS v2f*)a; }
DEVI void csw32(cf& a,cf& b){ float x=a.x,y=b.x,z=a.y,t=b.y; sw32(x,y); sw32(z,t); a=cf{x,z}; b=cf{y,t}; }
DEVI void sincos_(float x,float& s,float& c){
  const float n=__builtin_rintf(x*0.636619772f);
  float r=__builtin_fmaf(n,-1.57079601e+00f,x); r=__builtin_fmaf(n,-3.13916473e-07f,r); r=__builtin_fmaf(n,-5.39030253e-15f,r);
  const float z=r*r;
  float ps=__builtin_fmaf(__builtin_fmaf(__builtin_fmaf(2.86567956e-6f,z,-1.98559923e-4f),z,8.33338592e-3f),z,-1.66666672e-1f);
  ps=__builtin_fmaf(ps*z,r,r);
  float pc=__builtin_fmaf(__builtin_fmaf(__builtin_fmaf(2.44677067e-5f,z,-1.38877297e-3f),z,4.16666567e-2f),z,-0.5f);
  pc=__builtin_fmaf(pc,z,1.f);
  const int q=(int)n;
  const float a=(q&1)?pc:ps, b=(q&1)?ps:pc;
  s=(q&2)?-a:a; c=((q+1)&2)?-b:b;
}
typedef __amdgpu_buffer_rsrc_t rsrc_t;
DEVI rsrc_t mkrsrc(const void* p,u32 bytes){ return __builtin_amdgcn_make_buffer_rsrc((void*)p,0,bytes,0x00020000); }
DEVI void st128(v4u v,rsrc_t r,u32 off){ __builtin_amdgcn_raw_buffer_store_b128(v,r,off,0,16); }
DEVI u32 mktag(u32 e){ return (e*0x9E3779B1u+0x7F4A7C15u)|1u; }

#define STAMP(k) do{}while(0)
#define CSTAMP(k) do{}while(0)
#define TRACE(k) do{}while(0)
#define STAMP_DECL do{}while(0)
#define BAR() bar()
#define STAMP_FLUSH() do{}while(0)
#define TRARG
#define TRPASS
#define TSARG
#define TSPASS

DEVI void poll32(v4u srd,u32 voff,u32 soff,u32 tag){
  #pragma unroll 1
  for(int it=0;it<SPIN_CAP;++it){
    u32 v;
    asm volatile("buffer_load_dword %0, %1, %2, %3 offen sc1\n\ts_waitcnt vmcnt(0)" : "=&v"(v) : "v"(voff), "s"(srd), "s"(soff) : "memory");
    if(all_eq(v,tag)) break;
    __builtin_amdgcn_s_sleep(12);
  }
}
template<int I,int N,class F> DEVI void sfor(F&& f){ if constexpr(I<N){ f(std::integral_constant<int,I>{}); sfor<I+1,N>(f); } }
AI constexpr u32 wslot(int c){ return WB+(c&3)*WSZ; }
AI constexpr u32 pa_(int c){ return (c&3)==3?WB+5*XS:(((c&3)+2)%3)*XS; }
AI constexpr u32 pb_(int c){ return WB+(c%5)*XS; }
AI constexpr u32 sx_(int c){ return c>=5?(c-5)*XS:wslot(c+2); }
AI constexpr u32 sb_(int c){ return c>=5?wslot(3)+(c-5)*8192:wslot(c+2)+16384; }

#define SG(m,n) __builtin_amdgcn_sched_group_barrier(m,n,0)
template<int NB>
DEVI u32 g_main(v4u x0,v4u x1,u32 e,rsrc_t rws,u32 lbase,int lane,int gw,int tt,int cg,u32 hqo TRARG TSARG){
  const int h=lane>>5, r=lane&31, rh=gw&1, u0=(gw>>1)*NB;
  const int row=tt*128+cg*8+gw*2;
  v16f acc[3][2]; v4f cw[3];
  u32 ax[4],aw,aw2;
  const u32 on2=((lane&3)==((lane>>4)&1))?0x3f803f80u:0u;
  v4u ones={on2,on2,on2,on2};
  u32 tag;
  {
    u32 ab=lbase+sb_(0)+r*128+((((2*gw+h)^((r>>1)&7)))<<4);
    v4f c0={},c1={};
    sfor<0,8>([&](auto C) AI {
      constexpr int c=C.value;
      if constexpr(c!=0 || !LNFIRST){ waitl0(); BAR(); }
      v4u t0,t1;
      if constexpr(c!=0 || !LNFIRST){ t0=ldsr128(ab); t1=ldsr128(ab+4096); }
      if constexpr(c==0){
        const float z0=u2f(rfl(x0[0])<<16), z1=u2f(rfl(x1[0])<<16);
        float s0=0.f,s1=0.f,v0=0.f,v1=0.f;
        #pragma unroll
        for(int k=0;k<8;++k){
          const float d0=u2f((k&1)?x0[k>>1]&0xffff0000u:x0[k>>1]<<16)-z0, d1=u2f((k&1)?x1[k>>1]&0xffff0000u:x1[k>>1]<<16)-z1;
          s0+=d0; s1+=d1; v0=__builtin_fmaf(d0,d0,v0); v1=__builtin_fmaf(d1,d1,v1); }
        wsum4(s0,s1,v0,v1);
        const float sm=(lane?s1:s0)*(1.f/512.f);
        const u32 vm=pk2(relu(__builtin_fmaf(lane?v1:v0,1.f/512.f,-sm*sm)),sm+(lane?z1:z0));
        const float rs=__builtin_amdgcn_rcpf(rbf(__builtin_amdgcn_sqrtf(rbf(u2f(vm<<16)+1e-5f))));
        tag=mktag(rfl(e));
        if(lane<2) st128(v4u{f2u(rs),tag,vm&0xffff0000u,tag},rws,WS_FX+row*64+lane*16);
        if(gw==0) *(LAS v2u*)(lbase+L_TAG)=v2u{tag,e};
        STAMP(1);
        if(LNFIRST){ waitl0(); BAR(); t0=ldsr128(ab); t1=ldsr128(ab+4096); }
      }
      if constexpr(c<7) ab+=sb_(c+1)-sb_(c);
      if constexpr(c>=1 && c<=2*NB){ acc[(c-1)>>1][(c-1)&1]=v16f{}; asm volatile("":"+v"(acc[(c-1)>>1][(c-1)&1])); }
      c0=mfma16(ones,t0,c0); c1=mfma16(ones,t1,c1);
      asm volatile("":"+v"(c0),"+v"(c1),"+v"(ab));
    });
    asm volatile("s_nop 7\n\ts_nop 1":"+v"(c0),"+v"(c1));
    const bool hi=lane&16;
    *(LAS float*)(lbase+L_CSP+(r*8+h*4+gw)*4)=h?(hi?c1[1]:c1[0]):(hi?c0[1]:c0[0]);
    STAMP(2);
    waitl0(); BAR();
  }
  #pragma unroll
  for(int s=0;s<4;++s) ax[s]=lbase+rh*8192+r*128+((((2*(s^(2*rh))+h)^((r>>1)&7)))<<4);
  aw=lbase+WB+((lane>>4)&1)*(12288+32)+(8*h+((lane&15)>>2))*192+(lane&3)*8+u0*32;
  aw2=aw+6144-rh*6144; aw+=rh*6144;
  #pragma unroll
  for(int t=0;t<3;++t){ cw[t]=v4f{}; if(t<NB) asm volatile("":"+v"(cw[t])); }
  asm volatile("":"+v"(ax[0]),"+v"(ax[1]),"+v"(ax[2]),"+v"(ax[3]),"+v"(aw),"+v"(aw2));
  BAR();
  __builtin_amdgcn_s_setprio(2);
  v4u X[2][2],T[2][3];
  auto rd=[&](int b,int s) AI {
    const u32 ad=(s<2?aw:aw2)+(s&1)*3072;
    X[b][0]=ldsr128(ax[s]);
    #pragma unroll
    for(int t=0;t<NB;++t){ T[b][t]=ldstr(ad+t*32,ad+t*32+768); if(t==0) X[b][1]=ldsr128(ax[s]+4096); }
  };
  auto mm=[&](int b,bool cs) AI {
    #pragma unroll
    for(int t=0;t<NB;++t){ acc[t][0]=mfma(T[b][t],X[b][0],acc[t][0]); if(cs) cw[t]=mfma16(ones,T[b][t],cw[t]); acc[t][1]=mfma(T[b][t],X[b][1],acc[t][1]); }
  };
  auto mix=[&](int nv,bool cs) AI {
    SG(0x8,1); SG(0x100,2); if(cs) SG(0x8,2); else SG(0x8,1); SG(0x100,2); SG(0x8,1); SG(0x100,2);
    if(NB==3){ if(cs) SG(0x8,2); else SG(0x8,1); SG(0x100,2); }
    SG(0x8,1); if(nv) SG(0x2,3);
    SG(0x8,5);
  };
  u32 d0=XS, d1=XS, d2=(u32)(-2*(int)XS);
  STAMP(3); CSTAMP(6);
  rd(0,0);
  #pragma unroll 1
  for(int c=0;c<8;++c){
    const u32 dw=((c&3)==3)?(u32)(-3*(int)WSZ):WSZ;
    SB; rd(1,1); mm(0,true); ax[0]+=d0; ax[1]+=d0; aw+=dw; mix(1,true);
    SB; asm volatile("":"+v"(ax[0]),"+v"(ax[1]),"+v"(aw));
    rd(0,2); mm(1,true); mix(0,true);
    SB; rd(1,3); mm(0,false); ax[2]+=d0; ax[3]+=d0; aw2+=dw; mix(1,false);
    SB; asm volatile("":"+v"(ax[2]),"+v"(ax[3]),"+v"(aw2));
    { const u32 t=d0; d0=d1; d1=d2; d2=t; }
    if(c==7){
      const int ln=lane_now();
      #pragma unroll
      for(int t=0;t<NB;++t){ const float lo=cw[t][0], hi=cw[t][1]; cw[t][0]=(ln&16)?hi:lo; }
      *(LAS v4f*)(lbase+L_CW+(gw*32+(ln&31))*16)=v4f{cw[0][0],cw[1][0],cw[2][0],0.f};
      const u32 tm=lbase+L_TAB+(rh*64+16*((ln>>2)&1)+8*((ln>>4)&1)+4*((ln>>3)&1)+(ln&3))*16+8;
      cw[0][2]=*(LAS float*)tm; cw[0][3]=*(LAS float*)(tm+512);
      ax[0]=lbase+L_CW+((gw^1)*32+(ln&31))*16;
    }
    waitl0(); BAR();
    rd(0,0); mm(1,false); mix(0,false);
  }
  const int ln=lane_now();
  const u32 hm=(ln&32)?0u:0xffffffffu;
  v4u M[2];
  #pragma unroll
  for(int m=0;m<2;++m){ const u32 mu=f2u(cw[0][2+m]), nm=(((mu>>16)|mu)^0x80008000u)&hm; M[m]=v4u{nm,nm,0u,0u}; }
  #pragma unroll
  for(int t=0;t<NB;++t){
    const float c=cw[t][0]+u2f(X[0][0][t]);
    const u32 c0=f2u(c)&0xffff0000u; const float r1=c-u2f(c0);
    const u32 c1=f2u(r1)&0xffff0000u; const float r2=r1-u2f(c1);
    const v4u F={(c0>>16)|c1,f2u(r2)>>16,0u,0u};
    acc[t][0]=mfma(F,M[0],acc[t][0]); acc[t][1]=mfma(F,M[1],acc[t][1]);
  }
  STAMP(4); CSTAMP(7);
  #pragma unroll
  for(int t=0;t<NB;++t)
    #pragma unroll
    for(int m=0;m<2;++m){
      SB;
      v4u v;
      #pragma unroll
      for(int k=0;k<4;++k) v[k]=pk2(relu(acc[t][m][2*k])*acc[t][m][2*k+8],relu(acc[t][m][2*k+1])*acc[t][m][2*k+9]);
      st128(v,rws,hqo+(u0+t)*2048+m*1024+ln*16);
    }
  return tag;
}

__global__ NOPK __launch_bounds__(768,3) void fused_k(Args a){
  __shared__ __attribute__((aligned(256))) char smem[L_TOTAL];
  const u32 lbase=(u32)(size_t)(LAS char*)smem;
  const int tid=__builtin_amdgcn_workitem_id_x();
  const int wgid=__builtin_amdgcn_workgroup_id_x(), w=rfl(tid>>6);
  int lane=tid&63;
  const int xcd=wgid&7, idx=wgid>>3;
  if(CODEPF && w==CODEPF-1){
    u64 pc; u32 back; asm volatile("s_getpc_b64 %0\n.Lpcx%=:\n\ts_mov_b32 %1, .Lpcx%=-_Z7fused_k4Args":"=&s"(pc),"=s"(back));
    dma_dw<false>(mksrd((const void*)(pc-back),CODE_LEN),(u32)(((idx&3)*64+lane)*128),0,lbase+L_DUM);
  }
  const int tt=(xcd&1)*8+(idx&7), cg=(xcd>>1)*4+(idx>>3);
  const int nb=((cg&3)==3||cg==14)?2:3, blk0=11*(cg>>2)+3*(cg&3)-(cg==15?1:0);
  const int rt2=(xcd&3)*8+(idx&7), ct=(xcd>>2)*4+(idx>>3);
  const int ry=ROTP?2*(idx>>3):0, rz=ROTP?(11*(idx>>3))>>2:0;
  const u32 one=1;
  STAMP_DECL; STAMP(0); CSTAMP(12);

  v4u x0,x1; u32 ep0;
  if(w<4){ const int c_=cg*32+(lane&31); x0[0]=a.lre[c_]; x0[1]=a.lim[c_]; x0[2]=a.ldt[c_]; }
  else if(w<8){ const int row=tt*128+cg*8+(w-4)*2; x0=((const v4u*)a.x)[row*64+lane]; x1=((const v4u*)a.x)[row*64+64+lane];
    ep0=__builtin_amdgcn_raw_buffer_load_b32(mkrsrc(a.ws,WS_END),0,WS_EPOCH,16); }
  asm volatile("":::"memory");
  if(__builtin_expect(w>=8,1)){
    __builtin_amdgcn_s_setprio(3);
    const int lw=w-8, j=lane>>3, l7=lane&7;
    const v4u sx=mksrd(a.x,2048*1024);
    const v4u sb=mksrd(lw<2?a.bre:a.bim,512*1024);
    const u32 sz0=(u32)((l7^(j>>1))<<4), sz1=sz0^64u;
    const u32 xr=(tt*128+32*lw+16*(j>>2)+(j&3))*1024;
    const u32 vx0=xr+sz0, vx1=xr+3*1024+sz1, vx2=xr+6*1024+sz0, vx3=xr+9*1024+sz1;
    const u32 br=(cg*32+16*(lw&1)+j)*1024;
    const u32 vb0=br+sz0, vb1=br+7*1024+sz1;
    const u32 l4=lbase+lw*4096, l2=lbase+lw*2048;
    u32 kx=((tt*(ROT1&15)+cg*(ROT1>>4))&7)*128, kw=(kx+640)&0x380;
    #define SX(c) dma4<false,sx_(c)>(sx,vx0,vx1,vx2,vx3,kx,l4)
    #define SBB(c) do{ dma2<sb_(c)>(sb,vb0,vb1,kx,l2); kx=(kx+128)&0x380; }while(0)
    SX(0); SBB(0); SX(1); SBB(1);
    const v4u sws=mksrd(a.ws,WS_END);
    const v4u sw=mksrd(lw<2?a.wg:a.wu,512*2752);
    const v4u swd=mksrd(a.wd,1376*1024);
    const v4u sc=mksrd(lw<2?a.cre:a.cim,512*1024);
    const u32 vf=(lane&15)*128, lf=lbase+L_FLG+lw*256;
    const u32 fx=WS_FX+(tt*64+lw*16)*128, fh=WS_FH+(rt2*32+(lw&1)*16)*128, fhh=WS_FHH+rt2*4096;
    u32 vw[6];
    #pragma unroll
    for(int i=0;i<3;++i){ const u32 q=i*1024+lane*16, row=q/192, col=q%192; const bool ok=col<(u32)nb*64;
      vw[i]=ok?(row+32*(lw&1))*2752+col+blk0*64-i*1024:OOB; vw[i+3]=vw[i]+16*2752; asm volatile("":"+v"(vw[i]),"+v"(vw[i+3])); }
    const u32 lwv=lbase+(lw>>1)*(12288+32)+(lw&1)*6144;
    #define GWA(c) dma3<wslot(c)>(sw,vw[0],vw[1],vw[2],kw*1376,lwv)
    #define GWB(c) do{ dma3<wslot(c)+3072>(sw,vw[3],vw[4],vw[5],kw*1376,lwv); kw=(kw+128)&0x380; }while(0)
    #define GX(c) do{ dma4<false,((c)%3)*XS>(sx,vx0,vx1,vx2,vx3,kx,l4); kx=(kx+128)&0x380; }while(0)
    SX(2); waitv<10>(); BAR();
    SBB(2); SX(3); waitv<10>(); BAR();
    SBB(3); SX(4); waitv<10>(); BAR();
    SBB(4); SX(5); waitv<10>(); BAR();
    SBB(5); SX(6); SBB(6); waitv<12>(); BAR();
    SX(7); SBB(7); waitv<12>(); BAR();
    STAMP(2);
    const u32 vs=(lane>>1)*128+(lane&1)*16, lt=lbase+L_TAB+lw*512;
    const u32 tag=rfl(peek(lbase+L_TAG));
    if(lane<32) dma_x4(sws,vs,fx,lt);
    GWA(0); waitv<10>(); BAR();
    GWB(0); waitv<7>(); BAR();
    waitv<6>();
    #pragma unroll 1
    for(int it=0;it<SPIN_CAP;++it){
      const v4u t=*(volatile LAS v4u*)(lt+(lane&31)*16);
      if(all_eq((t[1]^tag)|(t[3]^tag),0u)) break;
      __builtin_amdgcn_s_sleep(2);
      if(lane<32) dma_x4(sws,vs,fx,lt);
      waitv<0>();
    }
    BAR();
    GWA(1); GWB(1);
    if(WARMP){
      dma_dw<false>(sc,lane<32?(u32)(j*1024+l7*128):OOB,(ct*64+(idx&7)*8+(lw&1)*4)*1024,lbase+L_DUM);
      dma_dw<false>(swd,lane<43?(u32)(lane*1024):OOB,((idx&7)*172+lw*43)*1024+ct*128,lbase+L_DUM);
    }
    waitv<WARMP?8:6>(); BAR();
    STAMP(3);
    GWA(2); GWB(2); GWA(3); GWB(3);
    if(LLCPF && nb==2){
      const u32 li=((cg<12?(cg>>2):cg-11)*16+tt)*2+(lw&1);
      dma_dw<false>(sc,lane<26?(li*26+lane)*128:OOB,0,lbase+L_DUM);
      dma_dw<false>(swd,lane<35?((li*2+(lw>>1))*35+lane)*128:OOB,0,lbase+L_DUM);
    }
    u32 va=WS_HS+rt2*131072+lw*4096+lane*16;
    const u32 cr=(ct*64+(lw&1)*32+j)*1024;
    u32 vp0=cr+sz0, vp1=cr+7*1024+sz1, vp2=cr+14*1024+sz0, vp3=cr+21*1024+sz1;
    const int gp0=tt-1-4*lw-(lane>>5);
    const u32 vg0=gp0>=0?(u32)(WS_GRAN+gp0*8192+(cg*32+(lane&31))*16):OOB, vg1=gp0>=2?vg0-2*8192-1024:OOB;
    #define SNAP() asm volatile("s_add_u32 m0, %[b], %[i]\n\ts_nop 0\n\tbuffer_load_dwordx4 %[v0], %[r], 0 offen sc1 lds\n\tbuffer_load_dwordx4 %[v1], %[r], 0 offen offset:1024 sc1 lds" \
        :: [v0]"v"(vg0),[v1]"v"(vg1),[r]"s"(sws),[b]"s"(l2),[i]"n"(L_GR) : "memory","scc")
    waitv<WARMP?14:12>(); BAR();
    GX(3); GWA(4); GWB(4); SNAP(); waitv<18>(); BAR();
    GX(4); GWA(5); GWB(5); waitv<RELAX?18:10>(); SNAP();
    if(WARMB) dma_dw<false>(sc,lane<32?(u32)(j*1024+l7*128):OOB,(ct*64+(idx&7)*8+(lw&1)*4)*1024,lbase+L_DUM);
    BAR();
    GX(5); GWA(6); GWB(6); waitv<RELAX?18:WARMB?11:10>(); SNAP(); BAR();
    GX(6); GWA(7); GWB(7); waitv<RELAX?18:10>(); BAR();
    u32 ka=ry, kb=ry;
    auto PA=[&](auto C) AI {
      constexpr int c=C.value;
      if constexpr(c==8){ va=WS_HH+rt2*86*2048+lw*4096+lane*16; ka=rz; }
      u32 so;
      if constexpr(c<8){ so=ka<<14; ka=(ka+1)&7; } else { so=(ka==10&&lw==3)?OOB:ka<<14; ka= ka==10?0:ka+1; }
      dma4<true,pa_(c)>(sws,va,va,va,va,so,l4);
    };
    auto PB=[&](auto C) AI {
      constexpr int c=C.value;
      if constexpr(c==8){ vp0=(lw*32+j)*1024+ct*128+((l7^(((j>>1)&1)<<2))<<4); vp1=vp0+7*1024; vp2=vp0+14*1024; vp3=vp0+21*1024; kb=rz; }
      u32 so;
      if constexpr(c<8){ so=kb<<7; kb=(kb+1)&7; } else { so=kb<<17; kb= kb==10?0:kb+1; }
      dma4<false,pb_(c)>(c<8?sc:swd,vp0,vp1,vp2,vp3,so,l4);
    };
    #define IC(n) std::integral_constant<int,(n)>{}
    GX(7); PB(IC(0));
    waitv<14>(); BAR();
    PB(IC(1)); PB(IC(2));
    waitv<12>(); BAR();
    STAMP(4);
    dma_dw<true>(sws,vf,fh,lf);
    PB(IC(3));
    waitv<4>();
    const bool early=all_eq(peek(lf+lane*4),tag);
    if(early){ PA(IC(0)); PA(IC(1)); }
    BAR();
    STAMP(5);
    if(!early){ poll32(sws,vf,fh,tag); PA(IC(0)); PA(IC(1)); }
    STAMP(6);
    if(!LATEA){ PA(IC(2)); PA(IC(3)); PB(IC(4)); }
    waitv<LATEA?4:16>(); BAR();
    if(LATEA){ PA(IC(2)); PA(IC(3)); PB(IC(4)); }
    if(WARMB) dma_dw<false>(swd,lane<43?(u32)(lane*1024):OOB,((idx&7)*172+lw*43)*1024+ct*128,lbase+L_DUM);
    if(lane<16) dma_x4(sws,vs,WS_FX+(rt2*32+lw*8)*128,lbase+L_TAB+lw*256);
    { const u32 vt=(rt2*64+lw*16+j)*1024+ct*128+l7*16; dma2<L_XT>(sx,vt,vt+7*1024,0,l2); }
    waitv<WARMB?16:15>(); BAR();
    waitv<WARMB?12:11>(); BAR();
    bool zok=false;
    sfor<2,18>([&](auto K) AI {
      constexpr int k=K.value;
      if constexpr(k==5) dma_dw<true>(sws,(lane&31)*128,fhh,lf);
      if constexpr(k==9){ if(wgid==0 && lw==0) dma_dw<true>(sws,vf*64,WS_FH,lf); }
      if constexpr(k==6){
        waitv<8>();
        zok=all_eq(peek(lf+lane*4),tag);
        if(zok) PA(IC(8));
        PB(IC(9));
        if(zok) waitv<12>(); else waitv<8>();
      } else if constexpr(k==7){
        STAMP(7);
        if(!zok){ poll32(sws,(lane&31)*128,fhh,tag); PA(IC(8)); }
        STAMP(8);
        PA(IC(9)); PB(IC(10));
        if(zok) waitv<12>(); else waitv<8>();
      } else {
        if constexpr(k<=16) PA(IC(k+2));
        if constexpr(k<=15) PB(IC(k+3));
        waitv<(k==2?(WARMB?16:15):k<=15?12:k==16?8:0)>();
      }
      if constexpr(k==12){
        if(wgid==0 && lw==0){
          if(!all_eq(peek(lf+lane*4),tag)) poll32(sws,vf*64,WS_FH,tag);
          const u32 e=peek(lbase+L_TAG+4)+1u;
          asm volatile("s_mov_b64 exec, 1\n\tbuffer_store_dword %0, off, %1, 0 sc1\n\ts_mov_b64 exec, -1" :: "v"(e), "s"(sws) : "memory");
        }
      }
      BAR();
    });
    STAMP(11);
    STAMP_FLUSH();
    return;
  }

  int h=lane>>5, r=lane&31;
  const rsrc_t rws=mkrsrc(a.ws,WS_END);
  const v4u sws=mksrd(a.ws,WS_END);
  u32 pend=0;
  u32 tag;
  auto try_flag=[&](bool force) AI {
    if(pend && (force || vm_idle())){
      waitv<0>();
      flag_st(sws,pend,tag);
      pend=0;
    }
  };
  if(w>=4){
    const int gw=w-4, rt=2*tt+(w&1);
    __builtin_amdgcn_s_setprio(3);
    const u32 hqo=WS_HH+(rt*86+2*blk0)*2048;
    if(nb==3) tag=g_main<3>(x0,x1,ep0,rws,lbase,lane,gw,tt,cg,hqo TRPASS TSPASS); else tag=g_main<2>(x0,x1,ep0,rws,lbase,lane,gw,tt,cg,hqo TRPASS TSPASS);
    pend=WS_FHH+(rt*32+cg*2+(gw>>1))*128;
    STAMP(5);
  } else {
    const int ch=cg*32+r;
    __builtin_amdgcn_s_setprio(SPRIO);
    const u16 hlr=(u16)x0[0], hli=(u16)x0[1], hdt=(u16)x0[2];
    *(LAS v4u*)(lbase+L_GR+w*2048+lane*16)=v4u{0u,0u,0u,0u}; *(LAS v4u*)(lbase+L_GR+w*2048+1024+lane*16)=v4u{0u,0u,0u,0u};
    const float lr=b2f(hlr), li=b2f(hli), dt=rbf(__builtin_expf(b2f(hdt)));
    cf lam,coef;
    { const float ex=__builtin_expf(dt*lr); float sn,cs_; sincos_(dt*li,sn,cs_); lam=cf{ex*cs_,ex*sn};
      const float dd=1.f/(lr*lr+li*li); coef=cmul(cf{lam.x-1.f,lam.y},cf{lr*dd,-li*dd}); }
    cf l16=cmul(lam,lam); l16=cmul(l16,l16); l16=cmul(l16,l16); l16=cmul(l16,l16);
    cf l32=cmul(l16,l16);
    cf l256,wq,pw;
    { const cf l64=cmul(l32,l32), l128=cmul(l64,l64), o={1.f,0.f};
      l256=cmul(l128,l128); wq=csel(h,l128,o); pw=cmul(csel(w&1,l32,o),csel(w&2,l64,o)); }
    asm volatile("":"+v"(lam),"+v"(coef),"+v"(l16),"+v"(l32),"+v"(l256),"+v"(wq),"+v"(pw));
    v16f are={},aim={};
    {
      u32 ax[4],ab[4];
      #pragma unroll
      for(int s=0;s<4;++s){ const u32 t=lbase+r*128+((((2*s+h)^((r>>1)&7)))<<4); ab[s]=t+sb_(0); ax[s]=t+sx_(0)+w*4096; }
      v4u X[2][2],T[2][2][2];
      auto rd=[&](int b) AI {
        #pragma unroll
        for(int s=0;s<2;++s){ X[b][s]=ldsr128(ax[2*b+s]); T[b][s][0]=ldsr128(ab[2*b+s]); T[b][s][1]=ldsr128(ab[2*b+s]+4096); } };
      auto mm=[&](int b) AI {
        #pragma unroll
        for(int s=0;s<2;++s){ are=mfma(X[b][s],T[b][s][0],are); aim=mfma(X[b][s],T[b][s][1],aim); }
        asm volatile("":"+v"(are),"+v"(aim)); };
      STAMP(1);
      waitl0(); BAR();
      rd(0);
      sfor<0,8>([&](auto C) AI {
        constexpr int c=C.value;
        SB; rd(1);
        SB; mm(0);
        if constexpr(c<7){
          #pragma unroll
          for(int s=0;s<4;++s){ ax[s]+=sx_(c+1)-sx_(c); ab[s]+=sb_(c+1)-sb_(c); asm volatile("":"+v"(ax[s]),"+v"(ab[s])); }
          SB;
          waitl0(); BAR();
          rd(0);
        }
        SB; mm(1);
      });
    }
    STAMP(2);
    tag=rfl(peek(lbase+L_TAG));
    u32 ta=lbase+L_TAB+(32*w+16*h)*16, tb=lbase+L_CSP+r*32, tg=lbase+L_AGG+r*8;
    asm volatile("":"+v"(ta),"+v"(tb),"+v"(tg));
    waitl0(); BAR();
    __builtin_amdgcn_s_setprio(3);
    int left=8;
    #define SBAR() do{ if(left>0){ BAR(); --left; } else __builtin_amdgcn_s_sleep(4); }while(0)
    cf uu[16], elo, Q;
    {
      const v4f c0=*(LAS v4f*)tb, c1=*(LAS v4f*)(tb+16);
      float rs[16],mu[16];
      #pragma unroll
      for(int k=0;k<16;++k){ rs[k]=*(LAS float*)(ta+k*16); mu[k]=*(LAS float*)(ta+k*16+8); }
      SB;
      const cf ncs={-((c0[0]+c0[1])+(c0[2]+c0[3])),-((c1[0]+c1[1])+(c1[2]+c1[3]))};
      cf ea={0.f,0.f};
      #pragma unroll
      for(int k=0;k<16;++k){
        uu[k]=cf{__builtin_fmaf(mu[k],ncs.x,are[k]),__builtin_fmaf(mu[k],ncs.y,aim[k])}*rs[k];
        ea= k? cfma(lam,ea,uu[k]) : uu[0];
      }
      cf ehi=ea; elo=ea; csw32(elo,ehi);
      *(LAS v2f*)(tg+w*256)=cfma(l16,elo,ehi);
    }
    STAMP(3);
    waitl0(); BAR();
    { const cf a0=ldsc(tg), a1=ldsc(tg+256), a2=ldsc(tg+512), a3=ldsc(tg+768);
      const cf Q2=cfma(l32,a0,a1), Q3=cfma(l32,Q2,a2);
      if(w==3){ const cf E=cfma(l32,Q3,a3); if(lane<32) st128(v4u{f2u(E.x),tag,f2u(E.y),tag},rws,ch*16+WS_GRAN+tt*8192); }
      Q= w==0?cf{0.f,0.f}: w==1?a0: w==2?Q2:Q3; }
    STAMP(4);
    #pragma unroll
    for(int k=0;k<16;++k){ uu[k]=cmul(coef,uu[k]); asm volatile("":"+v"(uu[k])); if(k==CSPLIT) SBAR(); }
    cf H={0.f,0.f};
    if(tt>0){
      SBAR();
      v4u gr[8];
      #pragma unroll 1
      for(int it=0;it<SPIN_CAP;++it){
        if(it<3){
          SBAR();
          #pragma unroll
          for(int i=0;i<8;++i) gr[i]=*(volatile LAS v4u*)(lbase+L_GR+lane*16+i*1024);
        } else {
          #pragma unroll
          for(int i=0;i<8;++i){ const int pd=tt-1-2*i-h; gr[i]=__builtin_amdgcn_raw_buffer_load_b128(rws,pd>=0?(u32)(pd*8192+ch*16):OOB,WS_GRAN,16); }
          SBAR();
        }
        u32 bad=0;
        #pragma unroll
        for(int i=0;i<8;++i) bad|=((gr[i][1]^tag)|(gr[i][3]^tag))&~(u32)((tt-1-2*i-h)>>31);
        if(all_eq(bad,0u)) break;
      }
      #pragma unroll
      for(int i=7;i>=0;--i) H=cfma(l256,H,cf{u2f(gr[i][0]),u2f(gr[i][2])});
      H=cmul(H,wq);
      cf h2=H; csw32(H,h2); H+=h2;
    }
    STAMP(5);
    cf hc=cfma(pw,H,Q);
    hc=cmul(coef,csel(h,cfma(l16,hc,elo),hc));
    const int rt=2*tt+(w>>1);
    const u32 hso=WS_HS+rt*131072+(cg>>1)*16384+(((cg&1)*2+(r>>4))*2+(w&1))*1024+h*512;
    #pragma unroll
    for(int m=0;m<2;++m){
      if(left>0 && (m==0 || FSPLIT)){ asm volatile("":"+v"(hc)); BAR(); --left; }
      cf hv[8];
      #pragma unroll
      for(int jj=0;jj<8;++jj){ hc=cfma(lam,hc,uu[8*m+jj]); hv[jj]=hc; }
      const u32 o=hso+m*256+(((lane&15)^(8*h+4*m))<<4);
      st128(v4u{pk2(hv[0].x,hv[1].x),pk2(hv[2].x,hv[3].x),pk2(hv[4].x,hv[5].x),pk2(hv[6].x,hv[7].x)},rws,o);
      st128(v4u{pk2(-hv[0].y,-hv[1].y),pk2(-hv[2].y,-hv[3].y),pk2(-hv[4].y,-hv[5].y),pk2(-hv[6].y,-hv[7].y)},rws,o+8192);
    }
    pend=WS_FH+(rt*32+(cg&1)*16+(cg>>1)*2+(w&1))*128;
    STAMP(6);
    #pragma unroll 1
    while(left>0){
      BAR(); --left;
      #pragma unroll 1
      for(int it=0;it<FWIN && pend;++it){ try_flag(false); if(pend) __builtin_amdgcn_s_sleep(1); else STAMP(7); }
    }
    if(pend){ try_flag(true); STAMP(7); }
  }
  lane=lane_now(); h=lane>>5; r=lane&31;
  const int g=lane>>4, m2=w&1, tc=(w>>1)&1, kh=w>>2;
  const int dcol=ct*64+tc*32+r;
  const float dsk=b2f(a.dsk[dcol]);
  v16f ay={},az={};
  const int q=(lane&15)>>2, p=lane&3, b4=g&1, gp=(b4^kh)+2*(p&1);
  u32 a0=lbase+pa_(0)+(kh*8+m2)*1024+(gp*16+((8*h+q)^(4*gp)))*16+8*(p>>1);
  u32 b0=lbase+pb_(0)+kh*8192+(tc*32+r)*128+((h^((r>>1)&7))<<4);
  u32 za=lbase+pa_(8)+(kh*8+m2)*1024+(lane^(kh*16))*16;
  u32 zb=lbase+pb_(8)+(64*kh+4*h+q)*128+((tc*64+b4*32+8*p)^(((q>>1)&1)<<6));
  u32 d0=pa_(1)-pa_(0), d1=pa_(2)-pa_(1), d2=pa_(3)-pa_(2), d3=pa_(0)-pa_(3), e0=XS; int eb=0;
  v4u A[2][4],B[2][4];
  auto rot=[&]() AI { const u32 t=d0; d0=d1; d1=d2; d2=d3; d3=t; eb= eb==4?0:eb+1; e0= eb==4?(u32)(-4*(int)XS):XS; };
  auto rdy=[&](int b) AI {
    #pragma unroll
    for(int s=0;s<4;++s){ A[b][s]=ldstr(a0+s*2048,(a0^64)+s*2048); B[b][s]=ldsr128(b0^(s*32)); }
    a0+=d0; b0+=e0; rot();
  };
  auto rdz=[&](int b) AI {
    #pragma unroll
    for(int s=0;s<4;++s){ A[b][s]=ldsr128(za+s*2048); B[b][s]=ldstr(zb+s*2048,zb+s*2048+1024); }
    za+=d0; zb+=e0; rot();
  };
  auto mm4=[&](int b,v16f& c) AI {
    #pragma unroll
    for(int s=0;s<4;++s) c=mfma(A[b][s],B[b][s],c);
    #pragma unroll
    for(int s=0;s<4;++s){ SG(0x8,1); SG(0x100,3); }
    SB; asm volatile("":"+v"(c));
  };
  #define NXT() BAR()
  const int tk=32*m2+16*h+8*kh;
  STAMP(8);
  __builtin_amdgcn_s_setprio(0);
  NXT(); rdy(0);
  STAMP(9);
  #pragma unroll 1
  for(int c=0;c<3;++c){
    NXT(); rdy(1); mm4(0,ay); try_flag(false);
    NXT(); rdy(0); mm4(1,ay); try_flag(c==2);
  }
  NXT(); rdy(1); mm4(0,ay);
  NXT(); rdz(0); mm4(1,ay);
  STAMP(10);
  float sv[8], xf[8], r2[8];
  u32 pt=lbase+L_TAB+tk*16, px=lbase+L_XT+tk*128+(tc*32+r)*2;
  asm volatile("":"+v"(pt),"+v"(px));
  #pragma unroll 1
  for(int c=0;c<5;++c){
    NXT(); rdz(1); mm4(0,az);
    if(c==1){
      u16 xr[8];
      #pragma unroll
      for(int k=0;k<8;++k){ r2[k]=*(LAS float*)(pt+k*16); sv[k]=*(LAS float*)(pt+k*16+8); xr[k]=*(LAS u16*)(px+k*128); }
      SB;
      #pragma unroll
      for(int k=0;k<8;++k){ xf[k]=b2f(xr[k]); sv[k]=(xf[k]-sv[k])*r2[k]*dsk; r2[k]*=r2[k]; }
    }
    NXT(); rdz(0); mm4(1,az);
  }
  {
    const u32 xo=lbase+WB+w*4096+lane*16, xi=lbase+WB+(w^4)*4096+lane*16;
    *(LAS v4f*)xo=v4f{ay[8],ay[9],ay[10],ay[11]}; *(LAS v4f*)(xo+1024)=v4f{ay[12],ay[13],ay[14],ay[15]};
    #pragma unroll
    for(int k=0;k<8;++k) sv[k]=__builtin_fmaf(2.f,ay[k],sv[k]);
    SB;
    #pragma unroll
    for(int s=0;s<4;++s) az=mfma(A[0][s],B[0][s],az);
    STAMP(11);
    *(LAS v4f*)(xo+2048)=v4f{az[8],az[9],az[10],az[11]}; *(LAS v4f*)(xo+3072)=v4f{az[12],az[13],az[14],az[15]};
    waitl0(); bar();
    const v4f i2=*(LAS v4f*)(xi+2048), i3=*(LAS v4f*)(xi+3072), i0=*(LAS v4f*)xi, i1=*(LAS v4f*)(xi+1024);
    const rsrc_t ro=mkrsrc(a.out,2048*512*4);
    const u32 oo=((rt2*64+tk)*512+dcol)*4;
    #pragma unroll
    for(int k=0;k<8;++k){
      const float z=(az[k]+(k<4?i2[k&3]:i3[k&3]))*r2[k];
      const float gt=z*__builtin_amdgcn_rcpf(1.f+__builtin_amdgcn_exp2f(z*-1.44269504f));
      __builtin_amdgcn_raw_buffer_store_b32(f2u(__builtin_fmaf(__builtin_fmaf(2.f,k<4?i0[k&3]:i1[k&3],sv[k]),gt,xf[k])),ro,oo+k*2048,0,OUTAUX);
    }
  }
  CSTAMP(14); STAMP(13); STAMP_FLUSH();
}

extern "C" void kernel_launch(void* const* d_in, const int* in_sizes, int n_in,
                              void* d_out, int out_size, void* d_ws, size_t ws_size,
                              hipStream_t stream) {
  Args a;
  a.x=(const u16*)d_in[0]; a.lre=(const u16*)d_in[1]; a.lim=(const u16*)d_in[2];
  a.bre=(const u16*)d_in[3]; a.bim=(const u16*)d_in[4]; a.cre=(const u16*)d_in[5]; a.cim=(const u16*)d_in[6];
  a.ldt=(const u16*)d_in[7]; a.dsk=(const u16*)d_in[8];
  a.wg=(const u16*)d_in[10]; a.wu=(const u16*)d_in[11]; a.wd=(const u16*)d_in[12];
  a.out=(float*)d_out; a.ws=(char*)d_ws;
  fused_k<<<256,768,0,stream>>>(a);
}
```

```cpp
#include <hip/hip_runtime.h>
#include <hip/hip_bf16.h>
#include <type_traits>

typedef unsigned u32; typedef unsigned long long u64; typedef unsigned short u16;
typedef u32 v4u __attribute__((ext_vector_type(4)));
typedef u32 v2u __attribute__((ext_vector_type(2)));
typedef short v4s __attribute__((ext_vector_type(4)));
typedef __bf16 v2bf __attribute__((ext_vector_type(2)));
typedef __bf16 v8bf __attribute__((ext_vector_type(8)));
typedef float v2f __attribute__((ext_vector_type(2)));
typedef float v4f __attribute__((ext_vector_type(4)));
typedef float v16f __attribute__((ext_vector_type(16)));
#define LAS __attribute__((address_space(3)))
#define DEVI __device__ __forceinline__
#define AI __attribute__((always_inline))
#define SB __builtin_amdgcn_sched_barrier(0)
#if defined(__HIP_DEVICE_COMPILE__)
#define NOPK __attribute__((target("no-packed-fp32-ops")))
#else
#define NOPK
#endif

#define ROT1 0x01
#define ROTP 1
#define SPRIO 2
#define OUTAUX 16
#define HSLEEP 2
#define FSPLIT 0
#define FWIN 10
#define LATEA 0
#define LLCPF 0
#define LNFIRST 1
#define WARMP 1
#define RELAX 1
#define CSPLIT 5
#define LBSLEEP 6
#define WARMB 0

constexpr u32 WS_EPOCH = 0;
constexpr u32 WS_FX    = 32768;
constexpr u32 WS_FH    = WS_FX + 131072;
constexpr u32 WS_FHH   = WS_FH + 131072;
constexpr u32 WS_GRAN  = WS_FHH + 131072;
constexpr u32 WS_HH    = WS_GRAN + 131072;
constexpr u32 WS_HS    = WS_HH + 32*86*2048;
constexpr u32 WS_END   = WS_HS + 32*131072;
constexpr u32 WS_STAMP = 16u<<20;
constexpr u32 XS = 16384;
constexpr u32 WB = 49152, WSZ = 24832;
constexpr u32 L_XT = WB+4*WSZ;
constexpr u32 L_TAB  = L_XT+8192;
constexpr u32 L_CSP  = L_TAB + 2048;
constexpr u32 L_FLG  = L_CSP;
constexpr u32 L_CW   = L_CSP + 1024;
constexpr u32 L_AGG  = L_CW + 2048;
constexpr u32 L_DUM  = L_AGG + 1024;
constexpr u32 L_TAG  = L_DUM + 256;
constexpr u32 L_GR   = L_XT;
constexpr u32 L_TOTAL = 163840;
static_assert(L_TAG+16<=L_TOTAL,"lds");
constexpr u32 OOB = 0x40000000u;
constexpr int SPIN_CAP = 1<<18;

#define CODEPF 12
constexpr u32 CODE_LEN=$n;
struct Args { const u16 *x,*lre,*lim,*bre,*bim,*cre,*cim,*ldt,*dsk,*wg,*wu,*wd; float* out; char* ws; };

DEVI u32 f2u(float f){ return __builtin_bit_cast(u32,f); }
DEVI float u2f(u32 u){ return __builtin_bit_cast(float,u); }
DEVI u32 rfl(u32 v){ return __builtin_amdgcn_readfirstlane(v); }
DEVI bool all_eq(u32 v,u32 t){ return __builtin_amdgcn_ballot_w64(v!=t)==0ull; }
DEVI v4u mksrd(const void* p, u32 bytes){ u64 a=(u64)p; v4u s; s.x=rfl((u32)a); s.y=rfl((u32)(a>>32)); s.z=bytes; s.w=0x00020000u; return s; }
#define NTM 0
#define POL1 ""
#define POL2 ""
#define POL3 ""
#define POL4 ""
#define DM0 "s_add_u32 m0, %[b], %[i]\n\ts_nop 0\n\t"
#define DLD(n,off,pol) "buffer_load_dwordx4 %[v" #n "], %[r], %[s] offen" off pol " lds\n\t"
template<bool SC1,u32 IMM> DEVI void dma4(v4u srd,u32 v0,u32 v1,u32 v2,u32 v3,u32 soff,u32 base){
  if(SC1) asm volatile(DM0 DLD(0,""," sc1") DLD(1," offset:1024"," sc1") DLD(2," offset:2048"," sc1") DLD(3," offset:3072"," sc1")
      :: [v0]"v"(v0),[v1]"v"(v1),[v2]"v"(v2),[v3]"v"(v3),[r]"s"(srd),[s]"s"(soff),[b]"s"(base),[i]"n"(IMM) : "memory","scc");
  else asm volatile(DM0 DLD(0,"",POL4) DLD(1," offset:1024",POL4) DLD(2," offset:2048",POL4) DLD(3," offset:3072",POL4)
      :: [v0]"v"(v0),[v1]"v"(v1),[v2]"v"(v2),[v3]"v"(v3),[r]"s"(srd),[s]"s"(soff),[b]"s"(base),[i]"n"(IMM) : "memory","scc");
}
template<u32 IMM> DEVI void dma3(v4u srd,u32 v0,u32 v1,u32 v2,u32 soff,u32 base){
  asm volatile(DM0 DLD(0,"",POL3) DLD(1," offset:1024",POL3) DLD(2," offset:2048",POL3)
      :: [v0]"v"(v0),[v1]"v"(v1),[v2]"v"(v2),[r]"s"(srd),[s]"s"(soff),[b]"s"(base),[i]"n"(IMM) : "memory","scc");
}
template<u32 IMM> DEVI void dma2(v4u srd,u32 v0,u32 v1,u32 soff,u32 base){
  asm volatile(DM0 DLD(0,"",POL2) DLD(1," offset:1024",POL2)
      :: [v0]"v"(v0),[v1]"v"(v1),[r]"s"(srd),[s]"s"(soff),[b]"s"(base),[i]"n"(IMM) : "memory","scc");
}
template<bool SC1> DEVI void dma_dw(v4u srd,u32 voff,u32 soff,u32 lds){
  if(SC1) asm volatile("s_mov_b32 m0, %3\n\ts_nop 0\n\tbuffer_load_dword %0, %1, %2 offen sc1 lds" :: "v"(voff), "s"(srd), "s"(soff), "s"(lds) : "memory");
  else    asm volatile("s_mov_b32 m0, %3\n\ts_nop 0\n\tbuffer_load_dword %0, %1, %2 offen" POL1 " lds" :: "v"(voff), "s"(srd), "s"(soff), "s"(lds) : "memory");
}
DEVI void dma_x4(v4u srd,u32 voff,u32 soff,u32 lds){
  asm volatile("s_mov_b32 m0, %3\n\ts_nop 0\n\tbuffer_load_dwordx4 %0, %1, %2 offen sc1 lds" :: "v"(voff), "s"(srd), "s"(soff), "s"(lds) : "memory");
}
template<int N> DEVI void waitv(){ asm volatile("s_waitcnt vmcnt(%0)"::"n"(N):"memory"); }
DEVI void waitl0(){ asm volatile("s_waitcnt lgkmcnt(0)":::"memory"); }
DEVI void bar(){ asm volatile("":::"memory"); __builtin_amdgcn_s_barrier(); asm volatile("":::"memory"); SB; }
template<u32 OFF> DEVI void sig(u32 vaddr,u32 one){ asm volatile("s_mov_b64 exec, 1\n\tds_add_u32 %0, %1 offset:%2\n\ts_mov_b64 exec, -1" :: "v"(vaddr), "v"(one), "n"(OFF) : "memory"); }
template<u32 OFF> DEVI void sigm(u32 vaddr){ asm volatile("s_mov_b64 exec, 1\n\tds_inc_u32 %0, %0 offset:%1\n\ts_mov_b64 exec, -1" :: "v"(vaddr), "n"(OFF) : "memory"); }
DEVI u32 peek(u32 vaddr){ return *(volatile LAS u32*)vaddr; }
DEVI bool vm_idle(){ u32 v; asm volatile("s_getreg_b32 %0, hwreg(HW_REG_IB_STS, 0, 4)":"=s"(v)); return v==0u; }
DEVI void flag_st(v4u srd,u32 soff,u32 val){ asm volatile("s_mov_b64 exec, 1\n\tbuffer_store_dword %0, off, %1, %2 sc1\n\ts_mov_b64 exec, -1" :: "v"(val), "s"(srd), "s"(soff) : "memory"); }
DEVI int lane_now(){ int l; asm volatile("v_mbcnt_lo_u32_b32 %0, -1, 0\n\tv_mbcnt_hi_u32_b32 %0, -1, %0":"=v"(l)); return l; }
DEVI float b2f(u16 v){ return u2f(((u32)v)<<16); }
DEVI u32 pk2(float lo,float hi){ v2f p={lo,hi}; return __builtin_bit_cast(u32,__builtin_convertvector(p,v2bf)); }
DEVI float rbf(float f){ return u2f(pk2(f,f)<<16); }
DEVI float relu(float x){ int i=__builtin_bit_cast(int,x); return __builtin_bit_cast(float,i>0?i:0); }
DEVI float dot2(u32 a,u32 b,float c){ return __builtin_amdgcn_fdot2_f32_bf16(__builtin_bit_cast(v2bf,a),__builtin_bit_cast(v2bf,b),c,false); }
DEVI v16f mfma(v4u a,v4u b,v16f c){ return __builtin_amdgcn_mfma_f32_32x32x16_bf16(__builtin_bit_cast(v8bf,a),__builtin_bit_cast(v8bf,b),c,0,0,0); }
DEVI v4f mfma16(v4u a,v4u b,v4f c){ return __builtin_amdgcn_mfma_f32_16x16x32_bf16(__builtin_bit_cast(v8bf,a),__builtin_bit_cast(v8bf,b),c,0,0,0); }
DEVI v4u ldsr128(u32 a){ return *(LAS v4u*)a; }
DEVI v4u ldstr(u32 a0,u32 a1){
  v4s x=__builtin_amdgcn_ds_read_tr16_b64_v4i16((LAS v4s*)a0);
  v4s y=__builtin_amdgcn_ds_read_tr16_b64_v4i16((LAS v4s*)a1);
  return __builtin_bit_cast(v4u,__builtin_shufflevector(x,y,0,1,2,3,4,5,6,7));
}
DEVI void sw16(float& a,float& b){ v2u r=__builtin_amdgcn_permlane16_swap(f2u(a),f2u(b),false,false); a=u2f(r[0]); b=u2f(r[1]); }
DEVI void sw32(float& a,float& b){ v2u r=__builtin_amdgcn_permlane32_swap(f2u(a),f2u(b),false,false); a=u2f(r[0]); b=u2f(r[1]); }
template<int CTRL> DEVI float dppadd(float v){ return v+u2f(__builtin_amdgcn_update_dpp(0u,f2u(v),CTRL,0xf,0xf,true)); }
DEVI void wsum4(float& a,float& b,float& c,float& d){
  sw32(a,b); sw32(c,d); float v=a+b, t=c+d;
  sw16(v,t); v+=t;
  v=dppadd<0xB1>(v); v=dppadd<0x4E>(v); v=dppadd<0x141>(v); v=dppadd<0x140>(v);
  a=u2f(__builtin_amdgcn_readlane(f2u(v),0)); c=u2f(__builtin_amdgcn_readlane(f2u(v),16));
  b=u2f(__builtin_amdgcn_readlane(f2u(v),32)); d=u2f(__builtin_amdgcn_readlane(f2u(v),48));
}
struct cf{ float r,i; };
DEVI cf cmul(cf a,cf b){ return cf{__builtin_fmaf(-a.i,b.i,a.r*b.r), __builtin_fmaf(a.i,b.r,a.r*b.i)}; }
DEVI cf cfma(cf a,cf b,cf c){ return cf{__builtin_fmaf(-a.i,b.i,__builtin_fmaf(a.r,b.r,c.r)), __builtin_fmaf(a.i,b.r,__builtin_fmaf(a.r,b.i,c.i))}; }
DEVI cf csel(bool c,cf a,cf b){ return cf{c?a.r:b.r, c?a.i:b.i}; }
DEVI cf ldsc(u32 a){ v2f v=*(LAS v2f*)a; return cf{v[0],v[1]}; }
DEVI void sincos_(float x,float& s,float& c){
  const float n=__builtin_rintf(x*0.636619772f);
  float r=__builtin_fmaf(n,-1.57079601e+00f,x); r=__builtin_fmaf(n,-3.13916473e-07f,r); r=__builtin_fmaf(n,-5.39030253e-15f,r);
  const float z=r*r;
  float ps=__builtin_fmaf(__builtin_fmaf(__builtin_fmaf(2.86567956e-6f,z,-1.98559923e-4f),z,8.33338592e-3f),z,-1.66666672e-1f);
  ps=__builtin_fmaf(ps*z,r,r);
  float pc=__builtin_fmaf(__builtin_fmaf(__builtin_fmaf(2.44677067e-5f,z,-1.38877297e-3f),z,4.16666567e-2f),z,-0.5f);
  pc=__builtin_fmaf(pc,z,1.f);
  const int q=(int)n;
  const float a=(q&1)?pc:ps, b=(q&1)?ps:pc;
  s=(q&2)?-a:a; c=((q+1)&2)?-b:b;
}
typedef __amdgpu_buffer_rsrc_t rsrc_t;
DEVI rsrc_t mkrsrc(const void* p,u32 bytes){ return __builtin_amdgcn_make_buffer_rsrc((void*)p,0,bytes,0x00020000); }
DEVI void st128(v4u v,rsrc_t r,u32 off){ __builtin_amdgcn_raw_buffer_store_b128(v,r,off,0,16); }
DEVI u32 mktag(u32 e){ return (e*0x9E3779B1u+0x7F4A7C15u)|1u; }

#define STAMP(k) do{}while(0)
#define CSTAMP(k) do{}while(0)
#define TRACE(k) do{}while(0)
#define STAMP_DECL do{}while(0)
#define BAR() bar()
#define STAMP_FLUSH() do{}while(0)
#define TRARG
#define TRPASS
#define TSARG
#define TSPASS

DEVI void poll32(v4u srd,u32 voff,u32 soff,u32 tag){
  #pragma unroll 1
  for(int it=0;it<SPIN_CAP;++it){
    u32 v;
    asm volatile("buffer_load_dword %0, %1, %2, %3 offen sc1\n\ts_waitcnt vmcnt(0)" : "=&v"(v) : "v"(voff), "s"(srd), "s"(soff) : "memory");
    if(all_eq(v,tag)) break;
    __builtin_amdgcn_s_sleep(12);
  }
}
template<int I,int N,class F> DEVI void sfor(F&& f){ if constexpr(I<N){ f(std::integral_constant<int,I>{}); sfor<I+1,N>(f); } }
AI constexpr u32 wslot(int c){ return WB+(c&3)*WSZ; }
AI constexpr u32 pa_(int c){ return (c&3)==3?WB+5*XS:(((c&3)+2)%3)*XS; }
AI constexpr u32 pb_(int c){ return WB+(c%5)*XS; }
AI constexpr u32 sx_(int c){ return c>=5?(c-5)*XS:wslot(c+2); }
AI constexpr u32 sb_(int c){ return c>=5?wslot(3)+(c-5)*8192:wslot(c+2)+16384; }

#define SG(m,n) __builtin_amdgcn_sched_group_barrier(m,n,0)
template<int NB>
DEVI u32 g_main(v4u x0,v4u x1,u32 e,rsrc_t rws,u32 lbase,int lane,int gw,int tt,int cg,u32 hqo TRARG TSARG){
  const int h=lane>>5, r=lane&31, rh=gw&1, u0=(gw>>1)*NB;
  const int row=tt*128+cg*8+gw*2;
  v16f acc[3][2]; v4f cw[3];
  u32 ax[4],aw,aw2;
  const u32 on2=((lane&3)==((lane>>4)&1))?0x3f803f80u:0u;
  v4u ones={on2,on2,on2,on2};
  u32 tag;
  {
    u32 ab=lbase+sb_(0)+r*128+((((2*gw+h)^((r>>1)&7)))<<4);
    v4f c0={},c1={};
    sfor<0,8>([&](auto C) AI {
      constexpr int c=C.value;
      if constexpr(c!=0 || !LNFIRST){ waitl0(); BAR(); }
      v4u t0,t1;
      if constexpr(c!=0 || !LNFIRST){ t0=ldsr128(ab); t1=ldsr128(ab+4096); }
      if constexpr(c==0){
        const float z0=u2f(rfl(x0[0])<<16), z1=u2f(rfl(x1[0])<<16);
        float s0=0.f,s1=0.f,v0=0.f,v1=0.f;
        #pragma unroll
        for(int k=0;k<8;++k){
          const float d0=u2f((k&1)?x0[k>>1]&0xffff0000u:x0[k>>1]<<16)-z0, d1=u2f((k&1)?x1[k>>1]&0xffff0000u:x1[k>>1]<<16)-z1;
          s0+=d0; s1+=d1; v0=__builtin_fmaf(d0,d0,v0); v1=__builtin_fmaf(d1,d1,v1); }
        wsum4(s0,s1,v0,v1);
        const float sm=(lane?s1:s0)*(1.f/512.f);
        const u32 vm=pk2(relu(__builtin_fmaf(lane?v1:v0,1.f/512.f,-sm*sm)),sm+(lane?z1:z0));
        const float rs=__builtin_amdgcn_rcpf(rbf(__builtin_amdgcn_sqrtf(rbf(u2f(vm<<16)+1e-5f))));
        tag=mktag(rfl(e));
        if(lane<2) st128(v4u{f2u(rs),tag,vm&0xffff0000u,tag},rws,WS_FX+row*64+lane*16);
        if(gw==0) *(LAS v2u*)(lbase+L_TAG)=v2u{tag,e};
        STAMP(1);
        if(LNFIRST){ waitl0(); BAR(); t0=ldsr128(ab); t1=ldsr128(ab+4096); }
      }
      if constexpr(c<7) ab+=sb_(c+1)-sb_(c);
      if constexpr(c>=1 && c<=2*NB){ acc[(c-1)>>1][(c-1)&1]=v16f{}; asm volatile("":"+v"(acc[(c-1)>>1][(c-1)&1])); }
      c0=mfma16(ones,t0,c0); c1=mfma16(ones,t1,c1);
      asm volatile("":"+v"(c0),"+v"(c1),"+v"(ab));
    });
    asm volatile("s_nop 7\n\ts_nop 1":"+v"(c0),"+v"(c1));
    const bool hi=lane&16;
    *(LAS float*)(lbase+L_CSP+(gw*64+lane)*4)=h?(hi?c1[1]:c1[0]):(hi?c0[1]:c0[0]);
    STAMP(2);
    waitl0(); BAR();
  }
  #pragma unroll
  for(int s=0;s<4;++s) ax[s]=lbase+rh*8192+r*128+((((2*(s^(2*rh))+h)^((r>>1)&7)))<<4);
  aw=lbase+WB+((lane>>4)&1)*(12288+32)+(8*h+((lane&15)>>2))*192+(lane&3)*8+u0*32;
  aw2=aw+6144-rh*6144; aw+=rh*6144;
  #pragma unroll
  for(int t=0;t<3;++t){ cw[t]=v4f{}; if(t<NB) asm volatile("":"+v"(cw[t])); }
  asm volatile("":"+v"(ax[0]),"+v"(ax[1]),"+v"(ax[2]),"+v"(ax[3]),"+v"(aw),"+v"(aw2));
  BAR();
  __builtin_amdgcn_s_setprio(2);
  v4u X[2][2],T[2][3];
  auto rd=[&](int b,int s) AI {
    const u32 ad=(s<2?aw:aw2)+(s&1)*3072;
    X[b][0]=ldsr128(ax[s]);
    #pragma unroll
    for(int t=0;t<NB;++t){ T[b][t]=ldstr(ad+t*32,ad+t*32+768); if(t==0) X[b][1]=ldsr128(ax[s]+4096); }
  };
  auto mm=[&](int b,bool cs) AI {
    #pragma unroll
    for(int t=0;t<NB;++t){ acc[t][0]=mfma(T[b][t],X[b][0],acc[t][0]); if(cs) cw[t]=mfma16(ones,T[b][t],cw[t]); acc[t][1]=mfma(T[b][t],X[b][1],acc[t][1]); }
  };
  auto mix=[&](int nv,bool cs) AI {
    SG(0x8,1); SG(0x100,2); if(cs) SG(0x8,2); else SG(0x8,1); SG(0x100,2); SG(0x8,1); SG(0x100,2);
    if(NB==3){ if(cs) SG(0x8,2); else SG(0x8,1); SG(0x100,2); }
    SG(0x8,1); if(nv) SG(0x2,3);
    SG(0x8,5);
  };
  u32 d0=XS, d1=XS, d2=(u32)(-2*(int)XS);
  STAMP(3); CSTAMP(6);
  rd(0,0);
  #pragma unroll 1
  for(int c=0;c<8;++c){
    const u32 dw=((c&3)==3)?(u32)(-3*(int)WSZ):WSZ;
    SB; rd(1,1); mm(0,true); ax[0]+=d0; ax[1]+=d0; aw+=dw; mix(1,true);
    SB; asm volatile("":"+v"(ax[0]),"+v"(ax[1]),"+v"(aw));
    rd(0,2); mm(1,true); mix(0,true);
    SB; rd(1,3); mm(0,false); ax[2]+=d0; ax[3]+=d0; aw2+=dw; mix(1,false);
    SB; asm volatile("":"+v"(ax[2]),"+v"(ax[3]),"+v"(aw2));
    { const u32 t=d0; d0=d1; d1=d2; d2=t; }
    if(c==7){
      const int ln=lane_now();
      #pragma unroll
      for(int t=0;t<NB;++t){ const float lo=cw[t][0], hi=cw[t][1]; cw[t][0]=(ln&16)?hi:lo; }
      *(LAS v4f*)(lbase+L_CW+(gw*32+(ln&31))*16)=v4f{cw[0][0],cw[1][0],cw[2][0],0.f};
    }
    waitl0(); BAR();
    rd(0,0); mm(1,false); mix(0,false);
  }
  const int ln=lane_now();
  const v4u pc=*(LAS v4u*)(lbase+L_CW+((gw^1)*32+(ln&31))*16);
  const u32 hm=(ln&32)?0u:0xffffffffu;
  v4u M[2];
  #pragma unroll
  for(int m=0;m<2;++m){ const u32 mu=peek(lbase+L_TAB+((rh*2+m)*32+16*((ln>>2)&1)+8*((ln>>4)&1)+4*((ln>>3)&1)+(ln&3))*16+8);
    const u32 nm=(((mu>>16)|mu)^0x80008000u)&hm; M[m]=v4u{nm,nm,0u,0u}; }
  #pragma unroll
  for(int t=0;t<NB;++t){
    const float c=cw[t][0]+u2f(pc[t]);
    const u32 c0=f2u(c)&0xffff0000u; const float r1=c-u2f(c0);
    const u32 c1=f2u(r1)&0xffff0000u; const float r2=r1-u2f(c1);
    const v4u F={(c0>>16)|c1,f2u(r2)>>16,0u,0u};
    acc[t][0]=mfma(F,M[0],acc[t][0]); acc[t][1]=mfma(F,M[1],acc[t][1]);
  }
  STAMP(4); CSTAMP(7);
  #pragma unroll
  for(int t=0;t<NB;++t)
    #pragma unroll
    for(int m=0;m<2;++m){
      v4u v;
      #pragma unroll
      for(int k=0;k<4;++k) v[k]=pk2(relu(acc[t][m][2*k])*acc[t][m][2*k+8],relu(acc[t][m][2*k+1])*acc[t][m][2*k+9]);
      st128(v,rws,hqo+(u0+t)*2048+m*1024+ln*16);
    }
  return tag;
}

__global__ NOPK __launch_bounds__(768,3) void fused_k(Args a){
  __shared__ __attribute__((aligned(256))) char smem[L_TOTAL];
  const u32 lbase=(u32)(size_t)(LAS char*)smem;
  const int tid=__builtin_amdgcn_workitem_id_x();
  const int wgid=__builtin_amdgcn_workgroup_id_x(), w=rfl(tid>>6);
  int lane=tid&63;
  const int xcd=wgid&7, idx=wgid>>3;
  if(CODEPF && w==CODEPF-1){
    u64 pc; u32 back; asm volatile("s_getpc_b64 %0\n.Lpcx%=:\n\ts_mov_b32 %1, .Lpcx%=-_Z7fused_k4Args":"=&s"(pc),"=s"(back));
    dma_dw<false>(mksrd((const void*)(pc-back),CODE_LEN),(u32)(((idx&3)*64+lane)*128),0,lbase+L_DUM);
  }
  const int tt=(xcd&1)*8+(idx&7), cg=(xcd>>1)*4+(idx>>3);
  const int nb=((cg&3)==3||cg==14)?2:3, blk0=11*(cg>>2)+3*(cg&3)-(cg==15?1:0);
  const int rt2=(xcd&3)*8+(idx&7), ct=(xcd>>2)*4+(idx>>3);
  const int ry=ROTP?2*(idx>>3):0, rz=ROTP?(11*(idx>>3))>>2:0;
  const u32 one=1;
  STAMP_DECL; STAMP(0); CSTAMP(12);

  v4u x0,x1; u32 ep0;
  if(w<4){ const int c_=cg*32+(lane&31); x0[0]=a.lre[c_]; x0[1]=a.lim[c_]; x0[2]=a.ldt[c_]; }
  else if(w<8){ const int row=tt*128+cg*8+(w-4)*2; x0=((const v4u*)a.x)[row*64+lane]; x1=((const v4u*)a.x)[row*64+64+lane];
    ep0=__builtin_amdgcn_raw_buffer_load_b32(mkrsrc(a.ws,WS_END),0,WS_EPOCH,16); }
  asm volatile("":::"memory");
  if(__builtin_expect(w>=8,1)){
    __builtin_amdgcn_s_setprio(3);
    const int lw=w-8, j=lane>>3, l7=lane&7;
    const v4u sx=mksrd(a.x,2048*1024);
    const v4u sb=mksrd(lw<2?a.bre:a.bim,512*1024);
    const u32 sz0=(u32)((l7^(j>>1))<<4), sz1=sz0^64u;
    const u32 xr=(tt*128+32*lw+16*(j>>2)+(j&3))*1024;
    const u32 vx0=xr+sz0, vx1=xr+3*1024+sz1, vx2=xr+6*1024+sz0, vx3=xr+9*1024+sz1;
    const u32 br=(cg*32+16*(lw&1)+j)*1024;
    const u32 vb0=br+sz0, vb1=br+7*1024+sz1;
    const u32 l4=lbase+lw*4096, l2=lbase+lw*2048;
    u32 kx=((tt*(ROT1&15)+cg*(ROT1>>4))&7)*128, kw=(kx+640)&0x380;
    #define SX(c) dma4<false,sx_(c)>(sx,vx0,vx1,vx2,vx3,kx,l4)
    #define SBB(c) do{ dma2<sb_(c)>(sb,vb0,vb1,kx,l2); kx=(kx+128)&0x380; }while(0)
    SX(0); SBB(0); SX(1); SBB(1);
    const v4u sws=mksrd(a.ws,WS_END);
    const v4u sw=mksrd(lw<2?a.wg:a.wu,512*2752);
    const v4u swd=mksrd(a.wd,1376*1024);
    const v4u sc=mksrd(lw<2?a.cre:a.cim,512*1024);
    const u32 vf=(lane&15)*128, lf=lbase+L_FLG+lw*256;
    const u32 fx=WS_FX+(tt*64+lw*16)*128, fh=WS_FH+(rt2*32+(lw&1)*16)*128, fhh=WS_FHH+rt2*4096;
    u32 vw[6];
    #pragma unroll
    for(int i=0;i<3;++i){ const u32 q=i*1024+lane*16, row=q/192, col=q%192; const bool ok=col<(u32)nb*64;
      vw[i]=ok?(row+32*(lw&1))*2752+col+blk0*64-i*1024:OOB; vw[i+3]=vw[i]+16*2752; asm volatile("":"+v"(vw[i]),"+v"(vw[i+3])); }
    const u32 lwv=lbase+(lw>>1)*(12288+32)+(lw&1)*6144;
    #define GWA(c) dma3<wslot(c)>(sw,vw[0],vw[1],vw[2],kw*1376,lwv)
    #define GWB(c) do{ dma3<wslot(c)+3072>(sw,vw[3],vw[4],vw[5],kw*1376,lwv); kw=(kw+128)&0x380; }while(0)
    #define GX(c) do{ dma4<false,((c)%3)*XS>(sx,vx0,vx1,vx2,vx3,kx,l4); kx=(kx+128)&0x380; }while(0)
    SX(2); waitv<10>(); BAR();
    SBB(2); SX(3); waitv<10>(); BAR();
    SBB(3); SX(4); waitv<10>(); BAR();
    SBB(4); SX(5); waitv<10>(); BAR();
    SBB(5); SX(6); SBB(6); waitv<12>(); BAR();
    SX(7); SBB(7); waitv<12>(); BAR();
    STAMP(2);
    const u32 vs=(lane>>1)*128+(lane&1)*16, lt=lbase+L_TAB+lw*512;
    const u32 tag=rfl(peek(lbase+L_TAG));
    if(lane<32) dma_x4(sws,vs,fx,lt);
    GWA(0); waitv<10>(); BAR();
    GWB(0); waitv<7>(); BAR();
    waitv<6>();
    #pragma unroll 1
    for(int it=0;it<SPIN_CAP;++it){
      const v4u t=*(volatile LAS v4u*)(lt+(lane&31)*16);
      if(all_eq((t[1]^tag)|(t[3]^tag),0u)) break;
      __builtin_amdgcn_s_sleep(2);
      if(lane<32) dma_x4(sws,vs,fx,lt);
      waitv<0>();
    }
    BAR();
    GWA(1); GWB(1);
    if(WARMP){
      dma_dw<false>(sc,lane<32?(u32)(j*1024+l7*128):OOB,(ct*64+(idx&7)*8+(lw&1)*4)*1024,lbase+L_DUM);
      dma_dw<false>(swd,lane<43?(u32)(lane*1024):OOB,((idx&7)*172+lw*43)*1024+ct*128,lbase+L_DUM);
    }
    waitv<WARMP?8:6>(); BAR();
    STAMP(3);
    GWA(2); GWB(2); GWA(3); GWB(3);
    if(LLCPF && nb==2){
      const u32 li=((cg<12?(cg>>2):cg-11)*16+tt)*2+(lw&1);
      dma_dw<false>(sc,lane<26?(li*26+lane)*128:OOB,0,lbase+L_DUM);
      dma_dw<false>(swd,lane<35?((li*2+(lw>>1))*35+lane)*128:OOB,0,lbase+L_DUM);
    }
    u32 va=WS_HS+rt2*131072+lw*4096+lane*16;
    const u32 cr=(ct*64+(lw&1)*32+j)*1024;
    u32 vp0=cr+sz0, vp1=cr+7*1024+sz1, vp2=cr+14*1024+sz0, vp3=cr+21*1024+sz1;
    const int gp0=tt-1-4*lw-(lane>>5);
    const u32 vg0=gp0>=0?(u32)(WS_GRAN+gp0*8192+(cg*32+(lane&31))*16):OOB, vg1=gp0>=2?vg0-2*8192-1024:OOB;
    #define SNAP() asm volatile("s_add_u32 m0, %[b], %[i]\n\ts_nop 0\n\tbuffer_load_dwordx4 %[v0], %[r], 0 offen sc1 lds\n\tbuffer_load_dwordx4 %[v1], %[r], 0 offen offset:1024 sc1 lds" \
        :: [v0]"v"(vg0),[v1]"v"(vg1),[r]"s"(sws),[b]"s"(l2),[i]"n"(L_GR) : "memory","scc")
    waitv<WARMP?14:12>(); BAR();
    GX(3); GWA(4); GWB(4); SNAP(); waitv<18>(); BAR();
    GX(4); GWA(5); GWB(5); waitv<RELAX?18:10>(); SNAP();
    if(WARMB) dma_dw<false>(sc,lane<32?(u32)(j*1024+l7*128):OOB,(ct*64+(idx&7)*8+(lw&1)*4)*1024,lbase+L_DUM);
    BAR();
    GX(5); GWA(6); GWB(6); waitv<RELAX?18:WARMB?11:10>(); SNAP(); BAR();
    GX(6); GWA(7); GWB(7); waitv<RELAX?18:10>(); BAR();
    u32 ka=ry, kb=ry;
    auto PA=[&](auto C) AI {
      constexpr int c=C.value;
      if constexpr(c==8){ va=WS_HH+rt2*86*2048+lw*4096+lane*16; ka=rz; }
      u32 so;
      if constexpr(c<8){ so=ka<<14; ka=(ka+1)&7; } else { so=(ka==10&&lw==3)?OOB:ka<<14; ka= ka==10?0:ka+1; }
      dma4<true,pa_(c)>(sws,va,va,va,va,so,l4);
    };
    auto PB=[&](auto C) AI {
      constexpr int c=C.value;
      if constexpr(c==8){ vp0=(lw*32+j)*1024+ct*128+((l7^(((j>>1)&1)<<2))<<4); vp1=vp0+7*1024; vp2=vp0+14*1024; vp3=vp0+21*1024; kb=rz; }
      u32 so;
      if constexpr(c<8){ so=kb<<7; kb=(kb+1)&7; } else { so=kb<<17; kb= kb==10?0:kb+1; }
      dma4<false,pb_(c)>(c<8?sc:swd,vp0,vp1,vp2,vp3,so,l4);
    };
    #define IC(n) std::integral_constant<int,(n)>{}
    GX(7); PB(IC(0));
    waitv<14>(); BAR();
    PB(IC(1)); PB(IC(2));
    waitv<12>(); BAR();
    STAMP(4);
    dma_dw<true>(sws,vf,fh,lf);
    PB(IC(3));
    waitv<4>();
    const bool early=all_eq(peek(lf+lane*4),tag);
    if(early){ PA(IC(0)); PA(IC(1)); }
    BAR();
    STAMP(5);
    if(!early){ poll32(sws,vf,fh,tag); PA(IC(0)); PA(IC(1)); }
    STAMP(6);
    if(!LATEA){ PA(IC(2)); PA(IC(3)); PB(IC(4)); }
    waitv<LATEA?4:16>(); BAR();
    if(LATEA){ PA(IC(2)); PA(IC(3)); PB(IC(4)); }
    if(WARMB) dma_dw<false>(swd,lane<43?(u32)(lane*1024):OOB,((idx&7)*172+lw*43)*1024+ct*128,lbase+L_DUM);
    if(lane<16) dma_x4(sws,vs,WS_FX+(rt2*32+lw*8)*128,lbase+L_TAB+lw*256);
    { const u32 vt=(rt2*64+lw*16+j)*1024+ct*128+l7*16; dma2<L_XT>(sx,vt,vt+7*1024,0,l2); }
    waitv<WARMB?16:15>(); BAR();
    waitv<WARMB?12:11>(); BAR();
    bool zok=false;
    sfor<2,18>([&](auto K) AI {
      constexpr int k=K.value;
      if constexpr(k==5) dma_dw<true>(sws,(lane&31)*128,fhh,lf);
      if constexpr(k==9){ if(wgid==0 && lw==0) dma_dw<true>(sws,vf*64,WS_FH,lf); }
      if constexpr(k==6){
        waitv<8>();
        zok=all_eq(peek(lf+lane*4),tag);
        if(zok) PA(IC(8));
        PB(IC(9));
        if(zok) waitv<12>(); else waitv<8>();
      } else if constexpr(k==7){
        STAMP(7);
        if(!zok){ poll32(sws,(lane&31)*128,fhh,tag); PA(IC(8)); }
        STAMP(8);
        PA(IC(9)); PB(IC(10));
        if(zok) waitv<12>(); else waitv<8>();
      } else {
        if constexpr(k<=16) PA(IC(k+2));
        if constexpr(k<=15) PB(IC(k+3));
        waitv<(k==2?(WARMB?16:15):k<=15?12:k==16?8:0)>();
      }
      if constexpr(k==12){
        if(wgid==0 && lw==0){
          if(!all_eq(peek(lf+lane*4),tag)) poll32(sws,vf*64,WS_FH,tag);
          const u32 e=peek(lbase+L_TAG+4)+1u;
          asm volatile("s_mov_b64 exec, 1\n\tbuffer_store_dword %0, off, %1, 0 sc1\n\ts_mov_b64 exec, -1" :: "v"(e), "s"(sws) : "memory");
        }
      }
      BAR();
    });
    STAMP(11);
    STAMP_FLUSH();
    return;
  }

  int h=lane>>5, r=lane&31;
  const rsrc_t rws=mkrsrc(a.ws,WS_END);
  const v4u sws=mksrd(a.ws,WS_END);
  u32 pend=0;
  u32 tag;
  auto try_flag=[&](bool force) AI {
    if(pend && (force || vm_idle())){
      waitv<0>();
      flag_st(sws,pend,tag);
      pend=0;
    }
  };
  if(w>=4){
    const int gw=w-4, rt=2*tt+(w&1);
    __builtin_amdgcn_s_setprio(3);
    const u32 hqo=WS_HH+(rt*86+2*blk0)*2048;
    if(nb==3) tag=g_main<3>(x0,x1,ep0,rws,lbase,lane,gw,tt,cg,hqo TRPASS TSPASS); else tag=g_main<2>(x0,x1,ep0,rws,lbase,lane,gw,tt,cg,hqo TRPASS TSPASS);
    pend=WS_FHH+(rt*32+cg*2+(gw>>1))*128;
    STAMP(5);
  } else {
    const int ch=cg*32+r;
    __builtin_amdgcn_s_setprio(SPRIO);
    const u16 hlr=(u16)x0[0], hli=(u16)x0[1], hdt=(u16)x0[2];
    *(LAS v4u*)(lbase+L_GR+w*2048+lane*16)=v4u{0u,0u,0u,0u}; *(LAS v4u*)(lbase+L_GR+w*2048+1024+lane*16)=v4u{0u,0u,0u,0u};
    const float lr=b2f(hlr), li=b2f(hli), dt=rbf(__builtin_expf(b2f(hdt)));
    cf lam,coef;
    { const float ex=__builtin_expf(dt*lr); float sn,cs_; sincos_(dt*li,sn,cs_); lam=cf{ex*cs_,ex*sn};
      const float dd=1.f/(lr*lr+li*li); coef=cmul(cf{lam.r-1.f,lam.i},cf{lr*dd,-li*dd}); }
    cf l16=cmul(lam,lam); l16=cmul(l16,l16); l16=cmul(l16,l16); l16=cmul(l16,l16);
    cf l32=cmul(l16,l16);
    cf l256,wq,pw;
    { const cf l64=cmul(l32,l32), l128=cmul(l64,l64), o={1.f,0.f};
      l256=cmul(l128,l128); wq=csel(h,l128,o); pw=cmul(csel(w&1,l32,o),csel(w&2,l64,o)); }
    asm volatile("":"+v"(lam.r),"+v"(lam.i),"+v"(coef.r),"+v"(coef.i),"+v"(l16.r),"+v"(l16.i),"+v"(l32.r),"+v"(l32.i));
    asm volatile("":"+v"(l256.r),"+v"(l256.i),"+v"(wq.r),"+v"(wq.i),"+v"(pw.r),"+v"(pw.i));
    v16f are={},aim={};
    {
      u32 ax[4],ab[4];
      #pragma unroll
      for(int s=0;s<4;++s){ const u32 t=lbase+r*128+((((2*s+h)^((r>>1)&7)))<<4); ab[s]=t+sb_(0); ax[s]=t+sx_(0)+w*4096; }
      v4u X[2][2],T[2][2][2];
      auto rd=[&](int b) AI {
        #pragma unroll
        for(int s=0;s<2;++s){ X[b][s]=ldsr128(ax[2*b+s]); T[b][s][0]=ldsr128(ab[2*b+s]); T[b][s][1]=ldsr128(ab[2*b+s]+4096); } };
      auto mm=[&](int b) AI {
        #pragma unroll
        for(int s=0;s<2;++s){ are=mfma(X[b][s],T[b][s][0],are); aim=mfma(X[b][s],T[b][s][1],aim); }
        asm volatile("":"+v"(are),"+v"(aim)); };
      STAMP(1);
      waitl0(); BAR();
      rd(0);
      sfor<0,8>([&](auto C) AI {
        constexpr int c=C.value;
        SB; rd(1);
        SB; mm(0);
        if constexpr(c<7){
          #pragma unroll
          for(int s=0;s<4;++s){ ax[s]+=sx_(c+1)-sx_(c); ab[s]+=sb_(c+1)-sb_(c); asm volatile("":"+v"(ax[s]),"+v"(ab[s])); }
          SB;
          waitl0(); BAR();
          rd(0);
        }
        SB; mm(1);
      });
    }
    STAMP(2);
    waitl0(); BAR();
    __builtin_amdgcn_s_setprio(3);
    tag=rfl(peek(lbase+L_TAG));
    cf cs={0.f,0.f};
    #pragma unroll
    for(int k=0;k<4;++k){ const u32 b=lbase+L_CSP+(k*64+r)*4; cs.r+=*(LAS float*)b; cs.i+=*(LAS float*)(b+128); }
    int left=8;
    #define SBAR() do{ if(left>0){ BAR(); --left; } else __builtin_amdgcn_s_sleep(4); }while(0)
    cf uu[16]; cf ea={0.f,0.f};
    #pragma unroll
    for(int k=0;k<16;++k){
      const v4f st=*(LAS v4f*)(lbase+L_TAB+(32*w+16*h+k)*16);
      const cf v={__builtin_fmaf(-st[2],cs.r,are[k])*st[0],__builtin_fmaf(-st[2],cs.i,aim[k])*st[0]};
      uu[k]=v; ea=cfma(lam,ea,v);
    }
    cf elo=ea,ehi=ea; sw32(elo.r,ehi.r); sw32(elo.i,ehi.i);
    { const cf ew=cfma(l16,elo,ehi); *(LAS v2f*)(lbase+L_AGG+(w*32+r)*8)=v2f{ew.r,ew.i}; }
    STAMP(3);
    waitl0(); BAR();
    cf Q;
    { const u32 b=lbase+L_AGG+r*8;
      const cf a0=ldsc(b), a1=ldsc(b+256), a2=ldsc(b+512), a3=ldsc(b+768);
      const cf Q2=cfma(l32,a0,a1), Q3=cfma(l32,Q2,a2);
      if(w==3){ const cf E=cfma(l32,Q3,a3); if(lane<32) st128(v4u{f2u(E.r),tag,f2u(E.i),tag},rws,ch*16+WS_GRAN+tt*8192); }
      Q= w==0?cf{0.f,0.f}: w==1?a0: w==2?Q2:Q3; }
    STAMP(4);
    #pragma unroll
    for(int k=0;k<16;++k){ uu[k]=cmul(coef,uu[k]); asm volatile("":"+v"(uu[k].r),"+v"(uu[k].i)); if(k==CSPLIT) SBAR(); }
    cf H={0.f,0.f};
    if(tt>0){
      SBAR();
      v4u gr[8];
      #pragma unroll 1
      for(int it=0;it<SPIN_CAP;++it){
        if(it<3){
          SBAR();
          #pragma unroll
          for(int i=0;i<8;++i) gr[i]=*(volatile LAS v4u*)(lbase+L_GR+lane*16+i*1024);
        } else {
          #pragma unroll
          for(int i=0;i<8;++i){ const int pd=tt-1-2*i-h; gr[i]=__builtin_amdgcn_raw_buffer_load_b128(rws,pd>=0?(u32)(pd*8192+ch*16):OOB,WS_GRAN,16); }
          SBAR();
        }
        u32 bad=0;
        #pragma unroll
        for(int i=0;i<8;++i) bad|=((gr[i][1]^tag)|(gr[i][3]^tag))&~(u32)((tt-1-2*i-h)>>31);
        if(all_eq(bad,0u)) break;
      }
      #pragma unroll
      for(int i=7;i>=0;--i) H=cfma(l256,H,cf{u2f(gr[i][0]),u2f(gr[i][2])});
      H=cmul(H,wq);
      cf h2=H; sw32(H.r,h2.r); sw32(H.i,h2.i); H.r+=h2.r; H.i+=h2.i;
    }
    STAMP(5);
    cf hc=cfma(pw,H,Q);
    hc=cmul(coef,csel(h,cfma(l16,hc,elo),hc));
    const int rt=2*tt+(w>>1);
    const u32 hso=WS_HS+rt*131072+(cg>>1)*16384+(((cg&1)*2+(r>>4))*2+(w&1))*1024+h*512;
    #pragma unroll
    for(int m=0;m<2;++m){
      if(left>0 && (m==0 || FSPLIT)){ asm volatile("":"+v"(hc.r),"+v"(hc.i)); BAR(); --left; }
      cf hv[8];
      #pragma unroll
      for(int jj=0;jj<8;++jj){ hc=cfma(lam,hc,uu[8*m+jj]); hv[jj]=hc; }
      const u32 o=hso+m*256+(((lane&15)^(8*h+4*m))<<4);
      st128(v4u{pk2(hv[0].r,hv[1].r),pk2(hv[2].r,hv[3].r),pk2(hv[4].r,hv[5].r),pk2(hv[6].r,hv[7].r)},rws,o);
      st128(v4u{pk2(-hv[0].i,-hv[1].i),pk2(-hv[2].i,-hv[3].i),pk2(-hv[4].i,-hv[5].i),pk2(-hv[6].i,-hv[7].i)},rws,o+8192);
    }
    pend=WS_FH+(rt*32+(cg&1)*16+(cg>>1)*2+(w&1))*128;
    STAMP(6);
    #pragma unroll 1
    while(left>0){
      BAR(); --left;
      #pragma unroll 1
      for(int it=0;it<FWIN && pend;++it){ try_flag(false); if(pend) __builtin_amdgcn_s_sleep(1); else STAMP(7); }
    }
    if(pend){ try_flag(true); STAMP(7); }
  }
  lane=lane_now(); h=lane>>5; r=lane&31;
  const int g=lane>>4, m2=w&1, tc=(w>>1)&1, kh=w>>2;
  const int dcol=ct*64+tc*32+r;
  const float dsk=b2f(a.dsk[dcol]);
  v16f ay={},az={};
  const int q=(lane&15)>>2, p=lane&3, b4=g&1, gp=(b4^kh)+2*(p&1);
  u32 a0=lbase+pa_(0)+(kh*8+m2)*1024+(gp*16+((8*h+q)^(4*gp)))*16+8*(p>>1);
  u32 b0=lbase+pb_(0)+kh*8192+(tc*32+r)*128+((h^((r>>1)&7))<<4);
  u32 za=lbase+pa_(8)+(kh*8+m2)*1024+(lane^(kh*16))*16;
  u32 zb=lbase+pb_(8)+(64*kh+4*h+q)*128+((tc*64+b4*32+8*p)^(((q>>1)&1)<<6));
  u32 d0=pa_(1)-pa_(0), d1=pa_(2)-pa_(1), d2=pa_(3)-pa_(2), d3=pa_(0)-pa_(3), e0=XS; int eb=0;
  v4u A[2][4],B[2][4];
  auto rot=[&]() AI { const u32 t=d0; d0=d1; d1=d2; d2=d3; d3=t; eb= eb==4?0:eb+1; e0= eb==4?(u32)(-4*(int)XS):XS; };
  auto rdy=[&](int b) AI {
    #pragma unroll
    for(int s=0;s<4;++s){ A[b][s]=ldstr(a0+s*2048,(a0^64)+s*2048); B[b][s]=ldsr128(b0^(s*32)); }
    a0+=d0; b0+=e0; rot();
  };
  auto rdz=[&](int b) AI {
    #pragma unroll
    for(int s=0;s<4;++s){ A[b][s]=ldsr128(za+s*2048); B[b][s]=ldstr(zb+s*2048,zb+s*2048+1024); }
    za+=d0; zb+=e0; rot();
  };
  auto mm4=[&](int b,v16f& c) AI {
    #pragma unroll
    for(int s=0;s<4;++s) c=mfma(A[b][s],B[b][s],c);
    #pragma unroll
    for(int s=0;s<4;++s){ SG(0x8,1); SG(0x100,3); }
    SB; asm volatile("":"+v"(c));
  };
  #define NXT() BAR()
  const int tk=32*m2+16*h+8*kh;
  STAMP(8);
  __builtin_amdgcn_s_setprio(0);
  NXT(); rdy(0);
  STAMP(9);
  #pragma unroll 1
  for(int c=0;c<3;++c){
    NXT(); rdy(1); mm4(0,ay); try_flag(false);
    NXT(); rdy(0); mm4(1,ay); try_flag(c==2);
  }
  NXT(); rdy(1); mm4(0,ay);
  NXT(); rdz(0); mm4(1,ay);
  STAMP(10);
  float sv[8], xf[8], r2[8];
  #pragma unroll 1
  for(int c=0;c<5;++c){
    NXT(); rdz(1); mm4(0,az);
    if(c==1){
      #pragma unroll
      for(int k=0;k<8;++k){
        const v4f st=*(LAS v4f*)(lbase+L_TAB+(tk+k)*16);
        xf[k]=b2f(*(LAS u16*)(lbase+L_XT+(tk+k)*128+(tc*32+r)*2));
        sv[k]=(xf[k]-st[2])*st[0]*dsk; r2[k]=st[0]*st[0];
      }
    }
    NXT(); rdz(0); mm4(1,az);
  }
  {
    const u32 xo=lbase+WB+w*4096+lane*16, xi=lbase+WB+(w^4)*4096+lane*16;
    *(LAS v4f*)xo=v4f{ay[8],ay[9],ay[10],ay[11]}; *(LAS v4f*)(xo+1024)=v4f{ay[12],ay[13],ay[14],ay[15]};
    #pragma unroll
    for(int k=0;k<8;++k) sv[k]=__builtin_fmaf(2.f,ay[k],sv[k]);
    SB;
    #pragma unroll
    for(int s=0;s<4;++s) az=mfma(A[0][s],B[0][s],az);
    STAMP(11);
    *(LAS v4f*)(xo+2048)=v4f{az[8],az[9],az[10],az[11]}; *(LAS v4f*)(xo+3072)=v4f{az[12],az[13],az[14],az[15]};
    waitl0(); bar();
    const v4f i2=*(LAS v4f*)(xi+2048), i3=*(LAS v4f*)(xi+3072), i0=*(LAS v4f*)xi, i1=*(LAS v4f*)(xi+1024);
    const rsrc_t ro=mkrsrc(a.out,2048*512*4);
    const u32 oo=((rt2*64+tk)*512+dcol)*4;
    #pragma unroll
    for(int k=0;k<8;++k){
      const float z=(az[k]+(k<4?i2[k&3]:i3[k&3]))*r2[k];
      const float gt=z*__builtin_amdgcn_rcpf(1.f+__builtin_amdgcn_exp2f(z*-1.44269504f));
      __builtin_amdgcn_raw_buffer_store_b32(f2u(__builtin_fmaf(__builtin_fmaf(2.f,k<4?i0[k&3]:i1[k&3],sv[k]),gt,xf[k])),ro,oo+k*2048,0,OUTAUX);
    }
  }
  CSTAMP(14); STAMP(13); STAMP_FLUSH();
}

extern "C" void kernel_launch(void* const* d_in, const int* in_sizes, int n_in,
                              void* d_out, int out_size, void* d_ws, size_t ws_size,
                              hipStream_t stream) {
  Args a;
  a.x=(const u16*)d_in[0]; a.lre=(const u16*)d_in[1]; a.lim=(const u16*)d_in[2];
  a.bre=(const u16*)d_in[3]; a.bim=(const u16*)d_in[4]; a.cre=(const u16*)d_in[5]; a.cim=(const u16*)d_in[6];
  a.ldt=(const u16*)d_in[7]; a.dsk=(const u16*)d_in[8];
  a.wg=(const u16*)d_in[10]; a.wu=(const u16*)d_in[11]; a.wd=(const u16*)d_in[12];
  a.out=(float*)d_out; a.ws=(char*)d_ws;
  fused_k<<<256,768,0,stream>>>(a);
}
```

```cpp
#include <hip/hip_runtime.h>
#include <hip/hip_bf16.h>
#include <type_traits>

typedef unsigned u32; typedef unsigned long long u64; typedef unsigned short u16;
typedef u32 v4u __attribute__((ext_vector_type(4)));
typedef u32 v2u __attribute__((ext_vector_type(2)));
typedef short v4s __attribute__((ext_vector_type(4)));
typedef __bf16 v2bf __attribute__((ext_vector_type(2)));
typedef __bf16 v8bf __attribute__((ext_vector_type(8)));
typedef float v2f __attribute__((ext_vector_type(2)));
typedef float v4f __attribute__((ext_vector_type(4)));
typedef float v16f __attribute__((ext_vector_type(16)));
#define LAS __attribute__((address_space(3)))
#define DEVI __device__ __forceinline__
#define AI __attribute__((always_inline))
#define SB __builtin_amdgcn_sched_barrier(0)
#if defined(__HIP_DEVICE_COMPILE__)
#define NOPK __attribute__((target("no-packed-fp32-ops")))
#else
#define NOPK
#endif

#define ROT1 0x01
#define ROTP 1
#define SPRIO 2
#define OUTAUX 16
#define HSLEEP 2
#define FSPLIT 0
#define FWIN 10
#define LATEA 0
#define LLCPF 0
#define LNFIRST 1
#define WARMP 1
#define RELAX 1
#define TX 760
#define ROTI 1
#define TPOS 0
#define CWIN 0
#define RY 1
#define RZ 2
#define CSPLIT 5
#define LBSLEEP 6
#define WARMB 0

constexpr u32 WS_EPOCH = 0;
constexpr u32 WS_FX    = 32768;
constexpr u32 WS_FH    = WS_FX + 131072;
constexpr u32 WS_FHH   = WS_FH + 131072;
constexpr u32 WS_GRAN  = WS_FHH + 131072;
constexpr u32 WS_HH    = WS_GRAN + 131072;
constexpr u32 WS_HS    = WS_HH + 32*86*2048;
constexpr u32 WS_END   = WS_HS + 32*131072;
constexpr u32 WS_STAMP = 16u<<20;
constexpr u32 XS = 16384;
constexpr u32 WB = 49152, WSZ = 24832;
constexpr u32 L_XT = WB+4*WSZ;
constexpr u32 L_TAB  = L_XT+8192;
constexpr u32 L_CSP  = L_TAB + 2048;
constexpr u32 L_FLG  = L_CSP;
constexpr u32 L_CW   = L_CSP + 1024;
constexpr u32 L_AGG  = L_CW + 2048;
constexpr u32 L_DUM  = L_AGG + 1024;
constexpr u32 L_TAG  = L_DUM + 256;
constexpr u32 L_GR   = L_XT;
constexpr u32 L_TOTAL = 163840;
static_assert(L_TAG+16<=L_TOTAL,"lds");
constexpr u32 OOB = 0x40000000u;
constexpr int SPIN_CAP = 1<<18;

#define CODEPF 12
constexpr u32 CODE_LEN=$n;
struct Args { const u16 *x,*lre,*lim,*bre,*bim,*cre,*cim,*ldt,*dsk,*wg,*wu,*wd; float* out; char* ws; };

DEVI u32 f2u(float f){ return __builtin_bit_cast(u32,f); }
DEVI float u2f(u32 u){ return __builtin_bit_cast(float,u); }
DEVI u32 rfl(u32 v){ return __builtin_amdgcn_readfirstlane(v); }
DEVI bool all_eq(u32 v,u32 t){ return __builtin_amdgcn_ballot_w64(v!=t)==0ull; }
DEVI v4u mksrd(const void* p, u32 bytes){ u64 a=(u64)p; v4u s; s.x=rfl((u32)a); s.y=rfl((u32)(a>>32)); s.z=bytes; s.w=0x00020000u; return s; }
#define NTM 0
#define POL1 ""
#define POL2 ""
#define POL3 ""
#define POL4 ""
#define DM0 "s_add_u32 m0, %[b], %[i]\n\ts_nop 0\n\t"
#define DLD(n,off,pol) "buffer_load_dwordx4 %[v" #n "], %[r], %[s] offen" off pol " lds\n\t"
template<bool SC1,u32 IMM> DEVI void dma4(v4u srd,u32 v0,u32 v1,u32 v2,u32 v3,u32 soff,u32 base){
  if(SC1) asm volatile(DM0 DLD(0,""," sc1") DLD(1," offset:1024"," sc1") DLD(2," offset:2048"," sc1") DLD(3," offset:3072"," sc1")
      :: [v0]"v"(v0),[v1]"v"(v1),[v2]"v"(v2),[v3]"v"(v3),[r]"s"(srd),[s]"s"(soff),[b]"s"(base),[i]"n"(IMM) : "memory","scc");
  else asm volatile(DM0 DLD(0,"",POL4) DLD(1," offset:1024",POL4) DLD(2," offset:2048",POL4) DLD(3," offset:3072",POL4)
      :: [v0]"v"(v0),[v1]"v"(v1),[v2]"v"(v2),[v3]"v"(v3),[r]"s"(srd),[s]"s"(soff),[b]"s"(base),[i]"n"(IMM) : "memory","scc");
}
template<u32 IMM> DEVI void dma3(v4u srd,u32 v0,u32 v1,u32 v2,u32 soff,u32 base){
  asm volatile(DM0 DLD(0,"",POL3) DLD(1," offset:1024",POL3) DLD(2," offset:2048",POL3)
      :: [v0]"v"(v0),[v1]"v"(v1),[v2]"v"(v2),[r]"s"(srd),[s]"s"(soff),[b]"s"(base),[i]"n"(IMM) : "memory","scc");
}
template<u32 IMM> DEVI void dma2(v4u srd,u32 v0,u32 v1,u32 soff,u32 base){
  asm volatile(DM0 DLD(0,"",POL2) DLD(1," offset:1024",POL2)
      :: [v0]"v"(v0),[v1]"v"(v1),[r]"s"(srd),[s]"s"(soff),[b]"s"(base),[i]"n"(IMM) : "memory","scc");
}
template<bool SC1> DEVI void dma_dw(v4u srd,u32 voff,u32 soff,u32 lds){
  if(SC1) asm volatile("s_mov_b32 m0, %3\n\ts_nop 0\n\tbuffer_load_dword %0, %1, %2 offen sc1 lds" :: "v"(voff), "s"(srd), "s"(soff), "s"(lds) : "memory");
  else    asm volatile("s_mov_b32 m0, %3\n\ts_nop 0\n\tbuffer_load_dword %0, %1, %2 offen" POL1 " lds" :: "v"(voff), "s"(srd), "s"(soff), "s"(lds) : "memory");
}
DEVI void dma_x4(v4u srd,u32 voff,u32 soff,u32 lds){
  asm volatile("s_mov_b32 m0, %3\n\ts_nop 0\n\tbuffer_load_dwordx4 %0, %1, %2 offen sc1 lds" :: "v"(voff), "s"(srd), "s"(soff), "s"(lds) : "memory");
}
template<int N> DEVI void waitv(){ asm volatile("s_waitcnt vmcnt(%0)"::"n"(N):"memory"); }
DEVI void waitl0(){ asm volatile("s_waitcnt lgkmcnt(0)":::"memory"); }
DEVI void bar(){ asm volatile("":::"memory"); __builtin_amdgcn_s_barrier(); asm volatile("":::"memory"); SB; }
template<u32 OFF> DEVI void sig(u32 vaddr,u32 one){ asm volatile("s_mov_b64 exec, 1\n\tds_add_u32 %0, %1 offset:%2\n\ts_mov_b64 exec, -1" :: "v"(vaddr), "v"(one), "n"(OFF) : "memory"); }
template<u32 OFF> DEVI void sigm(u32 vaddr){ asm volatile("s_mov_b64 exec, 1\n\tds_inc_u32 %0, %0 offset:%1\n\ts_mov_b64 exec, -1" :: "v"(vaddr), "n"(OFF) : "memory"); }
DEVI u32 peek(u32 vaddr){ return *(volatile LAS u32*)vaddr; }
DEVI bool vm_idle(){ u32 v; asm volatile("s_getreg_b32 %0, hwreg(HW_REG_IB_STS, 0, 4)":"=s"(v)); return v==0u; }
DEVI void flag_st(v4u srd,u32 soff,u32 val){ asm volatile("s_mov_b64 exec, 1\n\tbuffer_store_dword %0, off, %1, %2 sc1\n\ts_mov_b64 exec, -1" :: "v"(val), "s"(srd), "s"(soff) : "memory"); }
DEVI int lane_now(){ int l; asm volatile("v_mbcnt_lo_u32_b32 %0, -1, 0\n\tv_mbcnt_hi_u32_b32 %0, -1, %0":"=v"(l)); return l; }
DEVI float b2f(u16 v){ return u2f(((u32)v)<<16); }
DEVI u32 pk2(float lo,float hi){ v2f p={lo,hi}; return __builtin_bit_cast(u32,__builtin_convertvector(p,v2bf)); }
DEVI float rbf(float f){ return u2f(pk2(f,f)<<16); }
DEVI float relu(float x){ int i=__builtin_bit_cast(int,x); return __builtin_bit_cast(float,i>0?i:0); }
DEVI float dot2(u32 a,u32 b,float c){ return __builtin_amdgcn_fdot2_f32_bf16(__builtin_bit_cast(v2bf,a),__builtin_bit_cast(v2bf,b),c,false); }
DEVI v16f mfma(v4u a,v4u b,v16f c){ return __builtin_amdgcn_mfma_f32_32x32x16_bf16(__builtin_bit_cast(v8bf,a),__builtin_bit_cast(v8bf,b),c,0,0,0); }
DEVI v4f mfma16(v4u a,v4u b,v4f c){ return __builtin_amdgcn_mfma_f32_16x16x32_bf16(__builtin_bit_cast(v8bf,a),__builtin_bit_cast(v8bf,b),c,0,0,0); }
DEVI v4u ldsr128(u32 a){ return *(LAS v4u*)a; }
DEVI v4u ldstr(u32 a0,u32 a1){
  v4s x=__builtin_amdgcn_ds_read_tr16_b64_v4i16((LAS v4s*)a0);
  v4s y=__builtin_amdgcn_ds_read_tr16_b64_v4i16((LAS v4s*)a1);
  return __builtin_bit_cast(v4u,__builtin_shufflevector(x,y,0,1,2,3,4,5,6,7));
}
DEVI void sw16(float& a,float& b){ v2u r=__builtin_amdgcn_permlane16_swap(f2u(a),f2u(b),false,false); a=u2f(r[0]); b=u2f(r[1]); }
DEVI void sw32(float& a,float& b){ v2u r=__builtin_amdgcn_permlane32_swap(f2u(a),f2u(b),false,false); a=u2f(r[0]); b=u2f(r[1]); }
template<int CTRL> DEVI float dppadd(float v){ return v+u2f(__builtin_amdgcn_update_dpp(0u,f2u(v),CTRL,0xf,0xf,true)); }
DEVI void wsum4(float& a,float& b,float& c,float& d){
  sw32(a,b); sw32(c,d); float v=a+b, t=c+d;
  sw16(v,t); v+=t;
  v=dppadd<0xB1>(v); v=dppadd<0x4E>(v); v=dppadd<0x141>(v); v=dppadd<0x140>(v);
  a=u2f(__builtin_amdgcn_readlane(f2u(v),0)); c=u2f(__builtin_amdgcn_readlane(f2u(v),16));
  b=u2f(__builtin_amdgcn_readlane(f2u(v),32)); d=u2f(__builtin_amdgcn_readlane(f2u(v),48));
}
typedef v2f cf;
DEVI cf cfma(cf a,cf b,cf c){ const cf t=__builtin_elementwise_fma(cf{a.x,a.x},b,c); return __builtin_elementwise_fma(cf{-a.y,a.y},cf{b.y,b.x},t); }
DEVI cf cmul(cf a,cf b){ const cf t=cf{a.x,a.x}*b; return __builtin_elementwise_fma(cf{-a.y,a.y},cf{b.y,b.x},t); }
DEVI cf csel(bool c,cf a,cf b){ return cf{c?a.x:b.x, c?a.y:b.y}; }
DEVI cf ldsc(u32 a){ return *(LAS v2f*)a; }
DEVI void csw32(cf& a,cf& b){ float x=a.x,y=b.x,z=a.y,t=b.y; sw32(x,y); sw32(z,t); a=cf{x,z}; b=cf{y,t}; }
DEVI void sincos_(float x,float& s,float& c){
  const float n=__builtin_rintf(x*0.636619772f);
  float r=__builtin_fmaf(n,-1.57079601e+00f,x); r=__builtin_fmaf(n,-3.13916473e-07f,r); r=__builtin_fmaf(n,-5.39030253e-15f,r);
  const float z=r*r;
  float ps=__builtin_fmaf(__builtin_fmaf(__builtin_fmaf(2.86567956e-6f,z,-1.98559923e-4f),z,8.33338592e-3f),z,-1.66666672e-1f);
  ps=__builtin_fmaf(ps*z,r,r);
  float pc=__builtin_fmaf(__builtin_fmaf(__builtin_fmaf(2.44677067e-5f,z,-1.38877297e-3f),z,4.16666567e-2f),z,-0.5f);
  pc=__builtin_fmaf(pc,z,1.f);
  const int q=(int)n;
  const float a=(q&1)?pc:ps, b=(q&1)?ps:pc;
  s=(q&2)?-a:a; c=((q+1)&2)?-b:b;
}
typedef __amdgpu_buffer_rsrc_t rsrc_t;
DEVI rsrc_t mkrsrc(const void* p,u32 bytes){ return __builtin_amdgcn_make_buffer_rsrc((void*)p,0,bytes,0x00020000); }
DEVI void st128(v4u v,rsrc_t r,u32 off){ __builtin_amdgcn_raw_buffer_store_b128(v,r,off,0,16); }
DEVI u32 mktag(u32 e){ return (e*0x9E3779B1u+0x7F4A7C15u)|1u; }

#define STAMP(k) do{}while(0)
#define CSTAMP(k) do{}while(0)
#define TRACE(k) do{}while(0)
#define STAMP_DECL do{}while(0)
#define BAR() bar()
#define STAMP_FLUSH() do{}while(0)
#define TRARG
#define TRPASS
#define TSARG
#define TSPASS

DEVI void poll32(v4u srd,u32 voff,u32 soff,u32 tag){
  #pragma unroll 1
  for(int it=0;it<SPIN_CAP;++it){
    u32 v;
    asm volatile("buffer_load_dword %0, %1, %2, %3 offen sc1\n\ts_waitcnt vmcnt(0)" : "=&v"(v) : "v"(voff), "s"(srd), "s"(soff) : "memory");
    if(all_eq(v,tag)) break;
    __builtin_amdgcn_s_sleep(12);
  }
}
template<int I,int N,class F> DEVI void sfor(F&& f){ if constexpr(I<N){ f(std::integral_constant<int,I>{}); sfor<I+1,N>(f); } }
AI constexpr u32 wslot(int c){ return WB+(c&3)*WSZ; }
AI constexpr u32 pa_(int c){ return (c&3)==3?WB+5*XS:(((c&3)+2)%3)*XS; }
AI constexpr u32 pb_(int c){ return WB+(c%5)*XS; }
AI constexpr u32 sx_(int c){ return c>=5?(c-5)*XS:wslot(c+2); }
AI constexpr u32 sb_(int c){ return c>=5?wslot(3)+(c-5)*8192:wslot(c+2)+16384; }

#define SG(m,n) __builtin_amdgcn_sched_group_barrier(m,n,0)
template<int NB>
DEVI u32 g_main(v4u x0,v4u x1,u32 e,rsrc_t rws,u32 lbase,int lane,int gw,int tt,int cg,u32 hqo TRARG TSARG){
  const int h=lane>>5, r=lane&31, rh=gw&1, u0=(gw>>1)*NB;
  const int row=tt*128+cg*8+gw*2;
  v16f acc[3][2]; v4f cw[3];
  u32 ax[4],aw,aw2;
  const u32 on2=((lane&3)==((lane>>4)&1))?0x3f803f80u:0u;
  v4u ones={on2,on2,on2,on2};
  u32 tag;
  {
    u32 ab=lbase+sb_(0)+r*128+((((2*gw+h)^((r>>1)&7)))<<4);
    v4f c0={},c1={};
    sfor<0,8>([&](auto C) AI {
      constexpr int c=C.value;
      if constexpr(c!=0 || !LNFIRST){ waitl0(); BAR(); }
      v4u t0,t1;
      if constexpr(c!=0 || !LNFIRST){ t0=ldsr128(ab); t1=ldsr128(ab+4096); }
      if constexpr(c==0){
        const float z0=u2f(rfl(x0[0])<<16), z1=u2f(rfl(x1[0])<<16);
        float s0=0.f,s1=0.f,v0=0.f,v1=0.f;
        #pragma unroll
        for(int k=0;k<8;++k){
          const float d0=u2f((k&1)?x0[k>>1]&0xffff0000u:x0[k>>1]<<16)-z0, d1=u2f((k&1)?x1[k>>1]&0xffff0000u:x1[k>>1]<<16)-z1;
          s0+=d0; s1+=d1; v0=__builtin_fmaf(d0,d0,v0); v1=__builtin_fmaf(d1,d1,v1); }
        wsum4(s0,s1,v0,v1);
        const float sm=(lane?s1:s0)*(1.f/512.f);
        const u32 vm=pk2(relu(__builtin_fmaf(lane?v1:v0,1.f/512.f,-sm*sm)),sm+(lane?z1:z0));
        float rs=__builtin_amdgcn_rcpf(rbf(__builtin_amdgcn_sqrtf(rbf(u2f(vm<<16)+1e-5f))));
        asm volatile("":"+v"(rs));
        STAMP(1);
        if(LNFIRST==2){ BAR(); t0=ldsr128(ab); t1=ldsr128(ab+4096); }
        tag=mktag(rfl(e));
        if(lane<2) st128(v4u{f2u(rs),tag,vm&0xffff0000u,tag},rws,WS_FX+row*64+lane*16);
        if(gw==0) *(LAS v2u*)(lbase+L_TAG)=v2u{tag,e};
        if(LNFIRST==1){ waitl0(); BAR(); t0=ldsr128(ab); t1=ldsr128(ab+4096); }
      }
      if constexpr(c<7) ab+=sb_(c+1)-sb_(c);
      if constexpr(c>=1 && c<=2*NB){ acc[(c-1)>>1][(c-1)&1]=v16f{}; asm volatile("":"+v"(acc[(c-1)>>1][(c-1)&1])); }
      c0=mfma16(ones,t0,c0); c1=mfma16(ones,t1,c1);
      asm volatile("":"+v"(c0),"+v"(c1),"+v"(ab));
    });
    asm volatile("s_nop 7\n\ts_nop 1":"+v"(c0),"+v"(c1));
    const bool hi=lane&16;
    *(LAS float*)(lbase+L_CSP+(r*8+h*4+gw)*4)=h?(hi?c1[1]:c1[0]):(hi?c0[1]:c0[0]);
    STAMP(2);
    waitl0(); BAR();
  }
  #pragma unroll
  for(int s=0;s<4;++s) ax[s]=lbase+rh*8192+r*128+((((2*(s^(2*rh))+h)^((r>>1)&7)))<<4);
  aw=lbase+WB+((lane>>4)&1)*(12288+32)+(8*h+((lane&15)>>2))*192+(lane&3)*8+u0*32;
  aw2=aw+6144-rh*6144; aw+=rh*6144;
  #pragma unroll
  for(int t=0;t<3;++t){ cw[t]=v4f{}; if(t<NB) asm volatile("":"+v"(cw[t])); }
  asm volatile("":"+v"(ax[0]),"+v"(ax[1]),"+v"(ax[2]),"+v"(ax[3]),"+v"(aw),"+v"(aw2));
  BAR();
  __builtin_amdgcn_s_setprio(2);
  v4u X[2][2],T[2][3];
  auto rd=[&](int b,int s) AI {
    const u32 ad=(s<2?aw:aw2)+(s&1)*3072;
    X[b][0]=ldsr128(ax[s]);
    #pragma unroll
    for(int t=0;t<NB;++t){ T[b][t]=ldstr(ad+t*32,ad+t*32+768); if(t==0) X[b][1]=ldsr128(ax[s]+4096); }
  };
  auto mm=[&](int b,bool cs) AI {
    #pragma unroll
    for(int t=0;t<NB;++t){ acc[t][0]=mfma(T[b][t],X[b][0],acc[t][0]); if(cs) cw[t]=mfma16(ones,T[b][t],cw[t]); acc[t][1]=mfma(T[b][t],X[b][1],acc[t][1]); }
  };
  auto mix=[&](int nv,bool cs) AI {
    SG(0x8,1); SG(0x100,2); if(cs) SG(0x8,2); else SG(0x8,1); SG(0x100,2); SG(0x8,1); SG(0x100,2);
    if(NB==3){ if(cs) SG(0x8,2); else SG(0x8,1); SG(0x100,2); }
    SG(0x8,1); if(nv) SG(0x2,3);
    SG(0x8,5);
  };
  u32 d0=XS, d1=XS, d2=(u32)(-2*(int)XS);
  STAMP(3); CSTAMP(6);
  rd(0,0);
  #pragma unroll 1
  for(int c=0;c<8;++c){
    const u32 dw=((c&3)==3)?(u32)(-3*(int)WSZ):WSZ;
    SB; rd(1,1); mm(0,true); ax[0]+=d0; ax[1]+=d0; aw+=dw; mix(1,true);
    SB; asm volatile("":"+v"(ax[0]),"+v"(ax[1]),"+v"(aw));
    rd(0,2); mm(1,true); mix(0,true);
    SB; rd(1,3); mm(0,false); ax[2]+=d0; ax[3]+=d0; aw2+=dw; mix(1,false);
    SB; asm volatile("":"+v"(ax[2]),"+v"(ax[3]),"+v"(aw2));
    { const u32 t=d0; d0=d1; d1=d2; d2=t; }
    if(c==7){
      const int ln=lane_now();
      #pragma unroll
      for(int t=0;t<NB;++t){ const float lo=cw[t][0], hi=cw[t][1]; cw[t][0]=(ln&16)?hi:lo; }
      *(LAS v4f*)(lbase+L_CW+(gw*32+(ln&31))*16)=v4f{cw[0][0],cw[1][0],cw[2][0],0.f};
      const u32 tm=lbase+L_TAB+(rh*64+16*((ln>>2)&1)+8*((ln>>4)&1)+4*((ln>>3)&1)+(ln&3))*16+8;
      cw[0][2]=*(LAS float*)tm; cw[0][3]=*(LAS float*)(tm+512);
      ax[0]=lbase+L_CW+((gw^1)*32+(ln&31))*16;
    }
    waitl0(); BAR();
    rd(0,0); mm(1,false); mix(0,false);
  }
  const int ln=lane_now();
  const u32 hm=(ln&32)?0u:0xffffffffu;
  v4u M[2];
  #pragma unroll
  for(int m=0;m<2;++m){ const u32 mu=f2u(cw[0][2+m]), nm=(((mu>>16)|mu)^0x80008000u)&hm; M[m]=v4u{nm,nm,0u,0u}; }
  #pragma unroll
  for(int t=0;t<NB;++t){
    const float c=cw[t][0]+u2f(X[0][0][t]);
    const u32 c0=f2u(c)&0xffff0000u; const float r1=c-u2f(c0);
    const u32 c1=f2u(r1)&0xffff0000u; const float r2=r1-u2f(c1);
    const v4u F={(c0>>16)|c1,f2u(r2)>>16,0u,0u};
    acc[t][0]=mfma(F,M[0],acc[t][0]); acc[t][1]=mfma(F,M[1],acc[t][1]);
  }
  STAMP(4); CSTAMP(7);
  #pragma unroll
  for(int t=0;t<NB;++t)
    #pragma unroll
    for(int m=0;m<2;++m){
      SB;
      v4u v;
      #pragma unroll
      for(int k=0;k<4;++k) v[k]=pk2(relu(acc[t][m][2*k])*acc[t][m][2*k+8],relu(acc[t][m][2*k+1])*acc[t][m][2*k+9]);
      st128(v,rws,hqo+(u0+t)*2048+m*1024+ln*16);
    }
  return tag;
}

__global__ NOPK __launch_bounds__(768,3) void fused_k(Args a){
  __shared__ __attribute__((aligned(256))) char smem[L_TOTAL];
  const u32 lbase=(u32)(size_t)(LAS char*)smem;
  const int tid=__builtin_amdgcn_workitem_id_x();
  const int wgid=__builtin_amdgcn_workgroup_id_x(), w=rfl(tid>>6);
  int lane=tid&63;
  const int xcd=wgid&7, idx=wgid>>3;
  if(CODEPF && w==CODEPF-1){
    u64 pc; u32 back; asm volatile("s_getpc_b64 %0\n.Lpcx%=:\n\ts_mov_b32 %1, .Lpcx%=-_Z7fused_k4Args":"=&s"(pc),"=s"(back));
    dma_dw<false>(mksrd((const void*)(pc-back),CODE_LEN),(u32)(((idx&3)*64+lane)*128),0,lbase+L_DUM);
  }
  const int tt=(xcd&1)*8+(idx&7), cg=(xcd>>1)*4+(idx>>3);
  const int nb=((cg&3)==3||cg==14)?2:3, blk0=11*(cg>>2)+3*(cg&3)-(cg==15?1:0);
  const int rt2=(xcd&3)*8+(idx&7), ct=(xcd>>2)*4+(idx>>3);
  const int ry=((RY==4?2*(idx>>3):RY==2?4*((idx>>3)&1):0)+(idx&7))&7, rz=RZ==4?3*(idx>>3):RZ==2?6*((idx>>3)&1):RZ==3?(idx>>3):0;
  const u32 one=1;
  STAMP_DECL; STAMP(0); CSTAMP(12);

  v4u x0,x1; u32 ep0;
  if(w<4){ const int c_=cg*32+(lane&31); x0[0]=a.lre[c_]; x0[1]=a.lim[c_]; x0[2]=a.ldt[c_]; }
  else if(w<8){ const int row=tt*128+cg*8+(w-4)*2; x0=((const v4u*)a.x)[row*64+lane]; x1=((const v4u*)a.x)[row*64+64+lane];
    ep0=__builtin_amdgcn_raw_buffer_load_b32(mkrsrc(a.ws,WS_END),0,WS_EPOCH,16); }
  asm volatile("":::"memory");
  if(__builtin_expect(w>=8,1)){
    __builtin_amdgcn_s_setprio(3);
    const int lw=w-8, j=lane>>3, l7=lane&7;
    const v4u sx=mksrd(a.x,2048*1024);
    const v4u sb=mksrd(lw<2?a.bre:a.bim,512*1024);
    const u32 sz0=(u32)((l7^(j>>1))<<4), sz1=sz0^64u;
    const u32 xr=(tt*128+32*lw+16*(j>>2)+(j&3))*1024;
    const u32 vx0=xr+sz0, vx1=xr+3*1024+sz1, vx2=xr+6*1024+sz0, vx3=xr+9*1024+sz1;
    const u32 br=(cg*32+16*(lw&1)+j)*1024;
    const u32 vb0=br+sz0, vb1=br+7*1024+sz1;
    const u32 l4=lbase+lw*4096, l2=lbase+lw*2048;
    u32 kx=((tt*(ROT1&15)+cg*(ROT1>>4))&7)*128, kw=(kx+640)&0x380;
    #define SX(c) dma4<false,sx_(c)>(sx,vx0,vx1,vx2,vx3,kx,l4)
    #define SBB(c) do{ dma2<sb_(c)>(sb,vb0,vb1,kx,l2); kx=(kx+128)&0x380; }while(0)
    SX(0); SBB(0); SX(1); SBB(1);
    const v4u sws=mksrd(a.ws,WS_END);
    const v4u sw=mksrd(lw<2?a.wg:a.wu,512*2752);
    const v4u swd=mksrd(a.wd,1376*1024);
    const v4u sc=mksrd(lw<2?a.cre:a.cim,512*1024);
    const u32 vf=(lane&15)*128, lf=lbase+L_FLG+lw*256;
    const u32 fx=WS_FX+(tt*64+lw*16)*128, fh=WS_FH+(rt2*32+(lw&1)*16)*128, fhh=WS_FHH+rt2*4096;
    u32 vw[6];
    #pragma unroll
    for(int i=0;i<3;++i){ const u32 q=i*1024+lane*16, row=q/192, col=q%192; const bool ok=col<(u32)nb*64;
      vw[i]=ok?(row+32*(lw&1))*2752+col+blk0*64-i*1024:OOB; vw[i+3]=vw[i]+16*2752; asm volatile("":"+v"(vw[i]),"+v"(vw[i+3])); }
    const u32 lwv=lbase+(lw>>1)*(12288+32)+(lw&1)*6144;
    #define GWA(c) dma3<wslot(c)>(sw,vw[0],vw[1],vw[2],kw*1376,lwv)
    #define GWB(c) do{ dma3<wslot(c)+3072>(sw,vw[3],vw[4],vw[5],kw*1376,lwv); kw=(kw+128)&0x380; }while(0)
    #define GX(c) do{ dma4<false,((c)%3)*XS>(sx,vx0,vx1,vx2,vx3,kx,l4); kx=(kx+128)&0x380; }while(0)
    SX(2); waitv<10>(); BAR();
    SBB(2); SX(3); waitv<10>(); BAR();
    SBB(3); SX(4); waitv<10>(); BAR();
    SBB(4); SX(5); waitv<10>(); BAR();
    SBB(5); SX(6); SBB(6); waitv<12>(); BAR();
    SX(7); SBB(7); waitv<12>(); BAR();
    STAMP(2);
    const u32 vs=(lane>>1)*128+(lane&1)*16, lt=lbase+L_TAB+lw*512;
    const u32 tag=rfl(peek(lbase+L_TAG));
    if(lane<32) dma_x4(sws,vs,fx,lt);
    GWA(0); waitv<10>(); BAR();
    GWB(0); waitv<7>(); BAR();
    waitv<6>();
    #pragma unroll 1
    for(int it=0;it<SPIN_CAP;++it){
      const v4u t=*(volatile LAS v4u*)(lt+(lane&31)*16);
      if(all_eq((t[1]^tag)|(t[3]^tag),0u)) break;
      __builtin_amdgcn_s_sleep(2);
      if(lane<32) dma_x4(sws,vs,fx,lt);
      waitv<0>();
    }
    BAR();
    if(TPOS==1){
      dma_dw<false>(sc,(lane<32&&!(ROTI&1))?(u32)(j*1024+l7*128):OOB,(ct*64+(idx&7)*8+(lw&1)*4)*1024,lbase+L_DUM);
      dma_dw<false>(swd,(lane<43&&!(ROTI&2))?(u32)(lane*1024):OOB,((idx&7)*172+lw*43)*1024+ct*128,lbase+L_DUM);
    }
    GWA(1); GWB(1);
    if(TPOS==0){
      dma_dw<false>(sc,(lane<32&&!(ROTI&1))?(u32)(j*1024+l7*128):OOB,(ct*64+(idx&7)*8+(lw&1)*4)*1024,lbase+L_DUM);
      dma_dw<false>(swd,(lane<43&&!(ROTI&2))?(u32)(lane*1024):OOB,((idx&7)*172+lw*43)*1024+ct*128,lbase+L_DUM);
    }
    waitv<WARMP?8:6>(); BAR();
    STAMP(3);
    GWA(2); GWB(2); GWA(3); GWB(3);
    if(LLCPF && nb==2){
      const u32 li=((cg<12?(cg>>2):cg-11)*16+tt)*2+(lw&1);
      dma_dw<false>(sc,lane<26?(li*26+lane)*128:OOB,0,lbase+L_DUM);
      dma_dw<false>(swd,lane<35?((li*2+(lw>>1))*35+lane)*128:OOB,0,lbase+L_DUM);
    }
    u32 va=WS_HS+rt2*131072+lw*4096+lane*16;
    const u32 cr=(ct*64+(lw&1)*32+j)*1024;
    u32 vp0=cr+sz0, vp1=cr+7*1024+sz1, vp2=cr+14*1024+sz0, vp3=cr+21*1024+sz1;
    const int gp0=tt-1-4*lw-(lane>>5);
    const u32 vg0=gp0>=0?(u32)(WS_GRAN+gp0*8192+(cg*32+(lane&31))*16):OOB, vg1=gp0>=2?vg0-2*8192-1024:OOB;
    #define SNAP() asm volatile("s_add_u32 m0, %[b], %[i]\n\ts_nop 0\n\tbuffer_load_dwordx4 %[v0], %[r], 0 offen sc1 lds\n\tbuffer_load_dwordx4 %[v1], %[r], 0 offen offset:1024 sc1 lds" \
        :: [v0]"v"(vg0),[v1]"v"(vg1),[r]"s"(sws),[b]"s"(l2),[i]"n"(L_GR) : "memory","scc")
    waitv<TPOS==0?14:12>(); BAR();
    GX(3); GWA(4); GWB(4); SNAP(); waitv<18>(); BAR();
    GX(4); GWA(5); GWB(5); waitv<RELAX?18:10>(); SNAP();
    if(WARMB) dma_dw<false>(sc,lane<32?(u32)(j*1024+l7*128):OOB,(ct*64+(idx&7)*8+(lw&1)*4)*1024,lbase+L_DUM);
    BAR();
    GX(5); GWA(6); GWB(6); waitv<RELAX?18:WARMB?11:10>(); SNAP(); BAR();
    GX(6); GWA(7); GWB(7); waitv<RELAX?18:10>(); BAR();
    u32 ka=ry, kb=ry;
    auto PA=[&](auto C) AI {
      constexpr int c=C.value;
      if constexpr(c==8){ va=WS_HH+rt2*86*2048+lw*4096+lane*16; ka=rz; }
      u32 so;
      if constexpr(c<8){ so=ka<<14; ka=(ka+1)&7; } else { so=(ka==10&&lw==3)?OOB:ka<<14; ka= ka==10?0:ka+1; }
      dma4<true,pa_(c)>(sws,va,va,va,va,so,l4);
    };
    auto PB=[&](auto C) AI {
      constexpr int c=C.value;
      if constexpr(c==8){ vp0=(lw*32+j)*1024+ct*128+((l7^(((j>>1)&1)<<2))<<4); vp1=vp0+7*1024; vp2=vp0+14*1024; vp3=vp0+21*1024; kb=rz; }
      u32 so;
      if constexpr(c<8){ so=kb<<7; kb=(kb+1)&7; } else { so=kb<<17; kb= kb==10?0:kb+1; }
      dma4<false,pb_(c)>(c<8?sc:swd,vp0,vp1,vp2,vp3,so,l4);
    };
    #define IC(n) std::integral_constant<int,(n)>{}
    GX(7); PB(IC(0));
    waitv<14>(); BAR();
    PB(IC(1)); PB(IC(2));
    waitv<12>(); BAR();
    STAMP(4);
    dma_dw<true>(sws,vf,fh,lf);
    PB(IC(3));
    waitv<4>();
    const bool early=all_eq(peek(lf+lane*4),tag);
    if(early){ PA(IC(0)); PA(IC(1)); }
    BAR();
    STAMP(5);
    if(!early){ poll32(sws,vf,fh,tag); PA(IC(0)); PA(IC(1)); }
    STAMP(6);
    if(!LATEA){ PA(IC(2)); PA(IC(3)); PB(IC(4)); }
    waitv<LATEA?4:16>(); BAR();
    if(LATEA){ PA(IC(2)); PA(IC(3)); PB(IC(4)); }
    if(WARMB) dma_dw<false>(swd,lane<43?(u32)(lane*1024):OOB,((idx&7)*172+lw*43)*1024+ct*128,lbase+L_DUM);
    if(lane<16) dma_x4(sws,vs,WS_FX+(rt2*32+lw*8)*128,lbase+L_TAB+lw*256);
    { const u32 vt=(rt2*64+lw*16+j)*1024+ct*128+l7*16; dma2<L_XT>(sx,vt,vt+7*1024,0,l2); }
    waitv<WARMB?16:15>(); BAR();
    waitv<WARMB?12:11>(); BAR();
    bool zok=false;
    sfor<2,18>([&](auto K) AI {
      constexpr int k=K.value;
      if constexpr(k==5) dma_dw<true>(sws,(lane&31)*128,fhh,lf);
      if constexpr(k==9){ if(wgid==0 && lw==0) dma_dw<true>(sws,vf*64,WS_FH,lf); }
      if constexpr(k==6){
        waitv<8>();
        zok=all_eq(peek(lf+lane*4),tag);
        if(zok) PA(IC(8));
        PB(IC(9));
        if(zok) waitv<12>(); else waitv<8>();
      } else if constexpr(k==7){
        STAMP(7);
        if(!zok){ poll32(sws,(lane&31)*128,fhh,tag); PA(IC(8)); }
        STAMP(8);
        PA(IC(9)); PB(IC(10));
        if(zok) waitv<12>(); else waitv<8>();
      } else {
        if constexpr(k<=16) PA(IC(k+2));
        if constexpr(k<=15) PB(IC(k+3));
        waitv<(k==2?(WARMB?16:15):k<=15?12:k==16?8:0)>();
      }
      if constexpr(k==12){
        if(wgid==0 && lw==0){
          if(!all_eq(peek(lf+lane*4),tag)) poll32(sws,vf*64,WS_FH,tag);
          const u32 e=peek(lbase+L_TAG+4)+1u;
          asm volatile("s_mov_b64 exec, 1\n\tbuffer_store_dword %0, off, %1, 0 sc1\n\ts_mov_b64 exec, -1" :: "v"(e), "s"(sws) : "memory");
        }
      }
      BAR();
    });
    STAMP(11);
    STAMP_FLUSH();
    return;
  }

  int h=lane>>5, r=lane&31;
  const rsrc_t rws=mkrsrc(a.ws,WS_END);
  const v4u sws=mksrd(a.ws,WS_END);
  u32 pend=0;
  u32 tag;
  auto try_flag=[&](bool force) AI {
    if(pend && (force || vm_idle())){
      waitv<0>();
      flag_st(sws,pend,tag);
      pend=0;
    }
  };
  if(w>=4){
    const int gw=w-4, rt=2*tt+(w&1);
    __builtin_amdgcn_s_setprio(3);
    const u32 hqo=WS_HH+(rt*86+2*blk0)*2048;
    if(nb==3) tag=g_main<3>(x0,x1,ep0,rws,lbase,lane,gw,tt,cg,hqo TRPASS TSPASS); else tag=g_main<2>(x0,x1,ep0,rws,lbase,lane,gw,tt,cg,hqo TRPASS TSPASS);
    pend=WS_FHH+(rt*32+cg*2+(gw>>1))*128;
    STAMP(5);
  } else {
    const int ch=cg*32+r;
    __builtin_amdgcn_s_setprio(SPRIO);
    const u16 hlr=(u16)x0[0], hli=(u16)x0[1], hdt=(u16)x0[2];
    *(LAS v4u*)(lbase+L_GR+w*2048+lane*16)=v4u{0u,0u,0u,0u}; *(LAS v4u*)(lbase+L_GR+w*2048+1024+lane*16)=v4u{0u,0u,0u,0u};
    const float lr=b2f(hlr), li=b2f(hli), dt=rbf(__builtin_expf(b2f(hdt)));
    cf lam,coef;
    { const float ex=__builtin_expf(dt*lr); float sn,cs_; sincos_(dt*li,sn,cs_); lam=cf{ex*cs_,ex*sn};
      const float dd=1.f/(lr*lr+li*li); coef=cmul(cf{lam.x-1.f,lam.y},cf{lr*dd,-li*dd}); }
    cf l16=cmul(lam,lam); l16=cmul(l16,l16); l16=cmul(l16,l16); l16=cmul(l16,l16);
    cf l32=cmul(l16,l16);
    cf l256,wq,pw;
    { const cf l64=cmul(l32,l32), l128=cmul(l64,l64), o={1.f,0.f};
      l256=cmul(l128,l128); wq=csel(h,l128,o); pw=cmul(csel(w&1,l32,o),csel(w&2,l64,o)); }
    asm volatile("":"+v"(lam),"+v"(coef),"+v"(l16),"+v"(l32),"+v"(l256),"+v"(wq),"+v"(pw));
    v16f are={},aim={};
    {
      u32 ax[4],ab[4];
      #pragma unroll
      for(int s=0;s<4;++s){ const u32 t=lbase+r*128+((((2*s+h)^((r>>1)&7)))<<4); ab[s]=t+sb_(0); ax[s]=t+sx_(0)+w*4096; }
      v4u X[2][2],T[2][2][2];
      auto rd=[&](int b) AI {
        #pragma unroll
        for(int s=0;s<2;++s){ X[b][s]=ldsr128(ax[2*b+s]); T[b][s][0]=ldsr128(ab[2*b+s]); T[b][s][1]=ldsr128(ab[2*b+s]+4096); } };
      auto mm=[&](int b) AI {
        #pragma unroll
        for(int s=0;s<2;++s){ are=mfma(X[b][s],T[b][s][0],are); aim=mfma(X[b][s],T[b][s][1],aim); }
        asm volatile("":"+v"(are),"+v"(aim)); };
      STAMP(1);
      waitl0(); BAR();
      rd(0);
      sfor<0,8>([&](auto C) AI {
        constexpr int c=C.value;
        SB; rd(1);
        SB; mm(0);
        if constexpr(c<7){
          #pragma unroll
          for(int s=0;s<4;++s){ ax[s]+=sx_(c+1)-sx_(c); ab[s]+=sb_(c+1)-sb_(c); asm volatile("":"+v"(ax[s]),"+v"(ab[s])); }
          SB;
          waitl0(); BAR();
          rd(0);
        }
        SB; mm(1);
      });
    }
    STAMP(2);
    tag=rfl(peek(lbase+L_TAG));
    u32 ta=lbase+L_TAB+(32*w+16*h)*16, tb=lbase+L_CSP+r*32, tg=lbase+L_AGG+r*8;
    asm volatile("":"+v"(ta),"+v"(tb),"+v"(tg));
    waitl0(); BAR();
    __builtin_amdgcn_s_setprio(3);
    int left=8;
    #define SBAR() do{ if(left>0){ BAR(); --left; } else __builtin_amdgcn_s_sleep(4); }while(0)
    cf uu[16], elo, Q;
    {
      const v4f c0=*(LAS v4f*)tb, c1=*(LAS v4f*)(tb+16);
      float rs[16],mu[16];
      #pragma unroll
      for(int k=0;k<16;++k){ rs[k]=*(LAS float*)(ta+k*16); mu[k]=*(LAS float*)(ta+k*16+8); }
      SB;
      const cf ncs={-((c0[0]+c0[1])+(c0[2]+c0[3])),-((c1[0]+c1[1])+(c1[2]+c1[3]))};
      cf ea={0.f,0.f};
      #pragma unroll
      for(int k=0;k<16;++k){
        uu[k]=cf{__builtin_fmaf(mu[k],ncs.x,are[k]),__builtin_fmaf(mu[k],ncs.y,aim[k])}*rs[k];
        ea= k? cfma(lam,ea,uu[k]) : uu[0];
      }
      cf ehi=ea; elo=ea; csw32(elo,ehi);
      *(LAS v2f*)(tg+w*256)=cfma(l16,elo,ehi);
      const float hf=(float)h;
      elo*=hf; l16=cf{__builtin_fmaf(hf,l16.x,1.f-hf),hf*l16.y};
      asm volatile("":"+v"(elo),"+v"(l16));
    }
    SB;
    if(CWIN){
      #pragma unroll
      for(int k=0;k<16;++k){ uu[k]=cmul(coef,uu[k]); asm volatile("":"+v"(uu[k])); }
    }
    STAMP(3);
    waitl0(); BAR();
    { const cf a0=ldsc(tg), a1=ldsc(tg+256), a2=ldsc(tg+512), a3=ldsc(tg+768);
      const cf Q2=cfma(l32,a0,a1), Q3=cfma(l32,Q2,a2);
      if(w==3){ const cf E=cfma(l32,Q3,a3); if(lane<32) st128(v4u{f2u(E.x),tag,f2u(E.y),tag},rws,ch*16+WS_GRAN+tt*8192); }
      Q= w==0?cf{0.f,0.f}: w==1?a0: w==2?Q2:Q3; }
    STAMP(4);
    asm volatile("":"+v"(Q));
    SBAR();
    if(!CWIN){
      #pragma unroll
      for(int k=0;k<16;++k){ uu[k]=cmul(coef,uu[k]); asm volatile("":"+v"(uu[k])); if(k==7 && tt>0) SBAR(); }
    }
    const int rt=2*tt+(w>>1);
    u32 hso=WS_HS+rt*131072+(cg>>1)*16384+(((cg&1)*2+(r>>4))*2+(w&1))*1024+h*512+(((lane&15)^(8*h))<<4);
    asm volatile("":"+v"(hso));
    cf H={0.f,0.f};
    if(tt>0){
      if(CWIN) SBAR();
      v4u gr[8];
      #pragma unroll 1
      for(int it=0;it<SPIN_CAP;++it){
        if(it<3){
          SBAR();
          #pragma unroll
          for(int i=0;i<8;++i) gr[i]=*(volatile LAS v4u*)(lbase+L_GR+lane*16+i*1024);
        } else {
          #pragma unroll
          for(int i=0;i<8;++i){ const int pd=tt-1-2*i-h; gr[i]=__builtin_amdgcn_raw_buffer_load_b128(rws,pd>=0?(u32)(pd*8192+ch*16):OOB,WS_GRAN,16); }
          SBAR();
        }
        u32 bad=0;
        #pragma unroll
        for(int i=0;i<8;++i) bad|=((gr[i][1]^tag)|(gr[i][3]^tag))&~(u32)((tt-1-2*i-h)>>31);
        if(all_eq(bad,0u)){
          if(it<3){
            #pragma unroll
            for(int i=0;i<8;++i) gr[i]=*(volatile LAS v4u*)(lbase+L_GR+lane*16+i*1024);
          }
          break;
        }
      }
      cf gv[8];
      #pragma unroll
      for(int i=0;i<8;++i){ gv[i]=cf{u2f(gr[i][0]),u2f(gr[i][2])}; asm volatile("":"+v"(gv[i])); }
      #pragma unroll
      for(int i=7;i>=0;--i) H=cfma(l256,H,gv[i]);
      H=cmul(H,wq);
      cf h2=H; csw32(H,h2); H+=h2;
    }
    STAMP(5);
    cf hc=cfma(pw,H,Q);
    hc=cmul(coef,cfma(l16,hc,elo));
    #pragma unroll
    for(int m=0;m<2;++m){
      cf hv[8];
      #pragma unroll
      for(int jj=0;jj<8;++jj){ hc=cfma(lam,hc,uu[8*m+jj]); hv[jj]=hc; }
      const u32 o=(hso^(m*64))+m*256;
      st128(v4u{pk2(hv[0].x,hv[1].x),pk2(hv[2].x,hv[3].x),pk2(hv[4].x,hv[5].x),pk2(hv[6].x,hv[7].x)},rws,o);
      st128(v4u{pk2(-hv[0].y,-hv[1].y),pk2(-hv[2].y,-hv[3].y),pk2(-hv[4].y,-hv[5].y),pk2(-hv[6].y,-hv[7].y)},rws,o+8192);
    }
    pend=WS_FH+(rt*32+(cg&1)*16+(cg>>1)*2+(w&1))*128;
    STAMP(6);
    #pragma unroll 1
    while(left>0){
      BAR(); --left;
      #pragma unroll 1
      for(int it=0;it<FWIN && pend;++it){ try_flag(false); if(pend) __builtin_amdgcn_s_sleep(1); else STAMP(7); }
    }
    if(pend){ try_flag(true); STAMP(7); }
  }
  lane=lane_now(); h=lane>>5; r=lane&31;
  const int g=lane>>4, m2=w&1, tc=(w>>1)&1, kh=w>>2;
  const int dcol=ct*64+tc*32+r;
  const float dsk=b2f(a.dsk[dcol]);
  v16f ay={},az={};
  const int q=(lane&15)>>2, p=lane&3, b4=g&1, gp=(b4^kh)+2*(p&1);
  u32 a0=lbase+pa_(0)+(kh*8+m2)*1024+(gp*16+((8*h+q)^(4*gp)))*16+8*(p>>1);
  u32 b0=lbase+pb_(0)+kh*8192+(tc*32+r)*128+((h^((r>>1)&7))<<4);
  u32 za=lbase+pa_(8)+(kh*8+m2)*1024+(lane^(kh*16))*16;
  u32 zb=lbase+pb_(8)+(64*kh+4*h+q)*128+((tc*64+b4*32+8*p)^(((q>>1)&1)<<6));
  u32 d0=pa_(1)-pa_(0), d1=pa_(2)-pa_(1), d2=pa_(3)-pa_(2), d3=pa_(0)-pa_(3), e0=XS; int eb=0;
  v4u A[2][4],B[2][4];
  auto rot=[&]() AI { const u32 t=d0; d0=d1; d1=d2; d2=d3; d3=t; eb= eb==4?0:eb+1; e0= eb==4?(u32)(-4*(int)XS):XS; };
  auto rdy=[&](int b) AI {
    #pragma unroll
    for(int s=0;s<4;++s){ A[b][s]=ldstr(a0+s*2048,(a0^64)+s*2048); B[b][s]=ldsr128(b0^(s*32)); }
    a0+=d0; b0+=e0; rot();
  };
  auto rdz=[&](int b) AI {
    #pragma unroll
    for(int s=0;s<4;++s){ A[b][s]=ldsr128(za+s*2048); B[b][s]=ldstr(zb+s*2048,zb+s*2048+1024); }
    za+=d0; zb+=e0; rot();
  };
  auto mm4=[&](int b,v16f& c) AI {
    #pragma unroll
    for(int s=0;s<4;++s) c=mfma(A[b][s],B[b][s],c);
    #pragma unroll
    for(int s=0;s<4;++s){ SG(0x8,1); SG(0x100,3); }
    SB; asm volatile("":"+v"(c));
  };
  #define NXT() BAR()
  const int tk=32*m2+16*h+8*kh;
  STAMP(8);
  __builtin_amdgcn_s_setprio(0);
  NXT(); rdy(0);
  STAMP(9);
  #pragma unroll 1
  for(int c=0;c<3;++c){
    NXT(); rdy(1); mm4(0,ay); try_flag(false);
    NXT(); rdy(0); mm4(1,ay); try_flag(c==2);
  }
  NXT(); rdy(1); mm4(0,ay);
  NXT(); rdz(0); mm4(1,ay);
  STAMP(10);
  float sv[8], xf[8], r2[8];
  u32 pt=lbase+L_TAB+tk*16, px=lbase+L_XT+tk*128+(tc*32+r)*2;
  asm volatile("":"+v"(pt),"+v"(px));
  #pragma unroll 1
  for(int c=0;c<4;++c){
    NXT(); rdz(1); mm4(0,az);
    if(c==1){
      #pragma unroll
      for(int k=0;k<8;++k){ r2[k]=*(LAS float*)(pt+k*16); sv[k]=*(LAS float*)(pt+k*16+8); xf[k]=u2f(*(LAS u16*)(px+k*128)); }
    }
    NXT(); rdz(0); mm4(1,az);
    if(c==1){
      #pragma unroll
      for(int k=0;k<8;++k){ xf[k]=u2f(f2u(xf[k])<<16); sv[k]=(xf[k]-sv[k])*r2[k]*dsk; r2[k]*=r2[k]; }
    }
  }
  {
    const u32 xo=lbase+WB+w*2048+lane*16, xi=lbase+WB+(w^4)*2048+lane*16;
    const rsrc_t ro=mkrsrc(a.out,2048*512*4);
    u32 oo=((rt2*64+tk)*512+dcol)*4;
    NXT(); rdz(1); mm4(0,az);
    *(LAS v4f*)xo=v4f{ay[8],ay[9],ay[10],ay[11]}; *(LAS v4f*)(xo+1024)=v4f{ay[12],ay[13],ay[14],ay[15]};
    SB;
    #pragma unroll
    for(int s=0;s<4;++s) az=mfma(A[1][s],B[1][s],az);
    asm volatile("":"+v"(az),"+v"(oo));
    waitl0(); NXT(); rdz(0);
    const v4f i0=*(LAS v4f*)xi, i1=*(LAS v4f*)(xi+1024);
    SB;
    #pragma unroll
    for(int s=0;s<4;++s) az=mfma(A[0][s],B[0][s],az);
    float ce[8];
    #pragma unroll
    for(int k=0;k<8;++k){ sv[k]=__builtin_fmaf(2.f,ay[k]+(k<4?i0[k&3]:i1[k&3]),sv[k])*r2[k]; ce[k]=r2[k]*-1.44269504f; asm volatile("":"+v"(sv[k]),"+v"(ce[k])); }
    STAMP(11);
    *(LAS v4f*)(xo+XS)=v4f{az[8],az[9],az[10],az[11]}; *(LAS v4f*)(xo+XS+1024)=v4f{az[12],az[13],az[14],az[15]};
    waitl0(); bar();
    const v4f i2=*(LAS v4f*)(xi+XS), i3=*(LAS v4f*)(xi+XS+1024);
    #pragma unroll
    for(int k=0;k<8;++k){
      const float z=az[k]+(k<4?i2[k&3]:i3[k&3]);
      const float gt=z*__builtin_amdgcn_rcpf(1.f+__builtin_amdgcn_exp2f(z*ce[k]));
      __builtin_amdgcn_raw_buffer_store_b32(f2u(__builtin_fmaf(sv[k],gt,xf[k])),ro,oo+k*2048,0,OUTAUX);
    }
  }
  CSTAMP(14); STAMP(13); STAMP_FLUSH();
}

extern "C" void kernel_launch(void* const* d_in, const int* in_sizes, int n_in,
                              void* d_out, int out_size, void* d_ws, size_t ws_size,
                              hipStream_t stream) {
  Args a;
  a.x=(const u16*)d_in[0]; a.lre=(const u16*)d_in[1]; a.lim=(const u16*)d_in[2];
  a.bre=(const u16*)d_in[3]; a.bim=(const u16*)d_in[4]; a.cre=(const u16*)d_in[5]; a.cim=(const u16*)d_in[6];
  a.ldt=(const u16*)d_in[7]; a.dsk=(const u16*)d_in[8];
  a.wg=(const u16*)d_in[10]; a.wu=(const u16*)d_in[11]; a.wd=(const u16*)d_in[12];
  a.out=(float*)d_out; a.ws=(char*)d_ws;
  fused_k<<<256,768,0,stream>>>(a);
}
```

```cpp
#include <hip/hip_runtime.h>
#include <hip/hip_bf16.h>
#include <type_traits>

typedef unsigned u32; typedef unsigned long long u64; typedef unsigned short u16;
typedef u32 v4u __attribute__((ext_vector_type(4)));
typedef u32 v2u __attribute__((ext_vector_type(2)));
typedef short v4s __attribute__((ext_vector_type(4)));
typedef __bf16 v2bf __attribute__((ext_vector_type(2)));
typedef __bf16 v8bf __attribute__((ext_vector_type(8)));
typedef float v2f __attribute__((ext_vector_type(2)));
typedef float v4f __attribute__((ext_vector_type(4)));
typedef float v16f __attribute__((ext_vector_type(16)));
#define LAS __attribute__((address_space(3)))
#define DEVI __device__ __forceinline__
#define AI __attribute__((always_inline))
#define SB __builtin_amdgcn_sched_barrier(0)
#if defined(__HIP_DEVICE_COMPILE__)
#define NOPK __attribute__((target("no-packed-fp32-ops")))
#else
#define NOPK
#endif

#define ROT1 0x01
#define ROTP 1
#define SPRIO 2
#define OUTAUX 16
#define HSLEEP 2
#define FSPLIT 0
#define FWIN 6
#define LATEA 1
#define LLCPF 0
#define LNFIRST 1
#define WARMP 1
#define RELAX 1
#define TX 760
#define ROTI 1
#define TPOS 0
#define CWIN 0
#define RY 1
#define RZ 2
#define CSPLIT 5
#define LBSLEEP 6
#define WARMB 0

constexpr u32 WS_EPOCH = 0;
constexpr u32 WS_FX    = 32768;
constexpr u32 WS_FH    = WS_FX + 131072;
constexpr u32 WS_FHH   = WS_FH + 131072;
constexpr u32 WS_GRAN  = WS_FHH + 131072;
constexpr u32 WS_HH    = WS_GRAN + 131072;
constexpr u32 WS_HS    = WS_HH + 32*86*2048;
constexpr u32 WS_END   = WS_HS + 32*131072;
constexpr u32 WS_STAMP = 16u<<20;
constexpr u32 XS = 16384;
constexpr u32 WB = 49152, WSZ = 24832;
constexpr u32 L_XT = WB+4*WSZ;
constexpr u32 L_TAB  = L_XT+8192;
constexpr u32 L_CSP  = L_TAB + 2048;
constexpr u32 L_FLG  = L_CSP;
constexpr u32 L_CW   = L_CSP + 1024;
constexpr u32 L_AGG  = L_CW + 2048;
constexpr u32 L_DUM  = L_AGG + 1024;
constexpr u32 L_TAG  = L_DUM + 256;
constexpr u32 L_GR   = L_XT;
constexpr u32 L_TOTAL = 163840;
static_assert(L_TAG+16<=L_TOTAL,"lds");
constexpr u32 OOB = 0x40000000u;
constexpr int SPIN_CAP = 1<<18;

#define CODEPF 12
constexpr u32 CODE_LEN=$n;
struct Args { const u16 *x,*lre,*lim,*bre,*bim,*cre,*cim,*ldt,*dsk,*wg,*wu,*wd; float* out; char* ws; };

DEVI u32 f2u(float f){ return __builtin_bit_cast(u32,f); }
DEVI float u2f(u32 u){ return __builtin_bit_cast(float,u); }
DEVI u32 rfl(u32 v){ return __builtin_amdgcn_readfirstlane(v); }
DEVI bool all_eq(u32 v,u32 t){ return __builtin_amdgcn_ballot_w64(v!=t)==0ull; }
DEVI v4u mksrd(const void* p, u32 bytes){ u64 a=(u64)p; v4u s; s.x=rfl((u32)a); s.y=rfl((u32)(a>>32)); s.z=bytes; s.w=0x00020000u; return s; }
#define NTM 0
#define POL1 ""
#define POL2 ""
#define POL3 ""
#define POL4 ""
#define DM0 "s_add_u32 m0, %[b], %[i]\n\ts_nop 0\n\t"
#define DLD(n,off,pol) "buffer_load_dwordx4 %[v" #n "], %[r], %[s] offen" off pol " lds\n\t"
template<bool SC1,u32 IMM> DEVI void dma4(v4u srd,u32 v0,u32 v1,u32 v2,u32 v3,u32 soff,u32 base){
  if(SC1) asm volatile(DM0 DLD(0,""," sc1") DLD(1," offset:1024"," sc1") DLD(2," offset:2048"," sc1") DLD(3," offset:3072"," sc1")
      :: [v0]"v"(v0),[v1]"v"(v1),[v2]"v"(v2),[v3]"v"(v3),[r]"s"(srd),[s]"s"(soff),[b]"s"(base),[i]"n"(IMM) : "memory","scc");
  else asm volatile(DM0 DLD(0,"",POL4) DLD(1," offset:1024",POL4) DLD(2," offset:2048",POL4) DLD(3," offset:3072",POL4)
      :: [v0]"v"(v0),[v1]"v"(v1),[v2]"v"(v2),[v3]"v"(v3),[r]"s"(srd),[s]"s"(soff),[b]"s"(base),[i]"n"(IMM) : "memory","scc");
}
template<u32 IMM> DEVI void dma3(v4u srd,u32 v0,u32 v1,u32 v2,u32 soff,u32 base){
  asm volatile(DM0 DLD(0,"",POL3) DLD(1," offset:1024",POL3) DLD(2," offset:2048",POL3)
      :: [v0]"v"(v0),[v1]"v"(v1),[v2]"v"(v2),[r]"s"(srd),[s]"s"(soff),[b]"s"(base),[i]"n"(IMM) : "memory","scc");
}
template<u32 IMM> DEVI void dma2(v4u srd,u32 v0,u32 v1,u32 soff,u32 base){
  asm volatile(DM0 DLD(0,"",POL2) DLD(1," offset:1024",POL2)
      :: [v0]"v"(v0),[v1]"v"(v1),[r]"s"(srd),[s]"s"(soff),[b]"s"(base),[i]"n"(IMM) : "memory","scc");
}
template<bool SC1> DEVI void dma_dw(v4u srd,u32 voff,u32 soff,u32 lds){
  if(SC1) asm volatile("s_mov_b32 m0, %3\n\ts_nop 0\n\tbuffer_load_dword %0, %1, %2 offen sc1 lds" :: "v"(voff), "s"(srd), "s"(soff), "s"(lds) : "memory");
  else    asm volatile("s_mov_b32 m0, %3\n\ts_nop 0\n\tbuffer_load_dword %0, %1, %2 offen" POL1 " lds" :: "v"(voff), "s"(srd), "s"(soff), "s"(lds) : "memory");
}
DEVI void dma_x4(v4u srd,u32 voff,u32 soff,u32 lds){
  asm volatile("s_mov_b32 m0, %3\n\ts_nop 0\n\tbuffer_load_dwordx4 %0, %1, %2 offen sc1 lds" :: "v"(voff), "s"(srd), "s"(soff), "s"(lds) : "memory");
}
template<int N> DEVI void waitv(){ asm volatile("s_waitcnt vmcnt(%0)"::"n"(N):"memory"); }
DEVI void waitl0(){ asm volatile("s_waitcnt lgkmcnt(0)":::"memory"); }
DEVI void bar(){ asm volatile("":::"memory"); __builtin_amdgcn_s_barrier(); asm volatile("":::"memory"); SB; }
template<u32 OFF> DEVI void sig(u32 vaddr,u32 one){ asm volatile("s_mov_b64 exec, 1\n\tds_add_u32 %0, %1 offset:%2\n\ts_mov_b64 exec, -1" :: "v"(vaddr), "v"(one), "n"(OFF) : "memory"); }
template<u32 OFF> DEVI void sigm(u32 vaddr){ asm volatile("s_mov_b64 exec, 1\n\tds_inc_u32 %0, %0 offset:%1\n\ts_mov_b64 exec, -1" :: "v"(vaddr), "n"(OFF) : "memory"); }
DEVI u32 peek(u32 vaddr){ return *(volatile LAS u32*)vaddr; }
DEVI bool vm_idle(){ u32 v; asm volatile("s_getreg_b32 %0, hwreg(HW_REG_IB_STS, 0, 4)":"=s"(v)); return v==0u; }
DEVI void flag_st(v4u srd,u32 soff,u32 val){ asm volatile("s_mov_b64 exec, 1\n\tbuffer_store_dword %0, off, %1, %2 sc1\n\ts_mov_b64 exec, -1" :: "v"(val), "s"(srd), "s"(soff) : "memory"); }
DEVI int lane_now(){ int l; asm volatile("v_mbcnt_lo_u32_b32 %0, -1, 0\n\tv_mbcnt_hi_u32_b32 %0, -1, %0":"=v"(l)); return l; }
DEVI float b2f(u16 v){ return u2f(((u32)v)<<16); }
DEVI u32 pk2(float lo,float hi){ v2f p={lo,hi}; return __builtin_bit_cast(u32,__builtin_convertvector(p,v2bf)); }
DEVI float rbf(float f){ return u2f(pk2(f,f)<<16); }
DEVI float relu(float x){ int i=__builtin_bit_cast(int,x); return __builtin_bit_cast(float,i>0?i:0); }
DEVI float dot2(u32 a,u32 b,float c){ return __builtin_amdgcn_fdot2_f32_bf16(__builtin_bit_cast(v2bf,a),__builtin_bit_cast(v2bf,b),c,false); }
DEVI v16f mfma(v4u a,v4u b,v16f c){ return __builtin_amdgcn_mfma_f32_32x32x16_bf16(__builtin_bit_cast(v8bf,a),__builtin_bit_cast(v8bf,b),c,0,0,0); }
DEVI v4f mfma16(v4u a,v4u b,v4f c){ return __builtin_amdgcn_mfma_f32_16x16x32_bf16(__builtin_bit_cast(v8bf,a),__builtin_bit_cast(v8bf,b),c,0,0,0); }
DEVI v4u ldsr128(u32 a){ return *(LAS v4u*)a; }
DEVI v4u ldstr(u32 a0,u32 a1){
  v4s x=__builtin_amdgcn_ds_read_tr16_b64_v4i16((LAS v4s*)a0);
  v4s y=__builtin_amdgcn_ds_read_tr16_b64_v4i16((LAS v4s*)a1);
  return __builtin_bit_cast(v4u,__builtin_shufflevector(x,y,0,1,2,3,4,5,6,7));
}
DEVI void sw16(float& a,float& b){ v2u r=__builtin_amdgcn_permlane16_swap(f2u(a),f2u(b),false,false); a=u2f(r[0]); b=u2f(r[1]); }
DEVI void sw32(float& a,float& b){ v2u r=__builtin_amdgcn_permlane32_swap(f2u(a),f2u(b),false,false); a=u2f(r[0]); b=u2f(r[1]); }
template<int CTRL> DEVI float dppadd(float v){ return v+u2f(__builtin_amdgcn_update_dpp(0u,f2u(v),CTRL,0xf,0xf,true)); }
DEVI void wsum4(float& a,float& b,float& c,float& d){
  sw32(a,b); sw32(c,d); float v=a+b, t=c+d;
  sw16(v,t); v+=t;
  v=dppadd<0xB1>(v); v=dppadd<0x4E>(v); v=dppadd<0x141>(v); v=dppadd<0x140>(v);
  a=u2f(__builtin_amdgcn_readlane(f2u(v),0)); c=u2f(__builtin_amdgcn_readlane(f2u(v),16));
  b=u2f(__builtin_amdgcn_readlane(f2u(v),32)); d=u2f(__builtin_amdgcn_readlane(f2u(v),48));
}
typedef v2f cf;
DEVI cf cfma(cf a,cf b,cf c){ return cf{__builtin_fmaf(-a.y,b.y,__builtin_fmaf(a.x,b.x,c.x)), __builtin_fmaf(a.y,b.x,__builtin_fmaf(a.x,b.y,c.y))}; }
DEVI cf cmul(cf a,cf b){ return cf{__builtin_fmaf(-a.y,b.y,a.x*b.x), __builtin_fmaf(a.y,b.x,a.x*b.y)}; }
DEVI cf csel(bool c,cf a,cf b){ return cf{c?a.x:b.x, c?a.y:b.y}; }
DEVI cf ldsc(u32 a){ return *(LAS v2f*)a; }
DEVI void csw32(cf& a,cf& b){ float x=a.x,y=b.x,z=a.y,t=b.y; sw32(x,y); sw32(z,t); a=cf{x,z}; b=cf{y,t}; }
DEVI void sincos_(float x,float& s,float& c){
  const float n=__builtin_rintf(x*0.636619772f);
  float r=__builtin_fmaf(n,-1.57079601e+00f,x); r=__builtin_fmaf(n,-3.13916473e-07f,r); r=__builtin_fmaf(n,-5.39030253e-15f,r);
  const float z=r*r;
  float ps=__builtin_fmaf(__builtin_fmaf(__builtin_fmaf(2.86567956e-6f,z,-1.98559923e-4f),z,8.33338592e-3f),z,-1.66666672e-1f);
  ps=__builtin_fmaf(ps*z,r,r);
  float pc=__builtin_fmaf(__builtin_fmaf(__builtin_fmaf(2.44677067e-5f,z,-1.38877297e-3f),z,4.16666567e-2f),z,-0.5f);
  pc=__builtin_fmaf(pc,z,1.f);
  const int q=(int)n;
  const float a=(q&1)?pc:ps, b=(q&1)?ps:pc;
  s=(q&2)?-a:a; c=((q+1)&2)?-b:b;
}
typedef __amdgpu_buffer_rsrc_t rsrc_t;
DEVI rsrc_t mkrsrc(const void* p,u32 bytes){ return __builtin_amdgcn_make_buffer_rsrc((void*)p,0,bytes,0x00020000); }
DEVI void st128(v4u v,rsrc_t r,u32 off){ __builtin_amdgcn_raw_buffer_store_b128(v,r,off,0,16); }
DEVI u32 mktag(u32 e){ return (e*0x9E3779B1u+0x7F4A7C15u)|1u; }

#define STAMP(k) do{}while(0)
#define CSTAMP(k) do{}while(0)
#define TRACE(k) do{}while(0)
#define STAMP_DECL do{}while(0)
#define BAR() bar()
#define STAMP_FLUSH() do{}while(0)
#define TRARG
#define TRPASS
#define TSARG
#define TSPASS

DEVI void poll32(v4u srd,u32 voff,u32 soff,u32 tag){
  #pragma unroll 1
  for(int it=0;it<SPIN_CAP;++it){
    u32 v;
    asm volatile("buffer_load_dword %0, %1, %2, %3 offen sc1\n\ts_waitcnt vmcnt(0)" : "=&v"(v) : "v"(voff), "s"(srd), "s"(soff) : "memory");
    if(all_eq(v,tag)) break;
    __builtin_amdgcn_s_sleep(12);
  }
}
template<int I,int N,class F> DEVI void sfor(F&& f){ if constexpr(I<N){ f(std::integral_constant<int,I>{}); sfor<I+1,N>(f); } }
AI constexpr u32 wslot(int c){ return WB+(c&3)*WSZ; }
AI constexpr u32 pa_(int c){ return (c&3)==3?WB+5*XS:(((c&3)+2)%3)*XS; }
AI constexpr u32 pb_(int c){ return WB+(c%5)*XS; }
AI constexpr u32 sx_(int c){ return c>=5?(c-5)*XS:wslot(c+2); }
AI constexpr u32 sb_(int c){ return c>=5?wslot(3)+(c-5)*8192:wslot(c+2)+16384; }

#define SG(m,n) __builtin_amdgcn_sched_group_barrier(m,n,0)
template<int NB>
DEVI u32 g_main(v4u x0,v4u x1,u32 e,rsrc_t rws,u32 lbase,int lane,int gw,int tt,int cg,u32 hqo TRARG TSARG){
  const int h=lane>>5, r=lane&31, rh=gw&1, u0=(gw>>1)*NB;
  const int row=tt*128+cg*8+gw*2;
  v16f acc[3][2]; v4f cw[3];
  u32 ax[4],aw,aw2;
  const u32 on2=((lane&3)==((lane>>4)&1))?0x3f803f80u:0u;
  v4u ones={on2,on2,on2,on2};
  u32 tag;
  {
    u32 ab=lbase+sb_(0)+r*128+((((2*gw+h)^((r>>1)&7)))<<4);
    v4f c0={},c1={};
    sfor<0,8>([&](auto C) AI {
      constexpr int c=C.value;
      if constexpr(c!=0 || !LNFIRST){ waitl0(); BAR(); }
      v4u t0,t1;
      if constexpr(c!=0 || !LNFIRST){ t0=ldsr128(ab); t1=ldsr128(ab+4096); }
      if constexpr(c==0){
        const float z0=u2f(rfl(x0[0])<<16), z1=u2f(rfl(x1[0])<<16);
        float s0=0.f,s1=0.f,v0=0.f,v1=0.f;
        #pragma unroll
        for(int k=0;k<8;++k){
          const float d0=u2f((k&1)?x0[k>>1]&0xffff0000u:x0[k>>1]<<16)-z0, d1=u2f((k&1)?x1[k>>1]&0xffff0000u:x1[k>>1]<<16)-z1;
          s0+=d0; s1+=d1; v0=__builtin_fmaf(d0,d0,v0); v1=__builtin_fmaf(d1,d1,v1); }
        wsum4(s0,s1,v0,v1);
        const float sm=(lane?s1:s0)*(1.f/512.f);
        const u32 vm=pk2(relu(__builtin_fmaf(lane?v1:v0,1.f/512.f,-sm*sm)),sm+(lane?z1:z0));
        float rs=__builtin_amdgcn_rcpf(rbf(__builtin_amdgcn_sqrtf(rbf(u2f(vm<<16)+1e-5f))));
        asm volatile("":"+v"(rs));
        STAMP(1);
        if(LNFIRST==2){ BAR(); t0=ldsr128(ab); t1=ldsr128(ab+4096); }
        tag=mktag(rfl(e));
        if(lane<2) st128(v4u{f2u(rs),tag,vm&0xffff0000u,tag},rws,WS_FX+row*64+lane*16);
        if(gw==0) *(LAS v2u*)(lbase+L_TAG)=v2u{tag,e};
        if(LNFIRST==1){ waitl0(); BAR(); t0=ldsr128(ab); t1=ldsr128(ab+4096); }
      }
      if constexpr(c<7) ab+=sb_(c+1)-sb_(c);
      if constexpr(c>=1 && c<=2*NB){ acc[(c-1)>>1][(c-1)&1]=v16f{}; asm volatile("":"+v"(acc[(c-1)>>1][(c-1)&1])); }
      c0=mfma16(ones,t0,c0); c1=mfma16(ones,t1,c1);
      asm volatile("":"+v"(c0),"+v"(c1),"+v"(ab));
    });
    asm volatile("s_nop 7\n\ts_nop 1":"+v"(c0),"+v"(c1));
    const bool hi=lane&16;
    *(LAS float*)(lbase+L_CSP+(r*8+h*4+gw)*4)=h?(hi?c1[1]:c1[0]):(hi?c0[1]:c0[0]);
    STAMP(2);
    waitl0(); BAR();
  }
  #pragma unroll
  for(int s=0;s<4;++s) ax[s]=lbase+rh*8192+r*128+((((2*(s^(2*rh))+h)^((r>>1)&7)))<<4);
  aw=lbase+WB+((lane>>4)&1)*(12288+32)+(8*h+((lane&15)>>2))*192+(lane&3)*8+u0*32;
  aw2=aw+6144-rh*6144; aw+=rh*6144;
  #pragma unroll
  for(int t=0;t<3;++t){ cw[t]=v4f{}; if(t<NB) asm volatile("":"+v"(cw[t])); }
  asm volatile("":"+v"(ax[0]),"+v"(ax[1]),"+v"(ax[2]),"+v"(ax[3]),"+v"(aw),"+v"(aw2));
  BAR();
  __builtin_amdgcn_s_setprio(2);
  v4u X[2][2],T[2][3];
  auto rd=[&](int b,int s) AI {
    const u32 ad=(s<2?aw:aw2)+(s&1)*3072;
    X[b][0]=ldsr128(ax[s]);
    #pragma unroll
    for(int t=0;t<NB;++t){ T[b][t]=ldstr(ad+t*32,ad+t*32+768); if(t==0) X[b][1]=ldsr128(ax[s]+4096); }
  };
  auto mm=[&](int b,bool cs) AI {
    #pragma unroll
    for(int t=0;t<NB;++t){ acc[t][0]=mfma(T[b][t],X[b][0],acc[t][0]); if(cs) cw[t]=mfma16(ones,T[b][t],cw[t]); acc[t][1]=mfma(T[b][t],X[b][1],acc[t][1]); }
  };
  auto mix=[&](int nv,bool cs) AI {
    SG(0x8,1); SG(0x100,2); if(cs) SG(0x8,2); else SG(0x8,1); SG(0x100,2); SG(0x8,1); SG(0x100,2);
    if(NB==3){ if(cs) SG(0x8,2); else SG(0x8,1); SG(0x100,2); }
    SG(0x8,1); if(nv) SG(0x2,3);
    SG(0x8,5);
  };
  u32 d0=XS, d1=XS, d2=(u32)(-2*(int)XS);
  STAMP(3); CSTAMP(6);
  rd(0,0);
  #pragma unroll 1
  for(int c=0;c<8;++c){
    const u32 dw=((c&3)==3)?(u32)(-3*(int)WSZ):WSZ;
    SB; rd(1,1); mm(0,true); ax[0]+=d0; ax[1]+=d0; aw+=dw; mix(1,true);
    SB; asm volatile("":"+v"(ax[0]),"+v"(ax[1]),"+v"(aw));
    rd(0,2); mm(1,true); mix(0,true);
    SB; rd(1,3); mm(0,false); ax[2]+=d0; ax[3]+=d0; aw2+=dw; mix(1,false);
    SB; asm volatile("":"+v"(ax[2]),"+v"(ax[3]),"+v"(aw2));
    { const u32 t=d0; d0=d1; d1=d2; d2=t; }
    if(c==7){
      const int ln=lane_now();
      #pragma unroll
      for(int t=0;t<NB;++t){ const float lo=cw[t][0], hi=cw[t][1]; cw[t][0]=(ln&16)?hi:lo; }
      *(LAS v4f*)(lbase+L_CW+(gw*32+(ln&31))*16)=v4f{cw[0][0],cw[1][0],cw[2][0],0.f};
      const u32 tm=lbase+L_TAB+(rh*64+16*((ln>>2)&1)+8*((ln>>4)&1)+4*((ln>>3)&1)+(ln&3))*16+8;
      cw[0][2]=*(LAS float*)tm; cw[0][3]=*(LAS float*)(tm+512);
      ax[0]=lbase+L_CW+((gw^1)*32+(ln&31))*16;
    }
    waitl0(); BAR();
    rd(0,0); mm(1,false); mix(0,false);
  }
  const int ln=lane_now();
  const u32 hm=(ln&32)?0u:0xffffffffu;
  v4u M[2];
  #pragma unroll
  for(int m=0;m<2;++m){ const u32 mu=f2u(cw[0][2+m]), nm=(((mu>>16)|mu)^0x80008000u)&hm; M[m]=v4u{nm,nm,0u,0u}; }
  #pragma unroll
  for(int t=0;t<NB;++t){
    const float c=cw[t][0]+u2f(X[0][0][t]);
    const u32 c0=f2u(c)&0xffff0000u; const float r1=c-u2f(c0);
    const u32 c1=f2u(r1)&0xffff0000u; const float r2=r1-u2f(c1);
    const v4u F={(c0>>16)|c1,f2u(r2)>>16,0u,0u};
    acc[t][0]=mfma(F,M[0],acc[t][0]); acc[t][1]=mfma(F,M[1],acc[t][1]);
  }
  STAMP(4); CSTAMP(7);
  #pragma unroll
  for(int t=0;t<NB;++t)
    #pragma unroll
    for(int m=0;m<2;++m){
      SB;
      v4u v;
      #pragma unroll
      for(int k=0;k<4;++k) v[k]=pk2(relu(acc[t][m][2*k])*acc[t][m][2*k+8],relu(acc[t][m][2*k+1])*acc[t][m][2*k+9]);
      st128(v,rws,hqo+(u0+t)*2048+m*1024+ln*16);
    }
  return tag;
}

__global__ NOPK __launch_bounds__(768,3) void fused_k(Args a){
  __shared__ __attribute__((aligned(256))) char smem[L_TOTAL];
  const u32 lbase=(u32)(size_t)(LAS char*)smem;
  const int tid=__builtin_amdgcn_workitem_id_x();
  const int wgid=__builtin_amdgcn_workgroup_id_x(), w=rfl(tid>>6);
  int lane=tid&63;
  const int xcd=wgid&7, idx=wgid>>3;
  if(CODEPF && w==CODEPF-1){
    u64 pc; u32 back; asm volatile("s_getpc_b64 %0\n.Lpcx%=:\n\ts_mov_b32 %1, .Lpcx%=-_Z7fused_k4Args":"=&s"(pc),"=s"(back));
    dma_dw<false>(mksrd((const void*)(pc-back),CODE_LEN),(u32)(((idx&3)*64+lane)*128),0,lbase+L_DUM);
  }
  const int tt=(xcd&1)*8+(idx&7), cg=(xcd>>1)*4+(idx>>3);
  const int nb=((cg&3)==3||cg==14)?2:3, blk0=11*(cg>>2)+3*(cg&3)-(cg==15?1:0);
  const int rt2=(xcd&3)*8+(idx&7), ct=(xcd>>2)*4+(idx>>3);
  const int ry=((RY==4?2*(idx>>3):RY==2?4*((idx>>3)&1):0)+(idx&7))&7, rz=RZ==4?3*(idx>>3):RZ==2?6*((idx>>3)&1):RZ==3?(idx>>3):0;
  const u32 one=1;
  STAMP_DECL; STAMP(0); CSTAMP(12);

  v4u x0,x1; u32 ep0;
  if(w<4){ const int c_=cg*32+(lane&31); x0[0]=a.lre[c_]; x0[1]=a.lim[c_]; x0[2]=a.ldt[c_]; }
  else if(w<8){ const int row=tt*128+cg*8+(w-4)*2; x0=((const v4u*)a.x)[row*64+lane]; x1=((const v4u*)a.x)[row*64+64+lane];
    ep0=__builtin_amdgcn_raw_buffer_load_b32(mkrsrc(a.ws,WS_END),0,WS_EPOCH,16); }
  asm volatile("":::"memory");
  if(__builtin_expect(w>=8,1)){
    __builtin_amdgcn_s_setprio(3);
    const int lw=w-8, j=lane>>3, l7=lane&7;
    const v4u sx=mksrd(a.x,2048*1024);
    const v4u sb=mksrd(lw<2?a.bre:a.bim,512*1024);
    const u32 sz0=(u32)((l7^(j>>1))<<4), sz1=sz0^64u;
    const u32 xr=(tt*128+32*lw+16*(j>>2)+(j&3))*1024;
    const u32 vx0=xr+sz0, vx1=xr+3*1024+sz1, vx2=xr+6*1024+sz0, vx3=xr+9*1024+sz1;
    const u32 br=(cg*32+16*(lw&1)+j)*1024;
    const u32 vb0=br+sz0, vb1=br+7*1024+sz1;
    const u32 l4=lbase+lw*4096, l2=lbase+lw*2048;
    u32 kx=((tt*(ROT1&15)+cg*(ROT1>>4))&7)*128, kw=(kx+640)&0x380;
    #define SX(c) dma4<false,sx_(c)>(sx,vx0,vx1,vx2,vx3,kx,l4)
    #define SBB(c) do{ dma2<sb_(c)>(sb,vb0,vb1,kx,l2); kx=(kx+128)&0x380; }while(0)
    SX(0); SBB(0); SX(1); SBB(1);
    const v4u sws=mksrd(a.ws,WS_END);
    const v4u sw=mksrd(lw<2?a.wg:a.wu,512*2752);
    const v4u swd=mksrd(a.wd,1376*1024);
    const v4u sc=mksrd(lw<2?a.cre:a.cim,512*1024);
    const u32 vf=(lane&15)*128, lf=lbase+L_FLG+lw*256;
    const u32 fx=WS_FX+(tt*64+lw*16)*128, fh=WS_FH+(rt2*32+(lw&1)*16)*128, fhh=WS_FHH+rt2*4096;
    u32 vw[6];
    #pragma unroll
    for(int i=0;i<3;++i){ const u32 q=i*1024+lane*16, row=q/192, col=q%192; const bool ok=col<(u32)nb*64;
      vw[i]=ok?(row+32*(lw&1))*2752+col+blk0*64-i*1024:OOB; vw[i+3]=vw[i]+16*2752; asm volatile("":"+v"(vw[i]),"+v"(vw[i+3])); }
    const u32 lwv=lbase+(lw>>1)*(12288+32)+(lw&1)*6144;
    #define GWA(c) dma3<wslot(c)>(sw,vw[0],vw[1],vw[2],kw*1376,lwv)
    #define GWB(c) do{ dma3<wslot(c)+3072>(sw,vw[3],vw[4],vw[5],kw*1376,lwv); kw=(kw+128)&0x380; }while(0)
    #define GX(c) do{ dma4<false,((c)%3)*XS>(sx,vx0,vx1,vx2,vx3,kx,l4); kx=(kx+128)&0x380; }while(0)
    SX(2); waitv<10>(); BAR();
    SBB(2); SX(3); waitv<10>(); BAR();
    SBB(3); SX(4); waitv<10>(); BAR();
    SBB(4); SX(5); waitv<10>(); BAR();
    SBB(5); SX(6); SBB(6); waitv<12>(); BAR();
    SX(7); SBB(7); waitv<12>(); BAR();
    STAMP(2);
    const u32 vs=(lane>>1)*128+(lane&1)*16, lt=lbase+L_TAB+lw*512;
    const u32 tag=rfl(peek(lbase+L_TAG));
    if(lane<32) dma_x4(sws,vs,fx,lt);
    GWA(0); waitv<10>(); BAR();
    GWB(0); waitv<7>(); BAR();
    waitv<6>();
    #pragma unroll 1
    for(int it=0;it<SPIN_CAP;++it){
      const v4u t=*(volatile LAS v4u*)(lt+(lane&31)*16);
      if(all_eq((t[1]^tag)|(t[3]^tag),0u)) break;
      __builtin_amdgcn_s_sleep(2);
      if(lane<32) dma_x4(sws,vs,fx,lt);
      waitv<0>();
    }
    BAR();
    if(TPOS==1){
      dma_dw<false>(sc,(lane<32&&!(ROTI&1))?(u32)(j*1024+l7*128):OOB,(ct*64+(idx&7)*8+(lw&1)*4)*1024,lbase+L_DUM);
      dma_dw<false>(swd,(lane<43&&!(ROTI&2))?(u32)(lane*1024):OOB,((idx&7)*172+lw*43)*1024+ct*128,lbase+L_DUM);
    }
    GWA(1); GWB(1);
    if(TPOS==0){
      dma_dw<false>(sc,(lane<32&&!(ROTI&1))?(u32)(j*1024+l7*128):OOB,(ct*64+(idx&7)*8+(lw&1)*4)*1024,lbase+L_DUM);
      dma_dw<false>(swd,(lane<43&&!(ROTI&2))?(u32)(lane*1024):OOB,((idx&7)*172+lw*43)*1024+ct*128,lbase+L_DUM);
    }
    waitv<WARMP?8:6>(); BAR();
    STAMP(3);
    GWA(2); GWB(2); GWA(3); GWB(3);
    if(LLCPF && nb==2){
      const u32 li=((cg<12?(cg>>2):cg-11)*16+tt)*2+(lw&1);
      dma_dw<false>(sc,lane<26?(li*26+lane)*128:OOB,0,lbase+L_DUM);
      dma_dw<false>(swd,lane<35?((li*2+(lw>>1))*35+lane)*128:OOB,0,lbase+L_DUM);
    }
    u32 va=WS_HS+rt2*131072+lw*4096+lane*16;
    const u32 cr=(ct*64+(lw&1)*32+j)*1024;
    u32 vp0=cr+sz0, vp1=cr+7*1024+sz1, vp2=cr+14*1024+sz0, vp3=cr+21*1024+sz1;
    const int gp0=tt-1-4*lw-(lane>>5);
    const u32 vg0=gp0>=0?(u32)(WS_GRAN+gp0*8192+(cg*32+(lane&31))*16):OOB, vg1=gp0>=2?vg0-2*8192-1024:OOB;
    #define SNAP() asm volatile("s_add_u32 m0, %[b], %[i]\n\ts_nop 0\n\tbuffer_load_dwordx4 %[v0], %[r], 0 offen sc1 lds\n\tbuffer_load_dwordx4 %[v1], %[r], 0 offen offset:1024 sc1 lds" \
        :: [v0]"v"(vg0),[v1]"v"(vg1),[r]"s"(sws),[b]"s"(l2),[i]"n"(L_GR) : "memory","scc")
    waitv<TPOS==0?14:12>(); BAR();
    GX(3); GWA(4); GWB(4); SNAP(); waitv<18>(); BAR();
    GX(4); GWA(5); GWB(5); waitv<RELAX?18:10>(); SNAP();
    if(WARMB) dma_dw<false>(sc,lane<32?(u32)(j*1024+l7*128):OOB,(ct*64+(idx&7)*8+(lw&1)*4)*1024,lbase+L_DUM);
    BAR();
    GX(5); GWA(6); GWB(6); waitv<RELAX?18:WARMB?11:10>(); SNAP(); BAR();
    GX(6); GWA(7); GWB(7); waitv<RELAX?18:10>(); BAR();
    u32 ka=ry, kb=ry;
    auto PA=[&](auto C) AI {
      constexpr int c=C.value;
      if constexpr(c==8){ va=WS_HH+rt2*86*2048+lw*4096+lane*16; ka=rz; }
      u32 so;
      if constexpr(c<8){ so=ka<<14; ka=(ka+1)&7; } else { so=(ka==10&&lw==3)?OOB:ka<<14; ka= ka==10?0:ka+1; }
      dma4<true,pa_(c)>(sws,va,va,va,va,so,l4);
    };
    auto PB=[&](auto C) AI {
      constexpr int c=C.value;
      if constexpr(c==8){ vp0=(lw*32+j)*1024+ct*128+((l7^(((j>>1)&1)<<2))<<4); vp1=vp0+7*1024; vp2=vp0+14*1024; vp3=vp0+21*1024; kb=rz; }
      u32 so;
      if constexpr(c<8){ so=kb<<7; kb=(kb+1)&7; } else { so=kb<<17; kb= kb==10?0:kb+1; }
      dma4<false,pb_(c)>(c<8?sc:swd,vp0,vp1,vp2,vp3,so,l4);
    };
    #define IC(n) std::integral_constant<int,(n)>{}
    GX(7); PB(IC(0));
    waitv<14>(); BAR();
    PB(IC(1)); PB(IC(2));
    waitv<12>(); BAR();
    STAMP(4);
    dma_dw<true>(sws,vf,fh,lf);
    PB(IC(3));
    waitv<4>();
    const bool early=all_eq(peek(lf+lane*4),tag);
    if(early){ PA(IC(0)); PA(IC(1)); }
    BAR();
    STAMP(5);
    if(!early){ poll32(sws,vf,fh,tag); PA(IC(0)); PA(IC(1)); }
    STAMP(6);
    if(!LATEA){ PA(IC(2)); PA(IC(3)); PB(IC(4)); }
    waitv<LATEA?4:16>(); BAR();
    if(LATEA){ PA(IC(2)); PA(IC(3)); PB(IC(4)); }
    if(WARMB) dma_dw<false>(swd,lane<43?(u32)(lane*1024):OOB,((idx&7)*172+lw*43)*1024+ct*128,lbase+L_DUM);
    if(lane<16) dma_x4(sws,vs,WS_FX+(rt2*32+lw*8)*128,lbase+L_TAB+lw*256);
    { const u32 vt=(rt2*64+lw*16+j)*1024+ct*128+l7*16; dma2<L_XT>(sx,vt,vt+7*1024,0,l2); }
    waitv<WARMB?16:15>(); BAR();
    waitv<WARMB?12:11>(); BAR();
    bool zok=false;
    sfor<2,18>([&](auto K) AI {
      constexpr int k=K.value;
      if constexpr(k==5) dma_dw<true>(sws,(lane&31)*128,fhh,lf);
      if constexpr(k==9){ if(wgid==0 && lw==0) dma_dw<true>(sws,vf*64,WS_FH,lf); }
      if constexpr(k==6){
        waitv<8>();
        zok=all_eq(peek(lf+lane*4),tag);
        if(zok) PA(IC(8));
        PB(IC(9));
        if(zok) waitv<12>(); else waitv<8>();
      } else if constexpr(k==7){
        STAMP(7);
        if(!zok){ poll32(sws,(lane&31)*128,fhh,tag); PA(IC(8)); }
        STAMP(8);
        PA(IC(9)); PB(IC(10));
        if(zok) waitv<12>(); else waitv<8>();
      } else {
        if constexpr(k<=16) PA(IC(k+2));
        if constexpr(k<=15) PB(IC(k+3));
        waitv<(k==2?(WARMB?16:15):k<=15?12:k==16?8:0)>();
      }
      if constexpr(k==12){
        if(wgid==0 && lw==0){
          if(!all_eq(peek(lf+lane*4),tag)) poll32(sws,vf*64,WS_FH,tag);
          const u32 e=peek(lbase+L_TAG+4)+1u;
          asm volatile("s_mov_b64 exec, 1\n\tbuffer_store_dword %0, off, %1, 0 sc1\n\ts_mov_b64 exec, -1" :: "v"(e), "s"(sws) : "memory");
        }
      }
      BAR();
    });
    STAMP(11);
    STAMP_FLUSH();
    return;
  }

  int h=lane>>5, r=lane&31;
  const rsrc_t rws=mkrsrc(a.ws,WS_END);
  const v4u sws=mksrd(a.ws,WS_END);
  u32 pend=0;
  u32 tag;
  auto try_flag=[&](bool force) AI {
    if(pend && (force || vm_idle())){
      waitv<0>();
      flag_st(sws,pend,tag);
      pend=0;
    }
  };
  if(w>=4){
    const int gw=w-4, rt=2*tt+(w&1);
    __builtin_amdgcn_s_setprio(3);
    const u32 hqo=WS_HH+(rt*86+2*blk0)*2048;
    if(nb==3) tag=g_main<3>(x0,x1,ep0,rws,lbase,lane,gw,tt,cg,hqo TRPASS TSPASS); else tag=g_main<2>(x0,x1,ep0,rws,lbase,lane,gw,tt,cg,hqo TRPASS TSPASS);
    pend=WS_FHH+(rt*32+cg*2+(gw>>1))*128;
    STAMP(5);
  } else {
    const int ch=cg*32+r;
    __builtin_amdgcn_s_setprio(SPRIO);
    const u16 hlr=(u16)x0[0], hli=(u16)x0[1], hdt=(u16)x0[2];
    *(LAS v4u*)(lbase+L_GR+w*2048+lane*16)=v4u{0u,0u,0u,0u}; *(LAS v4u*)(lbase+L_GR+w*2048+1024+lane*16)=v4u{0u,0u,0u,0u};
    const float lr=b2f(hlr), li=b2f(hli), dt=rbf(__builtin_expf(b2f(hdt)));
    cf lam,coef;
    { const float ex=__builtin_expf(dt*lr); float sn,cs_; sincos_(dt*li,sn,cs_); lam=cf{ex*cs_,ex*sn};
      const float dd=1.f/(lr*lr+li*li); coef=cmul(cf{lam.x-1.f,lam.y},cf{lr*dd,-li*dd}); }
    cf l16=cmul(lam,lam); l16=cmul(l16,l16); l16=cmul(l16,l16); l16=cmul(l16,l16);
    cf l32=cmul(l16,l16);
    cf l256,wq,pw;
    { const cf l64=cmul(l32,l32), l128=cmul(l64,l64), o={1.f,0.f};
      l256=cmul(l128,l128); wq=csel(h,l128,o); pw=cmul(csel(w&1,l32,o),csel(w&2,l64,o)); }
    asm volatile("":"+v"(lam),"+v"(coef),"+v"(l16),"+v"(l32),"+v"(l256),"+v"(wq),"+v"(pw));
    v16f are={},aim={};
    {
      u32 ax[4],ab[4];
      #pragma unroll
      for(int s=0;s<4;++s){ const u32 t=lbase+r*128+((((2*s+h)^((r>>1)&7)))<<4); ab[s]=t+sb_(0); ax[s]=t+sx_(0)+w*4096; }
      v4u X[2][2],T[2][2][2];
      auto rd=[&](int b) AI {
        #pragma unroll
        for(int s=0;s<2;++s){ X[b][s]=ldsr128(ax[2*b+s]); T[b][s][0]=ldsr128(ab[2*b+s]); T[b][s][1]=ldsr128(ab[2*b+s]+4096); } };
      auto mm=[&](int b) AI {
        #pragma unroll
        for(int s=0;s<2;++s){ are=mfma(X[b][s],T[b][s][0],are); aim=mfma(X[b][s],T[b][s][1],aim); }
        asm volatile("":"+v"(are),"+v"(aim)); };
      STAMP(1);
      waitl0(); BAR();
      rd(0);
      sfor<0,8>([&](auto C) AI {
        constexpr int c=C.value;
        SB; rd(1);
        SB; mm(0);
        if constexpr(c<7){
          #pragma unroll
          for(int s=0;s<4;++s){ ax[s]+=sx_(c+1)-sx_(c); ab[s]+=sb_(c+1)-sb_(c); asm volatile("":"+v"(ax[s]),"+v"(ab[s])); }
          SB;
          waitl0(); BAR();
          rd(0);
        }
        SB; mm(1);
      });
    }
    STAMP(2);
    tag=rfl(peek(lbase+L_TAG));
    u32 ta=lbase+L_TAB+(32*w+16*h)*16, tb=lbase+L_CSP+r*32, tg=lbase+L_AGG+r*8;
    asm volatile("":"+v"(ta),"+v"(tb),"+v"(tg));
    waitl0(); BAR();
    __builtin_amdgcn_s_setprio(3);
    int left=8;
    #define SBAR() do{ if(left>0){ BAR(); --left; } else { __builtin_amdgcn_s_sleep(4); asm volatile("":::"memory"); } }while(0)
    cf uu[16], elo, Q;
    {
      const v4f c0=*(LAS v4f*)tb, c1=*(LAS v4f*)(tb+16);
      float rs[16],mu[16];
      #pragma unroll
      for(int k=0;k<16;++k){ rs[k]=*(LAS float*)(ta+k*16); mu[k]=*(LAS float*)(ta+k*16+8); }
      SB;
      const cf ncs={-((c0[0]+c0[1])+(c0[2]+c0[3])),-((c1[0]+c1[1])+(c1[2]+c1[3]))};
      cf ea={0.f,0.f};
      #pragma unroll
      for(int k=0;k<16;++k){
        uu[k]=cf{__builtin_fmaf(mu[k],ncs.x,are[k]),__builtin_fmaf(mu[k],ncs.y,aim[k])}*rs[k];
        ea= k? cfma(lam,ea,uu[k]) : uu[0];
      }
      cf ehi=ea; elo=ea; csw32(elo,ehi);
      *(LAS v2f*)(tg+w*256)=cfma(l16,elo,ehi);
      const float hf=(float)h;
      elo*=hf; l16=cf{__builtin_fmaf(hf,l16.x,1.f-hf),hf*l16.y};
      asm volatile("":"+v"(elo),"+v"(l16));
    }
    STAMP(3);
    waitl0(); BAR();
    { const cf a0=ldsc(tg), a1=ldsc(tg+256), a2=ldsc(tg+512), a3=ldsc(tg+768);
      const cf Q2=cfma(l32,a0,a1), Q3=cfma(l32,Q2,a2);
      if(w==3){ const cf E=cfma(l32,Q3,a3); if(lane<32) st128(v4u{f2u(E.x),tag,f2u(E.y),tag},rws,ch*16+WS_GRAN+tt*8192); }
      Q= w==0?cf{0.f,0.f}: w==1?a0: w==2?Q2:Q3; }
    STAMP(4);
    cf K0=cmul(coef,cfma(l16,Q,elo)), K1=cmul(coef,cmul(l16,pw));
    asm volatile("":"+v"(K0),"+v"(K1));
    SBAR();
    #pragma unroll
    for(int k=0;k<16;++k){
      uu[k]=cf{__builtin_fmaf(-coef.y,uu[k].y,coef.x*uu[k].x),__builtin_fmaf(-coef.y,uu[k].x,-coef.x*uu[k].y)};
      asm volatile("":"+v"(uu[k])); if(k==7 && tt>0) SBAR();
    }
    const int rt=2*tt+(w>>1);
    u32 hso=WS_HS+rt*131072+(cg>>1)*16384+(((cg&1)*2+(r>>4))*2+(w&1))*1024+h*512+(((lane&15)^(8*h))<<4), hs2=(hso^64)+256, ga=lbase+L_GR+lane*16;
    asm volatile("":"+v"(hso),"+v"(hs2),"+v"(ga));
    if(tt>0){
      u32 mk[8];
      #pragma unroll
      for(int i=0;i<8;++i){ mk[i]=~(u32)((tt-1-2*i-h)>>31); asm volatile("":"+v"(mk[i])); }
      #pragma unroll 1
      for(int it=0;it<SPIN_CAP;++it){
        if(__builtin_expect(it>=3,0)){
          #pragma unroll 1
          for(int i=2*w;i<2*w+2;++i){ const int pd=tt-1-2*i-h; dma_x4(sws,pd>=0?(u32)(WS_GRAN+pd*8192+ch*16):OOB,0,lbase+L_GR+i*1024); }
        }
        SBAR();
        if(it>=3) waitv<0>();
        v4u t[8];
        #pragma unroll
        for(int i=0;i<8;++i) t[i]=*(LAS v4u*)(ga+i*1024);
        SB;
        u32 bad=0;
        #pragma unroll
        for(int i=0;i<8;++i) bad|=((t[i][1]^tag)|(t[i][3]^tag))&mk[i];
        if(all_eq(bad,0u)) break;
      }
      asm volatile("":::"memory");
      cf gv[8];
      { v4f t[8];
        #pragma unroll
        for(int i=7;i>=0;--i) t[i]=*(LAS v4f*)(ga+i*1024);
        SB;
        #pragma unroll
        for(int i=0;i<8;++i) gv[i]=cf{t[i][0],t[i][2]}; }
      cf H=gv[7];
      #pragma unroll
      for(int i=6;i>=0;--i) H=cfma(l256,H,gv[i]);
      H=cmul(H,wq);
      cf h2=H; csw32(H,h2); H+=h2;
      K0=cfma(K1,H,K0);
    }
    STAMP(5);
    cf hc={K0.x,-K0.y};
    const cf lc={lam.x,-lam.y};
    #pragma unroll
    for(int m=0;m<2;++m){
      cf hv[8];
      #pragma unroll
      for(int jj=0;jj<8;++jj){ hc=cfma(lc,hc,uu[8*m+jj]); hv[jj]=hc; }
      st128(v4u{pk2(hv[0].x,hv[1].x),pk2(hv[2].x,hv[3].x),pk2(hv[4].x,hv[5].x),pk2(hv[6].x,hv[7].x)},rws,m?hs2:hso);
      st128(v4u{pk2(hv[0].y,hv[1].y),pk2(hv[2].y,hv[3].y),pk2(hv[4].y,hv[5].y),pk2(hv[6].y,hv[7].y)},rws,(m?hs2:hso)+8192);
    }
    pend=WS_FH+(rt*32+(cg&1)*16+(cg>>1)*2+(w&1))*128;
    STAMP(6);
    #pragma unroll 1
    while(left>0){
      BAR(); --left;
      #pragma unroll 1
      for(int it=0;it<FWIN && pend;++it){ try_flag(false); if(pend) __builtin_amdgcn_s_sleep(1); else STAMP(7); }
    }
    if(pend){ try_flag(true); STAMP(7); }
  }
  lane=lane_now(); h=lane>>5; r=lane&31;
  const int g=lane>>4, m2=w&1, tc=(w>>1)&1, kh=w>>2;
  const int dcol=ct*64+tc*32+r;
  const float dsk=b2f(a.dsk[dcol]);
  v16f ay={},az={};
  const int q=(lane&15)>>2, p=lane&3, b4=g&1, gp=(b4^kh)+2*(p&1);
  u32 a0=lbase+pa_(0)+(kh*8+m2)*1024+(gp*16+((8*h+q)^(4*gp)))*16+8*(p>>1);
  u32 b0=lbase+pb_(0)+kh*8192+(tc*32+r)*128+((h^((r>>1)&7))<<4);
  u32 za=lbase+pa_(8)+(kh*8+m2)*1024+(lane^(kh*16))*16;
  u32 zb=lbase+pb_(8)+(64*kh+4*h+q)*128+((tc*64+b4*32+8*p)^(((q>>1)&1)<<6));
  u32 d0=pa_(1)-pa_(0), d1=pa_(2)-pa_(1), d2=pa_(3)-pa_(2), d3=pa_(0)-pa_(3), e0=XS; int eb=0;
  v4u A[2][4],B[2][4];
  auto rot=[&]() AI { const u32 t=d0; d0=d1; d1=d2; d2=d3; d3=t; eb= eb==4?0:eb+1; e0= eb==4?(u32)(-4*(int)XS):XS; };
  auto rdy=[&](int b) AI {
    #pragma unroll
    for(int s=0;s<4;++s){ A[b][s]=ldstr(a0+s*2048,(a0^64)+s*2048); B[b][s]=ldsr128(b0^(s*32)); }
    a0+=d0; b0+=e0; rot();
  };
  auto rdz=[&](int b) AI {
    #pragma unroll
    for(int s=0;s<4;++s){ A[b][s]=ldsr128(za+s*2048); B[b][s]=ldstr(zb+s*2048,zb+s*2048+1024); }
    za+=d0; zb+=e0; rot();
  };
  auto mm4=[&](int b,v16f& c) AI {
    #pragma unroll
    for(int s=0;s<4;++s) c=mfma(A[b][s],B[b][s],c);
    #pragma unroll
    for(int s=0;s<4;++s){ SG(0x8,1); SG(0x100,3); }
    SB; asm volatile("":"+v"(c));
  };
  #define NXT() BAR()
  const int tk=32*m2+16*h+8*kh;
  STAMP(8);
  __builtin_amdgcn_s_setprio(0);
  NXT(); rdy(0);
  STAMP(9);
  #pragma unroll 1
  for(int c=0;c<3;++c){
    NXT(); rdy(1); mm4(0,ay); try_flag(false);
    NXT(); rdy(0); mm4(1,ay); try_flag(c==2);
  }
  NXT(); rdy(1); mm4(0,ay);
  NXT(); rdz(0); mm4(1,ay);
  STAMP(10);
  float sv[8], xf[8], r2[8];
  u32 pt=lbase+L_TAB+tk*16, px=lbase+L_XT+tk*128+(tc*32+r)*2;
  asm volatile("":"+v"(pt),"+v"(px));
  #pragma unroll 1
  for(int c=0;c<4;++c){
    NXT(); rdz(1); mm4(0,az);
    if(c==1){
      #pragma unroll
      for(int k=0;k<8;++k){ r2[k]=*(LAS float*)(pt+k*16); sv[k]=*(LAS float*)(pt+k*16+8); xf[k]=u2f(*(LAS u16*)(px+k*128)); }
    }
    NXT(); rdz(0); mm4(1,az);
    if(c==1){
      #pragma unroll
      for(int k=0;k<8;++k){ xf[k]=u2f(f2u(xf[k])<<16); sv[k]=(xf[k]-sv[k])*r2[k]*dsk; r2[k]*=r2[k]; }
    }
  }
  {
    const u32 xo=lbase+WB+w*2048+lane*16, xi=lbase+WB+(w^4)*2048+lane*16;
    const rsrc_t ro=mkrsrc(a.out,2048*512*4);
    u32 oo=((rt2*64+tk)*512+dcol)*4;
    NXT(); rdz(1); mm4(0,az);
    *(LAS v4f*)xo=v4f{ay[8],ay[9],ay[10],ay[11]}; *(LAS v4f*)(xo+1024)=v4f{ay[12],ay[13],ay[14],ay[15]};
    SB;
    #pragma unroll
    for(int s=0;s<4;++s) az=mfma(A[1][s],B[1][s],az);
    asm volatile("":"+v"(az),"+v"(oo));
    waitl0(); NXT(); rdz(0);
    const v4f i0=*(LAS v4f*)xi, i1=*(LAS v4f*)(xi+1024);
    SB;
    #pragma unroll
    for(int s=0;s<4;++s) az=mfma(A[0][s],B[0][s],az);
    float ce[8];
    #pragma unroll
    for(int k=0;k<8;++k){ sv[k]=__builtin_fmaf(2.f,ay[k]+(k<4?i0[k&3]:i1[k&3]),sv[k])*r2[k]; ce[k]=r2[k]*-1.44269504f; asm volatile("":"+v"(sv[k]),"+v"(ce[k])); }
    STAMP(11);
    *(LAS v4f*)(xo+XS)=v4f{az[8],az[9],az[10],az[11]}; *(LAS v4f*)(xo+XS+1024)=v4f{az[12],az[13],az[14],az[15]};
    waitl0(); bar();
    const v4f i2=*(LAS v4f*)(xi+XS), i3=*(LAS v4f*)(xi+XS+1024);
    #pragma unroll
    for(int k=0;k<8;++k){
      const float z=az[k]+(k<4?i2[k&3]:i3[k&3]);
      const float gt=z*__builtin_amdgcn_rcpf(1.f+__builtin_amdgcn_exp2f(z*ce[k]));
      __builtin_amdgcn_raw_buffer_store_b32(f2u(__builtin_fmaf(sv[k],gt,xf[k])),ro,oo+k*2048,0,OUTAUX);
    }
  }
  CSTAMP(14); STAMP(13); STAMP_FLUSH();
}

extern "C" void kernel_launch(void* const* d_in, const int* in_sizes, int n_in,
                              void* d_out, int out_size, void* d_ws, size_t ws_size,
                              hipStream_t stream) {
  Args a;
  a.x=(const u16*)d_in[0]; a.lre=(const u16*)d_in[1]; a.lim=(const u16*)d_in[2];
  a.bre=(const u16*)d_in[3]; a.bim=(const u16*)d_in[4]; a.cre=(const u16*)d_in[5]; a.cim=(const u16*)d_in[6];
  a.ldt=(const u16*)d_in[7]; a.dsk=(const u16*)d_in[8];
  a.wg=(const u16*)d_in[10]; a.wu=(const u16*)d_in[11]; a.wd=(const u16*)d_in[12];
  a.out=(float*)d_out; a.ws=(char*)d_ws;
  fused_k<<<256,768,0,stream>>>(a);
}
```

```cpp
#include <hip/hip_runtime.h>
#include <hip/hip_bf16.h>
#include <type_traits>

typedef unsigned u32; typedef unsigned long long u64; typedef unsigned short u16;
typedef u32 v4u __attribute__((ext_vector_type(4)));
typedef u32 v2u __attribute__((ext_vector_type(2)));
typedef short v4s __attribute__((ext_vector_type(4)));
typedef __bf16 v2bf __attribute__((ext_vector_type(2)));
typedef __bf16 v8bf __attribute__((ext_vector_type(8)));
typedef float v2f __attribute__((ext_vector_type(2)));
typedef float v4f __attribute__((ext_vector_type(4)));
typedef float v16f __attribute__((ext_vector_type(16)));
#define LAS __attribute__((address_space(3)))
#define DEVI __device__ __forceinline__
#define AI __attribute__((always_inline))
#define SB __builtin_amdgcn_sched_barrier(0)
#if defined(__HIP_DEVICE_COMPILE__)
#define NOPK __attribute__((target("no-packed-fp32-ops")))
#else
#define NOPK
#endif

#define ROT1 0x01
#define ROTP 1
#define SPRIO 2
#define OUTAUX 16
#define HSLEEP 2
#define FSPLIT 0
#define FWIN 10
#define LATEA 0
#define LLCPF 0
#define LNFIRST 1
#define WARMP 1
#define RELAX 1
#define TX 760
#define ROTI 1
#define TPOS 0
#define CWIN 0
#define RY 1
#define RZ 2
#define CSPLIT 5
#define LBSLEEP 6
#define WARMB 0

constexpr u32 WS_EPOCH = 0;
constexpr u32 WS_FX    = 32768;
constexpr u32 WS_FH    = WS_FX + 131072;
constexpr u32 WS_FHH   = WS_FH + 131072;
constexpr u32 WS_GRAN  = WS_FHH + 131072;
constexpr u32 WS_HH    = WS_GRAN + 131072;
constexpr u32 WS_HS    = WS_HH + 32*86*2048;
constexpr u32 WS_END   = WS_HS + 32*131072;
constexpr u32 WS_STAMP = 16u<<20;
constexpr u32 XS = 16384;
constexpr u32 WB = 49152, WSZ = 24832;
constexpr u32 L_XT = WB+4*WSZ;
constexpr u32 L_TAB  = L_XT+8192;
constexpr u32 L_CSP  = L_TAB + 2048;
constexpr u32 L_FLG  = L_CSP;
constexpr u32 L_AGG  = L_CSP + 1024;
constexpr u32 L_DUM  = L_TAB + 6144;
constexpr u32 L_TAG  = L_DUM + 256;
constexpr u32 L_GR   = L_XT;
constexpr u32 L_CWX  = L_TAB;
static_assert(L_CWX%1024==0,"address bits 8,9 carry lane and wave");
constexpr u32 L_TOTAL = 163840;
static_assert(L_TAG+16<=L_TOTAL,"lds");
constexpr u32 OOB = 0x40000000u;
constexpr int SPIN_CAP = 1<<18;

#define CODEPF 12
constexpr u32 CODE_LEN=$n;
#define CODE_EXTRA 0
struct Args { const u16 *x,*lre,*lim,*bre,*bim,*cre,*cim,*ldt,*dsk,*wg,*wu,*wd; float* out; char* ws; };

DEVI u32 f2u(float f){ return __builtin_bit_cast(u32,f); }
DEVI float u2f(u32 u){ return __builtin_bit_cast(float,u); }
DEVI u32 rfl(u32 v){ return __builtin_amdgcn_readfirstlane(v); }
DEVI bool all_eq(u32 v,u32 t){ return __builtin_amdgcn_ballot_w64(v!=t)==0ull; }
DEVI v4u mksrd(const void* p, u32 bytes){ u64 a=(u64)p; v4u s; s.x=rfl((u32)a); s.y=rfl((u32)(a>>32)); s.z=bytes; s.w=0x00020000u; return s; }
#define NTM 0
#define POL1 ""
#define POL2 ""
#define POL3 ""
#define POL4 ""
#define DM0 "s_add_u32 m0, %[b], %[i]\n\ts_nop 0\n\t"
#define DLD(n,off,pol) "buffer_load_dwordx4 %[v" #n "], %[r], %[s] offen" off pol " lds\n\t"
template<bool SC1,u32 IMM> DEVI void dma4(v4u srd,u32 v0,u32 v1,u32 v2,u32 v3,u32 soff,u32 base){
  if(SC1) asm volatile(DM0 DLD(0,""," sc1") DLD(1," offset:1024"," sc1") DLD(2," offset:2048"," sc1") DLD(3," offset:3072"," sc1")
      :: [v0]"v"(v0),[v1]"v"(v1),[v2]"v"(v2),[v3]"v"(v3),[r]"s"(srd),[s]"s"(soff),[b]"s"(base),[i]"n"(IMM) : "memory","scc");
  else asm volatile(DM0 DLD(0,"",POL4) DLD(1," offset:1024",POL4) DLD(2," offset:2048",POL4) DLD(3," offset:3072",POL4)
      :: [v0]"v"(v0),[v1]"v"(v1),[v2]"v"(v2),[v3]"v"(v3),[r]"s"(srd),[s]"s"(soff),[b]"s"(base),[i]"n"(IMM) : "memory","scc");
}
template<u32 IMM> DEVI void dma3(v4u srd,u32 v0,u32 v1,u32 v2,u32 soff,u32 base){
  asm volatile(DM0 DLD(0,"",POL3) DLD(1," offset:1024",POL3) DLD(2," offset:2048",POL3)
      :: [v0]"v"(v0),[v1]"v"(v1),[v2]"v"(v2),[r]"s"(srd),[s]"s"(soff),[b]"s"(base),[i]"n"(IMM) : "memory","scc");
}
template<u32 IMM> DEVI void dma2(v4u srd,u32 v0,u32 v1,u32 soff,u32 base){
  asm volatile(DM0 DLD(0,"",POL2) DLD(1," offset:1024",POL2)
      :: [v0]"v"(v0),[v1]"v"(v1),[r]"s"(srd),[s]"s"(soff),[b]"s"(base),[i]"n"(IMM) : "memory","scc");
}
template<bool SC1> DEVI void dma_dw(v4u srd,u32 voff,u32 soff,u32 lds){
  if(SC1) asm volatile("s_mov_b32 m0, %3\n\ts_nop 0\n\tbuffer_load_dword %0, %1, %2 offen sc1 lds" :: "v"(voff), "s"(srd), "s"(soff), "s"(lds) : "memory");
  else    asm volatile("s_mov_b32 m0, %3\n\ts_nop 0\n\tbuffer_load_dword %0, %1, %2 offen" POL1 " lds" :: "v"(voff), "s"(srd), "s"(soff), "s"(lds) : "memory");
}
DEVI void dma_x4(v4u srd,u32 voff,u32 soff,u32 lds){
  asm volatile("s_mov_b32 m0, %3\n\ts_nop 0\n\tbuffer_load_dwordx4 %0, %1, %2 offen sc1 lds" :: "v"(voff), "s"(srd), "s"(soff), "s"(lds) : "memory");
}
template<int N> DEVI void waitv(){ asm volatile("s_waitcnt vmcnt(%0)"::"n"(N):"memory"); }
DEVI void waitl0(){ asm volatile("s_waitcnt lgkmcnt(0)":::"memory"); }
DEVI void bar(){ asm volatile("":::"memory"); __builtin_amdgcn_s_barrier(); asm volatile("":::"memory"); SB; }
template<u32 OFF> DEVI void sig(u32 vaddr,u32 one){ asm volatile("s_mov_b64 exec, 1\n\tds_add_u32 %0, %1 offset:%2\n\ts_mov_b64 exec, -1" :: "v"(vaddr), "v"(one), "n"(OFF) : "memory"); }
template<u32 OFF> DEVI void sigm(u32 vaddr){ asm volatile("s_mov_b64 exec, 1\n\tds_inc_u32 %0, %0 offset:%1\n\ts_mov_b64 exec, -1" :: "v"(vaddr), "n"(OFF) : "memory"); }
DEVI u32 peek(u32 vaddr){ return *(volatile LAS u32*)vaddr; }
DEVI bool vm_idle(){ u32 v; asm volatile("s_getreg_b32 %0, hwreg(HW_REG_IB_STS, 0, 4)":"=s"(v)); return v==0u; }
DEVI void flag_st(v4u srd,u32 soff,u32 val){ asm volatile("s_mov_b64 exec, 1\n\tbuffer_store_dword %0, off, %1, %2 sc1\n\ts_mov_b64 exec, -1" :: "v"(val), "s"(srd), "s"(soff) : "memory"); }
DEVI int lane_now(){ int l; asm volatile("v_mbcnt_lo_u32_b32 %0, -1, 0\n\tv_mbcnt_hi_u32_b32 %0, -1, %0":"=v"(l)); return l; }
DEVI float b2f(u16 v){ return u2f(((u32)v)<<16); }
DEVI u32 pk2(float lo,float hi){ v2f p={lo,hi}; return __builtin_bit_cast(u32,__builtin_convertvector(p,v2bf)); }
DEVI float rbf(float f){ return u2f(pk2(f,f)<<16); }
DEVI float relu(float x){ int i=__builtin_bit_cast(int,x); return __builtin_bit_cast(float,i>0?i:0); }
DEVI float dot2(u32 a,u32 b,float c){ return __builtin_amdgcn_fdot2_f32_bf16(__builtin_bit_cast(v2bf,a),__builtin_bit_cast(v2bf,b),c,false); }
DEVI v16f mfma(v4u a,v4u b,v16f c){ return __builtin_amdgcn_mfma_f32_32x32x16_bf16(__builtin_bit_cast(v8bf,a),__builtin_bit_cast(v8bf,b),c,0,0,0); }
DEVI v4f mfma16(v4u a,v4u b,v4f c){ return __builtin_amdgcn_mfma_f32_16x16x32_bf16(__builtin_bit_cast(v8bf,a),__builtin_bit_cast(v8bf,b),c,0,0,0); }
DEVI v4u ldsr128(u32 a){ return *(LAS v4u*)a; }
DEVI v4u ldstr(u32 a0,u32 a1){
  v4s x=__builtin_amdgcn_ds_read_tr16_b64_v4i16((LAS v4s*)a0);
  v4s y=__builtin_amdgcn_ds_read_tr16_b64_v4i16((LAS v4s*)a1);
  return __builtin_bit_cast(v4u,__builtin_shufflevector(x,y,0,1,2,3,4,5,6,7));
}
DEVI void sw16(float& a,float& b){ v2u r=__builtin_amdgcn_permlane16_swap(f2u(a),f2u(b),false,false); a=u2f(r[0]); b=u2f(r[1]); }
DEVI void sw32(float& a,float& b){ v2u r=__builtin_amdgcn_permlane32_swap(f2u(a),f2u(b),false,false); a=u2f(r[0]); b=u2f(r[1]); }
template<int CTRL> DEVI float dppadd(float v){ return v+u2f(__builtin_amdgcn_update_dpp(0u,f2u(v),CTRL,0xf,0xf,true)); }
DEVI void wsum4(float& a,float& b,float& c,float& d){
  sw32(a,b); sw32(c,d); float v=a+b, t=c+d;
  sw16(v,t); v+=t;
  v=dppadd<0xB1>(v); v=dppadd<0x4E>(v); v=dppadd<0x141>(v); v=dppadd<0x140>(v);
  a=u2f(__builtin_amdgcn_readlane(f2u(v),0)); c=u2f(__builtin_amdgcn_readlane(f2u(v),16));
  b=u2f(__builtin_amdgcn_readlane(f2u(v),32)); d=u2f(__builtin_amdgcn_readlane(f2u(v),48));
}
typedef v2f cf;
DEVI cf cfma(cf a,cf b,cf c){ return cf{__builtin_fmaf(-a.y,b.y,__builtin_fmaf(a.x,b.x,c.x)), __builtin_fmaf(a.y,b.x,__builtin_fmaf(a.x,b.y,c.y))}; }
DEVI cf cmul(cf a,cf b){ return cf{__builtin_fmaf(-a.y,b.y,a.x*b.x), __builtin_fmaf(a.y,b.x,a.x*b.y)}; }
DEVI cf csel(bool c,cf a,cf b){ return cf{c?a.x:b.x, c?a.y:b.y}; }
DEVI cf ldsc(u32 a){ return *(LAS v2f*)a; }
DEVI void csw32(cf& a,cf& b){ float x=a.x,y=b.x,z=a.y,t=b.y; sw32(x,y); sw32(z,t); a=cf{x,z}; b=cf{y,t}; }
DEVI void sincos_(float x,float& s,float& c){
  const float n=__builtin_rintf(x*0.636619772f);
  float r=__builtin_fmaf(n,-1.57079601e+00f,x); r=__builtin_fmaf(n,-3.13916473e-07f,r); r=__builtin_fmaf(n,-5.39030253e-15f,r);
  const float z=r*r;
  float ps=__builtin_fmaf(__builtin_fmaf(__builtin_fmaf(2.86567956e-6f,z,-1.98559923e-4f),z,8.33338592e-3f),z,-1.66666672e-1f);
  ps=__builtin_fmaf(ps*z,r,r);
  float pc=__builtin_fmaf(__builtin_fmaf(__builtin_fmaf(2.44677067e-5f,z,-1.38877297e-3f),z,4.16666567e-2f),z,-0.5f);
  pc=__builtin_fmaf(pc,z,1.f);
  const int q=(int)n;
  const float a=(q&1)?pc:ps, b=(q&1)?ps:pc;
  s=(q&2)?-a:a; c=((q+1)&2)?-b:b;
}
typedef __amdgpu_buffer_rsrc_t rsrc_t;
DEVI rsrc_t mkrsrc(const void* p,u32 bytes){ return __builtin_amdgcn_make_buffer_rsrc((void*)p,0,bytes,0x00020000); }
DEVI void st128(v4u v,rsrc_t r,u32 off){ __builtin_amdgcn_raw_buffer_store_b128(v,r,off,0,16); }
DEVI u32 mktag(u32 e){ return (e*0x9E3779B1u+0x7F4A7C15u)|1u; }

#define STAMP(k) do{}while(0)
#define CSTAMP(k) do{}while(0)
#define TRACE(k) do{}while(0)
#define STAMP_DECL do{}while(0)
#define BAR() bar()
#define STAMP_FLUSH() do{}while(0)
#define TRARG
#define TRPASS
#define TSARG
#define TSPASS

DEVI void poll32(v4u srd,u32 voff,u32 soff,u32 tag){
  #pragma unroll 1
  for(int it=0;it<SPIN_CAP;++it){
    u32 v;
    asm volatile("buffer_load_dword %0, %1, %2, %3 offen sc1\n\ts_waitcnt vmcnt(0)" : "=&v"(v) : "v"(voff), "s"(srd), "s"(soff) : "memory");
    if(all_eq(v,tag)) break;
    __builtin_amdgcn_s_sleep(12);
  }
}
template<int I,int N,class F> DEVI void sfor(F&& f){ if constexpr(I<N){ f(std::integral_constant<int,I>{}); sfor<I+1,N>(f); } }
AI constexpr u32 wslot(int c){ return WB+(c&3)*WSZ; }
AI constexpr u32 pa_(int c){ return (c&3)==3?WB+5*XS:(((c&3)+2)%3)*XS; }
AI constexpr u32 pb_(int c){ return WB+(c%5)*XS; }
AI constexpr u32 sx_(int c){ return c>=5?(c-5)*XS:wslot(c+2); }
AI constexpr u32 sb_(int c){ return c>=5?wslot(3)+(c-5)*8192:wslot(c+2)+16384; }

#define SG(m,n) __builtin_amdgcn_sched_group_barrier(m,n,0)
template<int NB>
DEVI u32 g_main(v4u x0,v4u x1,u32 e,rsrc_t rws,u32 lbase,int lane,int gw,int tt,int cg,u32 hqo TRARG TSARG){
  const int h=lane>>5, r=lane&31, rh=gw&1, u0=(gw>>1)*NB;
  const int row=tt*128+cg*8+gw*2;
  v16f acc[3][2]; v4f cw[3];
  u32 ax[4],aw,aw2,nm[2],cwa;
  const u32 on2=((lane&3)==((lane>>4)&1))?0x3f803f80u:0u;
  v4u ones={on2,on2,on2,on2};
  u32 tag;
  {
    u32 ab=lbase+sb_(0)+r*128+((((2*gw+h)^((r>>1)&7)))<<4);
    v4f c0={},c1={};
    sfor<0,8>([&](auto C) AI {
      constexpr int c=C.value;
      if constexpr(c!=0 || !LNFIRST){ waitl0(); BAR(); }
      v4u t0,t1;
      if constexpr(c!=0 || !LNFIRST){ t0=ldsr128(ab); t1=ldsr128(ab+4096); }
      if constexpr(c==0){
        const float z0=u2f(rfl(x0[0])<<16), z1=u2f(rfl(x1[0])<<16);
        float s0=0.f,s1=0.f,v0=0.f,v1=0.f;
        #pragma unroll
        for(int k=0;k<8;++k){
          const float d0=u2f((k&1)?x0[k>>1]&0xffff0000u:x0[k>>1]<<16)-z0, d1=u2f((k&1)?x1[k>>1]&0xffff0000u:x1[k>>1]<<16)-z1;
          s0+=d0; s1+=d1; v0=__builtin_fmaf(d0,d0,v0); v1=__builtin_fmaf(d1,d1,v1); }
        wsum4(s0,s1,v0,v1);
        const float sm=(lane?s1:s0)*(1.f/512.f);
        const u32 vm=pk2(relu(__builtin_fmaf(lane?v1:v0,1.f/512.f,-sm*sm)),sm+(lane?z1:z0));
        float rs=__builtin_amdgcn_rcpf(rbf(__builtin_amdgcn_sqrtf(rbf(u2f(vm<<16)+1e-5f))));
        asm volatile("":"+v"(rs));
        STAMP(1);
        if(LNFIRST==2){ BAR(); t0=ldsr128(ab); t1=ldsr128(ab+4096); }
        tag=mktag(rfl(e));
        if(lane<2) st128(v4u{f2u(rs),tag,vm&0xffff0000u,tag},rws,WS_FX+row*64+lane*16);
        if(gw==0) *(LAS v2u*)(lbase+L_TAG)=v2u{tag,e};
        if(LNFIRST==1){ waitl0(); BAR(); t0=ldsr128(ab); t1=ldsr128(ab+4096); }
      }
      if constexpr(c<7) ab+=sb_(c+1)-sb_(c);
      if constexpr(c>=1 && c<=2*NB){ acc[(c-1)>>1][(c-1)&1]=v16f{}; asm volatile("":"+v"(acc[(c-1)>>1][(c-1)&1])); }
      c0=mfma16(ones,t0,c0); c1=mfma16(ones,t1,c1);
      asm volatile("":"+v"(c0),"+v"(c1),"+v"(ab));
    });
    asm volatile("s_nop 7\n\ts_nop 1":"+v"(c0),"+v"(c1));
    const bool hi=lane&16;
    *(LAS float*)(lbase+L_CSP+(r*8+h*4+gw)*4)=h?(hi?c1[1]:c1[0]):(hi?c0[1]:c0[0]);
    STAMP(2);
    waitl0(); BAR();
  }
  #pragma unroll
  for(int s=0;s<4;++s) ax[s]=lbase+rh*8192+r*128+((((2*(s^(2*rh))+h)^((r>>1)&7)))<<4);
  aw=lbase+WB+((lane>>4)&1)*(12288+32)+(8*h+((lane&15)>>2))*192+(lane&3)*8+u0*32;
  aw2=aw+6144-rh*6144; aw+=rh*6144;
  #pragma unroll
  for(int t=0;t<3;++t){ cw[t]=v4f{}; if(t<NB) asm volatile("":"+v"(cw[t])); }
  #pragma unroll
  for(int m=0;m<2;++m){
    const u32 mu=peek(lbase+L_TAB+(rh*64+m*32+16*((r>>2)&1)+8*(r>>4)+4*((r>>3)&1)+(r&3))*16+8);
    nm[m]=((mu>>16)|mu)^0x80008000u;
  }
  cwa=lbase+L_CWX+(gw*32+r)*16;
  asm volatile("":"+v"(ax[0]),"+v"(ax[1]),"+v"(ax[2]),"+v"(ax[3]),"+v"(aw),"+v"(aw2),"+v"(nm[0]),"+v"(nm[1]),"+v"(cwa));
  BAR();
  __builtin_amdgcn_s_setprio(2);
  v4u X[2][2],T[2][3];
  auto rd=[&](int b,int s) AI {
    const u32 ad=(s<2?aw:aw2)+(s&1)*3072;
    X[b][0]=ldsr128(ax[s]);
    #pragma unroll
    for(int t=0;t<NB;++t){ T[b][t]=ldstr(ad+t*32,ad+t*32+768); if(t==0) X[b][1]=ldsr128(ax[s]+4096); }
  };
  auto mm=[&](int b,bool cs) AI {
    #pragma unroll
    for(int t=0;t<NB;++t){ acc[t][0]=mfma(T[b][t],X[b][0],acc[t][0]); if(cs) cw[t]=mfma16(ones,T[b][t],cw[t]); acc[t][1]=mfma(T[b][t],X[b][1],acc[t][1]); }
  };
  auto mix=[&](int nv,bool cs) AI {
    SG(0x8,1); SG(0x100,2); if(cs) SG(0x8,2); else SG(0x8,1); SG(0x100,2); SG(0x8,1); SG(0x100,2);
    if(NB==3){ if(cs) SG(0x8,2); else SG(0x8,1); SG(0x100,2); }
    SG(0x8,1); if(nv) SG(0x2,3);
    SG(0x8,5);
  };
  u32 d0=XS, d1=XS, d2=(u32)(-2*(int)XS);
  STAMP(3); CSTAMP(6);
  rd(0,0);
  #pragma unroll 1
  for(int c=0;c<8;++c){
    const u32 dw=((c&3)==3)?(u32)(-3*(int)WSZ):WSZ;
    SB; rd(1,1); mm(0,true); ax[0]+=d0; ax[1]+=d0; aw+=dw; mix(1,true);
    SB; asm volatile("":"+v"(ax[0]),"+v"(ax[1]),"+v"(aw));
    rd(0,2); mm(1,true); mix(0,true);
    SB; rd(1,3); mm(0,false); ax[2]+=d0; ax[3]+=d0; aw2+=dw; mix(1,false);
    SB; asm volatile("":"+v"(ax[2]),"+v"(ax[3]),"+v"(aw2));
    { const u32 t=d0; d0=d1; d1=d2; d2=t; }
    if(c==7){
      u32 f[3][2]={};
      #pragma unroll
      for(int t=0;t<NB;++t){
        const float lo=cw[t][0], hi=cw[t][1], cs=(cwa&256u)?hi:lo;
        const u32 c0=f2u(cs)&0xffff0000u; const float r1=cs-u2f(c0);
        const u32 c1=f2u(r1)&0xffff0000u; const float r2=r1-u2f(c1);
        f[t][0]=(c0>>16)|c1; f[t][1]=f2u(r2)>>16;
      }
      *(LAS v4u*)cwa=v4u{f[0][0],f[0][1],f[1][0],f[1][1]};
      if(NB==3) *(LAS v2u*)(cwa+4096)=v2u{f[2][0],f[2][1]};
      ax[0]=cwa^((u32)(lane_now()&32)<<4);
    }
    waitl0(); BAR();
    rd(0,0); mm(1,false); mix(0,false);
  }
  const int ln=lane_now();
  #pragma unroll
  for(int t=0;t<NB;++t)
    #pragma unroll
    for(int m=0;m<2;++m) acc[t][m]=mfma(v4u{X[0][t>>1][2*(t&1)],X[0][t>>1][2*(t&1)+1],0u,0u},v4u{nm[m],nm[m],0u,0u},acc[t][m]);
  STAMP(4); CSTAMP(7);
  #pragma unroll
  for(int t=0;t<NB;++t)
    #pragma unroll
    for(int m=0;m<2;++m){
      SB;
      v4u v;
      #pragma unroll
      for(int k=0;k<4;++k) v[k]=pk2(relu(acc[t][m][2*k])*acc[t][m][2*k+8],relu(acc[t][m][2*k+1])*acc[t][m][2*k+9]);
      st128(v,rws,hqo+(u0+t)*2048+m*1024+ln*16);
    }
  return tag;
}

__global__ NOPK __launch_bounds__(768,3) void fused_k(Args a){
  __shared__ __attribute__((aligned(256))) char smem[L_TOTAL];
  const u32 lbase=(u32)(size_t)(LAS char*)smem;
  const int tid=__builtin_amdgcn_workitem_id_x();
  const int wgid=__builtin_amdgcn_workgroup_id_x(), w=rfl(tid>>6);
  int lane=tid&63;
  const int xcd=wgid&7, idx=wgid>>3;
  if(CODEPF && w==CODEPF-1){
    u64 pc; u32 back; asm volatile("s_getpc_b64 %0\n.Lpcx%=:\n\ts_mov_b32 %1, .Lpcx%=-_Z7fused_k4Args":"=&s"(pc),"=s"(back));
    dma_dw<false>(mksrd((const void*)(pc-back),CODE_LEN+CODE_EXTRA),(u32)(((idx&3)*64+lane)*128),0,lbase+L_DUM);
  }
  const int tt=(xcd&1)*8+(idx&7), cg=(xcd>>1)*4+(idx>>3);
  const int nb=((cg&3)==3||cg==14)?2:3, blk0=11*(cg>>2)+3*(cg&3)-(cg==15?1:0);
  const int rt2=(xcd&3)*8+(idx&7), ct=(xcd>>2)*4+(idx>>3);
  const int ry=((RY==4?2*(idx>>3):RY==2?4*((idx>>3)&1):0)+(idx&7))&7, rz=RZ==4?3*(idx>>3):RZ==2?6*((idx>>3)&1):RZ==3?(idx>>3):0;
  const u32 one=1;
  STAMP_DECL; STAMP(0); CSTAMP(12);

  v4u x0,x1; u32 ep0,dsk_;
  const u16* dp=a.dsk+ct*64+((w>>1)&1)*32+(lane&31);
  if(w<4){ const int c_=cg*32+(lane&31); x0[0]=a.lre[c_]; x0[1]=a.lim[c_]; x0[2]=a.ldt[c_]; dsk_=*dp; }
  else if(w<8){ const int row=tt*128+cg*8+(w-4)*2; x0=((const v4u*)a.x)[row*64+lane]; x1=((const v4u*)a.x)[row*64+64+lane];
    ep0=__builtin_amdgcn_raw_buffer_load_b32(mkrsrc(a.ws,WS_END),0,WS_EPOCH,16); dsk_=*dp; }
  asm volatile("":::"memory");
  if(__builtin_expect(w>=8,1)){
    __builtin_amdgcn_s_setprio(3);
    const int lw=w-8, j=lane>>3, l7=lane&7;
    const v4u sx=mksrd(a.x,2048*1024);
    const v4u sb=mksrd(lw<2?a.bre:a.bim,512*1024);
    const u32 sz0=(u32)((l7^(j>>1))<<4), sz1=sz0^64u;
    const u32 xr=(tt*128+32*lw+16*(j>>2)+(j&3))*1024;
    const u32 vx0=xr+sz0, vx1=xr+3*1024+sz1, vx2=xr+6*1024+sz0, vx3=xr+9*1024+sz1;
    const u32 br=(cg*32+16*(lw&1)+j)*1024;
    const u32 vb0=br+sz0, vb1=br+7*1024+sz1;
    const u32 l4=lbase+lw*4096, l2=lbase+lw*2048;
    u32 kx=((tt*(ROT1&15)+cg*(ROT1>>4))&7)*128, kw=(kx+640)&0x380;
    #define SX(c) dma4<false,sx_(c)>(sx,vx0,vx1,vx2,vx3,kx,l4)
    #define SBB(c) do{ dma2<sb_(c)>(sb,vb0,vb1,kx,l2); kx=(kx+128)&0x380; }while(0)
    SX(0); SBB(0); SX(1); SBB(1);
    const v4u sws=mksrd(a.ws,WS_END);
    const v4u sw=mksrd(lw<2?a.wg:a.wu,512*2752);
    const v4u swd=mksrd(a.wd,1376*1024);
    const v4u sc=mksrd(lw<2?a.cre:a.cim,512*1024);
    const u32 vf=(lane&15)*128, lf=lbase+L_FLG+lw*256;
    const u32 fx=WS_FX+(tt*64+lw*16)*128, fh=WS_FH+(rt2*32+(lw&1)*16)*128, fhh=WS_FHH+rt2*4096;
    u32 vw[6];
    #pragma unroll
    for(int i=0;i<3;++i){ const u32 q=i*1024+lane*16, row=q/192, col=q%192; const bool ok=col<(u32)nb*64;
      vw[i]=ok?(row+32*(lw&1))*2752+col+blk0*64-i*1024:OOB; vw[i+3]=vw[i]+16*2752; asm volatile("":"+v"(vw[i]),"+v"(vw[i+3])); }
    const u32 lwv=lbase+(lw>>1)*(12288+32)+(lw&1)*6144;
    #define GWA(c) dma3<wslot(c)>(sw,vw[0],vw[1],vw[2],kw*1376,lwv)
    #define GWB(c) do{ dma3<wslot(c)+3072>(sw,vw[3],vw[4],vw[5],kw*1376,lwv); kw=(kw+128)&0x380; }while(0)
    #define GX(c) do{ dma4<false,((c)%3)*XS>(sx,vx0,vx1,vx2,vx3,kx,l4); kx=(kx+128)&0x380; }while(0)
    SX(2); waitv<10>(); BAR();
    SBB(2); SX(3); waitv<10>(); BAR();
    SBB(3); SX(4); waitv<10>(); BAR();
    SBB(4); SX(5); waitv<10>(); BAR();
    SBB(5); SX(6); SBB(6); waitv<12>(); BAR();
    SX(7); SBB(7); waitv<12>(); BAR();
    STAMP(2);
    const u32 vs=(lane>>1)*128+(lane&1)*16, lt=lbase+L_TAB+lw*512;
    const u32 tag=rfl(peek(lbase+L_TAG));
    if(lane<32) dma_x4(sws,vs,fx,lt);
    GWA(0); waitv<10>(); BAR();
    GWB(0); waitv<7>(); BAR();
    waitv<6>();
    #pragma unroll 1
    for(int it=0;it<SPIN_CAP;++it){
      const v4u t=*(volatile LAS v4u*)(lt+(lane&31)*16);
      if(all_eq((t[1]^tag)|(t[3]^tag),0u)) break;
      __builtin_amdgcn_s_sleep(2);
      if(lane<32) dma_x4(sws,vs,fx,lt);
      waitv<0>();
    }
    BAR();
    if(TPOS==1){
      dma_dw<false>(sc,(lane<32&&!(ROTI&1))?(u32)(j*1024+l7*128):OOB,(ct*64+(idx&7)*8+(lw&1)*4)*1024,lbase+L_DUM);
      dma_dw<false>(swd,(lane<43&&!(ROTI&2))?(u32)(lane*1024):OOB,((idx&7)*172+lw*43)*1024+ct*128,lbase+L_DUM);
    }
    GWA(1); GWB(1);
    if(TPOS==0){
      dma_dw<false>(sc,(lane<32&&!(ROTI&1))?(u32)(j*1024+l7*128):OOB,(ct*64+(idx&7)*8+(lw&1)*4)*1024,lbase+L_DUM);
      dma_dw<false>(swd,(lane<43&&!(ROTI&2))?(u32)(lane*1024):OOB,((idx&7)*172+lw*43)*1024+ct*128,lbase+L_DUM);
    }
    waitv<WARMP?8:6>(); BAR();
    STAMP(3);
    GWA(2); GWB(2); GWA(3); GWB(3);
    if(LLCPF && nb==2){
      const u32 li=((cg<12?(cg>>2):cg-11)*16+tt)*2+(lw&1);
      dma_dw<false>(sc,lane<26?(li*26+lane)*128:OOB,0,lbase+L_DUM);
      dma_dw<false>(swd,lane<35?((li*2+(lw>>1))*35+lane)*128:OOB,0,lbase+L_DUM);
    }
    u32 va=WS_HS+rt2*131072+lw*4096+lane*16;
    const u32 cr=(ct*64+(lw&1)*32+j)*1024;
    u32 vp0=cr+sz0, vp1=cr+7*1024+sz1, vp2=cr+14*1024+sz0, vp3=cr+21*1024+sz1;
    const int gp0=tt-1-4*lw-(lane>>5);
    const u32 vg0=gp0>=0?(u32)(WS_GRAN+gp0*8192+(cg*32+(lane&31))*16):OOB, vg1=gp0>=2?vg0-2*8192-1024:OOB;
    #define SNAP() asm volatile("s_add_u32 m0, %[b], %[i]\n\ts_nop 0\n\tbuffer_load_dwordx4 %[v0], %[r], 0 offen sc1 lds\n\tbuffer_load_dwordx4 %[v1], %[r], 0 offen offset:1024 sc1 lds" \
        :: [v0]"v"(vg0),[v1]"v"(vg1),[r]"s"(sws),[b]"s"(l2),[i]"n"(L_GR) : "memory","scc")
    waitv<TPOS==0?14:12>(); BAR();
    GX(3); GWA(4); GWB(4); SNAP(); waitv<18>(); BAR();
    GX(4); GWA(5); GWB(5); waitv<RELAX?18:10>(); SNAP();
    if(WARMB) dma_dw<false>(sc,lane<32?(u32)(j*1024+l7*128):OOB,(ct*64+(idx&7)*8+(lw&1)*4)*1024,lbase+L_DUM);
    BAR();
    GX(5); GWA(6); GWB(6); waitv<RELAX?18:WARMB?11:10>(); SNAP(); BAR();
    GX(6); GWA(7); GWB(7); waitv<RELAX?18:10>(); BAR();
    u32 ka=ry, kb=ry;
    auto PA=[&](auto C) AI {
      constexpr int c=C.value;
      if constexpr(c==8){ va=WS_HH+rt2*86*2048+lw*4096+lane*16; ka=rz; }
      u32 so;
      if constexpr(c<8){ so=ka<<14; ka=(ka+1)&7; } else { so=(ka==10&&lw==3)?OOB:ka<<14; ka= ka==10?0:ka+1; }
      dma4<true,pa_(c)>(sws,va,va,va,va,so,l4);
    };
    auto PB=[&](auto C) AI {
      constexpr int c=C.value;
      if constexpr(c==8){ vp0=(lw*32+j)*1024+ct*128+((l7^(((j>>1)&1)<<2))<<4); vp1=vp0+7*1024; vp2=vp0+14*1024; vp3=vp0+21*1024; kb=rz; }
      u32 so;
      if constexpr(c<8){ so=kb<<7; kb=(kb+1)&7; } else { so=kb<<17; kb= kb==10?0:kb+1; }
      dma4<false,pb_(c)>(c<8?sc:swd,vp0,vp1,vp2,vp3,so,l4);
    };
    #define IC(n) std::integral_constant<int,(n)>{}
    GX(7); PB(IC(0));
    waitv<14>(); BAR();
    PB(IC(1)); PB(IC(2));
    waitv<12>(); BAR();
    STAMP(4);
    dma_dw<true>(sws,vf,fh,lf);
    PB(IC(3));
    waitv<4>();
    const bool early=all_eq(peek(lf+lane*4),tag);
    if(early){ PA(IC(0)); PA(IC(1)); }
    BAR();
    STAMP(5);
    if(!early){ poll32(sws,vf,fh,tag); PA(IC(0)); PA(IC(1)); }
    STAMP(6);
    if(!LATEA){ PA(IC(2)); PA(IC(3)); PB(IC(4)); }
    waitv<LATEA?4:16>(); BAR();
    if(LATEA){ PA(IC(2)); PA(IC(3)); PB(IC(4)); }
    if(WARMB) dma_dw<false>(swd,lane<43?(u32)(lane*1024):OOB,((idx&7)*172+lw*43)*1024+ct*128,lbase+L_DUM);
    if(lane<16) dma_x4(sws,vs,WS_FX+(rt2*32+lw*8)*128,lbase+L_TAB+lw*256);
    { const u32 vt=(rt2*64+lw*16+j)*1024+ct*128+l7*16; dma2<L_XT>(sx,vt,vt+7*1024,0,l2); }
    waitv<WARMB?16:15>(); BAR();
    waitv<WARMB?12:11>(); BAR();
    bool zok=false;
    sfor<2,18>([&](auto K) AI {
      constexpr int k=K.value;
      if constexpr(k==5) dma_dw<true>(sws,(lane&31)*128,fhh,lf);
      if constexpr(k==9){ if(wgid==0 && lw==0) dma_dw<true>(sws,vf*64,WS_FH,lf); }
      if constexpr(k==6){
        waitv<8>();
        zok=all_eq(peek(lf+lane*4),tag);
        if(zok) PA(IC(8));
        PB(IC(9));
        if(zok) waitv<12>(); else waitv<8>();
      } else if constexpr(k==7){
        STAMP(7);
        if(!zok){ poll32(sws,(lane&31)*128,fhh,tag); PA(IC(8)); }
        STAMP(8);
        PA(IC(9)); PB(IC(10));
        if(zok) waitv<12>(); else waitv<8>();
      } else {
        if constexpr(k<=16) PA(IC(k+2));
        if constexpr(k<=15) PB(IC(k+3));
        waitv<(k==2?(WARMB?16:15):k<=15?12:k==16?8:0)>();
      }
      if constexpr(k==12){
        if(wgid==0 && lw==0){
          if(!all_eq(peek(lf+lane*4),tag)) poll32(sws,vf*64,WS_FH,tag);
          const u32 e=peek(lbase+L_TAG+4)+1u;
          asm volatile("s_mov_b64 exec, 1\n\tbuffer_store_dword %0, off, %1, 0 sc1\n\ts_mov_b64 exec, -1" :: "v"(e), "s"(sws) : "memory");
        }
      }
      BAR();
    });
    STAMP(11);
    STAMP_FLUSH();
    return;
  }

  int h=lane>>5, r=lane&31;
  const rsrc_t rws=mkrsrc(a.ws,WS_END);
  const v4u sws=mksrd(a.ws,WS_END);
  const int m2=w&1, tc=(w>>1)&1, kh=w>>2;
  u32 a0,b0;
  { const int q=(lane&15)>>2, p=lane&3, gp=(((lane>>4)&1)^kh)+2*(p&1);
    a0=lbase+pa_(0)+(kh*8+m2)*1024+(gp*16+((8*h+q)^(4*gp)))*16+8*(p>>1);
    b0=lbase+pb_(0)+kh*8192+(tc*32+r)*128+((h^((r>>1)&7))<<4); }
  asm volatile("":"+v"(a0),"+v"(b0));
  u32 pend=0;
  u32 tag;
  auto try_flag=[&](bool force) AI {
    if(pend && (force || vm_idle())){
      waitv<0>();
      flag_st(sws,pend,tag);
      pend=0;
    }
  };
  if(w>=4){
    const int gw=w-4, rt=2*tt+(w&1);
    __builtin_amdgcn_s_setprio(3);
    const u32 hqo=WS_HH+(rt*86+2*blk0)*2048;
    if(nb==3) tag=g_main<3>(x0,x1,ep0,rws,lbase,lane,gw,tt,cg,hqo TRPASS TSPASS); else tag=g_main<2>(x0,x1,ep0,rws,lbase,lane,gw,tt,cg,hqo TRPASS TSPASS);
    pend=WS_FHH+(rt*32+cg*2+(gw>>1))*128;
    STAMP(5);
  } else {
    const int ch=cg*32+r;
    __builtin_amdgcn_s_setprio(SPRIO);
    const u16 hlr=(u16)x0[0], hli=(u16)x0[1], hdt=(u16)x0[2];
    *(LAS v4u*)(lbase+L_GR+w*2048+lane*16)=v4u{0u,0u,0u,0u}; *(LAS v4u*)(lbase+L_GR+w*2048+1024+lane*16)=v4u{0u,0u,0u,0u};
    const float lr=b2f(hlr), li=b2f(hli), dt=rbf(__builtin_expf(b2f(hdt)));
    cf lam,coef;
    { const float ex=__builtin_expf(dt*lr); float sn,cs_; sincos_(dt*li,sn,cs_); lam=cf{ex*cs_,ex*sn};
      const float dd=1.f/(lr*lr+li*li); coef=cmul(cf{lam.x-1.f,lam.y},cf{lr*dd,-li*dd}); }
    cf l16=cmul(lam,lam); l16=cmul(l16,l16); l16=cmul(l16,l16); l16=cmul(l16,l16);
    cf l32=cmul(l16,l16);
    cf l256,wq,pw;
    { const cf l64=cmul(l32,l32), l128=cmul(l64,l64), o={1.f,0.f};
      l256=cmul(l128,l128); wq=csel(h,l128,o); pw=cmul(csel(w&1,l32,o),csel(w&2,l64,o)); }
    asm volatile("":"+v"(lam),"+v"(coef),"+v"(l16),"+v"(l32),"+v"(l256),"+v"(wq),"+v"(pw));
    v16f are={},aim={};
    {
      u32 ax[4],ab[4];
      #pragma unroll
      for(int s=0;s<4;++s){ const u32 t=lbase+r*128+((((2*s+h)^((r>>1)&7)))<<4); ab[s]=t+sb_(0); ax[s]=t+sx_(0)+w*4096; }
      v4u X[2][2],T[2][2][2];
      auto rd=[&](int b) AI {
        #pragma unroll
        for(int s=0;s<2;++s){ X[b][s]=ldsr128(ax[2*b+s]); T[b][s][0]=ldsr128(ab[2*b+s]); T[b][s][1]=ldsr128(ab[2*b+s]+4096); } };
      auto mm=[&](int b) AI {
        #pragma unroll
        for(int s=0;s<2;++s){ are=mfma(X[b][s],T[b][s][0],are); aim=mfma(X[b][s],T[b][s][1],aim); }
        asm volatile("":"+v"(are),"+v"(aim)); };
      STAMP(1);
      waitl0(); BAR();
      rd(0);
      sfor<0,8>([&](auto C) AI {
        constexpr int c=C.value;
        SB; rd(1);
        SB; mm(0);
        if constexpr(c<7){
          #pragma unroll
          for(int s=0;s<4;++s){ ax[s]+=sx_(c+1)-sx_(c); ab[s]+=sb_(c+1)-sb_(c); asm volatile("":"+v"(ax[s]),"+v"(ab[s])); }
          SB;
          waitl0(); BAR();
          rd(0);
        }
        SB; mm(1);
      });
    }
    STAMP(2);
    tag=rfl(peek(lbase+L_TAG));
    u32 ta=lbase+L_TAB+(32*w+16*h)*16, tb=lbase+L_CSP+r*32, tg=lbase+L_AGG+r*8;
    asm volatile("":"+v"(ta),"+v"(tb),"+v"(tg));
    waitl0(); BAR();
    __builtin_amdgcn_s_setprio(3);
    int left=8;
    #define SBAR() do{ if(left>0){ BAR(); --left; } else { __builtin_amdgcn_s_sleep(4); asm volatile("":::"memory"); } }while(0)
    cf uu[16], elo, Q;
    {
      const v4f c0=*(LAS v4f*)tb, c1=*(LAS v4f*)(tb+16);
      float rs[16],mu[16];
      #pragma unroll
      for(int k=0;k<16;++k){ rs[k]=*(LAS float*)(ta+k*16); mu[k]=*(LAS float*)(ta+k*16+8); }
      SB;
      const cf ncs={-((c0[0]+c0[1])+(c0[2]+c0[3])),-((c1[0]+c1[1])+(c1[2]+c1[3]))};
      cf ea={0.f,0.f};
      #pragma unroll
      for(int k=0;k<16;++k){
        uu[k]=cf{__builtin_fmaf(mu[k],ncs.x,are[k]),__builtin_fmaf(mu[k],ncs.y,aim[k])}*rs[k];
        ea= k? cfma(lam,ea,uu[k]) : uu[0];
      }
      cf ehi=ea; elo=ea; csw32(elo,ehi);
      *(LAS v2f*)(tg+w*256)=cfma(l16,elo,ehi);
      const float hf=(float)h;
      elo*=hf; l16=cf{__builtin_fmaf(hf,l16.x,1.f-hf),hf*l16.y};
      asm volatile("":"+v"(elo),"+v"(l16));
    }
    STAMP(3);
    waitl0(); BAR();
    { const cf a0=ldsc(tg), a1=ldsc(tg+256), a2=ldsc(tg+512), a3=ldsc(tg+768);
      const cf Q2=cfma(l32,a0,a1), Q3=cfma(l32,Q2,a2);
      if(w==3){ const cf E=cfma(l32,Q3,a3); if(lane<32) st128(v4u{f2u(E.x),tag,f2u(E.y),tag},rws,ch*16+WS_GRAN+tt*8192); }
      Q= w==0?cf{0.f,0.f}: w==1?a0: w==2?Q2:Q3; }
    STAMP(4);
    cf K0=cmul(coef,cfma(l16,Q,elo)), K1=cmul(coef,cmul(l16,pw));
    asm volatile("":"+v"(K0),"+v"(K1));
    SBAR();
    #pragma unroll
    for(int k=0;k<16;++k){
      uu[k]=cf{__builtin_fmaf(-coef.y,uu[k].y,coef.x*uu[k].x),__builtin_fmaf(-coef.y,uu[k].x,-coef.x*uu[k].y)};
      asm volatile("":"+v"(uu[k])); if(k==7 && tt>0) SBAR();
    }
    const int rt=2*tt+(w>>1);
    u32 hso=WS_HS+rt*131072+(cg>>1)*16384+(((cg&1)*2+(r>>4))*2+(w&1))*1024+h*512+(((lane&15)^(8*h))<<4), hs2=(hso^64)+256, ga=lbase+L_GR+lane*16;
    asm volatile("":"+v"(hso),"+v"(hs2),"+v"(ga));
    if(tt>0){
      u32 mk[8];
      #pragma unroll
      for(int i=0;i<8;++i){ mk[i]=~(u32)((tt-1-2*i-h)>>31); asm volatile("":"+v"(mk[i])); }
      #pragma unroll 1
      for(int it=0;it<SPIN_CAP;++it){
        if(__builtin_expect(it>=3,0)){
          #pragma unroll 1
          for(int i=2*w;i<2*w+2;++i){ const int pd=tt-1-2*i-h; dma_x4(sws,pd>=0?(u32)(WS_GRAN+pd*8192+ch*16):OOB,0,lbase+L_GR+i*1024); }
        }
        SBAR();
        if(it>=3) waitv<0>();
        v4u t[8];
        #pragma unroll
        for(int i=0;i<8;++i) t[i]=*(LAS v4u*)(ga+i*1024);
        SB;
        u32 bad=0;
        #pragma unroll
        for(int i=0;i<8;++i) bad|=((t[i][1]^tag)|(t[i][3]^tag))&mk[i];
        if(all_eq(bad,0u)) break;
      }
      asm volatile("":::"memory");
      cf gv[8];
      { v4f t[8];
        #pragma unroll
        for(int i=7;i>=0;--i) t[i]=*(LAS v4f*)(ga+i*1024);
        SB;
        #pragma unroll
        for(int i=0;i<8;++i) gv[i]=cf{t[i][0],t[i][2]}; }
      cf H=gv[7];
      #pragma unroll
      for(int i=6;i>=0;--i) H=cfma(l256,H,gv[i]);
      H=cmul(H,wq);
      cf h2=H; csw32(H,h2); H+=h2;
      K0=cfma(K1,H,K0);
    }
    STAMP(5);
    cf hc={K0.x,-K0.y};
    const cf lc={lam.x,-lam.y};
    #pragma unroll
    for(int m=0;m<2;++m){
      cf hv[8];
      #pragma unroll
      for(int jj=0;jj<8;++jj){ hc=cfma(lc,hc,uu[8*m+jj]); hv[jj]=hc; }
      st128(v4u{pk2(hv[0].x,hv[1].x),pk2(hv[2].x,hv[3].x),pk2(hv[4].x,hv[5].x),pk2(hv[6].x,hv[7].x)},rws,m?hs2:hso);
      st128(v4u{pk2(hv[0].y,hv[1].y),pk2(hv[2].y,hv[3].y),pk2(hv[4].y,hv[5].y),pk2(hv[6].y,hv[7].y)},rws,(m?hs2:hso)+8192);
    }
    pend=WS_FH+(rt*32+(cg&1)*16+(cg>>1)*2+(w&1))*128;
    STAMP(6);
    #pragma unroll 1
    while(left>0){
      BAR(); --left;
      #pragma unroll 1
      for(int it=0;it<FWIN && pend;++it){ try_flag(false); if(pend) __builtin_amdgcn_s_sleep(1); else STAMP(7); }
    }
    if(pend){ try_flag(true); STAMP(7); }
  }
  lane=lane_now(); h=lane>>5; r=lane&31;
  const int g=lane>>4;
  const int dcol=ct*64+tc*32+r;
  asm volatile("":"+v"(dsk_));
  const float dsk=u2f(dsk_<<16);
  v16f ay={},az={};
  const int q=(lane&15)>>2, p=lane&3, b4=g&1;
  u32 za=lbase+pa_(8)+(kh*8+m2)*1024+(lane^(kh*16))*16;
  u32 zb=lbase+pb_(8)+(64*kh+4*h+q)*128+((tc*64+b4*32+8*p)^(((q>>1)&1)<<6));
  u32 d0=pa_(1)-pa_(0), d1=pa_(2)-pa_(1), d2=pa_(3)-pa_(2), d3=pa_(0)-pa_(3), e0=XS; int eb=0;
  v4u A[2][4],B[2][4];
  auto rot=[&]() AI { const u32 t=d0; d0=d1; d1=d2; d2=d3; d3=t; eb= eb==4?0:eb+1; e0= eb==4?(u32)(-4*(int)XS):XS; };
  auto rdy=[&](int b) AI {
    #pragma unroll
    for(int s=0;s<4;++s){ A[b][s]=ldstr(a0+s*2048,(a0^64)+s*2048); B[b][s]=ldsr128(b0^(s*32)); }
    a0+=d0; b0+=e0; rot();
  };
  auto rdz=[&](int b) AI {
    #pragma unroll
    for(int s=0;s<4;++s){ A[b][s]=ldsr128(za+s*2048); B[b][s]=ldstr(zb+s*2048,zb+s*2048+1024); }
    za+=d0; zb+=e0; rot();
  };
  auto mm4=[&](int b,v16f& c) AI {
    #pragma unroll
    for(int s=0;s<4;++s) c=mfma(A[b][s],B[b][s],c);
    #pragma unroll
    for(int s=0;s<4;++s){ SG(0x8,1); SG(0x100,3); }
    SB; asm volatile("":"+v"(c));
  };
  #define NXT() BAR()
  const int tk=32*m2+16*h+8*kh;
  STAMP(8);
  __builtin_amdgcn_s_setprio(0);
  NXT(); rdy(0);
  STAMP(9);
  #pragma unroll 1
  for(int c=0;c<3;++c){
    NXT(); rdy(1); mm4(0,ay); try_flag(false);
    NXT(); rdy(0); mm4(1,ay); try_flag(c==2);
  }
  NXT(); rdy(1); mm4(0,ay);
  NXT(); rdz(0); mm4(1,ay);
  STAMP(10);
  float sv[8], xf[8], r2[8];
  u32 pt=lbase+L_TAB+tk*16, px=lbase+L_XT+tk*128+(tc*32+r)*2;
  asm volatile("":"+v"(pt),"+v"(px));
  #pragma unroll 1
  for(int c=0;c<4;++c){
    NXT(); rdz(1); mm4(0,az);
    if(c==1){
      #pragma unroll
      for(int k=0;k<8;++k){ r2[k]=*(LAS float*)(pt+k*16); sv[k]=*(LAS float*)(pt+k*16+8); xf[k]=u2f(*(LAS u16*)(px+k*128)); }
    }
    NXT(); rdz(0); mm4(1,az);
    if(c==1){
      #pragma unroll
      for(int k=0;k<8;++k){ xf[k]=u2f(f2u(xf[k])<<16); sv[k]=(xf[k]-sv[k])*r2[k]*dsk; r2[k]*=r2[k]; }
    }
  }
  {
    const u32 xo=lbase+WB+w*2048+lane*16, xi=lbase+WB+(w^4)*2048+lane*16;
    const rsrc_t ro=mkrsrc(a.out,2048*512*4);
    u32 oo=((rt2*64+tk)*512+dcol)*4;
    NXT(); rdz(1); mm4(0,az);
    *(LAS v4f*)xo=v4f{ay[8],ay[9],ay[10],ay[11]}; *(LAS v4f*)(xo+1024)=v4f{ay[12],ay[13],ay[14],ay[15]};
    SB;
    #pragma unroll
    for(int s=0;s<4;++s) az=mfma(A[1][s],B[1][s],az);
    asm volatile("":"+v"(az),"+v"(oo));
    waitl0(); NXT(); rdz(0);
    const v4f i0=*(LAS v4f*)xi, i1=*(LAS v4f*)(xi+1024);
    SB;
    #pragma unroll
    for(int s=0;s<4;++s) az=mfma(A[0][s],B[0][s],az);
    float ce[8];
    #pragma unroll
    for(int k=0;k<8;++k){ sv[k]=__builtin_fmaf(2.f,ay[k]+(k<4?i0[k&3]:i1[k&3]),sv[k])*r2[k]; ce[k]=r2[k]*-1.44269504f; asm volatile("":"+v"(sv[k]),"+v"(ce[k])); }
    STAMP(11);
    *(LAS v4f*)(xo+XS)=v4f{az[8],az[9],az[10],az[11]}; *(LAS v4f*)(xo+XS+1024)=v4f{az[12],az[13],az[14],az[15]};
    waitl0(); bar();
    const v4f i2=*(LAS v4f*)(xi+XS), i3=*(LAS v4f*)(xi+XS+1024);
    #pragma unroll
    for(int k=0;k<8;++k){
      const float z=az[k]+(k<4?i2[k&3]:i3[k&3]);
      const float gt=z*__builtin_amdgcn_rcpf(1.f+__builtin_amdgcn_exp2f(z*ce[k]));
      __builtin_amdgcn_raw_buffer_store_b32(f2u(__builtin_fmaf(sv[k],gt,xf[k])),ro,oo+k*2048,0,OUTAUX);
    }
  }
  CSTAMP(14); STAMP(13); STAMP_FLUSH();
}

extern "C" void kernel_launch(void* const* d_in, const int* in_sizes, int n_in,
                              void* d_out, int out_size, void* d_ws, size_t ws_size,
                              hipStream_t stream) {
  Args a;
  a.x=(const u16*)d_in[0]; a.lre=(const u16*)d_in[1]; a.lim=(const u16*)d_in[2];
  a.bre=(const u16*)d_in[3]; a.bim=(const u16*)d_in[4]; a.cre=(const u16*)d_in[5]; a.cim=(const u16*)d_in[6];
  a.ldt=(const u16*)d_in[7]; a.dsk=(const u16*)d_in[8];
  a.wg=(const u16*)d_in[10]; a.wu=(const u16*)d_in[11]; a.wd=(const u16*)d_in[12];
  a.out=(float*)d_out; a.ws=(char*)d_ws;
  fused_k<<<256,768,0,stream>>>(a);
}
```

```cpp
#include <hip/hip_runtime.h>
#include <hip/hip_bf16.h>
#include <type_traits>

typedef unsigned u32; typedef unsigned long long u64; typedef unsigned short u16;
typedef u32 v4u __attribute__((ext_vector_type(4)));
typedef u32 v2u __attribute__((ext_vector_type(2)));
typedef short v4s __attribute__((ext_vector_type(4)));
typedef __bf16 v2bf __attribute__((ext_vector_type(2)));
typedef __bf16 v8bf __attribute__((ext_vector_type(8)));
typedef float v2f __attribute__((ext_vector_type(2)));
typedef float v4f __attribute__((ext_vector_type(4)));
typedef float v16f __attribute__((ext_vector_type(16)));
#define LAS __attribute__((address_space(3)))
#define DEVI __device__ __forceinline__
#define AI __attribute__((always_inline))
#define SB __builtin_amdgcn_sched_barrier(0)
#if defined(__HIP_DEVICE_COMPILE__)
#define NOPK __attribute__((target("no-packed-fp32-ops")))
#else
#define NOPK
#endif

#define ROT1 0x01
#define ROTP 1
#define SPRIO 2
#define OUTAUX 16
#define HSLEEP 2
#define FSPLIT 0
#define FWIN 10
#define LATEA 0
#define LLCPF 0
#define LNFIRST 1
#define WARMP 1
#define RELAX 1
#define TX 760
#define ROTI 1
#define TPOS 0
#define CWIN 0
#define RY 1
#define RZ 2
#define CSPLIT 5
#define LBSLEEP 6
#define WARMB 0

constexpr u32 WS_EPOCH = 0;
constexpr u32 WS_FX    = 32768;
constexpr u32 WS_FH    = WS_FX + 131072;
constexpr u32 WS_FHH   = WS_FH + 131072;
constexpr u32 WS_GRAN  = WS_FHH + 131072;
constexpr u32 WS_HH    = WS_GRAN + 131072;
constexpr u32 WS_HS    = WS_HH + 32*86*2048;
constexpr u32 WS_END   = WS_HS + 32*131072;
constexpr u32 WS_STAMP = 16u<<20;
constexpr u32 XS = 16384;
constexpr u32 WB = 49152, WSZ = 24832;
constexpr u32 L_XT = WB+4*WSZ;
constexpr u32 L_TAB  = L_XT+8192;
constexpr u32 L_CSP  = L_TAB + 2048;
constexpr u32 L_FLG  = L_CSP;
constexpr u32 L_AGG  = L_CSP + 1024;
constexpr u32 L_DUM  = L_TAB + 6144;
constexpr u32 L_TAG  = L_DUM + 256;
constexpr u32 L_GR   = L_XT;
constexpr u32 L_CWX  = L_TAB;
static_assert(L_CWX%1024==0,"address bits 8,9 carry lane and wave");
constexpr u32 L_TOTAL = 163840;
static_assert(L_TAG+16<=L_TOTAL,"lds");
constexpr u32 OOB = 0x40000000u;
constexpr int SPIN_CAP = 1<<18;

#define CODEPF 12
constexpr u32 CODE_LEN=$n;
#define CODE_EXTRA 0
struct Args { const u16 *x,*lre,*lim,*bre,*bim,*cre,*cim,*ldt,*dsk,*wg,*wu,*wd; float* out; char* ws; };

DEVI u32 f2u(float f){ return __builtin_bit_cast(u32,f); }
DEVI float u2f(u32 u){ return __builtin_bit_cast(float,u); }
DEVI u32 rfl(u32 v){ return __builtin_amdgcn_readfirstlane(v); }
DEVI bool all_eq(u32 v,u32 t){ return __builtin_amdgcn_ballot_w64(v!=t)==0ull; }
DEVI v4u mksrd(const void* p, u32 bytes){ u64 a=(u64)p; v4u s; s.x=rfl((u32)a); s.y=rfl((u32)(a>>32)); s.z=bytes; s.w=0x00020000u; return s; }
#define NTM 0
#define POL1 ""
#define POL2 ""
#define POL3 ""
#define POL4 ""
#define DM0 "s_add_u32 m0, %[b], %[i]\n\ts_nop 0\n\t"
#define DLD(n,off,pol) "buffer_load_dwordx4 %[v" #n "], %[r], %[s] offen" off pol " lds\n\t"
template<bool SC1,u32 IMM> DEVI void dma4(v4u srd,u32 v0,u32 v1,u32 v2,u32 v3,u32 soff,u32 base){
  if(SC1) asm volatile(DM0 DLD(0,""," sc1") DLD(1," offset:1024"," sc1") DLD(2," offset:2048"," sc1") DLD(3," offset:3072"," sc1")
      :: [v0]"v"(v0),[v1]"v"(v1),[v2]"v"(v2),[v3]"v"(v3),[r]"s"(srd),[s]"s"(soff),[b]"s"(base),[i]"n"(IMM) : "memory","scc");
  else asm volatile(DM0 DLD(0,"",POL4) DLD(1," offset:1024",POL4) DLD(2," offset:2048",POL4) DLD(3," offset:3072",POL4)
      :: [v0]"v"(v0),[v1]"v"(v1),[v2]"v"(v2),[v3]"v"(v3),[r]"s"(srd),[s]"s"(soff),[b]"s"(base),[i]"n"(IMM) : "memory","scc");
}
template<u32 IMM> DEVI void dma3(v4u srd,u32 v0,u32 v1,u32 v2,u32 soff,u32 base){
  asm volatile(DM0 DLD(0,"",POL3) DLD(1," offset:1024",POL3) DLD(2," offset:2048",POL3)
      :: [v0]"v"(v0),[v1]"v"(v1),[v2]"v"(v2),[r]"s"(srd),[s]"s"(soff),[b]"s"(base),[i]"n"(IMM) : "memory","scc");
}
template<u32 IMM> DEVI void dma2(v4u srd,u32 v0,u32 v1,u32 soff,u32 base){
  asm volatile(DM0 DLD(0,"",POL2) DLD(1," offset:1024",POL2)
      :: [v0]"v"(v0),[v1]"v"(v1),[r]"s"(srd),[s]"s"(soff),[b]"s"(base),[i]"n"(IMM) : "memory","scc");
}
template<bool SC1> DEVI void dma_dw(v4u srd,u32 voff,u32 soff,u32 lds){
  if(SC1) asm volatile("s_mov_b32 m0, %3\n\ts_nop 0\n\tbuffer_load_dword %0, %1, %2 offen sc1 lds" :: "v"(voff), "s"(srd), "s"(soff), "s"(lds) : "memory");
  else    asm volatile("s_mov_b32 m0, %3\n\ts_nop 0\n\tbuffer_load_dword %0, %1, %2 offen" POL1 " lds" :: "v"(voff), "s"(srd), "s"(soff), "s"(lds) : "memory");
}
DEVI void dma_x4(v4u srd,u32 voff,u32 soff,u32 lds){
  asm volatile("s_mov_b32 m0, %3\n\ts_nop 0\n\tbuffer_load_dwordx4 %0, %1, %2 offen sc1 lds" :: "v"(voff), "s"(srd), "s"(soff), "s"(lds) : "memory");
}
template<int N> DEVI void waitv(){ asm volatile("s_waitcnt vmcnt(%0)"::"n"(N):"memory"); }
DEVI void waitl0(){ asm volatile("s_waitcnt lgkmcnt(0)":::"memory"); }
DEVI void bar(){ asm volatile("":::"memory"); __builtin_amdgcn_s_barrier(); asm volatile("":::"memory"); SB; }
template<u32 OFF> DEVI void sig(u32 vaddr,u32 one){ asm volatile("s_mov_b64 exec, 1\n\tds_add_u32 %0, %1 offset:%2\n\ts_mov_b64 exec, -1" :: "v"(vaddr), "v"(one), "n"(OFF) : "memory"); }
template<u32 OFF> DEVI void sigm(u32 vaddr){ asm volatile("s_mov_b64 exec, 1\n\tds_inc_u32 %0, %0 offset:%1\n\ts_mov_b64 exec, -1" :: "v"(vaddr), "n"(OFF) : "memory"); }
DEVI u32 peek(u32 vaddr){ return *(volatile LAS u32*)vaddr; }
DEVI bool vm_idle(){ u32 v; asm volatile("s_getreg_b32 %0, hwreg(HW_REG_IB_STS, 0, 4)":"=s"(v)); return v==0u; }
DEVI void flag_st(v4u srd,u32 soff,u32 val){ asm volatile("s_mov_b64 exec, 1\n\tbuffer_store_dword %0, off, %1, %2 sc1\n\ts_mov_b64 exec, -1" :: "v"(val), "s"(srd), "s"(soff) : "memory"); }
DEVI int lane_now(){ int l; asm volatile("v_mbcnt_lo_u32_b32 %0, -1, 0\n\tv_mbcnt_hi_u32_b32 %0, -1, %0":"=v"(l)); return l; }
DEVI float b2f(u16 v){ return u2f(((u32)v)<<16); }
DEVI u32 pk2(float lo,float hi){ v2f p={lo,hi}; return __builtin_bit_cast(u32,__builtin_convertvector(p,v2bf)); }
DEVI float rbf(float f){ return u2f(pk2(f,f)<<16); }
DEVI float relu(float x){ int i=__builtin_bit_cast(int,x); return __builtin_bit_cast(float,i>0?i:0); }
DEVI float dot2(u32 a,u32 b,float c){ return __builtin_amdgcn_fdot2_f32_bf16(__builtin_bit_cast(v2bf,a),__builtin_bit_cast(v2bf,b),c,false); }
DEVI v16f mfma(v4u a,v4u b,v16f c){ return __builtin_amdgcn_mfma_f32_32x32x16_bf16(__builtin_bit_cast(v8bf,a),__builtin_bit_cast(v8bf,b),c,0,0,0); }
DEVI v4f mfma16(v4u a,v4u b,v4f c){ return __builtin_amdgcn_mfma_f32_16x16x32_bf16(__builtin_bit_cast(v8bf,a),__builtin_bit_cast(v8bf,b),c,0,0,0); }
DEVI v4u ldsr128(u32 a){ return *(LAS v4u*)a; }
DEVI v4u ldstr(u32 a0,u32 a1){
  v4s x=__builtin_amdgcn_ds_read_tr16_b64_v4i16((LAS v4s*)a0);
  v4s y=__builtin_amdgcn_ds_read_tr16_b64_v4i16((LAS v4s*)a1);
  return __builtin_bit_cast(v4u,__builtin_shufflevector(x,y,0,1,2,3,4,5,6,7));
}
DEVI void sw16(float& a,float& b){ v2u r=__builtin_amdgcn_permlane16_swap(f2u(a),f2u(b),false,false); a=u2f(r[0]); b=u2f(r[1]); }
DEVI void sw32(float& a,float& b){ v2u r=__builtin_amdgcn_permlane32_swap(f2u(a),f2u(b),false,false); a=u2f(r[0]); b=u2f(r[1]); }
template<int CTRL> DEVI float dppadd(float v){ return v+u2f(__builtin_amdgcn_update_dpp(0u,f2u(v),CTRL,0xf,0xf,true)); }
DEVI void wsum4(float& a,float& b,float& c,float& d){
  sw32(a,b); sw32(c,d); float v=a+b, t=c+d;
  sw16(v,t); v+=t;
  v=dppadd<0xB1>(v); v=dppadd<0x4E>(v); v=dppadd<0x141>(v); v=dppadd<0x140>(v);
  a=u2f(__builtin_amdgcn_readlane(f2u(v),0)); c=u2f(__builtin_amdgcn_readlane(f2u(v),16));
  b=u2f(__builtin_amdgcn_readlane(f2u(v),32)); d=u2f(__builtin_amdgcn_readlane(f2u(v),48));
}
typedef v2f cf;
DEVI cf cfma(cf a,cf b,cf c){ return cf{__builtin_fmaf(-a.y,b.y,__builtin_fmaf(a.x,b.x,c.x)), __builtin_fmaf(a.y,b.x,__builtin_fmaf(a.x,b.y,c.y))}; }
DEVI cf cmul(cf a,cf b){ return cf{__builtin_fmaf(-a.y,b.y,a.x*b.x), __builtin_fmaf(a.y,b.x,a.x*b.y)}; }
DEVI cf csel(bool c,cf a,cf b){ return cf{c?a.x:b.x, c?a.y:b.y}; }
DEVI cf ldsc(u32 a){ return *(LAS v2f*)a; }
DEVI void csw32(cf& a,cf& b){ float x=a.x,y=b.x,z=a.y,t=b.y; sw32(x,y); sw32(z,t); a=cf{x,z}; b=cf{y,t}; }
DEVI void sincos_(float x,float& s,float& c){
  const float n=__builtin_rintf(x*0.636619772f);
  float r=__builtin_fmaf(n,-1.57079601e+00f,x); r=__builtin_fmaf(n,-3.13916473e-07f,r); r=__builtin_fmaf(n,-5.39030253e-15f,r);
  const float z=r*r;
  float ps=__builtin_fmaf(__builtin_fmaf(__builtin_fmaf(2.86567956e-6f,z,-1.98559923e-4f),z,8.33338592e-3f),z,-1.66666672e-1f);
  ps=__builtin_fmaf(ps*z,r,r);
  float pc=__builtin_fmaf(__builtin_fmaf(__builtin_fmaf(2.44677067e-5f,z,-1.38877297e-3f),z,4.16666567e-2f),z,-0.5f);
  pc=__builtin_fmaf(pc,z,1.f);
  const int q=(int)n;
  const float a=(q&1)?pc:ps, b=(q&1)?ps:pc;
  s=(q&2)?-a:a; c=((q+1)&2)?-b:b;
}
typedef __amdgpu_buffer_rsrc_t rsrc_t;
DEVI rsrc_t mkrsrc(const void* p,u32 bytes){ return __builtin_amdgcn_make_buffer_rsrc((void*)p,0,bytes,0x00020000); }
DEVI void st128(v4u v,rsrc_t r,u32 off){ __builtin_amdgcn_raw_buffer_store_b128(v,r,off,0,16); }
DEVI u32 mktag(u32 e){ return (e*0x9E3779B1u+0x7F4A7C15u)|1u; }

#define STAMP(k) do{}while(0)
#define CSTAMP(k) do{}while(0)
#define TRACE(k) do{}while(0)
#define STAMP_DECL do{}while(0)
#define BAR() bar()
#define STAMP_FLUSH() do{}while(0)
#define TRARG
#define TRPASS
#define TSARG
#define TSPASS

DEVI void poll32(v4u srd,u32 voff,u32 soff,u32 tag){
  #pragma unroll 1
  for(int it=0;it<SPIN_CAP;++it){
    u32 v;
    asm volatile("buffer_load_dword %0, %1, %2, %3 offen sc1\n\ts_waitcnt vmcnt(0)" : "=&v"(v) : "v"(voff), "s"(srd), "s"(soff) : "memory");
    if(all_eq(v,tag)) break;
    __builtin_amdgcn_s_sleep(12);
  }
}
template<int I,int N,class F> DEVI void sfor(F&& f){ if constexpr(I<N){ f(std::integral_constant<int,I>{}); sfor<I+1,N>(f); } }
AI constexpr u32 wslot(int c){ return WB+(c&3)*WSZ; }
AI constexpr u32 pa_(int c){ return (c&3)==3?WB+5*XS:(((c&3)+2)%3)*XS; }
AI constexpr u32 pb_(int c){ return WB+(c%5)*XS; }
AI constexpr u32 sx_(int c){ return c>=5?(c-5)*XS:wslot(c+2); }
AI constexpr u32 sb_(int c){ return c>=5?wslot(3)+(c-5)*8192:wslot(c+2)+16384; }

#define SG(m,n) __builtin_amdgcn_sched_group_barrier(m,n,0)
template<int NB>
DEVI u32 g_main(v4u x0,v4u x1,u32 e,rsrc_t rws,u32 lbase,int lane,int gw,int tt,int cg,u32 hqo TRARG TSARG){
  const int h=lane>>5, r=lane&31, rh=gw&1, u0=(gw>>1)*NB;
  const int row=tt*128+cg*8+gw*2;
  v16f acc[3][2]; v4f cw[3];
  u32 ax[4],aw,aw2,nm[2],cwa;
  const u32 on2=((lane&3)==((lane>>4)&1))?0x3f803f80u:0u;
  v4u ones={on2,on2,on2,on2};
  u32 tag;
  {
    u32 ab=lbase+sb_(0)+r*128+((((2*gw+h)^((r>>1)&7)))<<4);
    v4f c0={},c1={};
    sfor<0,8>([&](auto C) AI {
      constexpr int c=C.value;
      if constexpr(c!=0 || !LNFIRST){ waitl0(); BAR(); }
      v4u t0,t1;
      if constexpr(c!=0 || !LNFIRST){ t0=ldsr128(ab); t1=ldsr128(ab+4096); }
      if constexpr(c==0){
        const float z0=u2f(rfl(x0[0])<<16), z1=u2f(rfl(x1[0])<<16);
        float s0=0.f,s1=0.f,v0=0.f,v1=0.f;
        #pragma unroll
        for(int k=0;k<8;++k){
          const float d0=u2f((k&1)?x0[k>>1]&0xffff0000u:x0[k>>1]<<16)-z0, d1=u2f((k&1)?x1[k>>1]&0xffff0000u:x1[k>>1]<<16)-z1;
          s0+=d0; s1+=d1; v0=__builtin_fmaf(d0,d0,v0); v1=__builtin_fmaf(d1,d1,v1); }
        wsum4(s0,s1,v0,v1);
        const float sm=(lane?s1:s0)*(1.f/512.f);
        const u32 vm=pk2(relu(__builtin_fmaf(lane?v1:v0,1.f/512.f,-sm*sm)),sm+(lane?z1:z0));
        float rs=__builtin_amdgcn_rcpf(rbf(__builtin_amdgcn_sqrtf(rbf(u2f(vm<<16)+1e-5f))));
        asm volatile("":"+v"(rs));
        STAMP(1);
        if(LNFIRST==2){ BAR(); t0=ldsr128(ab); t1=ldsr128(ab+4096); }
        tag=mktag(rfl(e));
        if(lane<2) st128(v4u{f2u(rs),tag,vm&0xffff0000u,tag},rws,WS_FX+row*64+lane*16);
        if(gw==0) *(LAS v2u*)(lbase+L_TAG)=v2u{tag,e};
        if(LNFIRST==1){ waitl0(); BAR(); t0=ldsr128(ab); t1=ldsr128(ab+4096); }
      }
      if constexpr(c<7) ab+=sb_(c+1)-sb_(c);
      if constexpr(c>=1 && c<=2*NB){ acc[(c-1)>>1][(c-1)&1]=v16f{}; asm volatile("":"+v"(acc[(c-1)>>1][(c-1)&1])); }
      c0=mfma16(ones,t0,c0); c1=mfma16(ones,t1,c1);
      asm volatile("":"+v"(c0),"+v"(c1),"+v"(ab));
    });
    asm volatile("s_nop 7\n\ts_nop 1":"+v"(c0),"+v"(c1));
    const bool hi=lane&16;
    *(LAS float*)(lbase+L_CSP+(r*8+h*4+gw)*4)=h?(hi?c1[1]:c1[0]):(hi?c0[1]:c0[0]);
    STAMP(2);
    waitl0(); BAR();
  }
  #pragma unroll
  for(int s=0;s<4;++s) ax[s]=lbase+rh*8192+r*128+((((2*(s^(2*rh))+h)^((r>>1)&7)))<<4);
  aw=lbase+WB+((lane>>4)&1)*(12288+32)+(8*h+((lane&15)>>2))*192+(lane&3)*8+u0*32;
  aw2=aw+6144-rh*6144; aw+=rh*6144;
  #pragma unroll
  for(int t=0;t<3;++t){ cw[t]=v4f{}; if(t<NB) asm volatile("":"+v"(cw[t])); }
  #pragma unroll
  for(int m=0;m<2;++m){
    const u32 mu=peek(lbase+L_TAB+(rh*64+m*32+16*((r>>2)&1)+8*(r>>4)+4*((r>>3)&1)+(r&3))*16+8);
    nm[m]=((mu>>16)|mu)^0x80008000u;
  }
  cwa=lbase+L_CWX+(gw*32+r)*16;
  asm volatile("":"+v"(ax[0]),"+v"(ax[1]),"+v"(ax[2]),"+v"(ax[3]),"+v"(aw),"+v"(aw2),"+v"(nm[0]),"+v"(nm[1]),"+v"(cwa));
  BAR();
  __builtin_amdgcn_s_setprio(2);
  v4u X[2][2],T[2][3];
  auto rd=[&](int b,int s) AI {
    const u32 ad=(s<2?aw:aw2)+(s&1)*3072;
    X[b][0]=ldsr128(ax[s]);
    #pragma unroll
    for(int t=0;t<NB;++t){ T[b][t]=ldstr(ad+t*32,ad+t*32+768); if(t==0) X[b][1]=ldsr128(ax[s]+4096); }
  };
  auto mm=[&](int b,bool cs) AI {
    #pragma unroll
    for(int t=0;t<NB;++t){ acc[t][0]=mfma(T[b][t],X[b][0],acc[t][0]); if(cs) cw[t]=mfma16(ones,T[b][t],cw[t]); acc[t][1]=mfma(T[b][t],X[b][1],acc[t][1]); }
  };
  auto mix=[&](int nv,bool cs) AI {
    SG(0x8,1); SG(0x100,2); if(cs) SG(0x8,2); else SG(0x8,1); SG(0x100,2); SG(0x8,1); SG(0x100,2);
    if(NB==3){ if(cs) SG(0x8,2); else SG(0x8,1); SG(0x100,2); }
    SG(0x8,1); if(nv) SG(0x2,3);
    SG(0x8,5);
  };
  u32 d0=XS, d1=XS, d2=(u32)(-2*(int)XS);
  STAMP(3); CSTAMP(6);
  rd(0,0);
  #pragma unroll 1
  for(int c=0;c<8;++c){
    const u32 dw=((c&3)==3)?(u32)(-3*(int)WSZ):WSZ;
    SB; rd(1,1); mm(0,true); ax[0]+=d0; ax[1]+=d0; aw+=dw; mix(1,true);
    SB; asm volatile("":"+v"(ax[0]),"+v"(ax[1]),"+v"(aw));
    rd(0,2); mm(1,true); mix(0,true);
    SB; rd(1,3); mm(0,false); ax[2]+=d0; ax[3]+=d0; aw2+=dw; mix(1,false);
    SB; asm volatile("":"+v"(ax[2]),"+v"(ax[3]),"+v"(aw2));
    { const u32 t=d0; d0=d1; d1=d2; d2=t; }
    if(c==7){
      u32 f[3][2]={};
      #pragma unroll
      for(int t=0;t<NB;++t){
        const float lo=cw[t][0], hi=cw[t][1], cs=(cwa&256u)?hi:lo;
        const u32 c0=f2u(cs)&0xffff0000u; const float r1=cs-u2f(c0);
        const u32 c1=f2u(r1)&0xffff0000u; const float r2=r1-u2f(c1);
        f[t][0]=(c0>>16)|c1; f[t][1]=f2u(r2)>>16;
      }
      *(LAS v4u*)cwa=v4u{f[0][0],f[0][1],f[1][0],f[1][1]};
      if(NB==3) *(LAS v2u*)(cwa+4096)=v2u{f[2][0],f[2][1]};
      ax[0]=cwa^((u32)(lane_now()&32)<<4);
    }
    waitl0(); BAR();
    rd(0,0); mm(1,false); mix(0,false);
  }
  const int ln=lane_now();
  #pragma unroll
  for(int t=0;t<NB;++t)
    #pragma unroll
    for(int m=0;m<2;++m) acc[t][m]=mfma(v4u{X[0][t>>1][2*(t&1)],X[0][t>>1][2*(t&1)+1],0u,0u},v4u{nm[m],nm[m],0u,0u},acc[t][m]);
  STAMP(4); CSTAMP(7);
  #pragma unroll
  for(int t=0;t<NB;++t)
    #pragma unroll
    for(int m=0;m<2;++m){
      SB;
      v4u v;
      #pragma unroll
      for(int k=0;k<4;++k) v[k]=pk2(relu(acc[t][m][2*k])*acc[t][m][2*k+8],relu(acc[t][m][2*k+1])*acc[t][m][2*k+9]);
      st128(v,rws,hqo+(u0+t)*2048+m*1024+ln*16);
    }
  return tag;
}

__global__ NOPK __launch_bounds__(768,3) void fused_k(Args a){
  __shared__ __attribute__((aligned(256))) char smem[L_TOTAL];
  const u32 lbase=(u32)(size_t)(LAS char*)smem;
  const int tid=__builtin_amdgcn_workitem_id_x();
  const int wgid=__builtin_amdgcn_workgroup_id_x(), w=rfl(tid>>6);
  int lane=tid&63;
  const int xcd=wgid&7, idx=wgid>>3;
  if(CODEPF && w==CODEPF-1){
    u64 pc; u32 back; asm volatile("s_getpc_b64 %0\n.Lpcx%=:\n\ts_mov_b32 %1, .Lpcx%=-_Z7fused_k4Args":"=&s"(pc),"=s"(back));
    dma_dw<false>(mksrd((const void*)(pc-back),CODE_LEN+CODE_EXTRA),(u32)(((idx&3)*64+lane)*128),0,lbase+L_DUM);
  }
  const int tt=(xcd&1)*8+(idx&7), cg=(xcd>>1)*4+(idx>>3);
  const int nb=((cg&3)==3||cg==14)?2:3, blk0=11*(cg>>2)+3*(cg&3)-(cg==15?1:0);
  const int rt2=(xcd&3)*8+(idx&7), ct=(xcd>>2)*4+(idx>>3);
  const int ry=((RY==4?2*(idx>>3):RY==2?4*((idx>>3)&1):0)+(idx&7))&7, rz=RZ==4?3*(idx>>3):RZ==2?6*((idx>>3)&1):RZ==3?(idx>>3):0;
  const u32 one=1;
  STAMP_DECL; STAMP(0); CSTAMP(12);
  const u64 tstart_=__builtin_amdgcn_s_memrealtime(); u64 tq_=tstart_;
  #define MARK() do{ asm volatile("s_memrealtime %0\n\ts_waitcnt lgkmcnt(0)":"=s"(tq_)::"memory"); }while(0)

  v4u x0,x1; u32 ep0,dsk_;
  const u16* dp=a.dsk+ct*64+((w>>1)&1)*32+(lane&31);
  if(w<4){ const int c_=cg*32+(lane&31); x0[0]=a.lre[c_]; x0[1]=a.lim[c_]; x0[2]=a.ldt[c_]; dsk_=*dp; }
  else if(w<8){ const int row=tt*128+cg*8+(w-4)*2; x0=((const v4u*)a.x)[row*64+lane]; x1=((const v4u*)a.x)[row*64+64+lane];
    ep0=__builtin_amdgcn_raw_buffer_load_b32(mkrsrc(a.ws,WS_END),0,WS_EPOCH,16); dsk_=*dp; }
  asm volatile("":::"memory");
  if(__builtin_expect(w>=8,1)){
    __builtin_amdgcn_s_setprio(3);
    const int lw=w-8, j=lane>>3, l7=lane&7;
    const v4u sx=mksrd(a.x,2048*1024);
    const v4u sb=mksrd(lw<2?a.bre:a.bim,512*1024);
    const u32 sz0=(u32)((l7^(j>>1))<<4), sz1=sz0^64u;
    const u32 xr=(tt*128+32*lw+16*(j>>2)+(j&3))*1024;
    const u32 vx0=xr+sz0, vx1=xr+3*1024+sz1, vx2=xr+6*1024+sz0, vx3=xr+9*1024+sz1;
    const u32 br=(cg*32+16*(lw&1)+j)*1024;
    const u32 vb0=br+sz0, vb1=br+7*1024+sz1;
    const u32 l4=lbase+lw*4096, l2=lbase+lw*2048;
    u32 kx=((tt*(ROT1&15)+cg*(ROT1>>4))&7)*128, kw=(kx+640)&0x380;
    #define SX(c) dma4<false,sx_(c)>(sx,vx0,vx1,vx2,vx3,kx,l4)
    #define SBB(c) do{ dma2<sb_(c)>(sb,vb0,vb1,kx,l2); kx=(kx+128)&0x380; }while(0)
    SX(0); SBB(0); SX(1); SBB(1);
    const v4u sws=mksrd(a.ws,WS_END);
    const v4u sw=mksrd(lw<2?a.wg:a.wu,512*2752);
    const v4u swd=mksrd(a.wd,1376*1024);
    const v4u sc=mksrd(lw<2?a.cre:a.cim,512*1024);
    const u32 vf=(lane&15)*128, lf=lbase+L_FLG+lw*256;
    const u32 fx=WS_FX+(tt*64+lw*16)*128, fh=WS_FH+(rt2*32+(lw&1)*16)*128, fhh=WS_FHH+rt2*4096;
    u32 vw[6];
    #pragma unroll
    for(int i=0;i<3;++i){ const u32 q=i*1024+lane*16, row=q/192, col=q%192; const bool ok=col<(u32)nb*64;
      vw[i]=ok?(row+32*(lw&1))*2752+col+blk0*64-i*1024:OOB; vw[i+3]=vw[i]+16*2752; asm volatile("":"+v"(vw[i]),"+v"(vw[i+3])); }
    const u32 lwv=lbase+(lw>>1)*(12288+32)+(lw&1)*6144;
    #define GWA(c) dma3<wslot(c)>(sw,vw[0],vw[1],vw[2],kw*1376,lwv)
    #define GWB(c) do{ dma3<wslot(c)+3072>(sw,vw[3],vw[4],vw[5],kw*1376,lwv); kw=(kw+128)&0x380; }while(0)
    #define GX(c) do{ dma4<false,((c)%3)*XS>(sx,vx0,vx1,vx2,vx3,kx,l4); kx=(kx+128)&0x380; }while(0)
    SX(2); waitv<10>(); BAR();
    SBB(2); SX(3); waitv<10>(); BAR();
    SBB(3); SX(4); waitv<10>(); BAR();
    SBB(4); SX(5); waitv<10>(); BAR();
    SBB(5); SX(6); SBB(6); waitv<12>(); BAR();
    SX(7); SBB(7); waitv<12>(); BAR();
    STAMP(2);
    const u32 vs=(lane>>1)*128+(lane&1)*16, lt=lbase+L_TAB+lw*512;
    const u32 tag=rfl(peek(lbase+L_TAG));
    if(lane<32) dma_x4(sws,vs,fx,lt);
    GWA(0); waitv<10>(); BAR();
    GWB(0); waitv<7>(); BAR();
    waitv<6>();
    #pragma unroll 1
    for(int it=0;it<SPIN_CAP;++it){
      const v4u t=*(volatile LAS v4u*)(lt+(lane&31)*16);
      if(all_eq((t[1]^tag)|(t[3]^tag),0u)) break;
      __builtin_amdgcn_s_sleep(2);
      if(lane<32) dma_x4(sws,vs,fx,lt);
      waitv<0>();
    }
    BAR();
    if(TPOS==1){
      dma_dw<false>(sc,(lane<32&&!(ROTI&1))?(u32)(j*1024+l7*128):OOB,(ct*64+(idx&7)*8+(lw&1)*4)*1024,lbase+L_DUM);
      dma_dw<false>(swd,(lane<43&&!(ROTI&2))?(u32)(lane*1024):OOB,((idx&7)*172+lw*43)*1024+ct*128,lbase+L_DUM);
    }
    GWA(1); GWB(1);
    if(TPOS==0){
      dma_dw<false>(sc,(lane<32&&!(ROTI&1))?(u32)(j*1024+l7*128):OOB,(ct*64+(idx&7)*8+(lw&1)*4)*1024,lbase+L_DUM);
      dma_dw<false>(swd,(lane<43&&!(ROTI&2))?(u32)(lane*1024):OOB,((idx&7)*172+lw*43)*1024+ct*128,lbase+L_DUM);
    }
    waitv<WARMP?8:6>(); BAR();
    STAMP(3);
    GWA(2); GWB(2); GWA(3); GWB(3);
    if(LLCPF && nb==2){
      const u32 li=((cg<12?(cg>>2):cg-11)*16+tt)*2+(lw&1);
      dma_dw<false>(sc,lane<26?(li*26+lane)*128:OOB,0,lbase+L_DUM);
      dma_dw<false>(swd,lane<35?((li*2+(lw>>1))*35+lane)*128:OOB,0,lbase+L_DUM);
    }
    u32 va=WS_HS+rt2*131072+lw*4096+lane*16;
    const u32 cr=(ct*64+(lw&1)*32+j)*1024;
    u32 vp0=cr+sz0, vp1=cr+7*1024+sz1, vp2=cr+14*1024+sz0, vp3=cr+21*1024+sz1;
    const int gp0=tt-1-4*lw-(lane>>5);
    const u32 vg0=gp0>=0?(u32)(WS_GRAN+gp0*8192+(cg*32+(lane&31))*16):OOB, vg1=gp0>=2?vg0-2*8192-1024:OOB;
    #define SNAP() asm volatile("s_add_u32 m0, %[b], %[i]\n\ts_nop 0\n\tbuffer_load_dwordx4 %[v0], %[r], 0 offen sc1 lds\n\tbuffer_load_dwordx4 %[v1], %[r], 0 offen offset:1024 sc1 lds" \
        :: [v0]"v"(vg0),[v1]"v"(vg1),[r]"s"(sws),[b]"s"(l2),[i]"n"(L_GR) : "memory","scc")
    waitv<TPOS==0?14:12>(); BAR();
    GX(3); GWA(4); GWB(4); SNAP(); waitv<18>(); BAR();
    GX(4); GWA(5); GWB(5); waitv<RELAX?18:10>(); SNAP();
    if(WARMB) dma_dw<false>(sc,lane<32?(u32)(j*1024+l7*128):OOB,(ct*64+(idx&7)*8+(lw&1)*4)*1024,lbase+L_DUM);
    BAR();
    GX(5); GWA(6); GWB(6); waitv<RELAX?18:WARMB?11:10>(); SNAP(); BAR();
    GX(6); GWA(7); GWB(7); waitv<RELAX?18:10>(); BAR();
    u32 ka=ry, kb=ry;
    auto PA=[&](auto C) AI {
      constexpr int c=C.value;
      if constexpr(c==8){ va=WS_HH+rt2*86*2048+lw*4096+lane*16; ka=rz; }
      u32 so;
      if constexpr(c<8){ so=ka<<14; ka=(ka+1)&7; } else { so=(ka==10&&lw==3)?OOB:ka<<14; ka= ka==10?0:ka+1; }
      dma4<true,pa_(c)>(sws,va,va,va,va,so,l4);
    };
    auto PB=[&](auto C) AI {
      constexpr int c=C.value;
      if constexpr(c==8){ vp0=(lw*32+j)*1024+ct*128+((l7^(((j>>1)&1)<<2))<<4); vp1=vp0+7*1024; vp2=vp0+14*1024; vp3=vp0+21*1024; kb=rz; }
      u32 so;
      if constexpr(c<8){ so=kb<<7; kb=(kb+1)&7; } else { so=kb<<17; kb= kb==10?0:kb+1; }
      dma4<false,pb_(c)>(c<8?sc:swd,vp0,vp1,vp2,vp3,so,l4);
    };
    #define IC(n) std::integral_constant<int,(n)>{}
    GX(7); PB(IC(0));
    waitv<14>(); BAR();
    PB(IC(1)); PB(IC(2));
    waitv<12>(); BAR();
    STAMP(4);
    dma_dw<true>(sws,vf,fh,lf);
    PB(IC(3));
    waitv<4>();
    const bool early=all_eq(peek(lf+lane*4),tag);
    if(early){ PA(IC(0)); PA(IC(1)); }
    BAR();
    STAMP(5);
    if(!early){ poll32(sws,vf,fh,tag); PA(IC(0)); PA(IC(1)); }
    STAMP(6);
    if(!LATEA){ PA(IC(2)); PA(IC(3)); PB(IC(4)); }
    waitv<LATEA?4:16>(); BAR();
    if(LATEA){ PA(IC(2)); PA(IC(3)); PB(IC(4)); }
    if(WARMB) dma_dw<false>(swd,lane<43?(u32)(lane*1024):OOB,((idx&7)*172+lw*43)*1024+ct*128,lbase+L_DUM);
    if(lane<16) dma_x4(sws,vs,WS_FX+(rt2*32+lw*8)*128,lbase+L_TAB+lw*256);
    { const u32 vt=(rt2*64+lw*16+j)*1024+ct*128+l7*16; dma2<L_XT>(sx,vt,vt+7*1024,0,l2); }
    waitv<WARMB?16:15>(); BAR();
    waitv<WARMB?12:11>(); BAR();
    bool zok=false;
    sfor<2,18>([&](auto K) AI {
      constexpr int k=K.value;
      if constexpr(k==5) dma_dw<true>(sws,(lane&31)*128,fhh,lf);
      if constexpr(k==9){ if(wgid==0 && lw==0) dma_dw<true>(sws,vf*64,WS_FH,lf); }
      if constexpr(k==6){
        waitv<8>();
        zok=all_eq(peek(lf+lane*4),tag);
        if(zok) PA(IC(8));
        PB(IC(9));
        if(zok) waitv<12>(); else waitv<8>();
      } else if constexpr(k==7){
        STAMP(7);
        if(!zok){ poll32(sws,(lane&31)*128,fhh,tag); PA(IC(8)); }
        MARK();
        PA(IC(9)); PB(IC(10));
        if(zok) waitv<12>(); else waitv<8>();
      } else {
        if constexpr(k<=16) PA(IC(k+2));
        if constexpr(k<=15) PB(IC(k+3));
        waitv<(k==2?(WARMB?16:15):k<=15?12:k==16?8:0)>();
      }
      if constexpr(k==12){
        if(wgid==0 && lw==0){
          if(!all_eq(peek(lf+lane*4),tag)) poll32(sws,vf*64,WS_FH,tag);
          const u32 e=peek(lbase+L_TAG+4)+1u;
          asm volatile("s_mov_b64 exec, 1\n\tbuffer_store_dword %0, off, %1, 0 sc1\n\ts_mov_b64 exec, -1" :: "v"(e), "s"(sws) : "memory");
        }
      }
      BAR();
    });
    STAMP(11);
    STAMP_FLUSH();
    return;
  }

  int h=lane>>5, r=lane&31;
  const rsrc_t rws=mkrsrc(a.ws,WS_END);
  const v4u sws=mksrd(a.ws,WS_END);
  const int m2=w&1, tc=(w>>1)&1, kh=w>>2;
  u32 a0,b0;
  { const int q=(lane&15)>>2, p=lane&3, gp=(((lane>>4)&1)^kh)+2*(p&1);
    a0=lbase+pa_(0)+(kh*8+m2)*1024+(gp*16+((8*h+q)^(4*gp)))*16+8*(p>>1);
    b0=lbase+pb_(0)+kh*8192+(tc*32+r)*128+((h^((r>>1)&7))<<4); }
  asm volatile("":"+v"(a0),"+v"(b0));
  u32 pend=0;
  u32 tag;
  auto try_flag=[&](bool force) AI {
    if(pend && (force || vm_idle())){
      waitv<0>();
      flag_st(sws,pend,tag);
      pend=0;
    }
  };
  if(w>=4){
    const int gw=w-4, rt=2*tt+(w&1);
    __builtin_amdgcn_s_setprio(3);
    const u32 hqo=WS_HH+(rt*86+2*blk0)*2048;
    if(nb==3) tag=g_main<3>(x0,x1,ep0,rws,lbase,lane,gw,tt,cg,hqo TRPASS TSPASS); else tag=g_main<2>(x0,x1,ep0,rws,lbase,lane,gw,tt,cg,hqo TRPASS TSPASS);
    pend=WS_FHH+(rt*32+cg*2+(gw>>1))*128;
    STAMP(5);
  } else {
    const int ch=cg*32+r;
    __builtin_amdgcn_s_setprio(SPRIO);
    const u16 hlr=(u16)x0[0], hli=(u16)x0[1], hdt=(u16)x0[2];
    *(LAS v4u*)(lbase+L_GR+w*2048+lane*16)=v4u{0u,0u,0u,0u}; *(LAS v4u*)(lbase+L_GR+w*2048+1024+lane*16)=v4u{0u,0u,0u,0u};
    const float lr=b2f(hlr), li=b2f(hli), dt=rbf(__builtin_expf(b2f(hdt)));
    cf lam,coef;
    { const float ex=__builtin_expf(dt*lr); float sn,cs_; sincos_(dt*li,sn,cs_); lam=cf{ex*cs_,ex*sn};
      const float dd=1.f/(lr*lr+li*li); coef=cmul(cf{lam.x-1.f,lam.y},cf{lr*dd,-li*dd}); }
    cf l16=cmul(lam,lam); l16=cmul(l16,l16); l16=cmul(l16,l16); l16=cmul(l16,l16);
    cf l32=cmul(l16,l16);
    cf l256,wq,pw;
    { const cf l64=cmul(l32,l32), l128=cmul(l64,l64), o={1.f,0.f};
      l256=cmul(l128,l128); wq=csel(h,l128,o); pw=cmul(csel(w&1,l32,o),csel(w&2,l64,o)); }
    asm volatile("":"+v"(lam),"+v"(coef),"+v"(l16),"+v"(l32),"+v"(l256),"+v"(wq),"+v"(pw));
    v16f are={},aim={};
    {
      u32 ax[4],ab[4];
      #pragma unroll
      for(int s=0;s<4;++s){ const u32 t=lbase+r*128+((((2*s+h)^((r>>1)&7)))<<4); ab[s]=t+sb_(0); ax[s]=t+sx_(0)+w*4096; }
      v4u X[2][2],T[2][2][2];
      auto rd=[&](int b) AI {
        #pragma unroll
        for(int s=0;s<2;++s){ X[b][s]=ldsr128(ax[2*b+s]); T[b][s][0]=ldsr128(ab[2*b+s]); T[b][s][1]=ldsr128(ab[2*b+s]+4096); } };
      auto mm=[&](int b) AI {
        #pragma unroll
        for(int s=0;s<2;++s){ are=mfma(X[b][s],T[b][s][0],are); aim=mfma(X[b][s],T[b][s][1],aim); }
        asm volatile("":"+v"(are),"+v"(aim)); };
      STAMP(1);
      waitl0(); BAR();
      rd(0);
      sfor<0,8>([&](auto C) AI {
        constexpr int c=C.value;
        SB; rd(1);
        SB; mm(0);
        if constexpr(c<7){
          #pragma unroll
          for(int s=0;s<4;++s){ ax[s]+=sx_(c+1)-sx_(c); ab[s]+=sb_(c+1)-sb_(c); asm volatile("":"+v"(ax[s]),"+v"(ab[s])); }
          SB;
          waitl0(); BAR();
          rd(0);
        }
        SB; mm(1);
      });
    }
    STAMP(2);
    tag=rfl(peek(lbase+L_TAG));
    u32 ta=lbase+L_TAB+(32*w+16*h)*16, tb=lbase+L_CSP+r*32, tg=lbase+L_AGG+r*8;
    asm volatile("":"+v"(ta),"+v"(tb),"+v"(tg));
    waitl0(); BAR();
    __builtin_amdgcn_s_setprio(3);
    int left=8;
    #define SBAR() do{ if(left>0){ BAR(); --left; } else { __builtin_amdgcn_s_sleep(4); asm volatile("":::"memory"); } }while(0)
    cf uu[16], elo, Q;
    {
      const v4f c0=*(LAS v4f*)tb, c1=*(LAS v4f*)(tb+16);
      float rs[16],mu[16];
      #pragma unroll
      for(int k=0;k<16;++k){ rs[k]=*(LAS float*)(ta+k*16); mu[k]=*(LAS float*)(ta+k*16+8); }
      SB;
      const cf ncs={-((c0[0]+c0[1])+(c0[2]+c0[3])),-((c1[0]+c1[1])+(c1[2]+c1[3]))};
      cf ea={0.f,0.f};
      #pragma unroll
      for(int k=0;k<16;++k){
        uu[k]=cf{__builtin_fmaf(mu[k],ncs.x,are[k]),__builtin_fmaf(mu[k],ncs.y,aim[k])}*rs[k];
        ea= k? cfma(lam,ea,uu[k]) : uu[0];
      }
      cf ehi=ea; elo=ea; csw32(elo,ehi);
      *(LAS v2f*)(tg+w*256)=cfma(l16,elo,ehi);
      const float hf=(float)h;
      elo*=hf; l16=cf{__builtin_fmaf(hf,l16.x,1.f-hf),hf*l16.y};
      asm volatile("":"+v"(elo),"+v"(l16));
    }
    STAMP(3);
    waitl0(); BAR();
    { const cf a0=ldsc(tg), a1=ldsc(tg+256), a2=ldsc(tg+512), a3=ldsc(tg+768);
      const cf Q2=cfma(l32,a0,a1), Q3=cfma(l32,Q2,a2);
      if(w==3){ const cf E=cfma(l32,Q3,a3); if(lane<32) st128(v4u{f2u(E.x),tag,f2u(E.y),tag},rws,ch*16+WS_GRAN+tt*8192); }
      Q= w==0?cf{0.f,0.f}: w==1?a0: w==2?Q2:Q3; }
    STAMP(4);
    cf K0=cmul(coef,cfma(l16,Q,elo)), K1=cmul(coef,cmul(l16,pw));
    asm volatile("":"+v"(K0),"+v"(K1));
    SBAR();
    #pragma unroll
    for(int k=0;k<16;++k){
      uu[k]=cf{__builtin_fmaf(-coef.y,uu[k].y,coef.x*uu[k].x),__builtin_fmaf(-coef.y,uu[k].x,-coef.x*uu[k].y)};
      asm volatile("":"+v"(uu[k])); if(k==7 && tt>0) SBAR();
    }
    const int rt=2*tt+(w>>1);
    u32 hso=WS_HS+rt*131072+(cg>>1)*16384+(((cg&1)*2+(r>>4))*2+(w&1))*1024+h*512+(((lane&15)^(8*h))<<4), hs2=(hso^64)+256, ga=lbase+L_GR+lane*16;
    asm volatile("":"+v"(hso),"+v"(hs2),"+v"(ga));
    if(tt>0){
      u32 mk[8];
      #pragma unroll
      for(int i=0;i<8;++i){ mk[i]=~(u32)((tt-1-2*i-h)>>31); asm volatile("":"+v"(mk[i])); }
      #pragma unroll 1
      for(int it=0;it<SPIN_CAP;++it){
        if(__builtin_expect(it>=3,0)){
          #pragma unroll 1
          for(int i=2*w;i<2*w+2;++i){ const int pd=tt-1-2*i-h; dma_x4(sws,pd>=0?(u32)(WS_GRAN+pd*8192+ch*16):OOB,0,lbase+L_GR+i*1024); }
        }
        SBAR();
        if(it>=3) waitv<0>();
        v4u t[8];
        #pragma unroll
        for(int i=0;i<8;++i) t[i]=*(LAS v4u*)(ga+i*1024);
        SB;
        u32 bad=0;
        #pragma unroll
        for(int i=0;i<8;++i) bad|=((t[i][1]^tag)|(t[i][3]^tag))&mk[i];
        if(all_eq(bad,0u)) break;
      }
      asm volatile("":::"memory");
      cf gv[8];
      { v4f t[8];
        #pragma unroll
        for(int i=7;i>=0;--i) t[i]=*(LAS v4f*)(ga+i*1024);
        SB;
        #pragma unroll
        for(int i=0;i<8;++i) gv[i]=cf{t[i][0],t[i][2]}; }
      cf H=gv[7];
      #pragma unroll
      for(int i=6;i>=0;--i) H=cfma(l256,H,gv[i]);
      H=cmul(H,wq);
      cf h2=H; csw32(H,h2); H+=h2;
      K0=cfma(K1,H,K0);
    }
    STAMP(5);
    cf hc={K0.x,-K0.y};
    const cf lc={lam.x,-lam.y};
    #pragma unroll
    for(int m=0;m<2;++m){
      cf hv[8];
      #pragma unroll
      for(int jj=0;jj<8;++jj){ hc=cfma(lc,hc,uu[8*m+jj]); hv[jj]=hc; }
      st128(v4u{pk2(hv[0].x,hv[1].x),pk2(hv[2].x,hv[3].x),pk2(hv[4].x,hv[5].x),pk2(hv[6].x,hv[7].x)},rws,m?hs2:hso);
      st128(v4u{pk2(hv[0].y,hv[1].y),pk2(hv[2].y,hv[3].y),pk2(hv[4].y,hv[5].y),pk2(hv[6].y,hv[7].y)},rws,(m?hs2:hso)+8192);
    }
    pend=WS_FH+(rt*32+(cg&1)*16+(cg>>1)*2+(w&1))*128;
    STAMP(6);
    #pragma unroll 1
    while(left>0){
      BAR(); --left;
      #pragma unroll 1
      for(int it=0;it<FWIN && pend;++it){ try_flag(false); if(pend) __builtin_amdgcn_s_sleep(1); else STAMP(7); }
    }
    if(pend){ try_flag(true); STAMP(7); }
  }
  lane=lane_now(); h=lane>>5; r=lane&31;
  const int g=lane>>4;
  const int dcol=ct*64+tc*32+r;
  asm volatile("":"+v"(dsk_));
  const float dsk=u2f(dsk_<<16);
  v16f ay={},az={};
  const int q=(lane&15)>>2, p=lane&3, b4=g&1;
  u32 za=lbase+pa_(8)+(kh*8+m2)*1024+(lane^(kh*16))*16;
  u32 zb=lbase+pb_(8)+(64*kh+4*h+q)*128+((tc*64+b4*32+8*p)^(((q>>1)&1)<<6));
  u32 d0=pa_(1)-pa_(0), d1=pa_(2)-pa_(1), d2=pa_(3)-pa_(2), d3=pa_(0)-pa_(3), e0=XS; int eb=0;
  v4u A[2][4],B[2][4];
  auto rot=[&]() AI { const u32 t=d0; d0=d1; d1=d2; d2=d3; d3=t; eb= eb==4?0:eb+1; e0= eb==4?(u32)(-4*(int)XS):XS; };
  auto rdy=[&](int b) AI {
    #pragma unroll
    for(int s=0;s<4;++s){ A[b][s]=ldstr(a0+s*2048,(a0^64)+s*2048); B[b][s]=ldsr128(b0^(s*32)); }
    a0+=d0; b0+=e0; rot();
  };
  auto rdz=[&](int b) AI {
    #pragma unroll
    for(int s=0;s<4;++s){ A[b][s]=ldsr128(za+s*2048); B[b][s]=ldstr(zb+s*2048,zb+s*2048+1024); }
    za+=d0; zb+=e0; rot();
  };
  auto mm4=[&](int b,v16f& c) AI {
    #pragma unroll
    for(int s=0;s<4;++s) c=mfma(A[b][s],B[b][s],c);
    #pragma unroll
    for(int s=0;s<4;++s){ SG(0x8,1); SG(0x100,3); }
    SB; asm volatile("":"+v"(c));
  };
  #define NXT() BAR()
  const int tk=32*m2+16*h+8*kh;
  STAMP(8);
  __builtin_amdgcn_s_setprio(0);
  NXT(); rdy(0);
  STAMP(9);
  #pragma unroll 1
  for(int c=0;c<3;++c){
    NXT(); rdy(1); mm4(0,ay); try_flag(false);
    NXT(); rdy(0); mm4(1,ay); try_flag(c==2);
  }
  NXT(); rdy(1); mm4(0,ay);
  NXT(); rdz(0); mm4(1,ay);
  STAMP(10);
  float sv[8], xf[8], r2[8];
  u32 pt=lbase+L_TAB+tk*16, px=lbase+L_XT+tk*128+(tc*32+r)*2;
  asm volatile("":"+v"(pt),"+v"(px));
  #pragma unroll 1
  for(int c=0;c<4;++c){
    NXT(); rdz(1); mm4(0,az);
    if(c==1){
      #pragma unroll
      for(int k=0;k<8;++k){ r2[k]=*(LAS float*)(pt+k*16); sv[k]=*(LAS float*)(pt+k*16+8); xf[k]=u2f(*(LAS u16*)(px+k*128)); }
    }
    NXT(); rdz(0); mm4(1,az);
    if(c==1){
      #pragma unroll
      for(int k=0;k<8;++k){ xf[k]=u2f(f2u(xf[k])<<16); sv[k]=(xf[k]-sv[k])*r2[k]*dsk; r2[k]*=r2[k]; }
    }
  }
  {
    const u32 xo=lbase+WB+w*2048+lane*16, xi=lbase+WB+(w^4)*2048+lane*16;
    const rsrc_t ro=mkrsrc(a.out,2048*512*4);
    u32 oo=((rt2*64+tk)*512+dcol)*4;
    NXT(); rdz(1); mm4(0,az);
    *(LAS v4f*)xo=v4f{ay[8],ay[9],ay[10],ay[11]}; *(LAS v4f*)(xo+1024)=v4f{ay[12],ay[13],ay[14],ay[15]};
    SB;
    #pragma unroll
    for(int s=0;s<4;++s) az=mfma(A[1][s],B[1][s],az);
    asm volatile("":"+v"(az),"+v"(oo));
    waitl0(); NXT(); rdz(0);
    const v4f i0=*(LAS v4f*)xi, i1=*(LAS v4f*)(xi+1024);
    SB;
    #pragma unroll
    for(int s=0;s<4;++s) az=mfma(A[0][s],B[0][s],az);
    float ce[8];
    #pragma unroll
    for(int k=0;k<8;++k){ sv[k]=__builtin_fmaf(2.f,ay[k]+(k<4?i0[k&3]:i1[k&3]),sv[k])*r2[k]; ce[k]=r2[k]*-1.44269504f; asm volatile("":"+v"(sv[k]),"+v"(ce[k])); }
    STAMP(11);
    *(LAS v4f*)(xo+XS)=v4f{az[8],az[9],az[10],az[11]}; *(LAS v4f*)(xo+XS+1024)=v4f{az[12],az[13],az[14],az[15]};
    waitl0(); bar();
    const v4f i2=*(LAS v4f*)(xi+XS), i3=*(LAS v4f*)(xi+XS+1024);
    #pragma unroll
    for(int k=0;k<8;++k){
      const float z=az[k]+(k<4?i2[k&3]:i3[k&3]);
      const float gt=z*__builtin_amdgcn_rcpf(1.f+__builtin_amdgcn_exp2f(z*ce[k]));
      __builtin_amdgcn_raw_buffer_store_b32(f2u(__builtin_fmaf(sv[k],gt,xf[k])),ro,oo+k*2048,0,OUTAUX);
    }
  }

  { const u64 tgt_=tstart_+3000+4*(tq_-tstart_); while(__builtin_amdgcn_s_memrealtime()<tgt_) __builtin_amdgcn_s_sleep(2); }
}

extern "C" void kernel_launch(void* const* d_in, const int* in_sizes, int n_in,
                              void* d_out, int out_size, void* d_ws, size_t ws_size,
                              hipStream_t stream) {
  Args a;
  a.x=(const u16*)d_in[0]; a.lre=(const u16*)d_in[1]; a.lim=(const u16*)d_in[2];
  a.bre=(const u16*)d_in[3]; a.bim=(const u16*)d_in[4]; a.cre=(const u16*)d_in[5]; a.cim=(const u16*)d_in[6];
  a.ldt=(const u16*)d_in[7]; a.dsk=(const u16*)d_in[8];
  a.wg=(const u16*)d_in[10]; a.wu=(const u16*)d_in[11]; a.wd=(const u16*)d_in[12];
  a.out=(float*)d_out; a.ws=(char*)d_ws;
  fused_k<<<256,768,0,stream>>>(a);
}
```

```cpp
#include <hip/hip_runtime.h>
#include <hip/hip_bf16.h>
#include <type_traits>

typedef unsigned u32; typedef unsigned long long u64; typedef unsigned short u16;
typedef u32 v4u __attribute__((ext_vector_type(4)));
typedef u32 v2u __attribute__((ext_vector_type(2)));
typedef short v4s __attribute__((ext_vector_type(4)));
typedef __bf16 v2bf __attribute__((ext_vector_type(2)));
typedef __bf16 v8bf __attribute__((ext_vector_type(8)));
typedef float v2f __attribute__((ext_vector_type(2)));
typedef float v4f __attribute__((ext_vector_type(4)));
typedef float v16f __attribute__((ext_vector_type(16)));
#define LAS __attribute__((address_space(3)))
#define DEVI __device__ __forceinline__
#define AI __attribute__((always_inline))
#define SB __builtin_amdgcn_sched_barrier(0)
#if defined(__HIP_DEVICE_COMPILE__)
#define NOPK __attribute__((target("no-packed-fp32-ops")))
#else
#define NOPK
#endif

#define ROT1 0x01
#define ROTP 1
#define SPRIO 2
#define OUTAUX 16
#define HSLEEP 2
#define FSPLIT 0
#define FWIN 10
#define LATEA 0
#define LLCPF 0
#define LNFIRST 1
#define WARMP 1
#define RELAX 1
#define TX 760
#define ROTI 1
#define TPOS 0
#define CWIN 0
#define RY 1
#define RZ 2
#define CSPLIT 5
#define LBSLEEP 6
#define WARMB 0

constexpr u32 WS_EPOCH = 0;
constexpr u32 WS_FX    = 32768;
constexpr u32 WS_FH    = WS_FX + 131072;
constexpr u32 WS_FHH   = WS_FH + 131072;
constexpr u32 WS_GRAN  = WS_FHH + 131072;
constexpr u32 WS_HH    = WS_GRAN + 131072;
constexpr u32 WS_HS    = WS_HH + 32*86*2048;
constexpr u32 WS_END   = WS_HS + 32*131072;
constexpr u32 WS_STAMP = 16u<<20;
constexpr u32 XS = 16384;
constexpr u32 WB = 49152, WSZ = 24832;
constexpr u32 L_XT = WB+4*WSZ;
constexpr u32 L_TAB  = L_XT+8192;
constexpr u32 L_CSP  = L_TAB + 2048;
constexpr u32 L_FLG  = L_CSP;
constexpr u32 L_AGG  = L_CSP + 1024;
constexpr u32 L_DUM  = L_TAB + 6144;
constexpr u32 L_TAG  = L_DUM + 256;
constexpr u32 L_GR   = L_XT;
constexpr u32 L_CWX  = L_TAB;
static_assert(L_CWX%1024==0,"address bits 8,9 carry lane and wave");
constexpr u32 L_TOTAL = 163840;
static_assert(L_TAG+16<=L_TOTAL,"lds");
constexpr u32 OOB = 0x40000000u;
constexpr int SPIN_CAP = 1<<18;

#define CODEPF 12
constexpr u32 CODE_LEN=$n;
#define CODE_EXTRA 0
struct Args { const u16 *x,*lre,*lim,*bre,*bim,*cre,*cim,*ldt,*dsk,*wg,*wu,*wd; float* out; char* ws; };

DEVI u32 f2u(float f){ return __builtin_bit_cast(u32,f); }
DEVI float u2f(u32 u){ return __builtin_bit_cast(float,u); }
DEVI u32 rfl(u32 v){ return __builtin_amdgcn_readfirstlane(v); }
DEVI bool all_eq(u32 v,u32 t){ return __builtin_amdgcn_ballot_w64(v!=t)==0ull; }
DEVI v4u mksrd(const void* p, u32 bytes){ u64 a=(u64)p; v4u s; s.x=rfl((u32)a); s.y=rfl((u32)(a>>32)); s.z=bytes; s.w=0x00020000u; return s; }
#define NTM 0
#define POL1 ""
#define POL2 ""
#define POL3 ""
#define POL4 ""
#define DM0 "s_add_u32 m0, %[b], %[i]\n\ts_nop 0\n\t"
#define DLD(n,off,pol) "buffer_load_dwordx4 %[v" #n "], %[r], %[s] offen" off pol " lds\n\t"
template<bool SC1,u32 IMM> DEVI void dma4(v4u srd,u32 v0,u32 v1,u32 v2,u32 v3,u32 soff,u32 base){
  if(SC1) asm volatile(DM0 DLD(0,""," sc1") DLD(1," offset:1024"," sc1") DLD(2," offset:2048"," sc1") DLD(3," offset:3072"," sc1")
      :: [v0]"v"(v0),[v1]"v"(v1),[v2]"v"(v2),[v3]"v"(v3),[r]"s"(srd),[s]"s"(soff),[b]"s"(base),[i]"n"(IMM) : "memory","scc");
  else asm volatile(DM0 DLD(0,"",POL4) DLD(1," offset:1024",POL4) DLD(2," offset:2048",POL4) DLD(3," offset:3072",POL4)
      :: [v0]"v"(v0),[v1]"v"(v1),[v2]"v"(v2),[v3]"v"(v3),[r]"s"(srd),[s]"s"(soff),[b]"s"(base),[i]"n"(IMM) : "memory","scc");
}
template<u32 IMM> DEVI void dma3(v4u srd,u32 v0,u32 v1,u32 v2,u32 soff,u32 base){
  asm volatile(DM0 DLD(0,"",POL3) DLD(1," offset:1024",POL3) DLD(2," offset:2048",POL3)
      :: [v0]"v"(v0),[v1]"v"(v1),[v2]"v"(v2),[r]"s"(srd),[s]"s"(soff),[b]"s"(base),[i]"n"(IMM) : "memory","scc");
}
template<u32 IMM> DEVI void dma2(v4u srd,u32 v0,u32 v1,u32 soff,u32 base){
  asm volatile(DM0 DLD(0,"",POL2) DLD(1," offset:1024",POL2)
      :: [v0]"v"(v0),[v1]"v"(v1),[r]"s"(srd),[s]"s"(soff),[b]"s"(base),[i]"n"(IMM) : "memory","scc");
}
template<bool SC1> DEVI void dma_dw(v4u srd,u32 voff,u32 soff,u32 lds){
  if(SC1) asm volatile("s_mov_b32 m0, %3\n\ts_nop 0\n\tbuffer_load_dword %0, %1, %2 offen sc1 lds" :: "v"(voff), "s"(srd), "s"(soff), "s"(lds) : "memory");
  else    asm volatile("s_mov_b32 m0, %3\n\ts_nop 0\n\tbuffer_load_dword %0, %1, %2 offen" POL1 " lds" :: "v"(voff), "s"(srd), "s"(soff), "s"(lds) : "memory");
}
DEVI void dma_x4(v4u srd,u32 voff,u32 soff,u32 lds){
  asm volatile("s_mov_b32 m0, %3\n\ts_nop 0\n\tbuffer_load_dwordx4 %0, %1, %2 offen sc1 lds" :: "v"(voff), "s"(srd), "s"(soff), "s"(lds) : "memory");
}
template<int N> DEVI void waitv(){ asm volatile("s_waitcnt vmcnt(%0)"::"n"(N):"memory"); }
DEVI void waitl0(){ asm volatile("s_waitcnt lgkmcnt(0)":::"memory"); }
DEVI void bar(){ asm volatile("":::"memory"); __builtin_amdgcn_s_barrier(); asm volatile("":::"memory"); SB; }
template<u32 OFF> DEVI void sig(u32 vaddr,u32 one){ asm volatile("s_mov_b64 exec, 1\n\tds_add_u32 %0, %1 offset:%2\n\ts_mov_b64 exec, -1" :: "v"(vaddr), "v"(one), "n"(OFF) : "memory"); }
template<u32 OFF> DEVI void sigm(u32 vaddr){ asm volatile("s_mov_b64 exec, 1\n\tds_inc_u32 %0, %0 offset:%1\n\ts_mov_b64 exec, -1" :: "v"(vaddr), "n"(OFF) : "memory"); }
DEVI u32 peek(u32 vaddr){ return *(volatile LAS u32*)vaddr; }
DEVI bool vm_idle(){ u32 v; asm volatile("s_getreg_b32 %0, hwreg(HW_REG_IB_STS, 0, 4)":"=s"(v)); return v==0u; }
DEVI void flag_st(v4u srd,u32 soff,u32 val){ asm volatile("s_mov_b64 exec, 1\n\tbuffer_store_dword %0, off, %1, %2 sc1\n\ts_mov_b64 exec, -1" :: "v"(val), "s"(srd), "s"(soff) : "memory"); }
DEVI int lane_now(){ int l; asm volatile("v_mbcnt_lo_u32_b32 %0, -1, 0\n\tv_mbcnt_hi_u32_b32 %0, -1, %0":"=v"(l)); return l; }
DEVI float b2f(u16 v){ return u2f(((u32)v)<<16); }
DEVI u32 pk2(float lo,float hi){ v2f p={lo,hi}; return __builtin_bit_cast(u32,__builtin_convertvector(p,v2bf)); }
DEVI float rbf(float f){ return u2f(pk2(f,f)<<16); }
DEVI float relu(float x){ int i=__builtin_bit_cast(int,x); return __builtin_bit_cast(float,i>0?i:0); }
DEVI float dot2(u32 a,u32 b,float c){ return __builtin_amdgcn_fdot2_f32_bf16(__builtin_bit_cast(v2bf,a),__builtin_bit_cast(v2bf,b),c,false); }
DEVI v16f mfma(v4u a,v4u b,v16f c){ return __builtin_amdgcn_mfma_f32_32x32x16_bf16(__builtin_bit_cast(v8bf,a),__builtin_bit_cast(v8bf,b),c,0,0,0); }
DEVI v4f mfma16(v4u a,v4u b,v4f c){ return __builtin_amdgcn_mfma_f32_16x16x32_bf16(__builtin_bit_cast(v8bf,a),__builtin_bit_cast(v8bf,b),c,0,0,0); }
DEVI v4u ldsr128(u32 a){ return *(LAS v4u*)a; }
DEVI v4u ldstr(u32 a0,u32 a1){
  v4s x=__builtin_amdgcn_ds_read_tr16_b64_v4i16((LAS v4s*)a0);
  v4s y=__builtin_amdgcn_ds_read_tr16_b64_v4i16((LAS v4s*)a1);
  return __builtin_bit_cast(v4u,__builtin_shufflevector(x,y,0,1,2,3,4,5,6,7));
}
DEVI void sw16(float& a,float& b){ v2u r=__builtin_amdgcn_permlane16_swap(f2u(a),f2u(b),false,false); a=u2f(r[0]); b=u2f(r[1]); }
DEVI void sw32(float& a,float& b){ v2u r=__builtin_amdgcn_permlane32_swap(f2u(a),f2u(b),false,false); a=u2f(r[0]); b=u2f(r[1]); }
template<int CTRL> DEVI float dppadd(float v){ return v+u2f(__builtin_amdgcn_update_dpp(0u,f2u(v),CTRL,0xf,0xf,true)); }
DEVI void wsum4(float& a,float& b,float& c,float& d){
  sw32(a,b); sw32(c,d); float v=a+b, t=c+d;
  sw16(v,t); v+=t;
  v=dppadd<0xB1>(v); v=dppadd<0x4E>(v); v=dppadd<0x141>(v); v=dppadd<0x140>(v);
  a=u2f(__builtin_amdgcn_readlane(f2u(v),0)); c=u2f(__builtin_amdgcn_readlane(f2u(v),16));
  b=u2f(__builtin_amdgcn_readlane(f2u(v),32)); d=u2f(__builtin_amdgcn_readlane(f2u(v),48));
}
typedef v2f cf;
DEVI cf cfma(cf a,cf b,cf c){ return cf{__builtin_fmaf(-a.y,b.y,__builtin_fmaf(a.x,b.x,c.x)), __builtin_fmaf(a.y,b.x,__builtin_fmaf(a.x,b.y,c.y))}; }
DEVI cf cmul(cf a,cf b){ return cf{__builtin_fmaf(-a.y,b.y,a.x*b.x), __builtin_fmaf(a.y,b.x,a.x*b.y)}; }
DEVI cf csel(bool c,cf a,cf b){ return cf{c?a.x:b.x, c?a.y:b.y}; }
DEVI cf ldsc(u32 a){ return *(LAS v2f*)a; }
DEVI void csw32(cf& a,cf& b){ float x=a.x,y=b.x,z=a.y,t=b.y; sw32(x,y); sw32(z,t); a=cf{x,z}; b=cf{y,t}; }
DEVI void sincos_(float x,float& s,float& c){
  const float n=__builtin_rintf(x*0.636619772f);
  float r=__builtin_fmaf(n,-1.57079601e+00f,x); r=__builtin_fmaf(n,-3.13916473e-07f,r); r=__builtin_fmaf(n,-5.39030253e-15f,r);
  const float z=r*r;
  float ps=__builtin_fmaf(__builtin_fmaf(__builtin_fmaf(2.86567956e-6f,z,-1.98559923e-4f),z,8.33338592e-3f),z,-1.66666672e-1f);
  ps=__builtin_fmaf(ps*z,r,r);
  float pc=__builtin_fmaf(__builtin_fmaf(__builtin_fmaf(2.44677067e-5f,z,-1.38877297e-3f),z,4.16666567e-2f),z,-0.5f);
  pc=__builtin_fmaf(pc,z,1.f);
  const int q=(int)n;
  const float a=(q&1)?pc:ps, b=(q&1)?ps:pc;
  s=(q&2)?-a:a; c=((q+1)&2)?-b:b;
}
typedef __amdgpu_buffer_rsrc_t rsrc_t;
DEVI rsrc_t mkrsrc(const void* p,u32 bytes){ return __builtin_amdgcn_make_buffer_rsrc((void*)p,0,bytes,0x00020000); }
DEVI void st128(v4u v,rsrc_t r,u32 off){ __builtin_amdgcn_raw_buffer_store_b128(v,r,off,0,16); }
DEVI u32 mktag(u32 e){ return (e*0x9E3779B1u+0x7F4A7C15u)|1u; }

#define STAMP(k) do{}while(0)
#define CSTAMP(k) do{}while(0)
#define TRACE(k) do{}while(0)
#define STAMP_DECL do{}while(0)
#define BAR() bar()
#define STAMP_FLUSH() do{}while(0)
#define TRARG
#define TRPASS
#define TSARG
#define TSPASS

DEVI void poll32(v4u srd,u32 voff,u32 soff,u32 tag){
  #pragma unroll 1
  for(int it=0;it<SPIN_CAP;++it){
    u32 v;
    asm volatile("buffer_load_dword %0, %1, %2, %3 offen sc1\n\ts_waitcnt vmcnt(0)" : "=&v"(v) : "v"(voff), "s"(srd), "s"(soff) : "memory");
    if(all_eq(v,tag)) break;
    __builtin_amdgcn_s_sleep(12);
  }
}
template<int I,int N,class F> DEVI void sfor(F&& f){ if constexpr(I<N){ f(std::integral_constant<int,I>{}); sfor<I+1,N>(f); } }
AI constexpr u32 wslot(int c){ return WB+(c&3)*WSZ; }
AI constexpr u32 pa_(int c){ return (c&3)==3?WB+5*XS:(((c&3)+2)%3)*XS; }
AI constexpr u32 pb_(int c){ return WB+(c%5)*XS; }
AI constexpr u32 sx_(int c){ return c>=5?(c-5)*XS:wslot(c+2); }
AI constexpr u32 sb_(int c){ return c>=5?wslot(3)+(c-5)*8192:wslot(c+2)+16384; }

#define SG(m,n) __builtin_amdgcn_sched_group_barrier(m,n,0)
template<int NB>
DEVI u32 g_main(v4u x0,v4u x1,u32 e,rsrc_t rws,u32 lbase,int lane,int gw,int tt,int cg,u32 hqo TRARG TSARG){
  const int h=lane>>5, r=lane&31, rh=gw&1, u0=(gw>>1)*NB;
  const int row=tt*128+cg*8+gw*2;
  v16f acc[3][2]; v4f cw[3];
  u32 ax[4],aw,aw2,nm[2],cwa;
  const u32 on2=((lane&3)==((lane>>4)&1))?0x3f803f80u:0u;
  v4u ones={on2,on2,on2,on2};
  u32 tag;
  {
    u32 ab=lbase+sb_(0)+r*128+((((2*gw+h)^((r>>1)&7)))<<4);
    v4f c0={},c1={};
    sfor<0,8>([&](auto C) AI {
      constexpr int c=C.value;
      if constexpr(c!=0 || !LNFIRST){ waitl0(); BAR(); }
      v4u t0,t1;
      if constexpr(c!=0 || !LNFIRST){ t0=ldsr128(ab); t1=ldsr128(ab+4096); }
      if constexpr(c==0){
        const float z0=u2f(rfl(x0[0])<<16), z1=u2f(rfl(x1[0])<<16);
        float s0=0.f,s1=0.f,v0=0.f,v1=0.f;
        #pragma unroll
        for(int k=0;k<8;++k){
          const float d0=u2f((k&1)?x0[k>>1]&0xffff0000u:x0[k>>1]<<16)-z0, d1=u2f((k&1)?x1[k>>1]&0xffff0000u:x1[k>>1]<<16)-z1;
          s0+=d0; s1+=d1; v0=__builtin_fmaf(d0,d0,v0); v1=__builtin_fmaf(d1,d1,v1); }
        wsum4(s0,s1,v0,v1);
        const float sm=(lane?s1:s0)*(1.f/512.f);
        const u32 vm=pk2(relu(__builtin_fmaf(lane?v1:v0,1.f/512.f,-sm*sm)),sm+(lane?z1:z0));
        float rs=__builtin_amdgcn_rcpf(rbf(__builtin_amdgcn_sqrtf(rbf(u2f(vm<<16)+1e-5f))));
        asm volatile("":"+v"(rs));
        STAMP(1);
        if(LNFIRST==2){ BAR(); t0=ldsr128(ab); t1=ldsr128(ab+4096); }
        tag=mktag(rfl(e));
        if(lane<2) st128(v4u{f2u(rs),tag,vm&0xffff0000u,tag},rws,WS_FX+row*64+lane*16);
        if(gw==0) *(LAS v2u*)(lbase+L_TAG)=v2u{tag,e};
        if(LNFIRST==1){ waitl0(); BAR(); t0=ldsr128(ab); t1=ldsr128(ab+4096); }
      }
      if constexpr(c<7) ab+=sb_(c+1)-sb_(c);
      if constexpr(c>=1 && c<=2*NB){ acc[(c-1)>>1][(c-1)&1]=v16f{}; asm volatile("":"+v"(acc[(c-1)>>1][(c-1)&1])); }
      c0=mfma16(ones,t0,c0); c1=mfma16(ones,t1,c1);
      asm volatile("":"+v"(c0),"+v"(c1),"+v"(ab));
    });
    asm volatile("s_nop 7\n\ts_nop 1":"+v"(c0),"+v"(c1));
    const bool hi=lane&16;
    *(LAS float*)(lbase+L_CSP+(r*8+h*4+gw)*4)=h?(hi?c1[1]:c1[0]):(hi?c0[1]:c0[0]);
    STAMP(2);
    waitl0(); BAR();
  }
  #pragma unroll
  for(int s=0;s<4;++s) ax[s]=lbase+rh*8192+r*128+((((2*(s^(2*rh))+h)^((r>>1)&7)))<<4);
  aw=lbase+WB+((lane>>4)&1)*(12288+32)+(8*h+((lane&15)>>2))*192+(lane&3)*8+u0*32;
  aw2=aw+6144-rh*6144; aw+=rh*6144;
  #pragma unroll
  for(int t=0;t<3;++t){ cw[t]=v4f{}; if(t<NB) asm volatile("":"+v"(cw[t])); }
  #pragma unroll
  for(int m=0;m<2;++m){
    const u32 mu=peek(lbase+L_TAB+(rh*64+m*32+16*((r>>2)&1)+8*(r>>4)+4*((r>>3)&1)+(r&3))*16+8);
    nm[m]=((mu>>16)|mu)^0x80008000u;
  }
  cwa=lbase+L_CWX+(gw*32+r)*16;
  asm volatile("":"+v"(ax[0]),"+v"(ax[1]),"+v"(ax[2]),"+v"(ax[3]),"+v"(aw),"+v"(aw2),"+v"(nm[0]),"+v"(nm[1]),"+v"(cwa));
  BAR();
  __builtin_amdgcn_s_setprio(2);
  v4u X[2][2],T[2][3];
  auto rd=[&](int b,int s) AI {
    const u32 ad=(s<2?aw:aw2)+(s&1)*3072;
    X[b][0]=ldsr128(ax[s]);
    #pragma unroll
    for(int t=0;t<NB;++t){ T[b][t]=ldstr(ad+t*32,ad+t*32+768); if(t==0) X[b][1]=ldsr128(ax[s]+4096); }
  };
  auto mm=[&](int b,bool cs) AI {
    #pragma unroll
    for(int t=0;t<NB;++t){ acc[t][0]=mfma(T[b][t],X[b][0],acc[t][0]); if(cs) cw[t]=mfma16(ones,T[b][t],cw[t]); acc[t][1]=mfma(T[b][t],X[b][1],acc[t][1]); }
  };
  auto mix=[&](int nv,bool cs) AI {
    SG(0x8,1); SG(0x100,2); if(cs) SG(0x8,2); else SG(0x8,1); SG(0x100,2); SG(0x8,1); SG(0x100,2);
    if(NB==3){ if(cs) SG(0x8,2); else SG(0x8,1); SG(0x100,2); }
    SG(0x8,1); if(nv) SG(0x2,3);
    SG(0x8,5);
  };
  u32 d0=XS, d1=XS, d2=(u32)(-2*(int)XS);
  STAMP(3); CSTAMP(6);
  rd(0,0);
  #pragma unroll 1
  for(int c=0;c<8;++c){
    const u32 dw=((c&3)==3)?(u32)(-3*(int)WSZ):WSZ;
    SB; rd(1,1); mm(0,true); ax[0]+=d0; ax[1]+=d0; aw+=dw; mix(1,true);
    SB; asm volatile("":"+v"(ax[0]),"+v"(ax[1]),"+v"(aw));
    rd(0,2); mm(1,true); mix(0,true);
    SB; rd(1,3); mm(0,false); ax[2]+=d0; ax[3]+=d0; aw2+=dw; mix(1,false);
    SB; asm volatile("":"+v"(ax[2]),"+v"(ax[3]),"+v"(aw2));
    { const u32 t=d0; d0=d1; d1=d2; d2=t; }
    if(c==7){
      u32 f[3][2]={};
      #pragma unroll
      for(int t=0;t<NB;++t){
        const float lo=cw[t][0], hi=cw[t][1], cs=(cwa&256u)?hi:lo;
        const u32 c0=f2u(cs)&0xffff0000u; const float r1=cs-u2f(c0);
        const u32 c1=f2u(r1)&0xffff0000u; const float r2=r1-u2f(c1);
        f[t][0]=(c0>>16)|c1; f[t][1]=f2u(r2)>>16;
      }
      *(LAS v4u*)cwa=v4u{f[0][0],f[0][1],f[1][0],f[1][1]};
      if(NB==3) *(LAS v2u*)(cwa+4096)=v2u{f[2][0],f[2][1]};
      ax[0]=cwa^((u32)(lane_now()&32)<<4);
    }
    waitl0(); BAR();
    rd(0,0); mm(1,false); mix(0,false);
  }
  const int ln=lane_now();
  #pragma unroll
  for(int t=0;t<NB;++t)
    #pragma unroll
    for(int m=0;m<2;++m) acc[t][m]=mfma(v4u{X[0][t>>1][2*(t&1)],X[0][t>>1][2*(t&1)+1],0u,0u},v4u{nm[m],nm[m],0u,0u},acc[t][m]);
  STAMP(4); CSTAMP(7);
  #pragma unroll
  for(int t=0;t<NB;++t)
    #pragma unroll
    for(int m=0;m<2;++m){
      SB;
      v4u v;
      #pragma unroll
      for(int k=0;k<4;++k) v[k]=pk2(relu(acc[t][m][2*k])*acc[t][m][2*k+8],relu(acc[t][m][2*k+1])*acc[t][m][2*k+9]);
      st128(v,rws,hqo+(u0+t)*2048+m*1024+ln*16);
    }
  return tag;
}

__global__ NOPK __launch_bounds__(768,3) void fused_k(Args a){
  __shared__ __attribute__((aligned(256))) char smem[L_TOTAL];
  const u32 lbase=(u32)(size_t)(LAS char*)smem;
  const int tid=__builtin_amdgcn_workitem_id_x();
  const int wgid=__builtin_amdgcn_workgroup_id_x(), w=rfl(tid>>6);
  int lane=tid&63;
  const int xcd=wgid&7, idx=wgid>>3;
  if(CODEPF && w==CODEPF-1){
    u64 pc; u32 back; asm volatile("s_getpc_b64 %0\n.Lpcx%=:\n\ts_mov_b32 %1, .Lpcx%=-_Z7fused_k4Args":"=&s"(pc),"=s"(back));
    dma_dw<false>(mksrd((const void*)(pc-back),CODE_LEN+CODE_EXTRA),(u32)(((idx&3)*64+lane)*128),0,lbase+L_DUM);
  }
  const int tt=(xcd&1)*8+(idx&7), cg=(xcd>>1)*4+(idx>>3);
  const int nb=((cg&3)==3||cg==14)?2:3, blk0=11*(cg>>2)+3*(cg&3)-(cg==15?1:0);
  const int rt2=(xcd&3)*8+(idx&7), ct=(xcd>>2)*4+(idx>>3);
  const int ry=((RY==4?2*(idx>>3):RY==2?4*((idx>>3)&1):0)+(idx&7))&7, rz=RZ==4?3*(idx>>3):RZ==2?6*((idx>>3)&1):RZ==3?(idx>>3):0;
  const u32 one=1;
  STAMP_DECL; STAMP(0); CSTAMP(12);

  v4u x0,x1; u32 ep0,dsk_;
  const u16* dp=a.dsk+ct*64+((w>>1)&1)*32+(lane&31);
  if(w<4){ const int c_=cg*32+(lane&31); x0[0]=a.lre[c_]; x0[1]=a.lim[c_]; x0[2]=a.ldt[c_]; dsk_=*dp; }
  else if(w<8){ const int row=tt*128+cg*8+(w-4)*2; x0=((const v4u*)a.x)[row*64+lane]; x1=((const v4u*)a.x)[row*64+64+lane];
    ep0=__builtin_amdgcn_raw_buffer_load_b32(mkrsrc(a.ws,WS_END),0,WS_EPOCH,16); dsk_=*dp; }
  asm volatile("":::"memory");
  if(__builtin_expect(w>=8,1)){
    __builtin_amdgcn_s_setprio(3);
    const int lw=w-8, j=lane>>3, l7=lane&7;
    const v4u sx=mksrd(a.x,2048*1024);
    const v4u sb=mksrd(lw<2?a.bre:a.bim,512*1024);
    const u32 sz0=(u32)((l7^(j>>1))<<4), sz1=sz0^64u;
    const u32 xr=(tt*128+32*lw+16*(j>>2)+(j&3))*1024;
    const u32 vx0=xr+sz0, vx1=xr+3*1024+sz1, vx2=xr+6*1024+sz0, vx3=xr+9*1024+sz1;
    const u32 br=(cg*32+16*(lw&1)+j)*1024;
    const u32 vb0=br+sz0, vb1=br+7*1024+sz1;
    const u32 l4=lbase+lw*4096, l2=lbase+lw*2048;
    u32 kx=((tt*(ROT1&15)+cg*(ROT1>>4))&7)*128, kw=(kx+640)&0x380;
    #define SX(c) dma4<false,sx_(c)>(sx,vx0,vx1,vx2,vx3,kx,l4)
    #define SBB(c) do{ dma2<sb_(c)>(sb,vb0,vb1,kx,l2); kx=(kx+128)&0x380; }while(0)
    SX(0); SBB(0); SX(1); SBB(1);
    const v4u sws=mksrd(a.ws,WS_END);
    const v4u sw=mksrd(lw<2?a.wg:a.wu,512*2752);
    const v4u swd=mksrd(a.wd,1376*1024);
    const v4u sc=mksrd(lw<2?a.cre:a.cim,512*1024);
    const u32 vf=(lane&15)*128, lf=lbase+L_FLG+lw*256;
    const u32 fx=WS_FX+(tt*64+lw*16)*128, fh=WS_FH+(rt2*32+(lw&1)*16)*128, fhh=WS_FHH+rt2*4096;
    u32 vw[6];
    #pragma unroll
    for(int i=0;i<3;++i){ const u32 q=i*1024+lane*16, row=q/192, col=q%192; const bool ok=col<(u32)nb*64;
      vw[i]=ok?(row+32*(lw&1))*2752+col+blk0*64-i*1024:OOB; vw[i+3]=vw[i]+16*2752; asm volatile("":"+v"(vw[i]),"+v"(vw[i+3])); }
    const u32 lwv=lbase+(lw>>1)*(12288+32)+(lw&1)*6144;
    #define GWA(c) dma3<wslot(c)>(sw,vw[0],vw[1],vw[2],kw*1376,lwv)
    #define GWB(c) do{ dma3<wslot(c)+3072>(sw,vw[3],vw[4],vw[5],kw*1376,lwv); kw=(kw+128)&0x380; }while(0)
    #define TOUCHD() dma_dw<false>(swd,lane<43?(u32)(lane*1024):OOB,((idx&7)*172+lw*43)*1024+ct*128,lbase+L_DUM)
    #define GX(c) do{ dma4<false,((c)%3)*XS>(sx,vx0,vx1,vx2,vx3,kx,l4); kx=(kx+128)&0x380; }while(0)
    SX(2); waitv<10>(); BAR();
    SBB(2); SX(3); waitv<10>(); BAR();
    SBB(3); SX(4); waitv<10>(); BAR();
    SBB(4); SX(5); waitv<10>(); BAR();
    SBB(5); SX(6); SBB(6); waitv<12>(); BAR();
    SX(7); SBB(7); waitv<12>(); BAR();
    STAMP(2);
    const u32 vs=(lane>>1)*128+(lane&1)*16, lt=lbase+L_TAB+lw*512;
    const u32 tag=rfl(peek(lbase+L_TAG));
    if(lane<32) dma_x4(sws,vs,fx,lt);
    GWA(0); waitv<10>(); BAR();
    GWB(0); waitv<7>(); BAR();
    waitv<6>();
    #pragma unroll 1
    for(int it=0;it<SPIN_CAP;++it){
      const v4u t=*(volatile LAS v4u*)(lt+(lane&31)*16);
      if(all_eq((t[1]^tag)|(t[3]^tag),0u)) break;
      __builtin_amdgcn_s_sleep(2);
      if(lane<32) dma_x4(sws,vs,fx,lt);
      waitv<0>();
    }
    BAR();
    GWA(1); GWB(1);
    if(TPOS==0) TOUCHD();
    waitv<TPOS==0?7:6>(); BAR();
    STAMP(3);
    GWA(2); GWB(2); GWA(3); GWB(3);
    if(LLCPF && nb==2){
      const u32 li=((cg<12?(cg>>2):cg-11)*16+tt)*2+(lw&1);
      dma_dw<false>(sc,lane<26?(li*26+lane)*128:OOB,0,lbase+L_DUM);
      dma_dw<false>(swd,lane<35?((li*2+(lw>>1))*35+lane)*128:OOB,0,lbase+L_DUM);
    }
    u32 va=WS_HS+rt2*131072+lw*4096+lane*16;
    const u32 cr=(ct*64+(lw&1)*32+j)*1024;
    u32 vp0=cr+sz0, vp1=cr+7*1024+sz1, vp2=cr+14*1024+sz0, vp3=cr+21*1024+sz1;
    const int gp0=tt-1-4*lw-(lane>>5);
    const u32 vg0=gp0>=0?(u32)(WS_GRAN+gp0*8192+(cg*32+(lane&31))*16):OOB, vg1=gp0>=2?vg0-2*8192-1024:OOB;
    #define SNAP() asm volatile("s_add_u32 m0, %[b], %[i]\n\ts_nop 0\n\tbuffer_load_dwordx4 %[v0], %[r], 0 offen sc1 lds\n\tbuffer_load_dwordx4 %[v1], %[r], 0 offen offset:1024 sc1 lds" \
        :: [v0]"v"(vg0),[v1]"v"(vg1),[r]"s"(sws),[b]"s"(l2),[i]"n"(L_GR) : "memory","scc")
    waitv<TPOS==0?13:12>(); BAR();
    GX(3); GWA(4); GWB(4); SNAP(); waitv<18>(); BAR();
    GX(4); GWA(5); GWB(5); waitv<RELAX?18:10>(); SNAP();
    if(WARMB) dma_dw<false>(sc,lane<32?(u32)(j*1024+l7*128):OOB,(ct*64+(idx&7)*8+(lw&1)*4)*1024,lbase+L_DUM);
    BAR();
    GX(5); GWA(6); GWB(6); waitv<RELAX?18:WARMB?11:10>(); SNAP(); BAR();
    GX(6); GWA(7); GWB(7); waitv<RELAX?18:10>();
    if(TPOS==4) TOUCHD();
    BAR();
    u32 ka=ry, kb=ry;
    auto PA=[&](auto C) AI {
      constexpr int c=C.value;
      if constexpr(c==8){ va=WS_HH+rt2*86*2048+lw*4096+lane*16; ka=rz; }
      u32 so;
      if constexpr(c<8){ so=ka<<14; ka=(ka+1)&7; } else { so=(ka==10&&lw==3)?OOB:ka<<14; ka= ka==10?0:ka+1; }
      dma4<true,pa_(c)>(sws,va,va,va,va,so,l4);
    };
    auto PB=[&](auto C) AI {
      constexpr int c=C.value;
      if constexpr(c==8){ vp0=(lw*32+j)*1024+ct*128+((l7^(((j>>1)&1)<<2))<<4); vp1=vp0+7*1024; vp2=vp0+14*1024; vp3=vp0+21*1024; kb=rz; }
      u32 so;
      if constexpr(c<8){ so=kb<<7; kb=(kb+1)&7; } else { so=kb<<17; kb= kb==10?0:kb+1; }
      dma4<false,pb_(c)>(c<8?sc:swd,vp0,vp1,vp2,vp3,so,l4);
    };
    #define IC(n) std::integral_constant<int,(n)>{}
    GX(7); PB(IC(0));
    waitv<TPOS==4?15:14>();
    if(TPOS==3) TOUCHD();
    BAR();
    PB(IC(1)); PB(IC(2));
    waitv<TPOS==3?13:12>(); BAR();
    STAMP(4);
    dma_dw<true>(sws,vf,fh,lf);
    PB(IC(3));
    waitv<4>();
    const bool early=all_eq(peek(lf+lane*4),tag);
    if(early){ PA(IC(0)); PA(IC(1)); }
    BAR();
    STAMP(5);
    if(!early){ poll32(sws,vf,fh,tag); PA(IC(0)); PA(IC(1)); }
    STAMP(6);
    if(!LATEA){ PA(IC(2)); PA(IC(3)); PB(IC(4)); }
    waitv<LATEA?4:16>(); BAR();
    if(LATEA){ PA(IC(2)); PA(IC(3)); PB(IC(4)); }
    if(TPOS==2) TOUCHD();
    if(lane<16) dma_x4(sws,vs,WS_FX+(rt2*32+lw*8)*128,lbase+L_TAB+lw*256);
    { const u32 vt=(rt2*64+lw*16+j)*1024+ct*128+l7*16; dma2<L_XT>(sx,vt,vt+7*1024,0,l2); }
    waitv<TPOS==2?16:15>(); BAR();
    waitv<TPOS==2?12:11>(); BAR();
    bool zok=false;
    sfor<2,18>([&](auto K) AI {
      constexpr int k=K.value;
      if constexpr(k==5) dma_dw<true>(sws,(lane&31)*128,fhh,lf);
      if constexpr(k==9){ if(wgid==0 && lw==0) dma_dw<true>(sws,vf*64,WS_FH,lf); }
      if constexpr(k==6){
        waitv<8>();
        zok=all_eq(peek(lf+lane*4),tag);
        if(zok) PA(IC(8));
        PB(IC(9));
        if(zok) waitv<12>(); else waitv<8>();
      } else if constexpr(k==7){
        STAMP(7);
        if(!zok){ poll32(sws,(lane&31)*128,fhh,tag); PA(IC(8)); }
        STAMP(8);
        PA(IC(9)); PB(IC(10));
        if(zok) waitv<12>(); else waitv<8>();
      } else {
        if constexpr(k<=16) PA(IC(k+2));
        if constexpr(k<=15) PB(IC(k+3));
        waitv<(k==2?(TPOS==2?16:15):k<=15?12:k==16?8:0)>();
      }
      if constexpr(k==12){
        if(wgid==0 && lw==0){
          if(!all_eq(peek(lf+lane*4),tag)) poll32(sws,vf*64,WS_FH,tag);
          const u32 e=peek(lbase+L_TAG+4)+1u;
          asm volatile("s_mov_b64 exec, 1\n\tbuffer_store_dword %0, off, %1, 0 sc1\n\ts_mov_b64 exec, -1" :: "v"(e), "s"(sws) : "memory");
        }
      }
      BAR();
    });
    STAMP(11);
    STAMP_FLUSH();
    return;
  }

  int h=lane>>5, r=lane&31;
  const rsrc_t rws=mkrsrc(a.ws,WS_END);
  const v4u sws=mksrd(a.ws,WS_END);
  const int m2=w&1, tc=(w>>1)&1, kh=w>>2;
  u32 a0,b0;
  { const int q=(lane&15)>>2, p=lane&3, gp=(((lane>>4)&1)^kh)+2*(p&1);
    a0=lbase+pa_(0)+(kh*8+m2)*1024+(gp*16+((8*h+q)^(4*gp)))*16+8*(p>>1);
    b0=lbase+pb_(0)+kh*8192+(tc*32+r)*128+((h^((r>>1)&7))<<4); }
  asm volatile("":"+v"(a0),"+v"(b0));
  u32 pend=0;
  u32 tag;
  auto try_flag=[&](bool force) AI {
    if(pend && (force || vm_idle())){
      waitv<0>();
      flag_st(sws,pend,tag);
      pend=0;
    }
  };
  if(w>=4){
    const int gw=w-4, rt=2*tt+(w&1);
    __builtin_amdgcn_s_setprio(3);
    const u32 hqo=WS_HH+(rt*86+2*blk0)*2048;
    if(nb==3) tag=g_main<3>(x0,x1,ep0,rws,lbase,lane,gw,tt,cg,hqo TRPASS TSPASS); else tag=g_main<2>(x0,x1,ep0,rws,lbase,lane,gw,tt,cg,hqo TRPASS TSPASS);
    pend=WS_FHH+(rt*32+cg*2+(gw>>1))*128;
    STAMP(5);
  } else {
    const int ch=cg*32+r;
    __builtin_amdgcn_s_setprio(SPRIO);
    const u16 hlr=(u16)x0[0], hli=(u16)x0[1], hdt=(u16)x0[2];
    *(LAS v4u*)(lbase+L_GR+w*2048+lane*16)=v4u{0u,0u,0u,0u}; *(LAS v4u*)(lbase+L_GR+w*2048+1024+lane*16)=v4u{0u,0u,0u,0u};
    const float lr=b2f(hlr), li=b2f(hli), dt=rbf(__builtin_expf(b2f(hdt)));
    cf lam,coef;
    { const float ex=__builtin_expf(dt*lr); float sn,cs_; sincos_(dt*li,sn,cs_); lam=cf{ex*cs_,ex*sn};
      const float dd=1.f/(lr*lr+li*li); coef=cmul(cf{lam.x-1.f,lam.y},cf{lr*dd,-li*dd}); }
    cf l16=cmul(lam,lam); l16=cmul(l16,l16); l16=cmul(l16,l16); l16=cmul(l16,l16);
    cf l32=cmul(l16,l16);
    cf l256,wq,pw;
    { const cf l64=cmul(l32,l32), l128=cmul(l64,l64), o={1.f,0.f};
      l256=cmul(l128,l128); wq=csel(h,l128,o); pw=cmul(csel(w&1,l32,o),csel(w&2,l64,o)); }
    asm volatile("":"+v"(lam),"+v"(coef),"+v"(l16),"+v"(l32),"+v"(l256),"+v"(wq),"+v"(pw));
    v16f are={},aim={};
    {
      u32 ax[4],ab[4];
      #pragma unroll
      for(int s=0;s<4;++s){ const u32 t=lbase+r*128+((((2*s+h)^((r>>1)&7)))<<4); ab[s]=t+sb_(0); ax[s]=t+sx_(0)+w*4096; }
      v4u X[2][2],T[2][2][2];
      auto rd=[&](int b) AI {
        #pragma unroll
        for(int s=0;s<2;++s){ X[b][s]=ldsr128(ax[2*b+s]); T[b][s][0]=ldsr128(ab[2*b+s]); T[b][s][1]=ldsr128(ab[2*b+s]+4096); } };
      auto mm=[&](int b) AI {
        #pragma unroll
        for(int s=0;s<2;++s){ are=mfma(X[b][s],T[b][s][0],are); aim=mfma(X[b][s],T[b][s][1],aim); }
        asm volatile("":"+v"(are),"+v"(aim)); };
      STAMP(1);
      waitl0(); BAR();
      rd(0);
      sfor<0,8>([&](auto C) AI {
        constexpr int c=C.value;
        SB; rd(1);
        SB; mm(0);
        if constexpr(c<7){
          #pragma unroll
          for(int s=0;s<4;++s){ ax[s]+=sx_(c+1)-sx_(c); ab[s]+=sb_(c+1)-sb_(c); asm volatile("":"+v"(ax[s]),"+v"(ab[s])); }
          SB;
          waitl0(); BAR();
          rd(0);
        }
        SB; mm(1);
      });
    }
    STAMP(2);
    tag=rfl(peek(lbase+L_TAG));
    u32 ta=lbase+L_TAB+(32*w+16*h)*16, tb=lbase+L_CSP+r*32, tg=lbase+L_AGG+r*8;
    asm volatile("":"+v"(ta),"+v"(tb),"+v"(tg));
    waitl0(); BAR();
    __builtin_amdgcn_s_setprio(3);
    int left=8;
    #define SBAR() do{ if(left>0){ BAR(); --left; } else { __builtin_amdgcn_s_sleep(4); asm volatile("":::"memory"); } }while(0)
    cf uu[16], elo, Q;
    {
      const v4f c0=*(LAS v4f*)tb, c1=*(LAS v4f*)(tb+16);
      float rs[16],mu[16];
      #pragma unroll
      for(int k=0;k<16;++k){ rs[k]=*(LAS float*)(ta+k*16); mu[k]=*(LAS float*)(ta+k*16+8); }
      SB;
      const cf ncs={-((c0[0]+c0[1])+(c0[2]+c0[3])),-((c1[0]+c1[1])+(c1[2]+c1[3]))};
      cf ea={0.f,0.f};
      #pragma unroll
      for(int k=0;k<16;++k){
        uu[k]=cf{__builtin_fmaf(mu[k],ncs.x,are[k]),__builtin_fmaf(mu[k],ncs.y,aim[k])}*rs[k];
        ea= k? cfma(lam,ea,uu[k]) : uu[0];
      }
      cf ehi=ea; elo=ea; csw32(elo,ehi);
      *(LAS v2f*)(tg+w*256)=cfma(l16,elo,ehi);
      const float hf=(float)h;
      elo*=hf; l16=cf{__builtin_fmaf(hf,l16.x,1.f-hf),hf*l16.y};
      asm volatile("":"+v"(elo),"+v"(l16));
    }
    STAMP(3);
    waitl0(); BAR();
    { const cf a0=ldsc(tg), a1=ldsc(tg+256), a2=ldsc(tg+512), a3=ldsc(tg+768);
      const cf Q2=cfma(l32,a0,a1), Q3=cfma(l32,Q2,a2);
      if(w==3){ const cf E=cfma(l32,Q3,a3); if(lane<32) st128(v4u{f2u(E.x),tag,f2u(E.y),tag},rws,ch*16+WS_GRAN+tt*8192); }
      Q= w==0?cf{0.f,0.f}: w==1?a0: w==2?Q2:Q3; }
    STAMP(4);
    cf K0=cmul(coef,cfma(l16,Q,elo)), K1=cmul(coef,cmul(l16,pw));
    asm volatile("":"+v"(K0),"+v"(K1));
    SBAR();
    #pragma unroll
    for(int k=0;k<16;++k){
      uu[k]=cf{__builtin_fmaf(-coef.y,uu[k].y,coef.x*uu[k].x),__builtin_fmaf(-coef.y,uu[k].x,-coef.x*uu[k].y)};
      asm volatile("":"+v"(uu[k])); if(k==7 && tt>0) SBAR();
    }
    const int rt=2*tt+(w>>1);
    u32 hso=WS_HS+rt*131072+(cg>>1)*16384+(((cg&1)*2+(r>>4))*2+(w&1))*1024+h*512+(((lane&15)^(8*h))<<4), hs2=(hso^64)+256, ga=lbase+L_GR+lane*16;
    asm volatile("":"+v"(hso),"+v"(hs2),"+v"(ga));
    if(tt>0){
      u32 mk[8];
      #pragma unroll
      for(int i=0;i<8;++i){ mk[i]=~(u32)((tt-1-2*i-h)>>31); asm volatile("":"+v"(mk[i])); }
      #pragma unroll 1
      for(int it=0;it<SPIN_CAP;++it){
        if(__builtin_expect(it>=3,0)){
          #pragma unroll 1
          for(int i=2*w;i<2*w+2;++i){ const int pd=tt-1-2*i-h; dma_x4(sws,pd>=0?(u32)(WS_GRAN+pd*8192+ch*16):OOB,0,lbase+L_GR+i*1024); }
        }
        SBAR();
        if(it>=3) waitv<0>();
        v4u t[8];
        #pragma unroll
        for(int i=0;i<8;++i) t[i]=*(LAS v4u*)(ga+i*1024);
        SB;
        u32 bad=0;
        #pragma unroll
        for(int i=0;i<8;++i) bad|=((t[i][1]^tag)|(t[i][3]^tag))&mk[i];
        if(all_eq(bad,0u)) break;
      }
      asm volatile("":::"memory");
      cf gv[8];
      { v4f t[8];
        #pragma unroll
        for(int i=7;i>=0;--i) t[i]=*(LAS v4f*)(ga+i*1024);
        SB;
        #pragma unroll
        for(int i=0;i<8;++i) gv[i]=cf{t[i][0],t[i][2]}; }
      cf H=gv[7];
      #pragma unroll
      for(int i=6;i>=0;--i) H=cfma(l256,H,gv[i]);
      H=cmul(H,wq);
      cf h2=H; csw32(H,h2); H+=h2;
      K0=cfma(K1,H,K0);
    }
    STAMP(5);
    cf hc={K0.x,-K0.y};
    const cf lc={lam.x,-lam.y};
    #pragma unroll
    for(int m=0;m<2;++m){
      cf hv[8];
      #pragma unroll
      for(int jj=0;jj<8;++jj){ hc=cfma(lc,hc,uu[8*m+jj]); hv[jj]=hc; }
      st128(v4u{pk2(hv[0].x,hv[1].x),pk2(hv[2].x,hv[3].x),pk2(hv[4].x,hv[5].x),pk2(hv[6].x,hv[7].x)},rws,m?hs2:hso);
      st128(v4u{pk2(hv[0].y,hv[1].y),pk2(hv[2].y,hv[3].y),pk2(hv[4].y,hv[5].y),pk2(hv[6].y,hv[7].y)},rws,(m?hs2:hso)+8192);
    }
    pend=WS_FH+(rt*32+(cg&1)*16+(cg>>1)*2+(w&1))*128;
    STAMP(6);
    #pragma unroll 1
    while(left>0){
      BAR(); --left;
      #pragma unroll 1
      for(int it=0;it<FWIN && pend;++it){ try_flag(false); if(pend) __builtin_amdgcn_s_sleep(1); else STAMP(7); }
    }
    if(pend){ try_flag(true); STAMP(7); }
  }
  lane=lane_now(); h=lane>>5; r=lane&31;
  const int g=lane>>4;
  const int dcol=ct*64+tc*32+r;
  asm volatile("":"+v"(dsk_));
  const float dsk=u2f(dsk_<<16);
  v16f ay={},az={};
  const int q=(lane&15)>>2, p=lane&3, b4=g&1;
  u32 za=lbase+pa_(8)+(kh*8+m2)*1024+(lane^(kh*16))*16;
  u32 zb=lbase+pb_(8)+(64*kh+4*h+q)*128+((tc*64+b4*32+8*p)^(((q>>1)&1)<<6));
  u32 d0=pa_(1)-pa_(0), d1=pa_(2)-pa_(1), d2=pa_(3)-pa_(2), d3=pa_(0)-pa_(3), e0=XS; int eb=0;
  v4u A[2][4],B[2][4];
  auto rot=[&]() AI { const u32 t=d0; d0=d1; d1=d2; d2=d3; d3=t; eb= eb==4?0:eb+1; e0= eb==4?(u32)(-4*(int)XS):XS; };
  auto rdy=[&](int b) AI {
    #pragma unroll
    for(int s=0;s<4;++s){ A[b][s]=ldstr(a0+s*2048,(a0^64)+s*2048); B[b][s]=ldsr128(b0^(s*32)); }
    a0+=d0; b0+=e0; rot();
  };
  auto rdz=[&](int b) AI {
    #pragma unroll
    for(int s=0;s<4;++s){ A[b][s]=ldsr128(za+s*2048); B[b][s]=ldstr(zb+s*2048,zb+s*2048+1024); }
    za+=d0; zb+=e0; rot();
  };
  auto mm4=[&](int b,v16f& c) AI {
    #pragma unroll
    for(int s=0;s<4;++s) c=mfma(A[b][s],B[b][s],c);
    #pragma unroll
    for(int s=0;s<4;++s){ SG(0x8,1); SG(0x100,3); }
    SB; asm volatile("":"+v"(c));
  };
  #define NXT() BAR()
  const int tk=32*m2+16*h+8*kh;
  STAMP(8);
  __builtin_amdgcn_s_setprio(0);
  NXT(); rdy(0);
  STAMP(9);
  #pragma unroll 1
  for(int c=0;c<3;++c){
    NXT(); rdy(1); mm4(0,ay); try_flag(false);
    NXT(); rdy(0); mm4(1,ay); try_flag(c==2);
  }
  NXT(); rdy(1); mm4(0,ay);
  NXT(); rdz(0); mm4(1,ay);
  STAMP(10);
  float sv[8], xf[8], r2[8];
  u32 pt=lbase+L_TAB+tk*16, px=lbase+L_XT+tk*128+(tc*32+r)*2;
  asm volatile("":"+v"(pt),"+v"(px));
  #pragma unroll 1
  for(int c=0;c<4;++c){
    NXT(); rdz(1); mm4(0,az);
    if(c==1){
      #pragma unroll
      for(int k=0;k<8;++k){ r2[k]=*(LAS float*)(pt+k*16); sv[k]=*(LAS float*)(pt+k*16+8); xf[k]=u2f(*(LAS u16*)(px+k*128)); }
    }
    NXT(); rdz(0); mm4(1,az);
    if(c==1){
      #pragma unroll
      for(int k=0;k<8;++k){ xf[k]=u2f(f2u(xf[k])<<16); sv[k]=(xf[k]-sv[k])*r2[k]*dsk; r2[k]*=r2[k]; }
    }
  }
  {
    const u32 xo=lbase+WB+w*2048+lane*16, xi=lbase+WB+(w^4)*2048+lane*16;
    const rsrc_t ro=mkrsrc(a.out,2048*512*4);
    u32 oo=((rt2*64+tk)*512+dcol)*4;
    NXT(); rdz(1); mm4(0,az);
    *(LAS v4f*)xo=v4f{ay[8],ay[9],ay[10],ay[11]}; *(LAS v4f*)(xo+1024)=v4f{ay[12],ay[13],ay[14],ay[15]};
    SB;
    #pragma unroll
    for(int s=0;s<4;++s) az=mfma(A[1][s],B[1][s],az);
    asm volatile("":"+v"(az),"+v"(oo));
    waitl0(); NXT(); rdz(0);
    const v4f i0=*(LAS v4f*)xi, i1=*(LAS v4f*)(xi+1024);
    SB;
    #pragma unroll
    for(int s=0;s<4;++s) az=mfma(A[0][s],B[0][s],az);
    float ce[8];
    #pragma unroll
    for(int k=0;k<8;++k){ sv[k]=__builtin_fmaf(2.f,ay[k]+(k<4?i0[k&3]:i1[k&3]),sv[k])*r2[k]; ce[k]=r2[k]*-1.44269504f; asm volatile("":"+v"(sv[k]),"+v"(ce[k])); }
    STAMP(11);
    *(LAS v4f*)(xo+XS)=v4f{az[8],az[9],az[10],az[11]}; *(LAS v4f*)(xo+XS+1024)=v4f{az[12],az[13],az[14],az[15]};
    waitl0(); bar();
    const v4f i2=*(LAS v4f*)(xi+XS), i3=*(LAS v4f*)(xi+XS+1024);
    #pragma unroll
    for(int k=0;k<8;++k){
      const float z=az[k]+(k<4?i2[k&3]:i3[k&3]);
      const float gt=z*__builtin_amdgcn_rcpf(1.f+__builtin_amdgcn_exp2f(z*ce[k]));
      __builtin_amdgcn_raw_buffer_store_b32(f2u(__builtin_fmaf(sv[k],gt,xf[k])),ro,oo+k*2048,0,OUTAUX);
    }
  }
  CSTAMP(14); STAMP(13); STAMP_FLUSH();
}

extern "C" void kernel_launch(void* const* d_in, const int* in_sizes, int n_in,
                              void* d_out, int out_size, void* d_ws, size_t ws_size,
                              hipStream_t stream) {
  Args a;
  a.x=(const u16*)d_in[0]; a.lre=(const u16*)d_in[1]; a.lim=(const u16*)d_in[2];
  a.bre=(const u16*)d_in[3]; a.bim=(const u16*)d_in[4]; a.cre=(const u16*)d_in[5]; a.cim=(const u16*)d_in[6];
  a.ldt=(const u16*)d_in[7]; a.dsk=(const u16*)d_in[8];
  a.wg=(const u16*)d_in[10]; a.wu=(const u16*)d_in[11]; a.wd=(const u16*)d_in[12];
  a.out=(float*)d_out; a.ws=(char*)d_ws;
  fused_k<<<256,768,0,stream>>>(a);
}
```

```cpp
#include <hip/hip_runtime.h>
#include <hip/hip_bf16.h>
#include <type_traits>

typedef unsigned u32; typedef unsigned long long u64; typedef unsigned short u16;
typedef u32 v4u __attribute__((ext_vector_type(4)));
typedef u32 v2u __attribute__((ext_vector_type(2)));
typedef short v4s __attribute__((ext_vector_type(4)));
typedef __bf16 v2bf __attribute__((ext_vector_type(2)));
typedef __bf16 v8bf __attribute__((ext_vector_type(8)));
typedef float v2f __attribute__((ext_vector_type(2)));
typedef float v4f __attribute__((ext_vector_type(4)));
typedef float v16f __attribute__((ext_vector_type(16)));
#define LAS __attribute__((address_space(3)))
#define DEVI __device__ __forceinline__
#define AI __attribute__((always_inline))
#define SB __builtin_amdgcn_sched_barrier(0)
#if defined(__HIP_DEVICE_COMPILE__)
#define NOPK __attribute__((target("no-packed-fp32-ops")))
#else
#define NOPK
#endif

constexpr u32 WS_EPOCH = 0;
constexpr u32 WS_FX    = 32768;
constexpr u32 WS_FH    = WS_FX + 131072;
constexpr u32 WS_FHH   = WS_FH + 131072;
constexpr u32 WS_GRAN  = WS_FHH + 131072;
constexpr u32 WS_HH    = WS_GRAN + 131072;
constexpr u32 WS_HS    = WS_HH + 32*86*2048;
constexpr u32 WS_END   = WS_HS + 32*131072;
constexpr u32 XS = 16384;
constexpr u32 WB = 49152, WSZ = 24832;
constexpr u32 L_XT = WB+4*WSZ;
constexpr u32 L_TAB  = L_XT+8192;
constexpr u32 L_CSP  = L_TAB + 2048;
constexpr u32 L_FLG  = L_CSP;
constexpr u32 L_AGG  = L_CSP + 1024;
constexpr u32 L_DUM  = L_TAB + 6144;
constexpr u32 L_TAG  = L_DUM + 256;
constexpr u32 L_GR   = L_XT;
constexpr u32 L_CWX  = L_TAB;
static_assert(L_CWX%1024==0,"address bits 8,9 carry lane and wave");
constexpr u32 L_TOTAL = 163840;
static_assert(L_TAG+16<=L_TOTAL,"lds");
constexpr u32 OOB = 0x40000000u;
constexpr int SPIN_CAP = 1<<18;

constexpr u32 CODE_LEN=24832;
struct Args { const u16 *x,*lre,*lim,*bre,*bim,*cre,*cim,*ldt,*dsk,*wg,*wu,*wd; float* out; char* ws; };

DEVI u32 f2u(float f){ return __builtin_bit_cast(u32,f); }
DEVI float u2f(u32 u){ return __builtin_bit_cast(float,u); }
DEVI u32 rfl(u32 v){ return __builtin_amdgcn_readfirstlane(v); }
DEVI bool all_eq(u32 v,u32 t){ return __builtin_amdgcn_ballot_w64(v!=t)==0ull; }
DEVI v4u mksrd(const void* p, u32 bytes){ u64 a=(u64)p; v4u s; s.x=rfl((u32)a); s.y=rfl((u32)(a>>32)); s.z=bytes; s.w=0x00020000u; return s; }
#define DM0 "s_add_u32 m0, %[b], %[i]\n\ts_nop 0\n\t"
#define DLD(n,off,pol) "buffer_load_dwordx4 %[v" #n "], %[r], %[s] offen" off pol " lds\n\t"
template<bool SC1,u32 IMM> DEVI void dma4(v4u srd,u32 v0,u32 v1,u32 v2,u32 v3,u32 soff,u32 base){
  if(SC1) asm volatile(DM0 DLD(0,""," sc1") DLD(1," offset:1024"," sc1") DLD(2," offset:2048"," sc1") DLD(3," offset:3072"," sc1")
      :: [v0]"v"(v0),[v1]"v"(v1),[v2]"v"(v2),[v3]"v"(v3),[r]"s"(srd),[s]"s"(soff),[b]"s"(base),[i]"n"(IMM) : "memory","scc");
  else asm volatile(DM0 DLD(0,"","") DLD(1," offset:1024","") DLD(2," offset:2048","") DLD(3," offset:3072","")
      :: [v0]"v"(v0),[v1]"v"(v1),[v2]"v"(v2),[v3]"v"(v3),[r]"s"(srd),[s]"s"(soff),[b]"s"(base),[i]"n"(IMM) : "memory","scc");
}
template<u32 IMM> DEVI void dma3(v4u srd,u32 v0,u32 v1,u32 v2,u32 soff,u32 base){
  asm volatile(DM0 DLD(0,"","") DLD(1," offset:1024","") DLD(2," offset:2048","")
      :: [v0]"v"(v0),[v1]"v"(v1),[v2]"v"(v2),[r]"s"(srd),[s]"s"(soff),[b]"s"(base),[i]"n"(IMM) : "memory","scc");
}
template<u32 IMM> DEVI void dma2(v4u srd,u32 v0,u32 v1,u32 soff,u32 base){
  asm volatile(DM0 DLD(0,"","") DLD(1," offset:1024","")
      :: [v0]"v"(v0),[v1]"v"(v1),[r]"s"(srd),[s]"s"(soff),[b]"s"(base),[i]"n"(IMM) : "memory","scc");
}
template<bool SC1> DEVI void dma_dw(v4u srd,u32 voff,u32 soff,u32 lds){
  if(SC1) asm volatile("s_mov_b32 m0, %3\n\ts_nop 0\n\tbuffer_load_dword %0, %1, %2 offen sc1 lds" :: "v"(voff), "s"(srd), "s"(soff), "s"(lds) : "memory");
  else    asm volatile("s_mov_b32 m0, %3\n\ts_nop 0\n\tbuffer_load_dword %0, %1, %2 offen lds" :: "v"(voff), "s"(srd), "s"(soff), "s"(lds) : "memory");
}
DEVI void dma_x4(v4u srd,u32 voff,u32 soff,u32 lds){
  asm volatile("s_mov_b32 m0, %3\n\ts_nop 0\n\tbuffer_load_dwordx4 %0, %1, %2 offen sc1 lds" :: "v"(voff), "s"(srd), "s"(soff), "s"(lds) : "memory");
}
template<int N> DEVI void waitv(){ asm volatile("s_waitcnt vmcnt(%0)"::"n"(N):"memory"); }
DEVI void waitl0(){ asm volatile("s_waitcnt lgkmcnt(0)":::"memory"); }
DEVI void bar(){ asm volatile("":::"memory"); __builtin_amdgcn_s_barrier(); asm volatile("":::"memory"); SB; }
DEVI u32 peek(u32 vaddr){ return *(volatile LAS u32*)vaddr; }
DEVI bool vm_idle(){ u32 v; asm volatile("s_getreg_b32 %0, hwreg(HW_REG_IB_STS, 0, 4)":"=s"(v)); return v==0u; }
DEVI void flag_st(v4u srd,u32 soff,u32 val){ asm volatile("s_mov_b64 exec, 1\n\tbuffer_store_dword %0, off, %1, %2 sc1\n\ts_mov_b64 exec, -1" :: "v"(val), "s"(srd), "s"(soff) : "memory"); }
DEVI int lane_now(){ int l; asm volatile("v_mbcnt_lo_u32_b32 %0, -1, 0\n\tv_mbcnt_hi_u32_b32 %0, -1, %0":"=v"(l)); return l; }
DEVI float b2f(u16 v){ return u2f(((u32)v)<<16); }
DEVI u32 pk2(float lo,float hi){ v2f p={lo,hi}; return __builtin_bit_cast(u32,__builtin_convertvector(p,v2bf)); }
DEVI float rbf(float f){ return u2f(pk2(f,f)<<16); }
DEVI float relu(float x){ int i=__builtin_bit_cast(int,x); return __builtin_bit_cast(float,i>0?i:0); }
DEVI v16f mfma(v4u a,v4u b,v16f c){ return __builtin_amdgcn_mfma_f32_32x32x16_bf16(__builtin_bit_cast(v8bf,a),__builtin_bit_cast(v8bf,b),c,0,0,0); }
DEVI v4f mfma16(v4u a,v4u b,v4f c){ return __builtin_amdgcn_mfma_f32_16x16x32_bf16(__builtin_bit_cast(v8bf,a),__builtin_bit_cast(v8bf,b),c,0,0,0); }
DEVI v4u ldsr128(u32 a){ return *(LAS v4u*)a; }
DEVI v4u ldstr(u32 a0,u32 a1){
  v4s x=__builtin_amdgcn_ds_read_tr16_b64_v4i16((LAS v4s*)a0);
  v4s y=__builtin_amdgcn_ds_read_tr16_b64_v4i16((LAS v4s*)a1);
  return __builtin_bit_cast(v4u,__builtin_shufflevector(x,y,0,1,2,3,4,5,6,7));
}
DEVI void sw16(float& a,float& b){ v2u r=__builtin_amdgcn_permlane16_swap(f2u(a),f2u(b),false,false); a=u2f(r[0]); b=u2f(r[1]); }
DEVI void sw32(float& a,float& b){ v2u r=__builtin_amdgcn_permlane32_swap(f2u(a),f2u(b),false,false); a=u2f(r[0]); b=u2f(r[1]); }
template<int CTRL> DEVI float dppadd(float v){ return v+u2f(__builtin_amdgcn_update_dpp(0u,f2u(v),CTRL,0xf,0xf,true)); }
DEVI void wsum4(float& a,float& b,float& c,float& d){
  sw32(a,b); sw32(c,d); float v=a+b, t=c+d;
  sw16(v,t); v+=t;
  v=dppadd<0xB1>(v); v=dppadd<0x4E>(v); v=dppadd<0x141>(v); v=dppadd<0x140>(v);
  a=u2f(__builtin_amdgcn_readlane(f2u(v),0)); c=u2f(__builtin_amdgcn_readlane(f2u(v),16));
  b=u2f(__builtin_amdgcn_readlane(f2u(v),32)); d=u2f(__builtin_amdgcn_readlane(f2u(v),48));
}
typedef v2f cf;
DEVI cf cfma(cf a,cf b,cf c){ return cf{__builtin_fmaf(-a.y,b.y,__builtin_fmaf(a.x,b.x,c.x)), __builtin_fmaf(a.y,b.x,__builtin_fmaf(a.x,b.y,c.y))}; }
DEVI cf cmul(cf a,cf b){ return cf{__builtin_fmaf(-a.y,b.y,a.x*b.x), __builtin_fmaf(a.y,b.x,a.x*b.y)}; }
DEVI cf csel(bool c,cf a,cf b){ return cf{c?a.x:b.x, c?a.y:b.y}; }
DEVI cf ldsc(u32 a){ return *(LAS v2f*)a; }
DEVI void csw32(cf& a,cf& b){ float x=a.x,y=b.x,z=a.y,t=b.y; sw32(x,y); sw32(z,t); a=cf{x,z}; b=cf{y,t}; }
DEVI void sincos_(float x,float& s,float& c){
  const float n=__builtin_rintf(x*0.636619772f);
  float r=__builtin_fmaf(n,-1.57079601e+00f,x); r=__builtin_fmaf(n,-3.13916473e-07f,r); r=__builtin_fmaf(n,-5.39030253e-15f,r);
  const float z=r*r;
  float ps=__builtin_fmaf(__builtin_fmaf(__builtin_fmaf(2.86567956e-6f,z,-1.98559923e-4f),z,8.33338592e-3f),z,-1.66666672e-1f);
  ps=__builtin_fmaf(ps*z,r,r);
  float pc=__builtin_fmaf(__builtin_fmaf(__builtin_fmaf(2.44677067e-5f,z,-1.38877297e-3f),z,4.16666567e-2f),z,-0.5f);
  pc=__builtin_fmaf(pc,z,1.f);
  const int q=(int)n;
  const float a=(q&1)?pc:ps, b=(q&1)?ps:pc;
  s=(q&2)?-a:a; c=((q+1)&2)?-b:b;
}
typedef __amdgpu_buffer_rsrc_t rsrc_t;
DEVI rsrc_t mkrsrc(const void* p,u32 bytes){ return __builtin_amdgcn_make_buffer_rsrc((void*)p,0,bytes,0x00020000); }
DEVI void st128(v4u v,rsrc_t r,u32 off){ __builtin_amdgcn_raw_buffer_store_b128(v,r,off,0,16); }
DEVI u32 mktag(u32 e){ return (e*0x9E3779B1u+0x7F4A7C15u)|1u; }

#define BAR() bar()

DEVI void poll32(v4u srd,u32 voff,u32 soff,u32 tag){
  #pragma unroll 1
  for(int it=0;it<SPIN_CAP;++it){
    u32 v;
    asm volatile("buffer_load_dword %0, %1, %2, %3 offen sc1\n\ts_waitcnt vmcnt(0)" : "=&v"(v) : "v"(voff), "s"(srd), "s"(soff) : "memory");
    if(all_eq(v,tag)) break;
    __builtin_amdgcn_s_sleep(12);
  }
}
template<int I,int N,class F> DEVI void sfor(F&& f){ if constexpr(I<N){ f(std::integral_constant<int,I>{}); sfor<I+1,N>(f); } }
AI constexpr u32 wslot(int c){ return WB+(c&3)*WSZ; }
AI constexpr u32 pa_(int c){ return (c&3)==3?WB+5*XS:(((c&3)+2)%3)*XS; }
AI constexpr u32 pb_(int c){ return WB+(c%5)*XS; }
AI constexpr u32 sx_(int c){ return c>=5?(c-5)*XS:wslot(c+2); }
AI constexpr u32 sb_(int c){ return c>=5?wslot(3)+(c-5)*8192:wslot(c+2)+16384; }

#define SG(m,n) __builtin_amdgcn_sched_group_barrier(m,n,0)
template<int NB>
DEVI u32 g_main(v4u x0,v4u x1,u32 e,rsrc_t rws,u32 lbase,int lane,int gw,int tt,int cg,u32 hqo){
  const int h=lane>>5, r=lane&31, rh=gw&1, u0=(gw>>1)*NB;
  const int row=tt*128+cg*8+gw*2;
  v16f acc[3][2]; v4f cw[3];
  u32 ax[4],aw,aw2,nm[2],cwa;
  const u32 on2=((lane&3)==((lane>>4)&1))?0x3f803f80u:0u;
  v4u ones={on2,on2,on2,on2};
  u32 tag;
  {
    u32 ab=lbase+sb_(0)+r*128+((((2*gw+h)^((r>>1)&7)))<<4);
    v4f c0={},c1={};
    sfor<0,8>([&](auto C) AI {
      constexpr int c=C.value;
      if constexpr(c!=0){ waitl0(); BAR(); }
      v4u t0,t1;
      if constexpr(c!=0){ t0=ldsr128(ab); t1=ldsr128(ab+4096); }
      if constexpr(c==0){
        const float z0=u2f(rfl(x0[0])<<16), z1=u2f(rfl(x1[0])<<16);
        float s0=0.f,s1=0.f,v0=0.f,v1=0.f;
        #pragma unroll
        for(int k=0;k<8;++k){
          const float d0=u2f((k&1)?x0[k>>1]&0xffff0000u:x0[k>>1]<<16)-z0, d1=u2f((k&1)?x1[k>>1]&0xffff0000u:x1[k>>1]<<16)-z1;
          s0+=d0; s1+=d1; v0=__builtin_fmaf(d0,d0,v0); v1=__builtin_fmaf(d1,d1,v1); }
        wsum4(s0,s1,v0,v1);
        const float sm=(lane?s1:s0)*(1.f/512.f);
        const u32 vm=pk2(relu(__builtin_fmaf(lane?v1:v0,1.f/512.f,-sm*sm)),sm+(lane?z1:z0));
        float rs=__builtin_amdgcn_rcpf(rbf(__builtin_amdgcn_sqrtf(rbf(u2f(vm<<16)+1e-5f))));
        asm volatile("":"+v"(rs));
        tag=mktag(rfl(e));
        if(lane<2) st128(v4u{f2u(rs),tag,vm&0xffff0000u,tag},rws,WS_FX+row*64+lane*16);
        if(gw==0) *(LAS v2u*)(lbase+L_TAG)=v2u{tag,e};
        waitl0(); BAR(); t0=ldsr128(ab); t1=ldsr128(ab+4096);
      }
      if constexpr(c<7) ab+=sb_(c+1)-sb_(c);
      if constexpr(c>=1 && c<=2*NB){ acc[(c-1)>>1][(c-1)&1]=v16f{}; asm volatile("":"+v"(acc[(c-1)>>1][(c-1)&1])); }
      c0=mfma16(ones,t0,c0); c1=mfma16(ones,t1,c1);
      asm volatile("":"+v"(c0),"+v"(c1),"+v"(ab));
    });
    asm volatile("s_nop 7\n\ts_nop 1":"+v"(c0),"+v"(c1));
    const bool hi=lane&16;
    *(LAS float*)(lbase+L_CSP+(r*8+h*4+gw)*4)=h?(hi?c1[1]:c1[0]):(hi?c0[1]:c0[0]);
    waitl0(); BAR();
  }
  #pragma unroll
  for(int s=0;s<4;++s) ax[s]=lbase+rh*8192+r*128+((((2*(s^(2*rh))+h)^((r>>1)&7)))<<4);
  aw=lbase+WB+((lane>>4)&1)*(12288+32)+(8*h+((lane&15)>>2))*192+(lane&3)*8+u0*32;
  aw2=aw+6144-rh*6144; aw+=rh*6144;
  #pragma unroll
  for(int t=0;t<3;++t){ cw[t]=v4f{}; if(t<NB) asm volatile("":"+v"(cw[t])); }
  #pragma unroll
  for(int m=0;m<2;++m){
    const u32 mu=peek(lbase+L_TAB+(rh*64+m*32+16*((r>>2)&1)+8*(r>>4)+4*((r>>3)&1)+(r&3))*16+8);
    nm[m]=((mu>>16)|mu)^0x80008000u;
  }
  cwa=lbase+L_CWX+(gw*32+r)*16;
  asm volatile("":"+v"(ax[0]),"+v"(ax[1]),"+v"(ax[2]),"+v"(ax[3]),"+v"(aw),"+v"(aw2),"+v"(nm[0]),"+v"(nm[1]),"+v"(cwa));
  BAR();
  __builtin_amdgcn_s_setprio(2);
  v4u X[2][2],T[2][3];
  auto rd=[&](int b,int s) AI {
    const u32 ad=(s<2?aw:aw2)+(s&1)*3072;
    X[b][0]=ldsr128(ax[s]);
    #pragma unroll
    for(int t=0;t<NB;++t){ T[b][t]=ldstr(ad+t*32,ad+t*32+768); if(t==0) X[b][1]=ldsr128(ax[s]+4096); }
  };
  auto mm=[&](int b,bool cs) AI {
    #pragma unroll
    for(int t=0;t<NB;++t){ acc[t][0]=mfma(T[b][t],X[b][0],acc[t][0]); if(cs) cw[t]=mfma16(ones,T[b][t],cw[t]); acc[t][1]=mfma(T[b][t],X[b][1],acc[t][1]); }
  };
  auto mix=[&](int nv,bool cs) AI {
    SG(0x8,1); SG(0x100,2); if(cs) SG(0x8,2); else SG(0x8,1); SG(0x100,2); SG(0x8,1); SG(0x100,2);
    if(NB==3){ if(cs) SG(0x8,2); else SG(0x8,1); SG(0x100,2); }
    SG(0x8,1); if(nv) SG(0x2,3);
    SG(0x8,5);
  };
  u32 d0=XS, d1=XS, d2=(u32)(-2*(int)XS);
  rd(0,0);
  #pragma unroll 1
  for(int c=0;c<8;++c){
    const u32 dw=((c&3)==3)?(u32)(-3*(int)WSZ):WSZ;
    SB; rd(1,1); mm(0,true); ax[0]+=d0; ax[1]+=d0; aw+=dw; mix(1,true);
    SB; asm volatile("":"+v"(ax[0]),"+v"(ax[1]),"+v"(aw));
    rd(0,2); mm(1,true); mix(0,true);
    SB; rd(1,3); mm(0,false); ax[2]+=d0; ax[3]+=d0; aw2+=dw; mix(1,false);
    SB; asm volatile("":"+v"(ax[2]),"+v"(ax[3]),"+v"(aw2));
    { const u32 t=d0; d0=d1; d1=d2; d2=t; }
    if(c==7){
      u32 f[3][2]={};
      #pragma unroll
      for(int t=0;t<NB;++t){
        const float lo=cw[t][0], hi=cw[t][1], cs=(cwa&256u)?hi:lo;
        const u32 c0=f2u(cs)&0xffff0000u; const float r1=cs-u2f(c0);
        const u32 c1=f2u(r1)&0xffff0000u; const float r2=r1-u2f(c1);
        f[t][0]=(c0>>16)|c1; f[t][1]=f2u(r2)>>16;
      }
      *(LAS v4u*)cwa=v4u{f[0][0],f[0][1],f[1][0],f[1][1]};
      if(NB==3) *(LAS v2u*)(cwa+4096)=v2u{f[2][0],f[2][1]};
      ax[0]=cwa^((u32)(lane_now()&32)<<4);
    }
    waitl0(); BAR();
    rd(0,0); mm(1,false); mix(0,false);
  }
  const int ln=lane_now();
  #pragma unroll
  for(int t=0;t<NB;++t)
    #pragma unroll
    for(int m=0;m<2;++m) acc[t][m]=mfma(v4u{X[0][t>>1][2*(t&1)],X[0][t>>1][2*(t&1)+1],0u,0u},v4u{nm[m],nm[m],0u,0u},acc[t][m]);
  #pragma unroll
  for(int t=0;t<NB;++t)
    #pragma unroll
    for(int m=0;m<2;++m){
      SB;
      v4u v;
      #pragma unroll
      for(int k=0;k<4;++k) v[k]=pk2(relu(acc[t][m][2*k])*acc[t][m][2*k+8],relu(acc[t][m][2*k+1])*acc[t][m][2*k+9]);
      st128(v,rws,hqo+(u0+t)*2048+m*1024+ln*16);
    }
  return tag;
}

__global__ NOPK __launch_bounds__(768,3) void fused_k(Args a){
  __shared__ __attribute__((aligned(256))) char smem[L_TOTAL];
  const u32 lbase=(u32)(size_t)(LAS char*)smem;
  const int tid=__builtin_amdgcn_workitem_id_x();
  const int wgid=__builtin_amdgcn_workgroup_id_x(), w=rfl(tid>>6);
  int lane=tid&63;
  const int xcd=wgid&7, idx=wgid>>3;
  if(w==11){
    u64 pc; u32 back; asm volatile("s_getpc_b64 %0\n.Lpcx%=:\n\ts_mov_b32 %1, .Lpcx%=-_Z7fused_k4Args":"=&s"(pc),"=s"(back));
    dma_dw<false>(mksrd((const void*)(pc-back),CODE_LEN),(u32)(((idx&3)*64+lane)*128),0,lbase+L_DUM);
  }
  const int tt=(xcd&1)*8+(idx&7), cg=(xcd>>1)*4+(idx>>3);
  const int nb=((cg&3)==3||cg==14)?2:3, blk0=11*(cg>>2)+3*(cg&3)-(cg==15?1:0);
  const int rt2=(xcd&3)*8+(idx&7), ct=(xcd>>2)*4+(idx>>3);
  const int ry=idx&7, rz=6*((idx>>3)&1);
  v4u x0,x1; u32 ep0,dsk_;
  const u16* dp=a.dsk+ct*64+((w>>1)&1)*32+(lane&31);
  if(w<4){ const int c_=cg*32+(lane&31); x0[0]=a.lre[c_]; x0[1]=a.lim[c_]; x0[2]=a.ldt[c_]; dsk_=*dp; }
  else if(w<8){ const int row=tt*128+cg*8+(w-4)*2; x0=((const v4u*)a.x)[row*64+lane]; x1=((const v4u*)a.x)[row*64+64+lane];
    ep0=__builtin_amdgcn_raw_buffer_load_b32(mkrsrc(a.ws,WS_END),0,WS_EPOCH,16); dsk_=*dp; }
  asm volatile("":::"memory");
  if(__builtin_expect(w>=8,1)){
    __builtin_amdgcn_s_setprio(3);
    const int lw=w-8, j=lane>>3, l7=lane&7;
    const v4u sx=mksrd(a.x,2048*1024);
    const v4u sb=mksrd(lw<2?a.bre:a.bim,512*1024);
    const u32 sz0=(u32)((l7^(j>>1))<<4), sz1=sz0^64u;
    const u32 xr=(tt*128+32*lw+16*(j>>2)+(j&3))*1024;
    const u32 vx0=xr+sz0, vx1=xr+3*1024+sz1, vx2=xr+6*1024+sz0, vx3=xr+9*1024+sz1;
    const u32 br=(cg*32+16*(lw&1)+j)*1024;
    const u32 vb0=br+sz0, vb1=br+7*1024+sz1;
    const u32 l4=lbase+lw*4096, l2=lbase+lw*2048;
    u32 kx=(tt&7)*128, kw=(kx+640)&0x380;
    #define SX(c) dma4<false,sx_(c)>(sx,vx0,vx1,vx2,vx3,kx,l4)
    #define SBB(c) do{ dma2<sb_(c)>(sb,vb0,vb1,kx,l2); kx=(kx+128)&0x380; }while(0)
    SX(0); SBB(0); SX(1); SBB(1);
    const v4u sws=mksrd(a.ws,WS_END);
    const v4u sw=mksrd(lw<2?a.wg:a.wu,512*2752);
    const v4u swd=mksrd(a.wd,1376*1024);
    const v4u sc=mksrd(lw<2?a.cre:a.cim,512*1024);
    const u32 vf=(lane&15)*128, lf=lbase+L_FLG+lw*256;
    const u32 fx=WS_FX+(tt*64+lw*16)*128, fh=WS_FH+(rt2*32+(lw&1)*16)*128, fhh=WS_FHH+rt2*4096;
    u32 vw[6];
    #pragma unroll
    for(int i=0;i<3;++i){ const u32 q=i*1024+lane*16, row=q/192, col=q%192; const bool ok=col<(u32)nb*64;
      vw[i]=ok?(row+32*(lw&1))*2752+col+blk0*64-i*1024:OOB; vw[i+3]=vw[i]+16*2752; asm volatile("":"+v"(vw[i]),"+v"(vw[i+3])); }
    const u32 lwv=lbase+(lw>>1)*(12288+32)+(lw&1)*6144;
    #define GWA(c) dma3<wslot(c)>(sw,vw[0],vw[1],vw[2],kw*1376,lwv)
    #define GWB(c) do{ dma3<wslot(c)+3072>(sw,vw[3],vw[4],vw[5],kw*1376,lwv); kw=(kw+128)&0x380; }while(0)
    #define TOUCHD() dma_dw<false>(swd,lane<43?(u32)(lane*1024):OOB,((idx&7)*172+lw*43)*1024+ct*128,lbase+L_DUM)
    #define GX(c) do{ dma4<false,((c)%3)*XS>(sx,vx0,vx1,vx2,vx3,kx,l4); kx=(kx+128)&0x380; }while(0)
    SX(2); waitv<10>(); BAR();
    SBB(2); SX(3); waitv<10>(); BAR();
    SBB(3); SX(4); waitv<10>(); BAR();
    SBB(4); SX(5); waitv<10>(); BAR();
    SBB(5); SX(6); SBB(6); waitv<12>(); BAR();
    SX(7); SBB(7); waitv<12>(); BAR();
    const u32 vs=(lane>>1)*128+(lane&1)*16, lt=lbase+L_TAB+lw*512;
    const u32 tag=rfl(peek(lbase+L_TAG));
    if(lane<32) dma_x4(sws,vs,fx,lt);
    GWA(0); waitv<10>(); BAR();
    GWB(0); waitv<7>(); BAR();
    waitv<6>();
    #pragma unroll 1
    for(int it=0;it<SPIN_CAP;++it){
      const v4u t=*(volatile LAS v4u*)(lt+(lane&31)*16);
      if(all_eq((t[1]^tag)|(t[3]^tag),0u)) break;
      __builtin_amdgcn_s_sleep(2);
      if(lane<32) dma_x4(sws,vs,fx,lt);
      waitv<0>();
    }
    BAR();
    GWA(1); GWB(1);
    TOUCHD();
    waitv<7>(); BAR();
    GWA(2); GWB(2); GWA(3); GWB(3);
    u32 va=WS_HS+rt2*131072+lw*4096+lane*16;
    const u32 cr=(ct*64+(lw&1)*32+j)*1024;
    u32 vp0=cr+sz0, vp1=cr+7*1024+sz1, vp2=cr+14*1024+sz0, vp3=cr+21*1024+sz1;
    const int gp0=tt-1-4*lw-(lane>>5);
    const u32 vg0=gp0>=0?(u32)(WS_GRAN+gp0*8192+(cg*32+(lane&31))*16):OOB, vg1=gp0>=2?vg0-2*8192-1024:OOB;
    #define SNAP() asm volatile("s_add_u32 m0, %[b], %[i]\n\ts_nop 0\n\tbuffer_load_dwordx4 %[v0], %[r], 0 offen sc1 lds\n\tbuffer_load_dwordx4 %[v1], %[r], 0 offen offset:1024 sc1 lds" \
        :: [v0]"v"(vg0),[v1]"v"(vg1),[r]"s"(sws),[b]"s"(l2),[i]"n"(L_GR) : "memory","scc")
    waitv<13>(); BAR();
    GX(3); GWA(4); GWB(4); SNAP(); waitv<18>(); BAR();
    GX(4); GWA(5); GWB(5); waitv<18>(); SNAP(); BAR();
    GX(5); GWA(6); GWB(6); waitv<18>(); SNAP(); BAR();
    GX(6); GWA(7); GWB(7); waitv<18>(); BAR();
    u32 ka=ry, kb=ry;
    auto PA=[&](auto C) AI {
      constexpr int c=C.value;
      if constexpr(c==8){ va=WS_HH+rt2*86*2048+lw*4096+lane*16; ka=rz; }
      u32 so;
      if constexpr(c<8){ so=ka<<14; ka=(ka+1)&7; } else { so=(ka==10&&lw==3)?OOB:ka<<14; ka= ka==10?0:ka+1; }
      dma4<true,pa_(c)>(sws,va,va,va,va,so,l4);
    };
    auto PB=[&](auto C) AI {
      constexpr int c=C.value;
      if constexpr(c==8){ vp0=(lw*32+j)*1024+ct*128+((l7^(((j>>1)&1)<<2))<<4); vp1=vp0+7*1024; vp2=vp0+14*1024; vp3=vp0+21*1024; kb=rz; }
      u32 so;
      if constexpr(c<8){ so=kb<<7; kb=(kb+1)&7; } else { so=kb<<17; kb= kb==10?0:kb+1; }
      dma4<false,pb_(c)>(c<8?sc:swd,vp0,vp1,vp2,vp3,so,l4);
    };
    #define IC(n) std::integral_constant<int,(n)>{}
    GX(7); PB(IC(0));
    waitv<14>(); BAR();
    PB(IC(1)); PB(IC(2));
    waitv<12>(); BAR();
    dma_dw<true>(sws,vf,fh,lf);
    PB(IC(3));
    waitv<4>();
    const bool early=all_eq(peek(lf+lane*4),tag);
    if(early){ PA(IC(0)); PA(IC(1)); }
    BAR();
    if(!early){ poll32(sws,vf,fh,tag); PA(IC(0)); PA(IC(1)); }
    PA(IC(2)); PA(IC(3)); PB(IC(4));
    waitv<16>(); BAR();
    if(lane<16) dma_x4(sws,vs,WS_FX+(rt2*32+lw*8)*128,lbase+L_TAB+lw*256);
    { const u32 vt=(rt2*64+lw*16+j)*1024+ct*128+l7*16; dma2<L_XT>(sx,vt,vt+7*1024,0,l2); }
    waitv<15>(); BAR();
    waitv<11>(); BAR();
    bool zok=false;
    sfor<2,18>([&](auto K) AI {
      constexpr int k=K.value;
      if constexpr(k==5) dma_dw<true>(sws,(lane&31)*128,fhh,lf);
      if constexpr(k==9){ if(wgid==0 && lw==0) dma_dw<true>(sws,vf*64,WS_FH,lf); }
      if constexpr(k==6){
        waitv<8>();
        zok=all_eq(peek(lf+lane*4),tag);
        if(zok) PA(IC(8));
        PB(IC(9));
        if(zok) waitv<12>(); else waitv<8>();
      } else if constexpr(k==7){
        if(!zok){ poll32(sws,(lane&31)*128,fhh,tag); PA(IC(8)); }
        PA(IC(9)); PB(IC(10));
        if(zok) waitv<12>(); else waitv<8>();
      } else {
        if constexpr(k<=16) PA(IC(k+2));
        if constexpr(k<=15) PB(IC(k+3));
        waitv<(k==2?15:k<=15?12:k==16?8:0)>();
      }
      if constexpr(k==12){
        if(wgid==0 && lw==0){
          if(!all_eq(peek(lf+lane*4),tag)) poll32(sws,vf*64,WS_FH,tag);
          const u32 e=peek(lbase+L_TAG+4)+1u;
          asm volatile("s_mov_b64 exec, 1\n\tbuffer_store_dword %0, off, %1, 0 sc1\n\ts_mov_b64 exec, -1" :: "v"(e), "s"(sws) : "memory");
        }
      }
      BAR();
    });
    return;
  }

  int h=lane>>5, r=lane&31;
  const rsrc_t rws=mkrsrc(a.ws,WS_END);
  const v4u sws=mksrd(a.ws,WS_END);
  const int m2=w&1, tc=(w>>1)&1, kh=w>>2;
  u32 a0,b0;
  { const int q=(lane&15)>>2, p=lane&3, gp=(((lane>>4)&1)^kh)+2*(p&1);
    a0=lbase+pa_(0)+(kh*8+m2)*1024+(gp*16+((8*h+q)^(4*gp)))*16+8*(p>>1);
    b0=lbase+pb_(0)+kh*8192+(tc*32+r)*128+((h^((r>>1)&7))<<4); }
  asm volatile("":"+v"(a0),"+v"(b0));
  u32 pend=0;
  u32 tag;
  auto try_flag=[&](bool force) AI {
    if(pend && (force || vm_idle())){
      waitv<0>();
      flag_st(sws,pend,tag);
      pend=0;
    }
  };
  if(w>=4){
    const int gw=w-4, rt=2*tt+(w&1);
    __builtin_amdgcn_s_setprio(3);
    const u32 hqo=WS_HH+(rt*86+2*blk0)*2048;
    if(nb==3) tag=g_main<3>(x0,x1,ep0,rws,lbase,lane,gw,tt,cg,hqo); else tag=g_main<2>(x0,x1,ep0,rws,lbase,lane,gw,tt,cg,hqo);
    pend=WS_FHH+(rt*32+cg*2+(gw>>1))*128;
  } else {
    const int ch=cg*32+r;
    __builtin_amdgcn_s_setprio(2);
    const u16 hlr=(u16)x0[0], hli=(u16)x0[1], hdt=(u16)x0[2];
    *(LAS v4u*)(lbase+L_GR+w*2048+lane*16)=v4u{0u,0u,0u,0u}; *(LAS v4u*)(lbase+L_GR+w*2048+1024+lane*16)=v4u{0u,0u,0u,0u};
    const float lr=b2f(hlr), li=b2f(hli), dt=rbf(__builtin_expf(b2f(hdt)));
    cf lam,coef;
    { const float ex=__builtin_expf(dt*lr); float sn,cs_; sincos_(dt*li,sn,cs_); lam=cf{ex*cs_,ex*sn};
      const float dd=1.f/(lr*lr+li*li); coef=cmul(cf{lam.x-1.f,lam.y},cf{lr*dd,-li*dd}); }
    cf l16=cmul(lam,lam); l16=cmul(l16,l16); l16=cmul(l16,l16); l16=cmul(l16,l16);
    cf l32=cmul(l16,l16);
    cf l256,wq,pw;
    { const cf l64=cmul(l32,l32), l128=cmul(l64,l64), o={1.f,0.f};
      l256=cmul(l128,l128); wq=csel(h,l128,o); pw=cmul(csel(w&1,l32,o),csel(w&2,l64,o)); }
    asm volatile("":"+v"(lam),"+v"(coef),"+v"(l16),"+v"(l32),"+v"(l256),"+v"(wq),"+v"(pw));
    v16f are={},aim={};
    {
      u32 ax[4],ab[4];
      #pragma unroll
      for(int s=0;s<4;++s){ const u32 t=lbase+r*128+((((2*s+h)^((r>>1)&7)))<<4); ab[s]=t+sb_(0); ax[s]=t+sx_(0)+w*4096; }
      v4u X[2][2],T[2][2][2];
      auto rd=[&](int b) AI {
        #pragma unroll
        for(int s=0;s<2;++s){ X[b][s]=ldsr128(ax[2*b+s]); T[b][s][0]=ldsr128(ab[2*b+s]); T[b][s][1]=ldsr128(ab[2*b+s]+4096); } };
      auto mm=[&](int b) AI {
        #pragma unroll
        for(int s=0;s<2;++s){ are=mfma(X[b][s],T[b][s][0],are); aim=mfma(X[b][s],T[b][s][1],aim); }
        asm volatile("":"+v"(are),"+v"(aim)); };
      waitl0(); BAR();
      rd(0);
      sfor<0,8>([&](auto C) AI {
        constexpr int c=C.value;
        SB; rd(1);
        SB; mm(0);
        if constexpr(c<7){
          #pragma unroll
          for(int s=0;s<4;++s){ ax[s]+=sx_(c+1)-sx_(c); ab[s]+=sb_(c+1)-sb_(c); asm volatile("":"+v"(ax[s]),"+v"(ab[s])); }
          SB;
          waitl0(); BAR();
          rd(0);
        }
        SB; mm(1);
      });
    }
    tag=rfl(peek(lbase+L_TAG));
    u32 ta=lbase+L_TAB+(32*w+16*h)*16, tb=lbase+L_CSP+r*32, tg=lbase+L_AGG+r*8;
    asm volatile("":"+v"(ta),"+v"(tb),"+v"(tg));
    waitl0(); BAR();
    __builtin_amdgcn_s_setprio(3);
    int left=8;
    #define SBAR() do{ if(left>0){ BAR(); --left; } else { __builtin_amdgcn_s_sleep(4); asm volatile("":::"memory"); } }while(0)
    cf uu[16], elo, Q;
    {
      const v4f c0=*(LAS v4f*)tb, c1=*(LAS v4f*)(tb+16);
      float rs[16],mu[16];
      #pragma unroll
      for(int k=0;k<16;++k){ rs[k]=*(LAS float*)(ta+k*16); mu[k]=*(LAS float*)(ta+k*16+8); }
      SB;
      const cf ncs={-((c0[0]+c0[1])+(c0[2]+c0[3])),-((c1[0]+c1[1])+(c1[2]+c1[3]))};
      cf ea={0.f,0.f};
      #pragma unroll
      for(int k=0;k<16;++k){
        uu[k]=cf{__builtin_fmaf(mu[k],ncs.x,are[k]),__builtin_fmaf(mu[k],ncs.y,aim[k])}*rs[k];
        ea= k? cfma(lam,ea,uu[k]) : uu[0];
      }
      cf ehi=ea; elo=ea; csw32(elo,ehi);
      *(LAS v2f*)(tg+w*256)=cfma(l16,elo,ehi);
      const float hf=(float)h;
      elo*=hf; l16=cf{__builtin_fmaf(hf,l16.x,1.f-hf),hf*l16.y};
      asm volatile("":"+v"(elo),"+v"(l16));
    }
    waitl0(); BAR();
    { const cf a0=ldsc(tg), a1=ldsc(tg+256), a2=ldsc(tg+512), a3=ldsc(tg+768);
      const cf Q2=cfma(l32,a0,a1), Q3=cfma(l32,Q2,a2);
      if(w==3){ const cf E=cfma(l32,Q3,a3); if(lane<32) st128(v4u{f2u(E.x),tag,f2u(E.y),tag},rws,ch*16+WS_GRAN+tt*8192); }
      Q= w==0?cf{0.f,0.f}: w==1?a0: w==2?Q2:Q3; }
    cf K0=cmul(coef,cfma(l16,Q,elo)), K1=cmul(coef,cmul(l16,pw));
    asm volatile("":"+v"(K0),"+v"(K1));
    SBAR();
    #pragma unroll
    for(int k=0;k<16;++k){
      uu[k]=cf{__builtin_fmaf(-coef.y,uu[k].y,coef.x*uu[k].x),__builtin_fmaf(-coef.y,uu[k].x,-coef.x*uu[k].y)};
      asm volatile("":"+v"(uu[k])); if(k==7 && tt>0) SBAR();
    }
    const int rt=2*tt+(w>>1);
    u32 hso=WS_HS+rt*131072+(cg>>1)*16384+(((cg&1)*2+(r>>4))*2+(w&1))*1024+h*512+(((lane&15)^(8*h))<<4), hs2=(hso^64)+256, ga=lbase+L_GR+lane*16;
    asm volatile("":"+v"(hso),"+v"(hs2),"+v"(ga));
    if(tt>0){
      u32 mk[8];
      #pragma unroll
      for(int i=0;i<8;++i){ mk[i]=~(u32)((tt-1-2*i-h)>>31); asm volatile("":"+v"(mk[i])); }
      #pragma unroll 1
      for(int it=0;it<SPIN_CAP;++it){
        if(__builtin_expect(it>=3,0)){
          #pragma unroll 1
          for(int i=2*w;i<2*w+2;++i){ const int pd=tt-1-2*i-h; dma_x4(sws,pd>=0?(u32)(WS_GRAN+pd*8192+ch*16):OOB,0,lbase+L_GR+i*1024); }
        }
        SBAR();
        if(it>=3) waitv<0>();
        v4u t[8];
        #pragma unroll
        for(int i=0;i<8;++i) t[i]=*(LAS v4u*)(ga+i*1024);
        SB;
        u32 bad=0;
        #pragma unroll
        for(int i=0;i<8;++i) bad|=((t[i][1]^tag)|(t[i][3]^tag))&mk[i];
        if(all_eq(bad,0u)) break;
      }
      asm volatile("":::"memory");
      cf gv[8];
      { v4f t[8];
        #pragma unroll
        for(int i=7;i>=0;--i) t[i]=*(LAS v4f*)(ga+i*1024);
        SB;
        #pragma unroll
        for(int i=0;i<8;++i) gv[i]=cf{t[i][0],t[i][2]}; }
      cf H=gv[7];
      #pragma unroll
      for(int i=6;i>=0;--i) H=cfma(l256,H,gv[i]);
      H=cmul(H,wq);
      cf h2=H; csw32(H,h2); H+=h2;
      K0=cfma(K1,H,K0);
    }
    cf hc={K0.x,-K0.y};
    const cf lc={lam.x,-lam.y};
    #pragma unroll
    for(int m=0;m<2;++m){
      cf hv[8];
      #pragma unroll
      for(int jj=0;jj<8;++jj){ hc=cfma(lc,hc,uu[8*m+jj]); hv[jj]=hc; }
      st128(v4u{pk2(hv[0].x,hv[1].x),pk2(hv[2].x,hv[3].x),pk2(hv[4].x,hv[5].x),pk2(hv[6].x,hv[7].x)},rws,m?hs2:hso);
      st128(v4u{pk2(hv[0].y,hv[1].y),pk2(hv[2].y,hv[3].y),pk2(hv[4].y,hv[5].y),pk2(hv[6].y,hv[7].y)},rws,(m?hs2:hso)+8192);
    }
    pend=WS_FH+(rt*32+(cg&1)*16+(cg>>1)*2+(w&1))*128;
    #pragma unroll 1
    while(left>0){
      BAR(); --left;
      #pragma unroll 1
      for(int it=0;it<10 && pend;++it){ try_flag(false); if(pend) __builtin_amdgcn_s_sleep(1);  }
    }
    if(pend){ try_flag(true); }
  }
  lane=lane_now(); h=lane>>5; r=lane&31;
  const int g=lane>>4;
  const int dcol=ct*64+tc*32+r;
  asm volatile("":"+v"(dsk_));
  const float dsk=u2f(dsk_<<16);
  v16f ay={},az={};
  const int q=(lane&15)>>2, p=lane&3, b4=g&1;
  u32 za=lbase+pa_(8)+(kh*8+m2)*1024+(lane^(kh*16))*16;
  u32 zb=lbase+pb_(8)+(64*kh+4*h+q)*128+((tc*64+b4*32+8*p)^(((q>>1)&1)<<6));
  u32 d0=pa_(1)-pa_(0), d1=pa_(2)-pa_(1), d2=pa_(3)-pa_(2), d3=pa_(0)-pa_(3), e0=XS; int eb=0;
  v4u A[2][4],B[2][4];
  auto rot=[&]() AI { const u32 t=d0; d0=d1; d1=d2; d2=d3; d3=t; eb= eb==4?0:eb+1; e0= eb==4?(u32)(-4*(int)XS):XS; };
  auto rdy=[&](int b) AI {
    #pragma unroll
    for(int s=0;s<4;++s){ A[b][s]=ldstr(a0+s*2048,(a0^64)+s*2048); B[b][s]=ldsr128(b0^(s*32)); }
    a0+=d0; b0+=e0; rot();
  };
  auto rdz=[&](int b) AI {
    #pragma unroll
    for(int s=0;s<4;++s){ A[b][s]=ldsr128(za+s*2048); B[b][s]=ldstr(zb+s*2048,zb+s*2048+1024); }
    za+=d0; zb+=e0; rot();
  };
  auto mm4=[&](int b,v16f& c) AI {
    #pragma unroll
    for(int s=0;s<4;++s) c=mfma(A[b][s],B[b][s],c);
    #pragma unroll
    for(int s=0;s<4;++s){ SG(0x8,1); SG(0x100,3); }
    SB; asm volatile("":"+v"(c));
  };
  #define NXT() BAR()
  const int tk=32*m2+16*h+8*kh;
  __builtin_amdgcn_s_setprio(0);
  NXT(); rdy(0);
  #pragma unroll 1
  for(int c=0;c<3;++c){
    NXT(); rdy(1); mm4(0,ay); try_flag(false);
    NXT(); rdy(0); mm4(1,ay); try_flag(c==2);
  }
  NXT(); rdy(1); mm4(0,ay);
  NXT(); rdz(0); mm4(1,ay);
  float sv[8], xf[8], r2[8];
  u32 pt=lbase+L_TAB+tk*16, px=lbase+L_XT+tk*128+(tc*32+r)*2;
  asm volatile("":"+v"(pt),"+v"(px));
  #pragma unroll 1
  for(int c=0;c<4;++c){
    NXT(); rdz(1); mm4(0,az);
    if(c==1){
      #pragma unroll
      for(int k=0;k<8;++k){ r2[k]=*(LAS float*)(pt+k*16); sv[k]=*(LAS float*)(pt+k*16+8); xf[k]=u2f(*(LAS u16*)(px+k*128)); }
    }
    NXT(); rdz(0); mm4(1,az);
    if(c==1){
      #pragma unroll
      for(int k=0;k<8;++k){ xf[k]=u2f(f2u(xf[k])<<16); sv[k]=(xf[k]-sv[k])*r2[k]*dsk; r2[k]*=r2[k]; }
    }
  }
  {
    const u32 xo=lbase+WB+w*2048+lane*16, xi=lbase+WB+(w^4)*2048+lane*16;
    const rsrc_t ro=mkrsrc(a.out,2048*512*4);
    u32 oo=((rt2*64+tk)*512+dcol)*4;
    NXT(); rdz(1); mm4(0,az);
    *(LAS v4f*)xo=v4f{ay[8],ay[9],ay[10],ay[11]}; *(LAS v4f*)(xo+1024)=v4f{ay[12],ay[13],ay[14],ay[15]};
    SB;
    #pragma unroll
    for(int s=0;s<4;++s) az=mfma(A[1][s],B[1][s],az);
    asm volatile("":"+v"(az),"+v"(oo));
    waitl0(); NXT(); rdz(0);
    const v4f i0=*(LAS v4f*)xi, i1=*(LAS v4f*)(xi+1024);
    SB;
    #pragma unroll
    for(int s=0;s<4;++s) az=mfma(A[0][s],B[0][s],az);
    float ce[8];
    #pragma unroll
    for(int k=0;k<8;++k){ sv[k]=__builtin_fmaf(2.f,ay[k]+(k<4?i0[k&3]:i1[k&3]),sv[k])*r2[k]; ce[k]=r2[k]*-1.44269504f; asm volatile("":"+v"(sv[k]),"+v"(ce[k])); }
    *(LAS v4f*)(xo+XS)=v4f{az[8],az[9],az[10],az[11]}; *(LAS v4f*)(xo+XS+1024)=v4f{az[12],az[13],az[14],az[15]};
    waitl0(); bar();
    const v4f i2=*(LAS v4f*)(xi+XS), i3=*(LAS v4f*)(xi+XS+1024);
    #pragma unroll
    for(int k=0;k<8;++k){
      const float z=az[k]+(k<4?i2[k&3]:i3[k&3]);
      const float gt=z*__builtin_amdgcn_rcpf(1.f+__builtin_amdgcn_exp2f(z*ce[k]));
      __builtin_amdgcn_raw_buffer_store_b32(f2u(__builtin_fmaf(sv[k],gt,xf[k])),ro,oo+k*2048,0,16);
    }
  }
}

extern "C" void kernel_launch(void* const* d_in, const int* in_sizes, int n_in,
                              void* d_out, int out_size, void* d_ws, size_t ws_size,
                              hipStream_t stream) {
  Args a;
  a.x=(const u16*)d_in[0]; a.lre=(const u16*)d_in[1]; a.lim=(const u16*)d_in[2];
  a.bre=(const u16*)d_in[3]; a.bim=(const u16*)d_in[4]; a.cre=(const u16*)d_in[5]; a.cim=(const u16*)d_in[6];
  a.ldt=(const u16*)d_in[7]; a.dsk=(const u16*)d_in[8];
  a.wg=(const u16*)d_in[10]; a.wu=(const u16*)d_in[11]; a.wd=(const u16*)d_in[12];
  a.out=(float*)d_out; a.ws=(char*)d_ws;
  fused_k<<<256,768,0,stream>>>(a);
}
```
